```python
import torch

def _softmax(x, axis=-1):
    return torch.softmax(x, dim=axis)

def _gelu(x):
    return torch.nn.functional.gelu(x, approximate='tanh')

def _silu(x):
    return torch.nn.functional.silu(x)

def _sigmoid(x):
    return torch.sigmoid(x)

def _layernorm(x, axis=-1, eps=1e-05):
    m = x.mean(dim=axis, keepdim=True)
    v = x.var(dim=axis, keepdim=True, unbiased=False)
    return (x - m) / torch.sqrt(v + eps)

def _scatter_add(tgt, idx, src, axis=0):
    return tgt.clone().index_add_(axis, idx, src)

def _segment_sum(src, seg, n):
    out = torch.zeros((n,) + src.shape[1:], dtype=src.dtype)
    return out.index_add_(0, seg, src)

def _pad(x, pw):
    flat = tuple((v for pair in reversed(pw) for v in pair))
    return torch.nn.functional.pad(x, flat)
kernel_name = 'recurrent_scan__ssm_conv_h3__mixer_gmlp__prompt'

def setup_inputs():

    _w = lambda a: (lambda t: t.to(torch.bfloat16) if t.is_floating_point() else t)(torch.as_tensor(a))

    def _A_setup():
        B, S, D = (1, 2048, 448)
        ks = 4
        return {'q': _w((lambda *s: torch.randn(*s, dtype=torch.bfloat16) * 0.05)(B, S, D)), 'k': _w((lambda *s: torch.randn(*s, dtype=torch.bfloat16) * 0.2)(B, S, D)), 'v': _w((lambda *s: torch.randn(*s, dtype=torch.bfloat16) * 0.2)(B, S, D)), 'shift_k': _w((lambda *s: torch.randn(*s, dtype=torch.bfloat16) * 0.2)(D, ks) * 0.5), 'ssm_k': _w((lambda *s: torch.randn(*s, dtype=torch.bfloat16) * 0.2)(D, S) * (1.0 / S ** 0.5)), 'ssm_D': _w((lambda *s: torch.randn(*s, dtype=torch.bfloat16) * 0.2)(D))}
    _A_in = _A_setup()

    def _B_setup():
        B, S, D, F = (1, 2048, 448, 1792)
        Fh = F // 2
        return {'x': _w((lambda *s: torch.randn(*s, dtype=torch.bfloat16) * 0.05)(B, S, D)), 'w_in': _w((lambda *s: torch.randn(*s, dtype=torch.bfloat16) * 0.8)(D, 2 * Fh)), 'w_sp': _w(0.01 * (lambda *s: torch.randn(*s, dtype=torch.bfloat16) * 0.8)(S, S)), 'b_sp': _w(1.0 + 0.01 * (lambda *s: torch.randn(*s, dtype=torch.bfloat16) * 0.1)(S)), 'w_out': _w((lambda *s: torch.randn(*s, dtype=torch.bfloat16) * 0.1)(Fh, D))}
    _B_in = _B_setup()
    return {'A_q': _A_in['q'], 'A_k': _A_in['k'], 'A_v': _A_in['v'], 'A_shift_k': _A_in['shift_k'], 'A_ssm_k': _A_in['ssm_k'], 'A_ssm_D': _A_in['ssm_D'], 'B_x': _B_in['x'], 'B_w_in': _B_in['w_in'], 'B_w_sp': _B_in['w_sp'], 'B_b_sp': _B_in['b_sp'], 'B_w_out': _B_in['w_out']}

def reference(A_q, A_k, A_v, A_shift_k, A_ssm_k, A_ssm_D, B_x, B_w_in, B_w_sp, B_b_sp, B_w_out):

    def _A_ref(q, k, v, shift_k, ssm_k, ssm_D):
        B, S, D = q.shape
        ks = shift_k.shape[-1]
        k_pad = _pad(k, ((0, 0), (ks - 1, 0), (0, 0)))
        k_shift = torch.zeros_like(k)
        for t in range(ks):
            k_shift = k_shift + k_pad[:, t:t + S, :] * shift_k[:, ks - 1 - t][None, None, :]
        kv = k_shift * v
        L = 2 * S
        kv_pad = _pad(kv, ((0, 0), (0, S), (0, 0)))
        kf_pad = _pad(ssm_k, ((0, 0), (0, S)))
        kv_f = (lambda x, axes=(-2, -1): torch.fft.rfft2(x.float(), dim=axes))(kv_pad[:, None, :, :], axes=(1, 2))
        kf_f = (lambda x, axes=(-2, -1): torch.fft.rfft2(x.float(), dim=axes))(kf_pad[:, None, :], axes=(1, 2))
        kf_f = (lambda x, *d: x.permute(*d))(kf_f, 1, 2, 0)
        y_f = kv_f * kf_f[None, :, :, :]
        y = (lambda x, s, axes=(-2, -1): torch.fft.irfft2(x, s=s, dim=axes).to(torch.bfloat16))(y_f, (1, L), axes=(1, 2))[:, 0, :S, :]
        y = y + kv * ssm_D[None, None, :]
        return q * y

    def _B_ref(x, w_in, w_sp, b_sp, w_out):
        B, S, D = x.shape
        F = w_in.shape[-1]
        Fh = F // 2
        z = _gelu(torch.einsum('bsd,df->bsf', x, w_in))
        z1 = z[..., :Fh].clone()
        z2 = _layernorm(z[..., Fh:], axis=-1)
        g = torch.einsum('ts,bsf->btf', w_sp, z2) + b_sp[None, :, None]
        return torch.einsum('bsf,fd->bsd', z1 * g, w_out)
    x = A_q
    xn = _layernorm(x, axis=-1)
    s = _A_ref(xn, A_k, A_v, A_shift_k, A_ssm_k, A_ssm_D)
    g = _silu(_B_ref(xn, B_w_in, B_w_sp, B_b_sp, B_w_out))
    return x + s * g
```

```cpp
#include <hip/hip_runtime.h>
#include <hip/hip_bf16.h>
#include <stdint.h>

typedef unsigned short u16;
static __device__ __forceinline__ float bf2f(u16 b){ return __uint_as_float(((unsigned)b)<<16); }
static __device__ __forceinline__ u16 f2bf(float f){ __hip_bfloat16 h = __float2bfloat16(f); return *(u16*)&h; }
static __device__ __forceinline__ float rbf(float f){ return bf2f(f2bf(f)); }

constexpr int S=2048, D=448, F=1792, FH=896;

__global__ void ln_kernel(const u16* in, int ld_in, u16* out, int ld_out, int n){
  int row = blockIdx.x; int lane = threadIdx.x;
  float s=0; for(int i=lane;i<n;i+=64) s+=bf2f(in[row*ld_in+i]);
  for(int o=32;o;o>>=1) s+=__shfl_xor(s,o);
  float m=s/n; float q=0;
  for(int i=lane;i<n;i+=64){ float d=bf2f(in[row*ld_in+i])-m; q+=d*d; }
  for(int o=32;o;o>>=1) q+=__shfl_xor(q,o);
  float v=rbf(q/n), mb=rbf(m);
  float sd=rbf(sqrtf(rbf(v+1e-5f)));
  for(int i=lane;i<n;i+=64){ float d=rbf(bf2f(in[row*ld_in+i])-mb); out[row*ld_out+i]=f2bf(d/sd); }
}
__global__ void kv_kernel(const u16* k,const u16* v,const u16* sk,u16* kv){
  int idx=blockIdx.x*256+threadIdx.x; if(idx>=S*D) return;
  int s=idx/D,d=idx%D; float acc=0;
  for(int t=0;t<4;t++){ int ss=s+t-3; float kk= ss>=0? bf2f(k[ss*D+d]):0.f; acc=rbf(acc+rbf(kk*bf2f(sk[d*4+3-t]))); }
  kv[idx]=f2bf(acc*bf2f(v[idx]));
}
template<int MODE>
__global__ void gemm_kernel(const u16* A,int lda,const u16* B,int ldb,int K,u16* C,int ldc,
   const u16* e0,const u16* e1,const u16* e2,const u16* e3){
  __shared__ float sa[16][17], sb[16][17];
  int tx=threadIdx.x&15, ty=threadIdx.x>>4; int row=blockIdx.y*16+ty, col=blockIdx.x*16+tx; float acc=0;
  for(int k0=0;k0<K;k0+=16){ sa[ty][tx]=bf2f(A[row*lda+k0+tx]); sb[ty][tx]=bf2f(B[(k0+ty)*ldb+col]); __syncthreads();
    for(int k=0;k<16;k++) acc+=sa[ty][k]*sb[k][tx]; __syncthreads(); }
  float a=rbf(acc);
  if(MODE==0){ float t=tanhf(0.7978845608028654f*(a+0.044715f*a*a*a)); C[row*ldc+col]=f2bf(0.5f*a*(1.f+t)); }
  if(MODE==1){ float g=rbf(a+bf2f(e0[row])); C[row*ldc+col]=f2bf(bf2f(e1[row*F+col])*g); }
  if(MODE==2){ float g=rbf(a/(1.f+expf(-a))); float sv=bf2f(e1[row*D+col]); C[row*ldc+col]=f2bf(bf2f(e0[row*D+col])+rbf(sv*g)); }
}
__global__ void conv_kernel(const u16* kv,const u16* h,const u16* Dv,const u16* xn,u16* sout){
  int idx=blockIdx.x*256+threadIdx.x; if(idx>=S*D) return;
  int s=idx/D,d=idx%D; float acc=0;
  for(int j=0;j<=s;j++) acc+=bf2f(h[d*S+j])*bf2f(kv[(s-j)*D+d]);
  float y=rbf(acc); y=rbf(y+rbf(bf2f(kv[idx])*bf2f(Dv[d])));
  sout[idx]=f2bf(bf2f(xn[idx])*y);
}
extern "C" void kernel_launch(void* const* d_in, const int* in_sizes, int n_in, void* d_out, int out_size, void* d_ws, size_t ws_size, hipStream_t stream){
  const u16* q=(const u16*)d_in[0],*k=(const u16*)d_in[1],*v=(const u16*)d_in[2],*sk=(const u16*)d_in[3],*h=(const u16*)d_in[4],*Dv=(const u16*)d_in[5];
  const u16* w_in=(const u16*)d_in[7],*w_sp=(const u16*)d_in[8],*b_sp=(const u16*)d_in[9],*w_out=(const u16*)d_in[10];
  u16* ws=(u16*)d_ws; u16* xn=ws; u16* kv=xn+S*D; u16* sv=kv+S*D; u16* z=sv+S*D; u16* z2n=z+S*F; u16* u=z2n+S*FH;
  ln_kernel<<<S,64,0,stream>>>(q,D,xn,D,D);
  kv_kernel<<<S*D/256,256,0,stream>>>(k,v,sk,kv);
  gemm_kernel<0><<<dim3(F/16,S/16),256,0,stream>>>(xn,D,w_in,F,D,z,F,0,0,0,0);
  ln_kernel<<<S,64,0,stream>>>(z+FH,F,z2n,FH,FH);
  gemm_kernel<1><<<dim3(FH/16,S/16),256,0,stream>>>(w_sp,S,z2n,FH,S,u,FH,b_sp,z,0,0);
  conv_kernel<<<S*D/256,256,0,stream>>>(kv,h,Dv,xn,sv);
  gemm_kernel<2><<<dim3(D/16,S/16),256,0,stream>>>(u,FH,w_out,D,FH,(u16*)d_out,D,q,sv,0,0);
}
```

```cpp
#include <hip/hip_runtime.h>
#include <stdint.h>
#include <type_traits>

typedef unsigned short u16;
typedef unsigned int u32;
typedef unsigned long long u64;
typedef u32 v4u __attribute__((ext_vector_type(4)));
typedef u32 v2u __attribute__((ext_vector_type(2)));
typedef float f4 __attribute__((ext_vector_type(4)));
typedef float f2 __attribute__((ext_vector_type(2)));
typedef short s4 __attribute__((ext_vector_type(4)));
typedef __bf16 bf8 __attribute__((ext_vector_type(8)));
typedef __bf16 bf2 __attribute__((ext_vector_type(2)));
#define LAS __attribute__((address_space(3)))
#define GAS __attribute__((address_space(1)))
typedef __amdgpu_buffer_rsrc_t rsrc_t;

constexpr int S_ = 2048, D_ = 448, F_ = 1792;
constexpr int WS_EPOCH = 0;
constexpr int WS_KVF = 4096;
constexpr int WS_ZF = 65536;
constexpr int WS_UF = 131072;
constexpr int WS_YF = 163840;
constexpr int WS_ST = 262144;
constexpr int WS_ZB = 1 << 20;
constexpr int WS_UB = 5 << 20;
constexpr int WS_KVR = 9 << 20;
constexpr int WS_YB = 11 << 20;
constexpr int WS_STAMP = 14 << 20;
constexpr int WS_BYTES = 16 << 20;
constexpr int LDS_BYTES = 163840;
constexpr int NV = 12;
constexpr u32 SPIN_CAP = 1u << 16;

struct Params {
  const u16 *x, *k, *v, *shk, *h, *dv, *win, *wsp, *bsp, *wout;
  u16 *out;
  unsigned char *ws;
};

#define SB() __builtin_amdgcn_sched_barrier(0)
#define BAR() asm volatile("s_waitcnt lgkmcnt(0)\n\ts_barrier" ::: "memory")
#define FRESH() do { asm volatile("" : "+v"(tid)); l = tid & 63; g = l >> 4; c = l & 15; } while (0)
#define DRAIN() asm volatile("s_waitcnt vmcnt(0)" ::: "memory")
#define STAMP(n) do { } while (0)

static __device__ __forceinline__ rsrc_t mk(const void *p, int bytes) { return __builtin_amdgcn_make_buffer_rsrc((void *)p, 0, bytes, 0x00020000); }
static __device__ __forceinline__ v4u ld16(rsrc_t r, int vo, int so) { return __builtin_amdgcn_raw_buffer_load_b128(r, vo, so, 0); }
static __device__ __forceinline__ v4u ld16c(rsrc_t r, int vo, int so) { return __builtin_amdgcn_raw_buffer_load_b128(r, vo, so, 16); }
static __device__ __forceinline__ void st16c(v4u v, rsrc_t r, int vo, int so) { __builtin_amdgcn_raw_buffer_store_b128(v, r, vo, so, 16); }
static __device__ __forceinline__ void st8c(v2u v, rsrc_t r, int vo, int so) { __builtin_amdgcn_raw_buffer_store_b64(v, r, vo, so, 16); }
template <int OFF> static __device__ __forceinline__ void lda(v4u &d, rsrc_t r, int vo, int so) {
  asm volatile("buffer_load_dwordx4 %0, %1, %2, %3 offen offset:%4" : "=a"(d) : "v"(vo), "s"(r), "s"(so), "n"(OFF));
}
template <int OFF> static __device__ __forceinline__ void ldv(v4u &d, rsrc_t r, int vo, int so) {
  asm volatile("buffer_load_dwordx4 %0, %1, %2, %3 offen offset:%4" : "=v"(d) : "v"(vo), "s"(r), "s"(so), "n"(OFF));
}
template <int OFF> static __device__ __forceinline__ void stsa(int a, const v4u &d) {
  asm volatile("ds_write_b128 %0, %1 offset:%2" ::"v"(a), "a"(d), "n"(OFF) : "memory");
}
template <int OFF> static __device__ __forceinline__ void stsv(int a, const v4u &d) {
  asm volatile("ds_write_b128 %0, %1 offset:%2" ::"v"(a), "v"(d), "n"(OFF) : "memory");
}
template <int N, typename Fn> static __device__ __forceinline__ void unr(Fn &&f) {
  if constexpr (N > 0) { unr<N - 1>(f); f(std::integral_constant<int, N - 1>{}); }
}
static __device__ __forceinline__ f4 mfma(v4u a, v4u b, f4 c) {
  return __builtin_amdgcn_mfma_f32_16x16x32_bf16(__builtin_bit_cast(bf8, a), __builtin_bit_cast(bf8, b), c, 0, 0, 0);
}
static __device__ __forceinline__ float asf(u32 u) { return __builtin_bit_cast(float, u); }
static __device__ __forceinline__ float lo16(u32 u) { return __builtin_bit_cast(float, u << 16); }
static __device__ __forceinline__ float hi16(u32 u) { return __builtin_bit_cast(float, u & 0xffff0000u); }
static __device__ __forceinline__ u32 pk(float a, float b) { f2 t = {a, b}; return __builtin_bit_cast(u32, __builtin_convertvector(t, bf2)); }
static __device__ __forceinline__ float rbf(float a) { return lo16(pk(a, a)); }
static __device__ __forceinline__ v4u lds16(LAS unsigned char *L, int off) { return *(LAS v4u *)(L + off); }
static __device__ __forceinline__ void sts16(LAS unsigned char *L, int off, v4u v) { *(LAS v4u *)(L + off) = v; }
static __device__ __forceinline__ v2u trd(LAS unsigned char *L, int off) {
  return __builtin_bit_cast(v2u, __builtin_amdgcn_ds_read_tr16_b64_v4i16((LAS s4 *)(L + off)));
}
static __device__ __forceinline__ float gelu(float x) {
  const float e = __builtin_amdgcn_exp2f(x * __builtin_fmaf(x * x, -0.1029432397f, -2.302208198f));
  return x * __builtin_amdgcn_rcpf(1.0f + e);
}
template <int CTRL> static __device__ __forceinline__ float dpp(float v) {
  return __builtin_bit_cast(float, __builtin_amdgcn_update_dpp(0, __builtin_bit_cast(int, v), CTRL, 0xf, 0xf, true));
}
static __device__ __forceinline__ float rowsum(float v) {
  v += dpp<0x128>(v);
  v += dpp<0x124>(v);
  v += dpp<0x122>(v);
  v += dpp<0x121>(v);
  return v;
}
static __device__ __forceinline__ u64 ldflag(unsigned char *ws, int off) { return __hip_atomic_load((GAS u64 *)(ws + off), __ATOMIC_RELAXED, __HIP_MEMORY_SCOPE_AGENT); }
static __device__ __forceinline__ void stflag(unsigned char *ws, int off, u64 v) { __hip_atomic_store((GAS u64 *)(ws + off), v, __ATOMIC_RELAXED, __HIP_MEMORY_SCOPE_AGENT); }
static __device__ __forceinline__ void wait_flags(unsigned char *ws, int off, u64 tag) {
#pragma unroll 1
  for (u32 s = 0; s < SPIN_CAP; ++s) {
    const u64 v = ldflag(ws, off);
    if (__all(v == tag)) break;
    __builtin_amdgcn_s_sleep(1);
  }
  __builtin_amdgcn_fence(__ATOMIC_ACQUIRE, "wavefront");
  asm volatile("" ::: "memory");
}
static __device__ __forceinline__ float inv_sigma(float var) {
  const float sd = rbf(__builtin_sqrtf(rbf(rbf(fmaxf(var, 0.f)) + 1e-5f)));
  return 1.0f / sd;
}

constexpr int L_XS = 114688;
constexpr int L_KVT = 147456;
constexpr int L_YT = 147456;
constexpr int L_CS = 156928;
constexpr int L_XST = 157696;
constexpr int L_ZST = 158208;

static __device__ __forceinline__ void p1_bases(int (&rb)[8], int rdoff) {
#pragma unroll
  for (int n = 0; n < 8; ++n) { rb[n] = rdoff ^ (n << 5); asm volatile("" : "+v"(rb[n])); }
}
static __device__ __forceinline__ void p1_read(LAS unsigned char *L, int ks, int (&rb)[8], v4u (&wf)[8]) {
#pragma unroll
  for (int n = 0; n < 8; ++n) {
    if (ks == 7) { rb[n] += 57344; asm volatile("" : "+v"(rb[n])); }
    const v2u a = trd(L, rb[n] + (ks % 7) * 8192);
    const v2u b = trd(L, rb[n] + (ks % 7) * 8192 + 1024);
    wf[n] = v4u{a.x, a.y, b.x, b.y};
  }
}
template <bool TR>
static __device__ __forceinline__ void p1_mma(const v4u (&wf)[8], v4u xf, f4 (&acc)[8], f4 (&cs)[2]) {
  const v4u ones = {0x3f803f80u, 0x3f803f80u, 0x3f803f80u, 0x3f803f80u};
#pragma unroll
  for (int n = 0; n < 8; ++n) {
    acc[n] = TR ? mfma(wf[n], xf, acc[n]) : mfma(xf, wf[n], acc[n]);
    if (n < 2) cs[n] = mfma(ones, wf[n], cs[n]);
  }
}
#define MIX() do { for (int q_ = 0; q_ < 8; ++q_) { __builtin_amdgcn_sched_group_barrier(8, 1, 0); __builtin_amdgcn_sched_group_barrier(0x100, 2, 0); } } while (0)

static __device__ __forceinline__ void gemm_wg(const Params &p, LAS unsigned char *L, int i, int j, u64 tag) {
  STAMP(0);
  int tid = threadIdx.x, l = tid & 63, g = l >> 4, c = l & 15;
  const int w = __builtin_amdgcn_readfirstlane(tid >> 6);
  const rsrc_t rws = mk(p.ws, WS_BYTES);
  const rsrc_t rx = mk(p.x, S_ * D_ * 2), rk = mk(p.k, S_ * D_ * 2), rv = mk(p.v, S_ * D_ * 2);
  const rsrc_t rwin = mk(p.win, D_ * F_ * 2), rwsp = mk(p.wsp, S_ * S_ * 2), rwout = mk(p.wout, 896 * D_ * 2);

  u32 kr[11], vr[8];
  const int c2 = tid & 31, rg = tid >> 5;
  {
    const int row0 = 64 * i + 8 * rg;
#pragma unroll
    for (int r = 0; r < 11; ++r) {
      const int row = row0 + r - 3;
      kr[r] = __builtin_amdgcn_raw_buffer_load_b32(rk, row >= 0 ? row * 896 + j * 128 + c2 * 4 : 0x40000000, 0, 0);
    }
#pragma unroll
    for (int r = 0; r < 8; ++r) vr[r] = __builtin_amdgcn_raw_buffer_load_b32(rv, (row0 + r) * 896 + j * 128 + c2 * 4, 0, 0);
  }
  const v4u shk = *(const v4u *)(p.shk + (64 * j + 2 * c2) * 4);
  v4u xr[14];
  {
    const int vo = (64 * i + 16 * w + (l >> 3)) * 896 + (l & 7) * 16;
#pragma unroll
    for (int cb = 0; cb < 7; ++cb) {
      xr[2 * cb] = ld16(rx, vo + cb * 128, 0);
      xr[2 * cb + 1] = ld16(rx, vo + cb * 128, 8 * 896);
    }
  }
  v4u wr[28];
  const int wvo = (4 * w + g) * 3584 + c * 16 + j * 256;
#pragma unroll
  for (int n = 0; n < 28; ++n) wr[n] = ld16(rwin, wvo + 1792, n * 16 * 3584);
  SB();

  {
    float o[2][8];
#pragma unroll
    for (int ch = 0; ch < 2; ++ch) {
      const u32 s01 = ch ? shk.z : shk.x, s23 = ch ? shk.w : shk.y;
      const float t0 = lo16(s01), t1 = hi16(s01), t2 = lo16(s23), t3 = hi16(s23);
#pragma unroll
      for (int r = 0; r < 8; ++r) {
        const float k3 = ch ? hi16(kr[r]) : lo16(kr[r]), k2 = ch ? hi16(kr[r + 1]) : lo16(kr[r + 1]);
        const float k1 = ch ? hi16(kr[r + 2]) : lo16(kr[r + 2]), k0 = ch ? hi16(kr[r + 3]) : lo16(kr[r + 3]);
        const float ks = __builtin_fmaf(k0, t0, __builtin_fmaf(k1, t1, __builtin_fmaf(k2, t2, k3 * t3)));
        o[ch][r] = ks * (ch ? hi16(vr[r]) : lo16(vr[r]));
      }
      const v4u q = {pk(o[ch][7], o[ch][6]), pk(o[ch][5], o[ch][4]), pk(o[ch][3], o[ch][2]), pk(o[ch][1], o[ch][0])};
      sts16(L, L_KVT + (2 * c2 + ch) * 144 + (7 - rg) * 16, q);
    }
  }
  STAMP(1);
  v4u xf[14];
  {
    const int wo = L_XS + w * 8192 + (l >> 3) * 128 + (((l & 7) ^ (l >> 3)) << 4);
    const int ro = L_XS + w * 8192 + c * 128;
#pragma unroll
    for (int cb = 0; cb < 7; ++cb) {
      const int sl = (cb & 3) * 2048;
      sts16(L, wo + sl, xr[2 * cb]);
      sts16(L, wo + sl + 1024, xr[2 * cb + 1]);
      xf[2 * cb] = lds16(L, ro + sl + ((g ^ (c & 7)) << 4));
      xf[2 * cb + 1] = lds16(L, ro + sl + (((4 + g) ^ (c & 7)) << 4));
    }
    const v4u ones = {0x3f803f80u, 0x3f803f80u, 0x3f803f80u, 0x3f803f80u};
    f4 sx = {0, 0, 0, 0}, gr = {0, 0, 0, 0};
#pragma unroll
    for (int ks = 0; ks < 14; ++ks) { sx = mfma(xf[ks], ones, sx); gr = mfma(xf[ks], xf[ks], gr); }
    const int e = c & 3;
    const float s1 = e == 0 ? sx.x : e == 1 ? sx.y : e == 2 ? sx.z : sx.w;
    const float s2 = e == 0 ? gr.x : e == 1 ? gr.y : e == 2 ? gr.z : gr.w;
    const float m = s1 * (1.0f / 448.0f);
    const float var = __builtin_fmaf(-m, m, s2 * (1.0f / 448.0f));
    if ((c >> 2) == g) *(LAS f2 *)(L + L_XST + (16 * w + c) * 8) = f2{rbf(m), inv_sigma(var)};
  }
  STAMP(2);

  const int sw = (g & 3) | ((w & 2) << 1);
  const int wwo = (4 * w + g) * 256 + ((((c >> 1) ^ sw) << 5) | ((c & 1) << 4));
  int wwh = wwo + 65536;
  asm volatile("" : "+v"(wwh));
  const int rdoff = (8 * g + (c >> 2)) * 256 + ((((c >> 2) | ((g & 1) << 2)) ^ (2 * w)) << 5) + (c & 3) * 8;
  f4 acc[8], cs[2];
#pragma unroll
  for (int n = 0; n < 8; ++n) acc[n] = f4{0, 0, 0, 0};
  cs[0] = cs[1] = f4{0, 0, 0, 0};
  v4u w1[28], wf[2][8];
  int rb[8];
  p1_bases(rb, rdoff);
  v4u ar[64];
  const int avo = (64 * i + (l >> 3)) * 4096 + (l & 7) * 16 + w * 1024;
#pragma unroll
  for (int st = 0; st < 7; ++st) {
#pragma unroll
    for (int q = 0; q < 4; ++q) sts16(L, (st < 4 ? wwo : wwh) + ((4 * st + q) & 15) * 4096, wr[4 * st + q]);
    BAR();
    if (st == 0) {
#pragma unroll
      for (int q = 0; q < 2; ++q) {
        const int ch = q * 32 + (tid >> 3);
        st16c(lds16(L, L_KVT + ch * 144 + (tid & 7) * 16), rws, (64 * j + ch) * 4096 + (31 - i) * 128 + (tid & 7) * 16, WS_KVR);
      }
    }
#pragma unroll
    for (int q = 0; q < 4; ++q) w1[4 * st + q] = ld16(rwin, wvo, (4 * st + q) * 16 * 3584);
    SB();
    p1_read(L, 2 * st, rb, wf[0]);
    if (st > 0) { p1_mma<false>(wf[1], xf[2 * st - 1], acc, cs); MIX(); }
    SB();
    p1_read(L, 2 * st + 1, rb, wf[1]);
    p1_mma<false>(wf[0], xf[2 * st], acc, cs);
    MIX();
    SB();
  }
  p1_mma<false>(wf[1], xf[13], acc, cs);
  STAMP(3);
  FRESH();
  if (g == 0) {
    *(LAS float *)(L + L_CS + ((2 * w) * 16 + c) * 4) = cs[0].x;
    *(LAS float *)(L + L_CS + ((2 * w + 1) * 16 + c) * 4) = cs[1].x;
  }
  asm volatile("s_waitcnt vmcnt(28)" ::: "memory");
  BAR();
  if (tid == 0) stflag(p.ws, WS_KVF + (j * 32 + i) * 128, tag);
  unr<32>([&](auto q) { lda<(q >> 3) * 128>(ar[q], rwsp, avo, (q & 7) * 32768); });
  SB();
  const u32 tagA = (u32)tag, tagB = (u32)(tag >> 32) ^ 0x9e3779b9u;
  {
    const f4 sa = *(LAS f4 *)(L + L_XST + (16 * w + 4 * g) * 8), sb = *(LAS f4 *)(L + L_XST + (16 * w + 4 * g) * 8 + 16);
    const float mm[4] = {sa.x, sa.z, sb.x, sb.z}, rr[4] = {sa.y, sa.w, sb.y, sb.w};
    float sum[4] = {0, 0, 0, 0};
#pragma unroll
    for (int n = 0; n < 8; ++n) {
      const float cv = *(LAS float *)(L + L_CS + (((n ^ (2 * w)) * 16) + c) * 4);
#pragma unroll
      for (int e = 0; e < 4; ++e) {
        const float z = gelu(rr[e] * __builtin_fmaf(-mm[e], cv, acc[n][e]));
        acc[n][e] = z;
        sum[e] += z;
      }
    }
    float mean[4], m2[4];
#pragma unroll
    for (int e = 0; e < 4; ++e) mean[e] = rowsum(sum[e]) * (1.0f / 128.0f);
#pragma unroll
    for (int e = 0; e < 4; ++e) {
      float q = 0;
#pragma unroll
      for (int n = 0; n < 8; ++n) { const float d = acc[n][e] - mean[e]; q = __builtin_fmaf(d, d, q); }
      m2[e] = rowsum(q);
    }
    const int e = c & 3;
    const float a = e == 0 ? mean[0] : e == 1 ? mean[1] : e == 2 ? mean[2] : mean[3];
    const float b = e == 0 ? m2[0] : e == 1 ? m2[1] : e == 2 ? m2[2] : m2[3];
    if (c < 4) st16c(v4u{__builtin_bit_cast(u32, a), tagA, __builtin_bit_cast(u32, b), tagB}, rws, ((i * 4 + w) * 8 + j) * 256 + (4 * g + c) * 16, WS_ST);
  }
  STAMP(4);
  FRESH();
#pragma unroll
  for (int n = 0; n < 28; ++n) sts16(L, (n < 16 ? wwo : wwh) + (n & 15) * 4096, w1[n]);
  {
    const int so = (i * 4 + w) * 2048 + c * 16;
    const int o0 = so + g * 256, o1 = so + (g == 3 ? 3 : 4 + g) * 256;
    v4u a, b;
#pragma unroll 1
    for (u32 s = 0; s < SPIN_CAP; ++s) {
      a = ld16c(rws, o0, WS_ST);
      b = ld16c(rws, o1, WS_ST);
      if (__all(a.y == tagA && a.w == tagB && b.y == tagA && b.w == tagB)) break;
      asm volatile("" ::: "memory");
    }
    const float ma = asf(a.x), mb = asf(b.x);
    float ms = g == 3 ? ma : ma + mb;
    ms += __shfl_xor(ms, 16);
    ms += __shfl_xor(ms, 32);
    const float mean = ms * (1.0f / 7.0f);
    const float da = ma - mean, db = mb - mean;
    float q = __builtin_fmaf(128.0f * da, da, asf(a.z));
    if (g != 3) q += __builtin_fmaf(128.0f * db, db, asf(b.z));
    q += __shfl_xor(q, 16);
    q += __shfl_xor(q, 32);
    const float r = inv_sigma(q * (1.0f / 896.0f));
    if (g == 0) *(LAS f2 *)(L + L_ZST + (16 * w + c) * 8) = f2{-rbf(mean) * r, r};
    const f4 sa = *(LAS f4 *)(L + L_ZST + (16 * w + 4 * g) * 8), sb = *(LAS f4 *)(L + L_ZST + (16 * w + 4 * g) * 8 + 16);
    STAMP(5);
    const int zo = (j * 64 + 2 * i + (w >> 1)) * 8192 + ((2 * (w & 1) + (g >> 1)) * 16 + c) * 16 + (g & 1) * 8;
#pragma unroll
    for (int n = 0; n < 8; ++n) {
      const v2u o = {pk(__builtin_fmaf(acc[n][0], sa.y, sa.x), __builtin_fmaf(acc[n][1], sa.w, sa.z)),
                     pk(__builtin_fmaf(acc[n][2], sb.y, sb.x), __builtin_fmaf(acc[n][3], sb.w, sb.z))};
      st8c(o, rws, zo + ((n ^ (2 * w)) << 10), WS_ZB);
    }
  }
  DRAIN();
  BAR();
  if (tid < 32) stflag(p.ws, WS_ZF + ((j * 32 + tid) * 32 + i) * 8, tag);
  STAMP(6);
  FRESH();
  unr<32>([&](auto q) {
    if constexpr (q < NV) ldv<(q >> 3) * 128 + 512>(ar[32 + q], rwsp, avo, (q & 7) * 32768);
    else lda<(q >> 3) * 128 + 512>(ar[32 + q], rwsp, avo, (q & 7) * 32768);
  });
  SB();

  u32 z1[8][2];
  {
#pragma unroll
    for (int n = 0; n < 8; ++n) acc[n] = f4{0, 0, 0, 0};
    cs[0] = cs[1] = f4{0, 0, 0, 0};
    p1_bases(rb, rdoff);
    p1_read(L, 0, rb, wf[0]);
    SB();
#pragma unroll
    for (int ks = 0; ks < 14; ++ks) {
      if (ks < 13) p1_read(L, ks + 1, rb, wf[(ks + 1) & 1]);
      p1_mma<true>(wf[ks & 1], xf[ks], acc, cs);
      MIX();
      SB();
    }
    if (g == 0) {
      *(LAS float *)(L + L_CS + ((2 * w) * 16 + c) * 4) = cs[0].x;
      *(LAS float *)(L + L_CS + ((2 * w + 1) * 16 + c) * 4) = cs[1].x;
    }
    BAR();
    const f2 st = *(LAS f2 *)(L + L_XST + (16 * w + c) * 8);
#pragma unroll
    for (int n = 0; n < 8; ++n) {
      const f4 cv = *(LAS f4 *)(L + L_CS + ((n ^ (2 * w)) * 16 + 4 * g) * 4);
      float z[4];
#pragma unroll
      for (int e = 0; e < 4; ++e) z[e] = gelu(st.y * __builtin_fmaf(-st.x, cv[e], acc[n][e]));
      z1[n][0] = pk(z[0], z[1]);
      z1[n][1] = pk(z[2], z[3]);
      asm volatile("" : "+v"(z1[n][0]), "+v"(z1[n][1]));
    }
  }
  STAMP(7);
  FRESH();

  f4 g2[4][8];
#pragma unroll
  for (int m = 0; m < 4; ++m)
#pragma unroll
    for (int n = 0; n < 8; ++n) g2[m][n] = f4{0, 0, 0, 0};
  const int awo = w * 32768 + (l >> 3) * 128 + (((l & 7) ^ (l >> 3)) << 4);
  unr<32>([&](auto q) { stsa<q * 1024>(awo, ar[q]); });
  wait_flags(p.ws, WS_ZF + ((j * 32 + i) * 32 + 8 * w + (l & 7)) * 8, tag);
  STAMP(8);
  v4u wo[28];
  {
    const int bvo = (j * 64 + 16 * w) * 8192 + l * 16;
    constexpr int AHEAD = 2;
    v4u bf[16][8];
#pragma unroll
    for (int ks = 0; ks < AHEAD; ++ks)
#pragma unroll
      for (int n = 0; n < 8; ++n) bf[ks][n] = ld16c(rws, bvo + (n & 3) * 1024, WS_ZB + ks * 8192 + (n >> 2) * 4096);
    int aro[2][4];
#pragma unroll
    for (int m = 0; m < 4; ++m) {
      const int base = w * 32768 + (((m + w) & 3) * 16 + c) * 128;
      aro[0][m] = base + ((g ^ (c & 7)) << 4);
      aro[1][m] = base + (((4 + g) ^ (c & 7)) << 4);
    }
    v4u af[2][4];
#pragma unroll
    for (int m = 0; m < 4; ++m) af[0][m] = lds16(L, aro[0][m]);
    SB();
    unr<16>([&](auto ksc) {
      constexpr int ks = ksc;
      if constexpr (ks + AHEAD < 16) {
#pragma unroll
        for (int n = 0; n < 8; ++n) bf[ks + AHEAD][n] = ld16c(rws, bvo + (n & 3) * 1024, WS_ZB + (ks + AHEAD) * 8192 + (n >> 2) * 4096);
      } else {
        const int ovo = (8 * w + (l >> 3)) * 896 + (l & 7) * 16 + j * 128;
#pragma unroll
        for (int q = 0; q < 14; ++q) wo[(ks - 14) * 14 + q] = ld16(rwout, ovo, ((ks - 14) * 14 + q) * 32 * 896);
      }
      if constexpr (ks + 1 < 16) {
#pragma unroll
        for (int m = 0; m < 4; ++m) af[(ks + 1) & 1][m] = lds16(L, aro[(ks + 1) & 1][m] + (((ks + 1) >> 1) & 3) * 8192);
      }
      if constexpr ((ks & 1) == 0 && ks < 8) {
        unr<8>([&](auto q) {
          constexpr int e = (ks >> 1) * 8 + q;
          if constexpr (e < NV) stsv<e * 1024>(awo, ar[32 + e]);
          else stsa<e * 1024>(awo, ar[32 + e]);
        });
      }
      SB();
#pragma unroll
      for (int n = 0; n < 8; ++n)
#pragma unroll
        for (int m = 0; m < 4; ++m) g2[m][n] = mfma(bf[ks][n], af[ks & 1][m], g2[m][n]);
      SB();
    });
  }
  STAMP(9);
  FRESH();
  BAR();
#pragma unroll
  for (int m = 1; m < 4; ++m)
#pragma unroll
    for (int n = 0; n < 8; ++n) *(LAS f4 *)(L + ((((m + w) & 3) * 3 + (m - 1)) * 8 + n) * 1024 + l * 16) = g2[m][n];
  const float bias = lo16((u32)p.bsp[64 * i + 16 * w + c]);
  BAR();
  {
    const int uo = ((i * 4 + w) * 28 + 4 * j) * 1024 + ((g >> 1) * 16 + c) * 16 + (g & 1) * 8;
#pragma unroll
    for (int n = 0; n < 8; ++n) {
      f4 s = g2[0][n];
#pragma unroll
      for (int q = 0; q < 3; ++q) s += *(LAS f4 *)(L + ((w * 3 + q) * 8 + n) * 1024 + l * 16);
      g2[0][n] = s;
    }
#pragma unroll
    for (int n = 0; n < 8; ++n) *(LAS f4 *)(L + 98304 + (w * 8 + n) * 1024 + l * 16) = g2[0][n];
#pragma unroll
    for (int n = 0; n < 8; ++n) {
      const int nt = n ^ (2 * w);
      const f4 s = *(LAS f4 *)(L + 98304 + (w * 8 + nt) * 1024 + l * 16);
      const v2u o = {pk(lo16(z1[n][0]) * (s.x + bias), hi16(z1[n][0]) * (s.y + bias)), pk(lo16(z1[n][1]) * (s.z + bias), hi16(z1[n][1]) * (s.w + bias))};
      st8c(o, rws, uo + (nt << 9), WS_UB);
    }
  }
  DRAIN();
  if (l == 0) stflag(p.ws, WS_UF + (i * 4 + w) * 128 + j * 8, tag);
  STAMP(10);
  FRESH();
  BAR();
  {
    const int s2 = ((l >> 4) & 1) | ((w & 1) << 1);
    const int owo = (8 * w + (l >> 3)) * 128 + (((((l & 7) >> 1) ^ s2) << 5) | ((l & 1) << 4));
    int owh = owo + 65536;
    asm volatile("" : "+v"(owh));
#pragma unroll
    for (int q = 0; q < 28; ++q) sts16(L, (q < 16 ? owo : owh) + (q & 15) * 4096, wo[q]);
  }
  wait_flags(p.ws, WS_YF + ((64 * j + l) / 14) * 128, tag);
  {
#pragma unroll
    for (int q = 0; q < 2; ++q) {
      const int ch = q * 32 + (tid >> 3);
      sts16(L, L_YT + ch * 144 + (tid & 7) * 16, ld16c(rws, (64 * j + ch) * 4096 + i * 128 + (tid & 7) * 16, WS_YB));
    }
  }
  v2u xe[4];
#pragma unroll
  for (int n = 0; n < 4; ++n) xe[n] = __builtin_amdgcn_raw_buffer_load_b64(rx, (64 * i + 16 * w + c) * 896 + j * 128 + g * 8 + n * 32, 0, 0);
  BAR();
  STAMP(11);
  wait_flags(p.ws, WS_UF + (i * 4 + w) * 128 + (l < 7 ? l : 0) * 8, tag);
  STAMP(12);
  FRESH();
  {
    v4u uf[28];
#pragma unroll
    for (int ks = 0; ks < 28; ++ks) uf[ks] = ld16c(rws, (i * 4 + w) * 28672 + l * 16 + (ks & 3) * 1024, WS_UB + (ks >> 2) * 4096);
    SB();
    f4 o[4];
#pragma unroll
    for (int n = 0; n < 4; ++n) o[n] = f4{0, 0, 0, 0};
    const int oro = (8 * g + (c >> 2)) * 128 + ((((c >> 3) & 1) | ((g & 1) << 1)) << 5) + (c & 3) * 8;
    int ob[4];
#pragma unroll
    for (int n = 0; n < 4; ++n) { ob[n] = oro ^ (n << 5); asm volatile("" : "+v"(ob[n])); }
    v4u of[2][8];
    auto rd = [&](int kp, v4u (&f)[8]) {
#pragma unroll
      for (int q = 0; q < 8; ++q) {
        const int n = q & 3, ks = 2 * kp + (q >> 2);
        if (ks == 14) { ob[n] += 57344; asm volatile("" : "+v"(ob[n])); }
        const v2u a = trd(L, ob[n] + (ks % 14) * 4096);
        const v2u b = trd(L, ob[n] + (ks % 14) * 4096 + 512);
        f[q] = v4u{a.x, a.y, b.x, b.y};
      }
    };
    rd(0, of[0]);
    SB();
#pragma unroll
    for (int kp = 0; kp < 14; ++kp) {
      if (kp < 13) rd(kp + 1, of[(kp + 1) & 1]);
#pragma unroll
      for (int q = 0; q < 8; ++q) o[q & 3] = mfma(of[kp & 1][q], uf[2 * kp + (q >> 2)], o[q & 3]);
      MIX();
      SB();
    }
    STAMP(13);
  FRESH();
    const f2 st = *(LAS f2 *)(L + L_XST + (16 * w + c) * 8);
#pragma unroll
    for (int n = 0; n < 4; ++n) {
      const v2u y = trd(L, L_YT + (16 * n + 4 * g + (c >> 2)) * 144 + w * 32 + (c & 3) * 8);
      const float xv[4] = {lo16(xe[n].x), hi16(xe[n].x), lo16(xe[n].y), hi16(xe[n].y)};
      const float yv[4] = {lo16(y.x), hi16(y.x), lo16(y.y), hi16(y.y)};
      float r[4];
#pragma unroll
      for (int e = 0; e < 4; ++e) {
        const float a = o[n][e];
        const float sg = a * __builtin_amdgcn_rcpf(1.0f + __builtin_amdgcn_exp2f(a * -1.442695041f));
        r[e] = __builtin_fmaf((xv[e] - st.x) * st.y * yv[e], sg, xv[e]);
      }
      *(v2u *)((unsigned char *)p.out + (64 * i + 16 * w + c) * 896 + j * 128 + n * 32 + g * 8) = v2u{pk(r[0], r[1]), pk(r[2], r[3])};
    }
  }
}

constexpr int C_BUF = 37568, C_KV = 32992;

static __device__ __forceinline__ void conv_wg(const Params &p, LAS unsigned char *L, int v, u64 tag) {
  STAMP(0);
  const int tid = threadIdx.x, l = tid & 63, w = __builtin_amdgcn_readfirstlane(tid >> 6), g = l >> 4, c = l & 15;
  const int ch = w >> 1, hf = w & 1;
  const rsrc_t rws = mk(p.ws, WS_BYTES), rh = mk(p.h, D_ * S_ * 2);
  {
    const v4u z = {0, 0, 0, 0};
    if (l < 8) sts16(L, w * C_BUF + l * 4112 + (l >> 1) * 32, z);
    if (l < 30) sts16(L, w * C_BUF + C_KV + 4096 + l * 16, z);
  }
  v4u hc[2], hp[2], kq[2];
  auto load_h = [&](int rho) {
    const int cch = 14 * v + 2 * rho + ch;
#pragma unroll
    for (int q = 0; q < 2; ++q) {
      const int m = 128 * hf + 64 * q + l;
      hc[q] = ld16(rh, m * 16, cch * 4096);
      hp[q] = ld16(rh, m > 0 ? m * 16 - 16 : 0x40000000, cch * 4096);
    }
  };
  auto load_kv = [&](int rho) {
    const int cch = 14 * v + 2 * rho + ch;
#pragma unroll
    for (int q = 0; q < 2; ++q) kq[q] = ld16c(rws, (128 * hf + 64 * q + l) * 16, WS_KVR + cch * 4096);
  };
  auto stage_h = [&](int buf) {
#pragma unroll
    for (int q = 0; q < 2; ++q) {
      const int o = buf + (128 * hf + 64 * q + l + 1) * 16;
      const u32 d[8] = {hp[q].x, hp[q].y, hp[q].z, hp[q].w, hc[q].x, hc[q].y, hc[q].z, hc[q].w};
      u32 e[7];
#pragma unroll
      for (int t = 0; t < 7; ++t) e[t] = __builtin_amdgcn_alignbit(d[t + 1], d[t], 16);
#pragma unroll
      for (int s = 0; s < 8; ++s) {
        const int t = s >> 1;
        const v4u val = (s & 1) ? v4u{d[t + 1], d[t + 2], d[t + 3], d[t + 4]} : v4u{e[t], e[t + 1], e[t + 2], e[t + 3]};
        sts16(L, o + s * 4112 + (s >> 1) * 32, val);
      }
    }
  };
  auto stage_kv = [&](int buf) {
#pragma unroll
    for (int q = 0; q < 2; ++q) sts16(L, buf + C_KV + (128 * hf + 64 * q + l) * 16, kq[q]);
  };
  load_h(0);
  stage_h(ch * C_BUF);
  load_h(1);
  {
    const int j0 = (14 * v) >> 6, j1 = (14 * v + 13) >> 6;
    wait_flags(p.ws, WS_KVF + ((l < 32 ? j0 : j1) * 32 + (l & 31)) * 128, tag);
  }
  STAMP(1);
  load_kv(0);
  stage_kv(ch * C_BUF);
  load_kv(1);
  BAR();
  STAMP(2);
  const int hro = (c & 7) * 4112 + ((c & 7) >> 1) * 32 + (g + (c >> 3)) * 16 + hf * 256;
  const int kro = C_KV + (254 - 2 * c + g) * 16 + hf * 256;
  for (int rho = 0; rho < 7; ++rho) {
    const int buf = ((rho & 1) * 2 + ch) * C_BUF, nbuf = (((rho & 1) ^ 1) * 2 + ch) * C_BUF;
    f4 acc[8];
#pragma unroll
    for (int n = 0; n < 8; ++n) acc[n] = f4{0, 0, 0, 0};
    v4u hfr[2][8], kfr[2][8];
#pragma unroll
    for (int d = 0; d < 8; ++d) { hfr[0][d] = lds16(L, buf + hro + d * 512); kfr[0][d] = lds16(L, buf + kro - d * 512); }
#pragma unroll
    for (int q = 0; q < 4; ++q) {
      if (q < 3) {
#pragma unroll
        for (int d = 0; d < 8; ++d) {
          hfr[(q + 1) & 1][d] = lds16(L, buf + hro + d * 512 + (q + 1) * 64);
          kfr[(q + 1) & 1][d] = lds16(L, buf + kro - d * 512 + (q + 1) * 64);
        }
      }
      if (q == 1 && rho < 6) { stage_h(nbuf); stage_kv(nbuf); }
      if (q == 2 && rho < 5) { load_h(rho + 2); load_kv(rho + 2); }
#pragma unroll
      for (int P = 0; P < 8; ++P)
#pragma unroll
        for (int d = 0; d + P < 8; ++d) acc[P + d] = mfma(hfr[q & 1][P], kfr[q & 1][d], acc[P + d]);
    }
    BAR();
#pragma unroll
    for (int n = 0; n < 4; ++n) {
      f4 a = acc[n], b = acc[4 + n];
      asm volatile("" : "+v"(a), "+v"(b));
      *(LAS f4 *)(L + buf + 16 + hf * 8256 + n * 1024 + l * 16) = hf ? a : b;
      acc[n] = hf ? b : a;
    }
    BAR();
    const int cch = 14 * v + 2 * rho + ch;
    const float dv = lo16((u32)p.dv[cch]);
#pragma unroll
    for (int n = 0; n < 4; ++n) {
      const f4 s = acc[n] + *(LAS f4 *)(L + buf + 16 + (hf ^ 1) * 8256 + n * 1024 + l * 16);
      const int t = 256 * (4 * hf + n) + 16 * c + 4 * g;
      const v2u kv = *(LAS v2u *)(L + buf + C_KV + (2044 - t) * 2);
      const v2u o = {pk(__builtin_fmaf(hi16(kv.y), dv, s.x), __builtin_fmaf(lo16(kv.y), dv, s.y)),
                     pk(__builtin_fmaf(hi16(kv.x), dv, s.z), __builtin_fmaf(lo16(kv.x), dv, s.w))};
      st8c(o, rws, t * 2, WS_YB + cch * 4096);
    }
    BAR();
    if (rho == 0) STAMP(3); if (rho == 1) STAMP(4); if (rho == 3) STAMP(5); if (rho == 5) STAMP(6);
  }
  DRAIN();
  BAR();
  if (tid == 0) stflag(p.ws, WS_YF + v * 128, tag);
  STAMP(10);
  if (v == 0 && w == 0) {
#pragma unroll
    for (int q = 0; q < 4; ++q) {
      const int f = q * 64 + l;
      wait_flags(p.ws, f < 224 ? WS_KVF + f * 128 : WS_YF + (f - 224) * 128, tag);
    }
    if (l == 0) stflag(p.ws, WS_EPOCH, tag);
  }
}

__global__ void __launch_bounds__(256, 1) fused_kernel(Params p) {
  extern __shared__ __attribute__((aligned(16))) unsigned char smem[];
  LAS unsigned char *L = (LAS unsigned char *)smem;
  const u64 tag = ldflag(p.ws, WS_EPOCH) + 1;
  const int b = blockIdx.x, x = b & 7, r = b >> 3;
  if (r >= 28) {
    conv_wg(p, L, x * 4 + (r - 28), tag);
  } else {
    const int pg = x >> 1, o = x & 1;
    int i, j;
    if (r < 24) { i = 8 * pg + r / 3; j = 4 * o + r % 3; }
    else { i = 8 * pg + 4 * o + (r - 24); j = 3; }
    gemm_wg(p, L, i, j, tag);
  }
}

extern "C" void kernel_launch(void *const *d_in, const int *in_sizes, int n_in, void *d_out, int out_size, void *d_ws, size_t ws_size,
                              hipStream_t stream) {
  Params p;
  p.x = (const u16 *)d_in[0];
  p.k = (const u16 *)d_in[1];
  p.v = (const u16 *)d_in[2];
  p.shk = (const u16 *)d_in[3];
  p.h = (const u16 *)d_in[4];
  p.dv = (const u16 *)d_in[5];
  p.win = (const u16 *)d_in[7];
  p.wsp = (const u16 *)d_in[8];
  p.bsp = (const u16 *)d_in[9];
  p.wout = (const u16 *)d_in[10];
  p.out = (u16 *)d_out;
  p.ws = (unsigned char *)d_ws;
  (void)hipFuncSetAttribute((const void *)fused_kernel, hipFuncAttributeMaxDynamicSharedMemorySize, LDS_BYTES);
  fused_kernel<<<256, 256, LDS_BYTES, stream>>>(p);
}
```

```cpp
#include <hip/hip_runtime.h>
#include <stdint.h>
#include <type_traits>

typedef unsigned short u16;
typedef unsigned int u32;
typedef unsigned long long u64;
typedef u32 v4u __attribute__((ext_vector_type(4)));
typedef u32 v2u __attribute__((ext_vector_type(2)));
typedef float f4 __attribute__((ext_vector_type(4)));
typedef float f2 __attribute__((ext_vector_type(2)));
typedef short s4 __attribute__((ext_vector_type(4)));
typedef __bf16 bf8 __attribute__((ext_vector_type(8)));
typedef __bf16 bf2 __attribute__((ext_vector_type(2)));
#define LAS __attribute__((address_space(3)))
#define GAS __attribute__((address_space(1)))
typedef __amdgpu_buffer_rsrc_t rsrc_t;

constexpr int S_ = 2048, D_ = 448, F_ = 1792;
constexpr int WS_EPOCH = 0;
constexpr int WS_KVF = 4096;
constexpr int WS_HI = 11264;
constexpr int WS_ZF = 16384;
constexpr int WS_UF = 32768;
constexpr int WS_YF = 49152;
constexpr int WS_ST = 262144;
constexpr int WS_ZB = 1 << 20;
constexpr int WS_UB = 5 << 20;
constexpr int WS_KVR = 9 << 20;
constexpr int WS_YB = 11 << 20;
constexpr int WS_STAMP = 14 << 20;
constexpr int WS_BYTES = 16 << 20;
constexpr int LDS_BYTES = 163840;
constexpr u32 SPIN_CAP = 1u << 16;

struct Params {
  const u16 *x, *k, *v, *shk, *h, *dv, *win, *wsp, *bsp, *wout;
  u16 *out;
  unsigned char *ws;
};

#define SB() __builtin_amdgcn_sched_barrier(0)
#define BARW() do { SB(); asm volatile("s_waitcnt lgkmcnt(0)\n\ts_barrier" ::: "memory"); SB(); } while (0)
#define BAR0() do { SB(); asm volatile("s_barrier" ::: "memory"); SB(); } while (0)
#define FRESH() do { asm volatile("" : "+v"(l)); g = l >> 4; c = l & 15; } while (0)
#define DRAIN() asm volatile("s_waitcnt vmcnt(0)" ::: "memory")
#define PIN(x) asm volatile("" : "+v"(x))
#define STAMP(n) do { } while (0)
#define STAMPA(n) do { } while (0)
#define STAMP_DECL do { } while (0)
#define STAMP_OUT(slot) do { } while (0)

static __device__ __forceinline__ rsrc_t mk(const void *p, int bytes) { return __builtin_amdgcn_make_buffer_rsrc((void *)p, 0, bytes, 0x00020000); }
static __device__ __forceinline__ v4u ld16(rsrc_t r, int vo, int so) { return __builtin_amdgcn_raw_buffer_load_b128(r, vo, so, 0); }
static __device__ __forceinline__ v4u ld16c(rsrc_t r, int vo, int so) { return __builtin_amdgcn_raw_buffer_load_b128(r, vo, so, 16); }
static __device__ __forceinline__ u32 ld4(rsrc_t r, int vo, int so) { return __builtin_amdgcn_raw_buffer_load_b32(r, vo, so, 0); }
static __device__ __forceinline__ void st16c(v4u v, rsrc_t r, int vo, int so) { __builtin_amdgcn_raw_buffer_store_b128(v, r, vo + so, 0, 16); }
template <int N, typename Fn> static __device__ __forceinline__ void unr(Fn &&f) {
  if constexpr (N > 0) { unr<N - 1>(f); f(std::integral_constant<int, N - 1>{}); }
}
static __device__ __forceinline__ f4 mfma(v4u a, v4u b, f4 c) {
  return __builtin_amdgcn_mfma_f32_16x16x32_bf16(__builtin_bit_cast(bf8, a), __builtin_bit_cast(bf8, b), c, 0, 0, 0);
}
static __device__ __forceinline__ float asf(u32 u) { return __builtin_bit_cast(float, u); }
static __device__ __forceinline__ u32 asu(float f) { return __builtin_bit_cast(u32, f); }
static __device__ __forceinline__ float lo16(u32 u) { return asf(u << 16); }
static __device__ __forceinline__ float hi16(u32 u) { return asf(u & 0xffff0000u); }
static __device__ __forceinline__ u32 pk(float a, float b) { f2 t = {a, b}; return __builtin_bit_cast(u32, __builtin_convertvector(t, bf2)); }
static __device__ __forceinline__ float rbf(float a) { return lo16(pk(a, a)); }
static __device__ __forceinline__ v4u lds16(LAS unsigned char *L, int off) { return *(LAS v4u *)(L + off); }
static __device__ __forceinline__ f4 ldsf4(LAS unsigned char *L, int off) { return *(LAS f4 *)(L + off); }
static __device__ __forceinline__ void sts16(LAS unsigned char *L, int off, v4u v) { *(LAS v4u *)(L + off) = v; }
static __device__ __forceinline__ v2u trd(LAS unsigned char *L, int off) {
  return __builtin_bit_cast(v2u, __builtin_amdgcn_ds_read_tr16_b64_v4i16((LAS s4 *)(L + off)));
}
static __device__ __forceinline__ v4u trd2(LAS unsigned char *L, int off, int step) {
  const v2u a = trd(L, off), b = trd(L, off + step);
  return v4u{a.x, a.y, b.x, b.y};
}
static __device__ __forceinline__ float gelu(float x) {
  const float e = __builtin_amdgcn_exp2f(x * __builtin_fmaf(x * x, -0.1029432397f, -2.302208198f));
  return x * __builtin_amdgcn_rcpf(1.0f + e);
}
template <int CTRL> static __device__ __forceinline__ float dpp(float v) {
  return asf(__builtin_amdgcn_update_dpp(0, asu(v), CTRL, 0xf, 0xf, true));
}
static __device__ __forceinline__ float rowsum(float v) {
  v += dpp<0x128>(v);
  v += dpp<0x124>(v);
  v += dpp<0x122>(v);
  v += dpp<0x121>(v);
  return v;
}
static __device__ __forceinline__ u64 ldflag(unsigned char *ws, int off) { return __hip_atomic_load((GAS u64 *)(ws + off), __ATOMIC_RELAXED, __HIP_MEMORY_SCOPE_AGENT); }
static __device__ __forceinline__ void stflag(unsigned char *ws, int off, u64 v) { __hip_atomic_store((GAS u64 *)(ws + off), v, __ATOMIC_RELAXED, __HIP_MEMORY_SCOPE_AGENT); }
static __device__ __forceinline__ void wait_flags(unsigned char *ws, int off, u64 tag) {
#pragma unroll 1
  for (u32 s = 0; s < SPIN_CAP; ++s) {
    const u64 v = ldflag(ws, off);
    if (__all(v == tag)) break;
    __builtin_amdgcn_s_sleep(1);
  }
  asm volatile("" ::: "memory");
}
static __device__ __forceinline__ float inv_sigma(float var) {
  const float sd = rbf(__builtin_amdgcn_sqrtf(rbf(rbf(fmaxf(var, 0.f)) + 1e-5f)));
  return __builtin_amdgcn_rcpf(sd);
}
static __device__ __forceinline__ int mod7(int a) { return a >= 7 ? a - 7 : a; }

constexpr int L_XR = 114688;
constexpr int L_Z1 = 131072;
constexpr int L_YT = 147456;
constexpr int L_CS = 156672;
constexpr int L_XST = 160768;
constexpr int L_ZST = 161280;
constexpr int L_CNT = 161792;

#define MIX(cnt, n) do { for (int q_ = 0; q_ < (cnt); ++q_) { __builtin_amdgcn_sched_group_barrier(8, 1, 0); __builtin_amdgcn_sched_group_barrier(0x100, (n), 0); } } while (0)

struct Tile { int i, j, rot1, rot2, rot3; };

static __device__ __forceinline__ void gemm_compute(const Params &p, LAS unsigned char *L, const Tile t, int w, u64 tag) {
  STAMP_DECL;
  STAMP(0);
  const int i = t.i, j = t.j, rh = w >> 1, ch = w & 1;
  int l = threadIdx.x & 63, g = l >> 4, c = l & 15;
  const rsrc_t rws = mk(p.ws, WS_BYTES);

  {
    const rsrc_t rk = mk(p.k, S_ * D_ * 2), rv = mk(p.v, S_ * D_ * 2);
    const int c2 = l & 31, rg = 2 * w + (l >> 5), row0 = 64 * i + 8 * rg;
    const int vo = row0 * 896 + j * 128 + c2 * 4, vh = row0 > 0 ? vo - 3 * 896 : 0x40000000;
    u32 kr[11], vr[8];
#pragma unroll
    for (int r = 0; r < 3; ++r) kr[r] = ld4(rk, vh + r * 896, 0);
#pragma unroll
    for (int r = 0; r < 8; ++r) kr[3 + r] = ld4(rk, vo + (r & 3) * 896, (r >> 2) * 3584);
#pragma unroll
    for (int r = 0; r < 8; ++r) vr[r] = ld4(rv, vo + (r & 3) * 896, (r >> 2) * 3584);
    const v4u shk = *(const v4u *)(p.shk + (64 * j + 2 * c2) * 4);
    if (threadIdx.x == 0) *(LAS u32 *)(L + L_CNT) = 0;
    SB();
#pragma unroll
    for (int h = 0; h < 2; ++h) {
      float o[8];
      const u32 s01 = h ? shk.z : shk.x, s23 = h ? shk.w : shk.y;
      const float t0 = lo16(s01), t1 = hi16(s01), t2 = lo16(s23), t3 = hi16(s23);
#pragma unroll
      for (int r = 0; r < 8; ++r) {
        const float k3 = h ? hi16(kr[r]) : lo16(kr[r]), k2 = h ? hi16(kr[r + 1]) : lo16(kr[r + 1]);
        const float k1 = h ? hi16(kr[r + 2]) : lo16(kr[r + 2]), k0 = h ? hi16(kr[r + 3]) : lo16(kr[r + 3]);
        const float ks = __builtin_fmaf(k0, t0, __builtin_fmaf(k1, t1, __builtin_fmaf(k2, t2, k3 * t3)));
        o[r] = ks * (h ? hi16(vr[r]) : lo16(vr[r]));
      }
      sts16(L, L_YT + (2 * c2 + h) * 144 + (7 - rg) * 16, v4u{pk(o[7], o[6]), pk(o[5], o[4]), pk(o[3], o[2]), pk(o[1], o[0])});
    }
  }
  STAMP(1);

  v4u xf[2][14], wf[2][4];
  f4 a2[2][4], a1[2][4];
  int rb[4];
  const int rdoff = (8 * g + (c >> 2)) * 256 + ((((c >> 2) | ((g & 1) << 2)) ^ (4 * ch)) << 5) + (c & 3) * 8;
  auto bases = [&]() {
#pragma unroll
    for (int n = 0; n < 4; ++n) { rb[n] = rdoff ^ (n << 5); PIN(rb[n]); }
  };
  auto rdw = [&](int ks, v4u (&f)[4]) {
#pragma unroll
    for (int n = 0; n < 4; ++n) {
      if (ks == 7) { rb[n] += 57344; PIN(rb[n]); }
      f[n] = trd2(L, rb[n] + (ks % 7) * 8192, 1024);
    }
  };
  auto mma = [&](int ks, const v4u (&f)[4], f4 (&a)[2][4]) {
#pragma unroll
    for (int n = 0; n < 4; ++n)
#pragma unroll
      for (int m = 0; m < 2; ++m) a[m][n] = mfma(xf[m][ks], f[n], a[m][n]);
  };
#pragma unroll
  for (int n = 0; n < 4; ++n) a2[0][n] = a2[1][n] = a1[0][n] = a1[1][n] = f4{0, 0, 0, 0};
  bases();
  {
    const v4u ones = {0x3f803f80u, 0x3f803f80u, 0x3f803f80u, 0x3f803f80u};
    f4 sx = {0, 0, 0, 0}, gr = {0, 0, 0, 0};
    int xo0 = L_XR + (32 * rh + c) * 128 + ((g ^ (c & 7)) << 4), xo1 = xo0 ^ 64, xs0 = xo0 + ch * 2048, xs1 = xo1 + ch * 2048;
    PIN(xo0); PIN(xo1); PIN(xs0); PIN(xs1);
    v4u xs[2];
    auto step = [&](int ks) {
      mma(ks, wf[ks & 1], a2);
      sx = mfma(xs[ks & 1], ones, sx);
      gr = mfma(xs[ks & 1], xs[ks & 1], gr);
    };
    auto rd = [&](int ks) {
      const int so = ((ks >> 1) & 3) * 8192;
      xf[0][ks] = lds16(L, ((ks & 1) ? xo1 : xo0) + so);
      xf[1][ks] = lds16(L, ((ks & 1) ? xo1 : xo0) + so + 2048);
      rdw(ks, wf[ks & 1]);
      xs[ks & 1] = lds16(L, ((ks & 1) ? xs1 : xs0) + so);
    };
#pragma unroll
    for (int st = 0; st < 7; ++st) {
      STAMPA(2 * st + 1);
      if (st == 0) BARW(); else BAR0();
      STAMPA(2 * st + 2);
      if (st == 0) {
#pragma unroll
        for (int q = 0; q < 2; ++q) {
          const int cc = 16 * w + 8 * q + (l >> 3);
          st16c(lds16(L, L_YT + cc * 144 + (l & 7) * 16), rws, (64 * j + cc) * 4096 + (31 - i) * 128 + (l & 7) * 16, WS_KVR);
        }
        STAMP(2);
      }
      if (st == 2) {
        DRAIN();
        if (l == 0) stflag(p.ws, WS_KVF + ((4 * j + w) * 32 + i) * 8, tag);
        SB();
      }
      rd(2 * st);
      if (st > 0) { step(2 * st - 1); MIX(10, 2); }
      SB();
      rd(2 * st + 1);
      step(2 * st);
      MIX(10, 2);
      SB();
    }
    step(13);
    const int e = c & 3;
    const float s1 = e == 0 ? sx.x : e == 1 ? sx.y : e == 2 ? sx.z : sx.w;
    const float s2 = e == 0 ? gr.x : e == 1 ? gr.y : e == 2 ? gr.z : gr.w;
    const float m = s1 * (1.0f / 448.0f);
    const float var = __builtin_fmaf(-m, m, s2 * (1.0f / 448.0f));
    if ((c >> 2) == g) *(LAS f2 *)(L + L_XST + (32 * rh + 8 * g + 4 * ch + e) * 8) = f2{rbf(m), inv_sigma(var)};
  }
  BARW();
  STAMP(3);
  FRESH();
  const u32 tagA = (u32)tag, tagB = (u32)(tag >> 32) ^ 0x9e3779b9u;
  auto act = [&](f4 (&a)[2][4], int cso, int n0, int n1) {
    float mm[8], rr[8];
#pragma unroll
    for (int q = 0; q < 4; ++q) {
      const f4 s = ldsf4(L, L_XST + (32 * rh + 8 * g) * 8 + q * 16);
      mm[2 * q] = s.x; rr[2 * q] = s.y; mm[2 * q + 1] = s.z; rr[2 * q + 1] = s.w;
    }
#pragma unroll
    for (int n = n0; n < n1; ++n) {
      const f4 s = ldsf4(L, L_CS + cso + ((4 * ch + n) * 16 + c) * 16);
      const float cv = (s.x + s.y) + (s.z + s.w);
#pragma unroll
      for (int m = 0; m < 2; ++m)
#pragma unroll
        for (int e = 0; e < 4; ++e) a[m][n][e] = gelu(rr[4 * m + e] * __builtin_fmaf(-mm[4 * m + e], cv, a[m][n][e]));
    }
  };
  {
    act(a2, 0, 0, 4);
    float sa = 0, sq = 0;
#pragma unroll
    for (int q = 0; q < 8; ++q) {
      float s = 0, u = 0;
#pragma unroll
      for (int n = 0; n < 4; ++n) { const float z = a2[q >> 2][n][q & 3]; s += z; u = __builtin_fmaf(z, z, u); }
      s = rowsum(s);
      u = rowsum(u);
      if ((c & 7) == q) { sa = s; sq = u; }
    }
    if (c < 8) st16c(v4u{asu(sa), tagA, asu(sq), tagB}, rws, ((i * 2 + rh) * 16 + 2 * j + ch) * 512 + (8 * g + c) * 16, WS_ST);
  }
  STAMP(4);
  BAR0();
  STAMP(5);
  v4u sg[7];
  const int sto = (i * 2 + rh) * 8192 + l * 16;
  bases();
  rdw(0, wf[0]);
  SB();
#pragma unroll
  for (int ks = 0; ks < 14; ++ks) {
    if (ks < 13) rdw(ks + 1, wf[(ks + 1) & 1]);
    if (ks == 9) {
#pragma unroll
      for (int q = 0; q < 7; ++q) sg[q] = ld16c(rws, sto + (q & 3) * 1024, WS_ST + (q >> 2) * 4096);
    }
    mma(ks, wf[ks & 1], a1);
    MIX(8, 1);
    SB();
  }
  if (l == 0) __hip_atomic_fetch_add((LAS u32 *)(L + L_CNT), 1u, __ATOMIC_RELAXED, __HIP_MEMORY_SCOPE_WORKGROUP);
  STAMP(6);
  FRESH();
  {
#pragma unroll 1
    for (u32 s = 0; s < SPIN_CAP; ++s) {
      bool ok = true;
#pragma unroll
      for (int q = 0; q < 7; ++q) ok = ok && sg[q].y == tagA && sg[q].w == tagB;
      if (__all(ok)) break;
      asm volatile("" ::: "memory");
#pragma unroll
      for (int q = 0; q < 7; ++q) sg[q] = ld16c(rws, sto + (q & 3) * 1024, WS_ST + (q >> 2) * 4096);
    }
    float s = 0, u = 0;
#pragma unroll
    for (int q = 0; q < 7; ++q) { s += asf(sg[q].x); u += asf(sg[q].z); }
    s += __shfl_xor(s, 32);
    u += __shfl_xor(u, 32);
    const float mean = s * (1.0f / 896.0f);
    const float r = inv_sigma(__builtin_fmaf(-mean, mean, u * (1.0f / 896.0f)));
    *(LAS f2 *)(L + L_ZST + (32 * rh + (l & 31)) * 8) = f2{-rbf(mean) * r, r};
    f4 zs[4];
#pragma unroll
    for (int q = 0; q < 4; ++q) zs[q] = ldsf4(L, L_ZST + (32 * rh + 8 * g) * 8 + q * 16);
    STAMP(7);
    const int zo = (j * 64 + 2 * i + rh) * 8192 + (4 * ch + ((c >> 2) & 1)) * 1024 + (g * 16 + 4 * (c >> 3) + (c & 3)) * 16;
#pragma unroll
    for (int n = 0; n < 4; ++n) {
      u32 o[4];
#pragma unroll
      for (int q = 0; q < 4; ++q)
        o[q] = pk(__builtin_fmaf(a2[q >> 1][n][(2 * q) & 3], zs[q].y, zs[q].x), __builtin_fmaf(a2[q >> 1][n][(2 * q + 1) & 3], zs[q].w, zs[q].z));
      st16c(v4u{o[0], o[1], o[2], o[3]}, rws, zo + (n >> 1) * 2048 + (n & 1) * 128, WS_ZB);
    }
  }
  SB();
  {
    const int po = L_Z1 + (64 * ch + c) * 128 + ((((2 * rh + (g >> 1)) ^ (((c >> 1) & 1) | ((c >> 2) & 2))) << 5) | ((g & 1) << 4));
#pragma unroll
    for (int h = 0; h < 2; ++h) {
      act(a1, 2048, 2 * h, 2 * h + 2);
#pragma unroll
      for (int n = 2 * h; n < 2 * h + 2; ++n)
        sts16(L, po + n * 2048, v4u{pk(a1[0][n][0], a1[0][n][1]), pk(a1[0][n][2], a1[0][n][3]), pk(a1[1][n][0], a1[1][n][1]), pk(a1[1][n][2], a1[1][n][3])});
      SB();
      if (h == 0) {
        DRAIN();
        if (l == 0) stflag(p.ws, WS_ZF + ((j * 2 + ch) * 64 + 2 * i + rh) * 8, tag);
        SB();
      }
    }
  }
  STAMP(8);
  FRESH();
  v2u xe[4];
  u32 bias[4];
  {
    const rsrc_t rx = mk(p.x, S_ * D_ * 2);
#pragma unroll
    for (int n = 0; n < 4; ++n) {
      xe[n] = __builtin_amdgcn_raw_buffer_load_b64(rx, (64 * i + 16 * w + c) * 896 + j * 128 + g * 8 + n * 32, 0, 0);
      bias[n] = p.bsp[64 * i + 16 * n + c];
    }
  }
  wait_flags(p.ws, WS_ZF + ((j * 2 + rh) * 64 + l) * 8, tag);
  STAMP(9);
  f4 g2[4][2];
  {
#define G2_AH 6
#define G2_AD 1
    constexpr int AH = G2_AH, AD = G2_AD;
    v4u bf[64][2], af[AD + 1][4];
    const int bvo = w * 2048 + l * 16;
    int cso[8];
#pragma unroll
    for (int q = 0; q < 8; ++q) cso[q] = WS_ZB + (j * 8 + ((q + t.rot2) & 7)) * 65536;
    auto ldb = [&](int ks) {
      bf[ks][0] = ld16c(rws, bvo, cso[ks >> 3] + (ks & 7) * 8192);
      bf[ks][1] = ld16c(rws, bvo + 1024, cso[ks >> 3] + (ks & 7) * 8192);
    };
#pragma unroll
    for (int ks = 0; ks < AH; ++ks) ldb(ks);
    int ab[2][2];
    ab[0][0] = c * 128 + ((g ^ (c & 7)) << 4);
    ab[1][0] = ab[0][0] ^ 64;
    ab[0][1] = ab[0][0] + 65536;
    ab[1][1] = ab[1][0] + 65536;
    PIN(ab[0][0]); PIN(ab[1][0]); PIN(ab[0][1]); PIN(ab[1][1]);
    auto rda = [&](int ks) {
#pragma unroll
      for (int m = 0; m < 4; ++m) af[ks % (AD + 1)][m] = lds16(L, ab[ks & 1][(ks >> 4) & 1] + ((ks >> 1) & 7) * 8192 + m * 2048);
    };
#pragma unroll
    for (int m = 0; m < 4; ++m) g2[m][0] = g2[m][1] = f4{0, 0, 0, 0};
    BARW();
    STAMP(10);
#pragma unroll
    for (int ks = 0; ks < AD; ++ks) rda(ks);
    SB();
    unr<64>([&](auto ksc) {
      constexpr int ks = ksc;
      if constexpr (ks + AH < 64) ldb(ks + AH);
      if constexpr (ks + AD < 64) rda(ks + AD);
#pragma unroll
      for (int b = 0; b < 2; ++b)
#pragma unroll
        for (int m = 0; m < 4; ++m) g2[m][b] = mfma(bf[ks][b], af[ks % (AD + 1)][m], g2[m][b]);
      __builtin_amdgcn_sched_group_barrier(0x20, 2, 0);
      MIX(4, 1);
      SB();
      if constexpr ((ks & 7) == 7 && ks != 47) BAR0();
    });
  }
  STAMP(11);
  FRESH();
  {
    const int zr = L_Z1 + (32 * w + 8 * g + (c >> 2)) * 128 + (c & 3) * 8;
    const int sw = ((c >> 3) & 1) | ((g & 1) << 1);
#pragma unroll
    for (int m = 0; m < 4; ++m) {
      const v4u z = trd2(L, zr + ((m ^ sw) << 5), 512);
      const float b = lo16(bias[m]);
      const v4u o = {pk(lo16(z.x) * (g2[m][0].x + b), hi16(z.x) * (g2[m][0].y + b)), pk(lo16(z.y) * (g2[m][0].z + b), hi16(z.y) * (g2[m][0].w + b)),
                     pk(lo16(z.z) * (g2[m][1].x + b), hi16(z.z) * (g2[m][1].y + b)), pk(lo16(z.w) * (g2[m][1].z + b), hi16(z.w) * (g2[m][1].w + b))};
      st16c(o, rws, l * 16, WS_UB + ((i * 4 + m) * 28 + 4 * j + w) * 1024);
    }
  }
  DRAIN();
  if (l == 0) stflag(p.ws, WS_UF + (i * 32 + 4 * j + w) * 8, tag);
  wait_flags(p.ws, WS_UF + (i * 32 + (l < 28 ? l : 0)) * 8, tag);
  STAMP(12);
  FRESH();
  {
    constexpr int AH = 12;
    v4u uf[28], of[2][8];
    int uso[7];
#pragma unroll
    for (int q = 0; q < 7; ++q) uso[q] = WS_UB + (i * 4 + w) * 28672 + mod7(q + t.rot3) * 4096;
    auto ldu = [&](int ks) { uf[ks] = ld16c(rws, l * 16 + (ks & 3) * 1024, uso[ks >> 2]); };
#pragma unroll
    for (int ks = 0; ks < AH; ++ks) ldu(ks);
    f4 o[4];
    int ob[4];
    const int oro = (8 * g + (c >> 2)) * 128 + ((((c >> 3) & 1) | ((g & 1) << 1)) << 5) + (c & 3) * 8;
#pragma unroll
    for (int n = 0; n < 4; ++n) { o[n] = f4{0, 0, 0, 0}; ob[n] = oro ^ (n << 5); PIN(ob[n]); }
    auto rd = [&](int kp, v4u (&f)[8]) {
#pragma unroll
      for (int q = 0; q < 8; ++q) {
        const int n = q & 3, ks = 2 * kp + (q >> 2);
        if (ks == 14) { ob[n] += 57344; PIN(ob[n]); }
        f[q] = trd2(L, ob[n] + (ks % 14) * 4096, 512);
      }
    };
    BAR0();
    rd(0, of[0]);
    SB();
#pragma unroll
    for (int kp = 0; kp < 14; ++kp) {
      if (2 * kp + AH < 28) { ldu(2 * kp + AH); ldu(2 * kp + AH + 1); }
      if (kp < 13) rd(kp + 1, of[(kp + 1) & 1]);
#pragma unroll
      for (int q = 0; q < 8; ++q) o[q & 3] = mfma(of[kp & 1][q], uf[2 * kp + (q >> 2)], o[q & 3]);
      __builtin_amdgcn_sched_group_barrier(0x20, 2, 0);
      MIX(8, 2);
      SB();
    }
    STAMP(13);
    BAR0();
    FRESH();
    const f2 st = *(LAS f2 *)(L + L_XST + (16 * w + c) * 8);
#pragma unroll
    for (int n = 0; n < 4; ++n) {
      const v2u y = trd(L, L_YT + (16 * n + 4 * g + (c >> 2)) * 144 + w * 32 + (c & 3) * 8);
      const float xv[4] = {lo16(xe[n].x), hi16(xe[n].x), lo16(xe[n].y), hi16(xe[n].y)};
      const float yv[4] = {lo16(y.x), hi16(y.x), lo16(y.y), hi16(y.y)};
      float r[4];
#pragma unroll
      for (int e = 0; e < 4; ++e) {
        const float a = o[n][e];
        const float sg = a * __builtin_amdgcn_rcpf(1.0f + __builtin_amdgcn_exp2f(a * -1.442695041f));
        r[e] = __builtin_fmaf((xv[e] - st.x) * st.y * yv[e], sg, xv[e]);
      }
      *(v2u *)((unsigned char *)p.out + (64 * i + 16 * w + c) * 896 + j * 128 + n * 32 + g * 8) = v2u{pk(r[0], r[1]), pk(r[2], r[3])};
    }
  }
  STAMP_OUT(0);
}

static __device__ __forceinline__ void gemm_move(const Params &p, LAS unsigned char *L, const Tile t, int m, u64 tag) {
  STAMP_DECL;
  STAMP(0);
  const int i = t.i, j = t.j;
  int l = threadIdx.x & 63, g = l >> 4, c = l & 15;
  const rsrc_t rx = mk(p.x, S_ * D_ * 2), rwin = mk(p.win, D_ * F_ * 2), rwsp = mk(p.wsp, S_ * S_ * 2);
  v4u xr[14], w2[28], w1[28], ar[64];
  const int xvo = (64 * i + 32 * (m >> 1) + 4 * (m & 1) + 8 * (l >> 5) + ((l >> 3) & 3)) * 896 + (l & 7) * 16;
  const int wvo = (4 * m + g) * 3584 + c * 16 + j * 256;
  const int avo = (64 * i + (l >> 3)) * 4096 + (l & 7) * 16 + m * 128;
  int sb[7];
#pragma unroll
  for (int s = 0; s < 7; ++s) sb[s] = mod7(s + t.rot1);
  auto ld_stage = [&](int s) {
    xr[2 * s] = ld16(rx, xvo, sb[s] * 128);
    xr[2 * s + 1] = ld16(rx, xvo, sb[s] * 128 + 16 * 896);
#pragma unroll
    for (int q = 0; q < 4; ++q) w2[4 * s + q] = ld16(rwin, wvo + 1792, (sb[s] * 4 + q) * 57344);
  };
  auto ld_next = [&](int n) {
    if (n < 28) w1[n] = ld16(rwin, wvo, (sb[n >> 2] * 4 + (n & 3)) * 57344);
    else if (n < 92) ar[n - 28] = ld16(rwsp, avo, ((((n - 28) >> 3) + t.rot2) & 7) * 512 + ((n - 28) & 7) * 32768);
  };
  ld_stage(0);
  ld_stage(1);
  SB();
  const int rsw = ((l & 7) ^ (l >> 3)) << 4;
  int xwo = L_XR + (16 * m + (l >> 3)) * 128 + rsw;
  int wwo = (4 * m + g) * 256 + ((((c >> 1) ^ (g | ((m & 2) << 1))) << 5) | ((c & 1) << 4)), wwh = wwo + 65536;
  PIN(xwo); PIN(wwo); PIN(wwh);
  const u32 one = (c & 1) ? 0x3f800000u : 0x3f80u;
  const v4u sel = {(c >> 1) == 0 ? one : 0u, (c >> 1) == 1 ? one : 0u, (c >> 1) == 2 ? one : 0u, (c >> 1) == 3 ? one : 0u};
  f4 cs = {0, 0, 0, 0};
  auto put_w = [&](int n, v4u v) {
    sts16(L, (n < 16 ? wwo : wwh) + (n & 15) * 4096, v);
    cs = mfma(sel, v, cs);
  };
  auto put_cs = [&](int off) {
    if (g < 2) {
#pragma unroll
      for (int e = 0; e < 4; ++e) *(LAS float *)(L + L_CS + off + (8 * c + 4 * g + e) * 16 + m * 4) = cs[e];
    }
    cs = f4{0, 0, 0, 0};
  };
#pragma unroll
  for (int s = 0; s < 7; ++s) {
    sts16(L, xwo + (s & 3) * 8192, xr[2 * s]);
    sts16(L, xwo + (s & 3) * 8192 + 1024, xr[2 * s + 1]);
#pragma unroll
    for (int q = 0; q < 4; ++q) put_w(4 * s + q, w2[4 * s + q]);
    SB();
    STAMPA(2 * s + 1);
    if (s + 2 < 7) ld_stage(s + 2);
    else {
#pragma unroll
      for (int q = 0; q < 6; ++q) ld_next(6 * (s - 5) + q);
    }
    STAMPA(2 * s + 2);
    BARW();
    if (s == 0) STAMP(1);
  }
  STAMP(2);
  put_cs(0);
#pragma unroll
  for (int q = 12; q < 16; ++q) ld_next(q);
  BARW();
  STAMP(3);
#pragma unroll
  for (int s = 0; s < 7; ++s) {
#pragma unroll
    for (int q = 0; q < 4; ++q) put_w(4 * s + q, w1[4 * s + q]);
    SB();
    if (s > 0) {
#pragma unroll
      for (int q = 0; q < 4; ++q) ld_next(12 + 4 * s + q);
    }
    SB();
  }
  put_cs(2048);
  STAMP(4);
  BARW();
  STAMP(5);
#pragma unroll
  for (int n = 40; n < 72; ++n) ld_next(n);
  SB();
  STAMP(6);
#pragma unroll 1
  for (u32 s = 0; s < SPIN_CAP; ++s) {
    if (__hip_atomic_load((LAS u32 *)(L + L_CNT), __ATOMIC_RELAXED, __HIP_MEMORY_SCOPE_WORKGROUP) >= 4) break;
    __builtin_amdgcn_s_sleep(1);
  }
  asm volatile("" ::: "memory");
  FRESH();
  int awo = m * 8192 + (l >> 3) * 128 + (((l & 7) ^ (l >> 3)) << 4), awh = awo + 65536;
  PIN(awo); PIN(awh);
  auto put_a = [&](int q) {
#pragma unroll
    for (int r = 0; r < 8; ++r) sts16(L, ((q & 2) ? awh : awo) + (q & 1) * 32768 + r * 1024, ar[8 * q + r]);
  };
#pragma unroll
  for (int q = 0; q < 4; ++q) {
    put_a(q);
    SB();
#pragma unroll
    for (int n = 0; n < 5; ++n) ld_next(72 + 5 * q + n);
    SB();
  }
  STAMP(7);
  BARW();
  STAMP(8);
#pragma unroll
  for (int q = 4; q < 8; ++q) {
    BARW();
    put_a(q);
  }
  BARW();
  BAR0();
  STAMP(9);
  {
    const rsrc_t rwout = mk(p.wout, 896 * D_ * 2);
    v4u wo[28];
    int lg[7];
#pragma unroll
    for (int q = 0; q < 7; ++q) lg[q] = mod7(q + 2 * (i & 3));
    const int ovo = (8 * m + (l >> 3)) * 896 + (l & 7) * 16 + j * 128;
    auto ldo = [&](int n) { wo[n] = ld16(rwout, ovo, (mod7(lg[n >> 2] + t.rot3) * 4 + (n & 3)) * 28672); };
#pragma unroll
    for (int n = 0; n < 12; ++n) ldo(n);
    BAR0();
    STAMP(10);
    const int owo = (8 * m + (l >> 3)) * 128 + (((((l & 7) >> 1) ^ ((g & 1) | ((m & 1) << 1))) << 5) | ((l & 1) << 4));
#pragma unroll
    for (int q = 0; q < 7; ++q) {
#pragma unroll
      for (int n = 0; n < 4; ++n) sts16(L, owo + lg[q] * 16384 + n * 4096, wo[4 * q + n]);
      SB();
      if (q < 4) {
#pragma unroll
        for (int n = 0; n < 4; ++n) ldo(12 + 4 * q + n);
      }
      SB();
    }
  }
  STAMP(11);
  BARW();
  wait_flags(p.ws, WS_YF + (((64 * j + l) / 14) * 4 + 2 * (l & 1) + (i >> 4)) * 8, tag);
  STAMP(12);
  {
    const rsrc_t rws = mk(p.ws, WS_BYTES);
#pragma unroll
    for (int q = 0; q < 2; ++q) {
      const int cc = 16 * m + 8 * q + (l >> 3);
      sts16(L, L_YT + cc * 144 + (l & 7) * 16, ld16c(rws, (64 * j + cc) * 4096 + i * 128 + (l & 7) * 16, WS_YB));
    }
  }
  BARW();
  STAMP_OUT(1);
}

constexpr int C_BUF = 37568, C_KV = 32992;
static __device__ __forceinline__ constexpr int copy_off(int s) { return s * 4112 + (s >> 1) * 32; }

static __device__ __forceinline__ void conv_compute(const Params &p, LAS unsigned char *L, int w) {
  STAMP_DECL;
  STAMP(0);
  const int l = threadIdx.x & 63, g = l >> 4, c = l & 15, ch = w >> 1, hf = w & 1;
  {
    const v4u z = {0, 0, 0, 0};
    if (l < 8) sts16(L, w * C_BUF + l * 4112 + (l >> 1) * 32, z);
    if (l < 30) sts16(L, w * C_BUF + C_KV + 4096 + l * 16, z);
  }
  const int hro = ch * C_BUF + (c & 7) * 4112 + ((c & 7) >> 1) * 32 + (g + (c >> 3)) * 16 + hf * 256;
  const int kro = ch * C_BUF + C_KV + (254 - 2 * c + g) * 16 + hf * 256 - 7 * 512;
  const int dwo = ch * C_BUF + copy_off(4) * hf + 16 + l * 16;
  v4u hfr[2][8], kfr[2][8];
  auto rd = [&](int set, int q) {
#pragma unroll
    for (int d = 0; d < 8; ++d) {
      hfr[q & 1][d] = lds16(L, hro + set + d * 512 + q * 64);
      kfr[q & 1][7 - d] = lds16(L, kro + set + d * 512 + q * 64);
    }
  };
  BARW();
  STAMP(2);
  rd(0, 0);
#pragma unroll 1
  for (int rho = 0; rho < 7; ++rho) {
    const int set = (rho & 1) * 2 * C_BUF, nset = 2 * C_BUF - set;
    f4 acc[8];
#pragma unroll
    for (int n = 0; n < 8; ++n) acc[n] = f4{0, 0, 0, 0};
#pragma unroll
    for (int q = 0; q < 4; ++q) {
      if (rho == 3) STAMP(7 + q);
      if (q == 3) BARW();
      if (q == 3 && rho == 3) STAMP(11);
      SB();
      if (q < 3) rd(set, q + 1); else rd(nset, 0);
#pragma unroll
      for (int P = 0; P < 8; ++P)
#pragma unroll
        for (int d = 7 - P; d >= 0; --d) acc[P + d] = mfma(hfr[q & 1][P], kfr[q & 1][d], acc[P + d]);
      for (int q_ = 0; q_ < 16; ++q_) { __builtin_amdgcn_sched_group_barrier(8, 2, 0); __builtin_amdgcn_sched_group_barrier(0x100, 1, 0); }
      SB();
    }
#pragma unroll
    for (int n = 0; n < 8; ++n) *(LAS f4 *)(L + dwo + set + copy_off(n & 3) + (n >> 2) * 2048) = acc[n];
    if (rho == 3) STAMP(12);
    BARW();
    if (rho == 0) STAMP(3);
    if (rho == 1) STAMP(4);
    if (rho == 3) STAMP(5);
    if (rho == 5) STAMP(6);
  }
  STAMP_OUT(0);
}

static __device__ __forceinline__ void conv_stage(const Params &p, LAS unsigned char *L, int v, int m, u64 tag) {
  STAMP_DECL;
  STAMP(0);
  const int l = threadIdx.x & 63, ch = m >> 1, hf = m & 1;
  const rsrc_t rws = mk(p.ws, WS_BYTES), rh = mk(p.h, D_ * S_ * 2);
  v4u hc[2], hp[2], kq[2];
  const int hvo = (128 * hf + l) * 16, hpo = hvo > 0 ? hvo - 16 : 0x40000000, kvo = (128 * (hf ^ 1) + l) * 16;
  auto load_h = [&](int rho) {
    const int so = (14 * v + 2 * rho + ch) * 4096;
    hc[0] = ld16(rh, hvo, so);
    hp[0] = ld16(rh, hpo, so);
    hc[1] = ld16(rh, hvo + 1024, so);
    hp[1] = ld16(rh, hvo + 1008, so);
  };
  auto load_kv = [&](int rho) {
    const int so = WS_KVR + (14 * v + 2 * rho + ch) * 4096;
    kq[0] = ld16c(rws, kvo, so);
    kq[1] = ld16c(rws, kvo + 1024, so);
  };
  auto stage_h = [&](int set) {
#pragma unroll
    for (int q = 0; q < 2; ++q) {
      const int o = set + ch * C_BUF + hvo + q * 1024 + 16;
      const u32 d[8] = {hp[q].x, hp[q].y, hp[q].z, hp[q].w, hc[q].x, hc[q].y, hc[q].z, hc[q].w};
      u32 e[7];
#pragma unroll
      for (int r = 0; r < 7; ++r) e[r] = __builtin_amdgcn_alignbit(d[r + 1], d[r], 16);
#pragma unroll
      for (int s = 0; s < 8; ++s) {
        const int r = s >> 1;
        const v4u val = (s & 1) ? v4u{d[r + 1], d[r + 2], d[r + 3], d[r + 4]} : v4u{e[r], e[r + 1], e[r + 2], e[r + 3]};
        sts16(L, o + copy_off(s), val);
      }
    }
  };
  auto stage_kv = [&](int set) {
    sts16(L, set + ch * C_BUF + C_KV + kvo, kq[0]);
    sts16(L, set + ch * C_BUF + C_KV + kvo + 1024, kq[1]);
  };
  const u32 dvl = p.dv[14 * v + (l < 14 ? l : 0)];
  load_h(0);
  stage_h(0);
  SB();
  load_h(1);
  stage_h(2 * C_BUF);
  SB();
  load_h(2);
  if (l == 0) stflag(p.ws, WS_HI + (v * 4 + m) * 8, tag);
  wait_flags(p.ws, WS_KVF + (((14 * v + (l < 32 ? 0 : 13)) >> 4) * 32 + (l & 31)) * 8, tag);
  STAMP(1);
  load_kv(0);
  v4u k1[2] = {kq[0], kq[1]};
  load_kv(1);
  sts16(L, ch * C_BUF + C_KV + kvo, k1[0]);
  sts16(L, ch * C_BUF + C_KV + kvo + 1024, k1[1]);
  BARW();
  STAMP(2);
  stage_kv(2 * C_BUF);
  load_kv(2);
  const int dro = ch * C_BUF + (l >> 5) * copy_off(1) + hf * 2048 + 16 + ((l & 1) * 32 + ((l >> 1) & 15)) * 16;
  const int kvr = ch * C_BUF + C_KV + (255 - 128 * hf - l) * 16;
#pragma unroll 1
  for (int rho = 0; rho < 7; ++rho) {
    const int set = (rho & 1) * 2 * C_BUF;
    if (rho == 3) STAMP(7);
    BARW();
    BAR0();
    if (rho == 2) STAMP(3);
    const float dv = lo16(__builtin_amdgcn_readlane(dvl, 2 * rho + ch));
    f4 s[2][4];
    v4u kv[2];
#pragma unroll
    for (int q = 0; q < 2; ++q) {
#pragma unroll
      for (int r = 0; r < 4; ++r) s[q][r] = ldsf4(L, dro + set + copy_off(4 * (r >> 1) + 2 * q) + (r & 1) * 256);
      kv[q] = lds16(L, kvr + set - q * 1024);
    }
#pragma unroll
    for (int q = 0; q < 2; ++q) {
      const f4 a = s[q][0] + s[q][2], b = s[q][1] + s[q][3];
      const v4u o = {pk(__builtin_fmaf(hi16(kv[q].w), dv, a.x), __builtin_fmaf(lo16(kv[q].w), dv, a.y)),
                     pk(__builtin_fmaf(hi16(kv[q].z), dv, a.z), __builtin_fmaf(lo16(kv[q].z), dv, a.w)),
                     pk(__builtin_fmaf(hi16(kv[q].y), dv, b.x), __builtin_fmaf(lo16(kv[q].y), dv, b.y)),
                     pk(__builtin_fmaf(hi16(kv[q].x), dv, b.z), __builtin_fmaf(lo16(kv[q].x), dv, b.w))};
      st16c(o, rws, hf * 2048 + q * 1024 + l * 16, WS_YB + (14 * v + 2 * rho + ch) * 4096);
    }
    SB();
    if (rho == 2) STAMP(4);
    if (rho < 5) {
      stage_h(set);
      stage_kv(set);
      SB();
      if (rho == 2) STAMP(5);
      if (rho < 4) { load_h(rho + 3); load_kv(rho + 3); }
    }
  }
  DRAIN();
  if (l == 0) stflag(p.ws, WS_YF + (v * 4 + m) * 8, tag);
  STAMP(10);
  if (v == 0 && m == 0) {
#pragma unroll 1
    for (u32 s = 0; s < SPIN_CAP; ++s) {
      bool ok = true;
#pragma unroll
      for (int q = 0; q < 8; ++q) {
        const v4u f = ld16c(rws, l * 16 + (q & 3) * 1024, WS_KVF + (q >> 2) * 4096);
        ok = ok && f.x == (u32)tag && f.y == (u32)(tag >> 32) && f.z == (u32)tag && f.w == (u32)(tag >> 32);
      }
      if (__all(ok)) break;
      asm volatile("" ::: "memory");
    }
    if (l == 0) stflag(p.ws, WS_EPOCH, tag);
  }
  STAMP_OUT(1);
}

__global__ void __launch_bounds__(512, 2) fused_kernel(Params p) {
  extern __shared__ __attribute__((aligned(16))) unsigned char smem[];
  LAS unsigned char *L = (LAS unsigned char *)smem;
  const u64 tag = ldflag(p.ws, WS_EPOCH) + 1;
  const int b = blockIdx.x, x = b & 7, r = b >> 3, wv = __builtin_amdgcn_readfirstlane(threadIdx.x >> 6);
  if (r >= 28) {
    if (wv < 4) conv_compute(p, L, wv);
    else conv_stage(p, L, x * 4 + (r - 28), wv - 4, tag);
  } else {
    const int pg = x >> 1, o = x & 1;
    Tile t;
    if (r < 24) { t.i = 8 * pg + r / 3; t.j = 4 * o + r % 3; }
    else { t.i = 8 * pg + 4 * o + (r - 24); t.j = 3; }
    const int s = 2 * ((t.i + t.j) & 3);
    t.rot1 = s;
    t.rot2 = s;
    t.rot3 = 2 * (t.j & 3);
    if (wv < 4) gemm_compute(p, L, t, wv, tag);
    else gemm_move(p, L, t, wv - 4, tag);
  }
}

extern "C" void kernel_launch(void *const *d_in, const int *in_sizes, int n_in, void *d_out, int out_size, void *d_ws, size_t ws_size,
                              hipStream_t stream) {
  Params p;
  p.x = (const u16 *)d_in[0];
  p.k = (const u16 *)d_in[1];
  p.v = (const u16 *)d_in[2];
  p.shk = (const u16 *)d_in[3];
  p.h = (const u16 *)d_in[4];
  p.dv = (const u16 *)d_in[5];
  p.win = (const u16 *)d_in[7];
  p.wsp = (const u16 *)d_in[8];
  p.bsp = (const u16 *)d_in[9];
  p.wout = (const u16 *)d_in[10];
  p.out = (u16 *)d_out;
  p.ws = (unsigned char *)d_ws;
  (void)hipFuncSetAttribute((const void *)fused_kernel, hipFuncAttributeMaxDynamicSharedMemorySize, LDS_BYTES);
  fused_kernel<<<256, 512, LDS_BYTES, stream>>>(p);
}
```

```cpp
#include <hip/hip_runtime.h>
#include <stdint.h>
#include <type_traits>

typedef unsigned short u16;
typedef unsigned int u32;
typedef unsigned long long u64;
typedef u32 v4u __attribute__((ext_vector_type(4)));
typedef u32 v2u __attribute__((ext_vector_type(2)));
typedef float f4 __attribute__((ext_vector_type(4)));
typedef float f2 __attribute__((ext_vector_type(2)));
typedef short s4 __attribute__((ext_vector_type(4)));
typedef __bf16 bf8 __attribute__((ext_vector_type(8)));
typedef __bf16 bf2 __attribute__((ext_vector_type(2)));
#define LAS __attribute__((address_space(3)))
#define GAS __attribute__((address_space(1)))
typedef __amdgpu_buffer_rsrc_t rsrc_t;

constexpr int S_ = 2048, D_ = 448, F_ = 1792;
constexpr int WS_EPOCH = 0;
constexpr int WS_KVF = 4096;
constexpr int WS_HI = 11264;
constexpr int WS_ZF = 16384;
constexpr int WS_UF = 32768;
constexpr int WS_YF = 49152;
constexpr int WS_ST = 262144;
constexpr int WS_ZB = 1 << 20;
constexpr int WS_UB = 5 << 20;
constexpr int WS_KVR = 9 << 20;
constexpr int WS_YB = 11 << 20;
constexpr int WS_STAMP = 14 << 20;
constexpr int WS_BYTES = 16 << 20;
constexpr int LDS_BYTES = 163840;
constexpr u32 SPIN_CAP = 1u << 16;

struct Params {
  const u16 *x, *k, *v, *shk, *h, *dv, *win, *wsp, *bsp, *wout;
  u16 *out;
  unsigned char *ws;
};

#define SB() __builtin_amdgcn_sched_barrier(0)
#define BARW() do { SB(); asm volatile("s_waitcnt lgkmcnt(0)\n\ts_barrier" ::: "memory"); SB(); } while (0)
#define BAR0() do { SB(); asm volatile("s_barrier" ::: "memory"); SB(); } while (0)
#define FRESH() do { asm volatile("" : "+v"(l)); g = l >> 4; c = l & 15; } while (0)
#define DRAIN() asm volatile("s_waitcnt vmcnt(0)" ::: "memory")
#define PIN(x) asm volatile("" : "+v"(x))
#define STAMP(n) do { } while (0)
#define STAMPA(n) do { } while (0)
#define STAMP_DECL do { } while (0)
#define STAMP_OUT(slot) do { } while (0)

static __device__ __forceinline__ rsrc_t mk(const void *p, int bytes) { return __builtin_amdgcn_make_buffer_rsrc((void *)p, 0, bytes, 0x00020000); }
static __device__ __forceinline__ v4u ld16(rsrc_t r, int vo, int so) { return __builtin_amdgcn_raw_buffer_load_b128(r, vo, so, 0); }
static __device__ __forceinline__ v4u ld16c(rsrc_t r, int vo, int so) { return __builtin_amdgcn_raw_buffer_load_b128(r, vo, so, 16); }
static __device__ __forceinline__ u32 ld4(rsrc_t r, int vo, int so) { return __builtin_amdgcn_raw_buffer_load_b32(r, vo, so, 0); }
static __device__ __forceinline__ void st16c(v4u v, rsrc_t r, int vo, int so) { __builtin_amdgcn_raw_buffer_store_b128(v, r, vo + so, 0, 16); }
template <int N, typename Fn> static __device__ __forceinline__ void unr(Fn &&f) {
  if constexpr (N > 0) { unr<N - 1>(f); f(std::integral_constant<int, N - 1>{}); }
}
static __device__ __forceinline__ f4 mfma(v4u a, v4u b, f4 c) {
  return __builtin_amdgcn_mfma_f32_16x16x32_bf16(__builtin_bit_cast(bf8, a), __builtin_bit_cast(bf8, b), c, 0, 0, 0);
}
static __device__ __forceinline__ float asf(u32 u) { return __builtin_bit_cast(float, u); }
static __device__ __forceinline__ u32 asu(float f) { return __builtin_bit_cast(u32, f); }
static __device__ __forceinline__ float lo16(u32 u) { return asf(u << 16); }
static __device__ __forceinline__ float hi16(u32 u) { return asf(u & 0xffff0000u); }
static __device__ __forceinline__ u32 pk(float a, float b) { f2 t = {a, b}; return __builtin_bit_cast(u32, __builtin_convertvector(t, bf2)); }
static __device__ __forceinline__ float rbf(float a) { return lo16(pk(a, a)); }
static __device__ __forceinline__ v4u lds16(LAS unsigned char *L, int off) { return *(LAS v4u *)(L + off); }
static __device__ __forceinline__ f4 ldsf4(LAS unsigned char *L, int off) { return *(LAS f4 *)(L + off); }
static __device__ __forceinline__ void sts16(LAS unsigned char *L, int off, v4u v) { *(LAS v4u *)(L + off) = v; }
static __device__ __forceinline__ v2u trd(LAS unsigned char *L, int off) {
  return __builtin_bit_cast(v2u, __builtin_amdgcn_ds_read_tr16_b64_v4i16((LAS s4 *)(L + off)));
}
static __device__ __forceinline__ v4u trd2(LAS unsigned char *L, int off, int step) {
  const v2u a = trd(L, off), b = trd(L, off + step);
  return v4u{a.x, a.y, b.x, b.y};
}
static __device__ __forceinline__ float gelu(float x) {
  const float e = __builtin_amdgcn_exp2f(x * __builtin_fmaf(x * x, -0.1029432397f, -2.302208198f));
  return x * __builtin_amdgcn_rcpf(1.0f + e);
}
template <int CTRL> static __device__ __forceinline__ float dpp(float v) {
  return asf(__builtin_amdgcn_update_dpp(0, asu(v), CTRL, 0xf, 0xf, true));
}
static __device__ __forceinline__ float rowsum(float v) {
  v += dpp<0x128>(v);
  v += dpp<0x124>(v);
  v += dpp<0x122>(v);
  v += dpp<0x121>(v);
  return v;
}
static __device__ __forceinline__ u64 ldflag(unsigned char *ws, int off) { return __hip_atomic_load((GAS u64 *)(ws + off), __ATOMIC_RELAXED, __HIP_MEMORY_SCOPE_AGENT); }
static __device__ __forceinline__ void stflag(unsigned char *ws, int off, u64 v) { __hip_atomic_store((GAS u64 *)(ws + off), v, __ATOMIC_RELAXED, __HIP_MEMORY_SCOPE_AGENT); }
static __device__ __forceinline__ void wait_flags(unsigned char *ws, int off, u64 tag) {
#pragma unroll 1
  for (u32 s = 0; s < SPIN_CAP; ++s) {
    const u64 v = ldflag(ws, off);
    if (__all(v == tag)) break;
    __builtin_amdgcn_s_sleep(1);
  }
  asm volatile("" ::: "memory");
}
static __device__ __forceinline__ float inv_sigma(float var) {
  const float sd = rbf(__builtin_amdgcn_sqrtf(rbf(rbf(fmaxf(var, 0.f)) + 1e-5f)));
  return __builtin_amdgcn_rcpf(sd);
}
static __device__ __forceinline__ int mod7(int a) { return a >= 7 ? a - 7 : a; }

constexpr int L_XR = 114688;
constexpr int L_Z1 = 131072;
constexpr int L_YT = 147456;
constexpr int L_CS = 156672;
constexpr int L_XST = 160768;
constexpr int L_ZST = 161280;
constexpr int L_CNT = 161792;

#define MIX(cnt, n) do { for (int q_ = 0; q_ < (cnt); ++q_) { __builtin_amdgcn_sched_group_barrier(8, 1, 0); __builtin_amdgcn_sched_group_barrier(0x100, (n), 0); } } while (0)

struct Tile { int i, j, rot1, rot2, rot3; };

static __device__ __forceinline__ void gemm_compute(const Params &p, LAS unsigned char *L, const Tile t, int w, u64 tag) {
  STAMP_DECL;
  STAMP(0);
  const int i = t.i, j = t.j, rh = w >> 1, ch = w & 1;
  int l = threadIdx.x & 63, g = l >> 4, c = l & 15;
  const rsrc_t rws = mk(p.ws, WS_BYTES);

  {
    const rsrc_t rk = mk(p.k, S_ * D_ * 2), rv = mk(p.v, S_ * D_ * 2);
    const int c2 = l & 31, rg = 2 * w + (l >> 5), row0 = 64 * i + 8 * rg;
    const int vo = row0 * 896 + j * 128 + c2 * 4, vh = row0 > 0 ? vo - 3 * 896 : 0x40000000;
    u32 kr[11], vr[8];
#pragma unroll
    for (int r = 0; r < 3; ++r) kr[r] = ld4(rk, vh + r * 896, 0);
#pragma unroll
    for (int r = 0; r < 8; ++r) kr[3 + r] = ld4(rk, vo + (r & 3) * 896, (r >> 2) * 3584);
#pragma unroll
    for (int r = 0; r < 8; ++r) vr[r] = ld4(rv, vo + (r & 3) * 896, (r >> 2) * 3584);
    const v4u shk = *(const v4u *)(p.shk + (64 * j + 2 * c2) * 4);
    if (threadIdx.x == 0) *(LAS u32 *)(L + L_CNT) = 0;
    SB();
#pragma unroll
    for (int h = 0; h < 2; ++h) {
      float o[8];
      const u32 s01 = h ? shk.z : shk.x, s23 = h ? shk.w : shk.y;
      const float t0 = lo16(s01), t1 = hi16(s01), t2 = lo16(s23), t3 = hi16(s23);
#pragma unroll
      for (int r = 0; r < 8; ++r) {
        const float k3 = h ? hi16(kr[r]) : lo16(kr[r]), k2 = h ? hi16(kr[r + 1]) : lo16(kr[r + 1]);
        const float k1 = h ? hi16(kr[r + 2]) : lo16(kr[r + 2]), k0 = h ? hi16(kr[r + 3]) : lo16(kr[r + 3]);
        const float ks = __builtin_fmaf(k0, t0, __builtin_fmaf(k1, t1, __builtin_fmaf(k2, t2, k3 * t3)));
        o[r] = ks * (h ? hi16(vr[r]) : lo16(vr[r]));
      }
      sts16(L, L_YT + (2 * c2 + h) * 144 + (7 - rg) * 16, v4u{pk(o[7], o[6]), pk(o[5], o[4]), pk(o[3], o[2]), pk(o[1], o[0])});
    }
  }
  STAMP(1);

  v4u xf[2][14], wf[2][4];
  f4 a2[2][4], a1[2][4];
  int rb[4];
  const int rdoff = (8 * g + (c >> 2)) * 256 + ((((c >> 2) | ((g & 1) << 2)) ^ (4 * ch)) << 5) + (c & 3) * 8;
  auto bases = [&]() {
#pragma unroll
    for (int n = 0; n < 4; ++n) { rb[n] = rdoff ^ (n << 5); PIN(rb[n]); }
  };
  auto rdw = [&](int ks, v4u (&f)[4]) {
#pragma unroll
    for (int n = 0; n < 4; ++n) {
      if (ks == 7) { rb[n] += 57344; PIN(rb[n]); }
      f[n] = trd2(L, rb[n] + (ks % 7) * 8192, 1024);
    }
  };
  auto mma = [&](int ks, const v4u (&f)[4], f4 (&a)[2][4]) {
#pragma unroll
    for (int n = 0; n < 4; ++n)
#pragma unroll
      for (int m = 0; m < 2; ++m) a[m][n] = mfma(xf[m][ks], f[n], a[m][n]);
  };
#pragma unroll
  for (int n = 0; n < 4; ++n) a2[0][n] = a2[1][n] = a1[0][n] = a1[1][n] = f4{0, 0, 0, 0};
  bases();
  {
    const v4u ones = {0x3f803f80u, 0x3f803f80u, 0x3f803f80u, 0x3f803f80u};
    f4 sx = {0, 0, 0, 0}, gr = {0, 0, 0, 0};
    int xo0 = L_XR + (32 * rh + c) * 128 + ((g ^ (c & 7)) << 4), xo1 = xo0 ^ 64, xs0 = xo0 + ch * 2048, xs1 = xo1 + ch * 2048;
    PIN(xo0); PIN(xo1); PIN(xs0); PIN(xs1);
    v4u xs[2];
    auto step = [&](int ks) {
      mma(ks, wf[ks & 1], a2);
      sx = mfma(xs[ks & 1], ones, sx);
      gr = mfma(xs[ks & 1], xs[ks & 1], gr);
    };
    auto rd = [&](int ks) {
      const int so = ((ks >> 1) & 3) * 8192;
      xf[0][ks] = lds16(L, ((ks & 1) ? xo1 : xo0) + so);
      xf[1][ks] = lds16(L, ((ks & 1) ? xo1 : xo0) + so + 2048);
      rdw(ks, wf[ks & 1]);
      xs[ks & 1] = lds16(L, ((ks & 1) ? xs1 : xs0) + so);
    };
#pragma unroll
    for (int st = 0; st < 7; ++st) {
      STAMPA(2 * st + 1);
      if (st == 0) BARW(); else BAR0();
      STAMPA(2 * st + 2);
      if (st == 0) {
#pragma unroll
        for (int q = 0; q < 2; ++q) {
          const int cc = 16 * w + 8 * q + (l >> 3);
          st16c(lds16(L, L_YT + cc * 144 + (l & 7) * 16), rws, (64 * j + cc) * 4096 + (31 - i) * 128 + (l & 7) * 16, WS_KVR);
        }
        STAMP(2);
      }
      if (st == 2) {
        DRAIN();
        if (l == 0) stflag(p.ws, WS_KVF + ((4 * j + w) * 32 + i) * 8, tag);
        SB();
      }
      rd(2 * st);
      if (st > 0) { step(2 * st - 1); MIX(10, 2); }
      SB();
      rd(2 * st + 1);
      step(2 * st);
      MIX(10, 2);
      SB();
    }
    step(13);
    const int e = c & 3;
    const float s1 = e == 0 ? sx.x : e == 1 ? sx.y : e == 2 ? sx.z : sx.w;
    const float s2 = e == 0 ? gr.x : e == 1 ? gr.y : e == 2 ? gr.z : gr.w;
    const float m = s1 * (1.0f / 448.0f);
    const float var = __builtin_fmaf(-m, m, s2 * (1.0f / 448.0f));
    if ((c >> 2) == g) *(LAS f2 *)(L + L_XST + (32 * rh + 8 * g + 4 * ch + e) * 8) = f2{rbf(m), inv_sigma(var)};
  }
  BARW();
  STAMP(3);
  FRESH();
  const u32 tagA = (u32)tag, tagB = (u32)(tag >> 32) ^ 0x9e3779b9u;
  auto act = [&](f4 (&a)[2][4], int cso, int n0, int n1) {
    float mm[8], rr[8];
#pragma unroll
    for (int q = 0; q < 4; ++q) {
      const f4 s = ldsf4(L, L_XST + (32 * rh + 8 * g) * 8 + q * 16);
      mm[2 * q] = s.x; rr[2 * q] = s.y; mm[2 * q + 1] = s.z; rr[2 * q + 1] = s.w;
    }
#pragma unroll
    for (int n = n0; n < n1; ++n) {
      const f4 s = ldsf4(L, L_CS + cso + ((4 * ch + n) * 16 + c) * 16);
      const float cv = (s.x + s.y) + (s.z + s.w);
#pragma unroll
      for (int m = 0; m < 2; ++m)
#pragma unroll
        for (int e = 0; e < 4; ++e) a[m][n][e] = gelu(rr[4 * m + e] * __builtin_fmaf(-mm[4 * m + e], cv, a[m][n][e]));
    }
  };
  {
    act(a2, 0, 0, 4);
    float sa = 0, sq = 0;
#pragma unroll
    for (int q = 0; q < 8; ++q) {
      float s = 0, u = 0;
#pragma unroll
      for (int n = 0; n < 4; ++n) { const float z = a2[q >> 2][n][q & 3]; s += z; u = __builtin_fmaf(z, z, u); }
      s = rowsum(s);
      u = rowsum(u);
      if ((c & 7) == q) { sa = s; sq = u; }
    }
    if (c < 8) st16c(v4u{asu(sa), tagA, asu(sq), tagB}, rws, ((i * 2 + rh) * 16 + 2 * j + ch) * 512 + (8 * g + c) * 16, WS_ST);
  }
  STAMP(4);
  BAR0();
  STAMP(5);
  v4u sg[7];
  const int sto = (i * 2 + rh) * 8192 + l * 16;
  bases();
  rdw(0, wf[0]);
  SB();
#pragma unroll
  for (int ks = 0; ks < 14; ++ks) {
    if (ks < 13) rdw(ks + 1, wf[(ks + 1) & 1]);
    if (ks == 9) {
#pragma unroll
      for (int q = 0; q < 7; ++q) sg[q] = ld16c(rws, sto + (q & 3) * 1024, WS_ST + (q >> 2) * 4096);
    }
    mma(ks, wf[ks & 1], a1);
    MIX(8, 1);
    SB();
  }
  if (l == 0) __hip_atomic_fetch_add((LAS u32 *)(L + L_CNT), 1u, __ATOMIC_RELAXED, __HIP_MEMORY_SCOPE_WORKGROUP);
  STAMP(6);
  FRESH();
  {
#pragma unroll 1
    for (u32 s = 0; s < SPIN_CAP; ++s) {
      bool ok = true;
#pragma unroll
      for (int q = 0; q < 7; ++q) ok = ok && sg[q].y == tagA && sg[q].w == tagB;
      if (__all(ok)) break;
      asm volatile("" ::: "memory");
#pragma unroll
      for (int q = 0; q < 7; ++q) sg[q] = ld16c(rws, sto + (q & 3) * 1024, WS_ST + (q >> 2) * 4096);
    }
    float s = 0, u = 0;
#pragma unroll
    for (int q = 0; q < 7; ++q) { s += asf(sg[q].x); u += asf(sg[q].z); }
    s += __shfl_xor(s, 32);
    u += __shfl_xor(u, 32);
    const float mean = s * (1.0f / 896.0f);
    const float r = inv_sigma(__builtin_fmaf(-mean, mean, u * (1.0f / 896.0f)));
    *(LAS f2 *)(L + L_ZST + (32 * rh + (l & 31)) * 8) = f2{-rbf(mean) * r, r};
    f4 zs[4];
#pragma unroll
    for (int q = 0; q < 4; ++q) zs[q] = ldsf4(L, L_ZST + (32 * rh + 8 * g) * 8 + q * 16);
    STAMP(7);
    const int zo = (j * 64 + 2 * i + rh) * 8192 + (4 * ch + ((c >> 2) & 1)) * 1024 + (g * 16 + 4 * (c >> 3) + (c & 3)) * 16;
#pragma unroll
    for (int n = 0; n < 4; ++n) {
      u32 o[4];
#pragma unroll
      for (int q = 0; q < 4; ++q)
        o[q] = pk(__builtin_fmaf(a2[q >> 1][n][(2 * q) & 3], zs[q].y, zs[q].x), __builtin_fmaf(a2[q >> 1][n][(2 * q + 1) & 3], zs[q].w, zs[q].z));
      st16c(v4u{o[0], o[1], o[2], o[3]}, rws, zo + (n >> 1) * 2048 + (n & 1) * 128, WS_ZB);
    }
  }
  SB();
  {
    const int po = L_Z1 + (64 * ch + c) * 128 + ((((2 * rh + (g >> 1)) ^ (((c >> 1) & 1) | ((c >> 2) & 2))) << 5) | ((g & 1) << 4));
#pragma unroll
    for (int h = 0; h < 2; ++h) {
      act(a1, 2048, 2 * h, 2 * h + 2);
#pragma unroll
      for (int n = 2 * h; n < 2 * h + 2; ++n)
        sts16(L, po + n * 2048, v4u{pk(a1[0][n][0], a1[0][n][1]), pk(a1[0][n][2], a1[0][n][3]), pk(a1[1][n][0], a1[1][n][1]), pk(a1[1][n][2], a1[1][n][3])});
      SB();
      if (h == 0) {
        DRAIN();
        if (l == 0) stflag(p.ws, WS_ZF + ((j * 2 + ch) * 64 + 2 * i + rh) * 8, tag);
        SB();
      }
    }
  }
  STAMP(8);
  FRESH();
  v2u xe[4];
  u32 bias[4];
  {
    const rsrc_t rx = mk(p.x, S_ * D_ * 2);
#pragma unroll
    for (int n = 0; n < 4; ++n) {
      xe[n] = __builtin_amdgcn_raw_buffer_load_b64(rx, (64 * i + 16 * w + c) * 896 + j * 128 + g * 8 + n * 32, 0, 0);
      bias[n] = p.bsp[64 * i + 16 * n + c];
    }
  }
  wait_flags(p.ws, WS_ZF + ((j * 2 + rh) * 64 + l) * 8, tag);
  STAMP(9);
  f4 g2[4][2];
  {
#define G2_AH 6
#define G2_AD 1
    constexpr int AH = G2_AH, AD = G2_AD;
    v4u bf[64][2], af[AD + 1][4];
    const int bvo = w * 2048 + l * 16;
    int cso[8];
#pragma unroll
    for (int q = 0; q < 8; ++q) cso[q] = WS_ZB + (j * 8 + ((q + t.rot2) & 7)) * 65536;
    auto ldb = [&](int ks) {
      bf[ks][0] = ld16c(rws, bvo, cso[ks >> 3] + (ks & 7) * 8192);
      bf[ks][1] = ld16c(rws, bvo + 1024, cso[ks >> 3] + (ks & 7) * 8192);
    };
#pragma unroll
    for (int ks = 0; ks < AH; ++ks) ldb(ks);
    int ab[2][2];
    ab[0][0] = c * 128 + ((g ^ (c & 7)) << 4);
    ab[1][0] = ab[0][0] ^ 64;
    ab[0][1] = ab[0][0] + 65536;
    ab[1][1] = ab[1][0] + 65536;
    PIN(ab[0][0]); PIN(ab[1][0]); PIN(ab[0][1]); PIN(ab[1][1]);
    auto rda = [&](int ks) {
#pragma unroll
      for (int m = 0; m < 4; ++m) af[ks % (AD + 1)][m] = lds16(L, ab[ks & 1][(ks >> 4) & 1] + ((ks >> 1) & 7) * 8192 + m * 2048);
    };
#pragma unroll
    for (int m = 0; m < 4; ++m) g2[m][0] = g2[m][1] = f4{0, 0, 0, 0};
    BARW();
    STAMP(10);
#pragma unroll
    for (int ks = 0; ks < AD; ++ks) rda(ks);
    SB();
    unr<64>([&](auto ksc) {
      constexpr int ks = ksc;
      if constexpr (ks + AH < 64) ldb(ks + AH);
      if constexpr (ks + AD < 64) rda(ks + AD);
#pragma unroll
      for (int b = 0; b < 2; ++b)
#pragma unroll
        for (int m = 0; m < 4; ++m) g2[m][b] = mfma(bf[ks][b], af[ks % (AD + 1)][m], g2[m][b]);
      __builtin_amdgcn_sched_group_barrier(0x20, 2, 0);
      MIX(4, 1);
      SB();
      if constexpr ((ks & 7) == 7 && ks != 47) BAR0();
    });
  }
  STAMP(11);
  FRESH();
  {
    const int zr = L_Z1 + (32 * w + 8 * g + (c >> 2)) * 128 + (c & 3) * 8;
    const int sw = ((c >> 3) & 1) | ((g & 1) << 1);
#pragma unroll
    for (int m = 0; m < 4; ++m) {
      const v4u z = trd2(L, zr + ((m ^ sw) << 5), 512);
      const float b = lo16(bias[m]);
      const v4u o = {pk(lo16(z.x) * (g2[m][0].x + b), hi16(z.x) * (g2[m][0].y + b)), pk(lo16(z.y) * (g2[m][0].z + b), hi16(z.y) * (g2[m][0].w + b)),
                     pk(lo16(z.z) * (g2[m][1].x + b), hi16(z.z) * (g2[m][1].y + b)), pk(lo16(z.w) * (g2[m][1].z + b), hi16(z.w) * (g2[m][1].w + b))};
      st16c(o, rws, l * 16, WS_UB + ((i * 4 + m) * 28 + 4 * j + w) * 1024);
    }
  }
  DRAIN();
  if (l == 0) stflag(p.ws, WS_UF + (i * 32 + 4 * j + w) * 8, tag);
  wait_flags(p.ws, WS_UF + (i * 32 + (l < 28 ? l : 0)) * 8, tag);
  STAMP(12);
  FRESH();
  {
    constexpr int AH = 12;
    v4u uf[28], of[2][8];
    int uso[7];
#pragma unroll
    for (int q = 0; q < 7; ++q) uso[q] = WS_UB + (i * 4 + w) * 28672 + mod7(q + t.rot3) * 4096;
    auto ldu = [&](int ks) { uf[ks] = ld16c(rws, l * 16 + (ks & 3) * 1024, uso[ks >> 2]); };
#pragma unroll
    for (int ks = 0; ks < AH; ++ks) ldu(ks);
    f4 o[4];
    int ob[4];
    const int oro = (8 * g + (c >> 2)) * 128 + ((((c >> 3) & 1) | ((g & 1) << 1)) << 5) + (c & 3) * 8;
#pragma unroll
    for (int n = 0; n < 4; ++n) { o[n] = f4{0, 0, 0, 0}; ob[n] = oro ^ (n << 5); PIN(ob[n]); }
    auto rd = [&](int kp, v4u (&f)[8]) {
#pragma unroll
      for (int q = 0; q < 8; ++q) {
        const int n = q & 3, ks = 2 * kp + (q >> 2);
        if (ks == 14) { ob[n] += 57344; PIN(ob[n]); }
        f[q] = trd2(L, ob[n] + (ks % 14) * 4096, 512);
      }
    };
    BAR0();
    rd(0, of[0]);
    SB();
#pragma unroll
    for (int kp = 0; kp < 14; ++kp) {
      if (2 * kp + AH < 28) { ldu(2 * kp + AH); ldu(2 * kp + AH + 1); }
      if (kp < 13) rd(kp + 1, of[(kp + 1) & 1]);
#pragma unroll
      for (int q = 0; q < 8; ++q) o[q & 3] = mfma(of[kp & 1][q], uf[2 * kp + (q >> 2)], o[q & 3]);
      __builtin_amdgcn_sched_group_barrier(0x20, 2, 0);
      MIX(8, 2);
      SB();
    }
    STAMP(13);
    BAR0();
    FRESH();
    const f2 st = *(LAS f2 *)(L + L_XST + (16 * w + c) * 8);
#pragma unroll
    for (int n = 0; n < 4; ++n) {
      const v2u y = trd(L, L_YT + (16 * n + 4 * g + (c >> 2)) * 144 + w * 32 + (c & 3) * 8);
      const float xv[4] = {lo16(xe[n].x), hi16(xe[n].x), lo16(xe[n].y), hi16(xe[n].y)};
      const float yv[4] = {lo16(y.x), hi16(y.x), lo16(y.y), hi16(y.y)};
      float r[4];
#pragma unroll
      for (int e = 0; e < 4; ++e) {
        const float a = o[n][e];
        const float sg = a * __builtin_amdgcn_rcpf(1.0f + __builtin_amdgcn_exp2f(a * -1.442695041f));
        r[e] = __builtin_fmaf((xv[e] - st.x) * st.y * yv[e], sg, xv[e]);
      }
      *(v2u *)((unsigned char *)p.out + (64 * i + 16 * w + c) * 896 + j * 128 + n * 32 + g * 8) = v2u{pk(r[0], r[1]), pk(r[2], r[3])};
    }
  }
  STAMP_OUT(0);
}

static __device__ __forceinline__ void gemm_move(const Params &p, LAS unsigned char *L, const Tile t, int m, u64 tag) {
  STAMP_DECL;
  STAMP(0);
  const int i = t.i, j = t.j;
  int l = threadIdx.x & 63, g = l >> 4, c = l & 15;
  const rsrc_t rx = mk(p.x, S_ * D_ * 2), rwin = mk(p.win, D_ * F_ * 2), rwsp = mk(p.wsp, S_ * S_ * 2);
  v4u xr[14], w2[28], w1[28], ar[64];
  const int xvo = (64 * i + 32 * (m >> 1) + 4 * (m & 1) + 8 * (l >> 5) + ((l >> 3) & 3)) * 896 + (l & 7) * 16;
  const int wvo = (4 * m + g) * 3584 + c * 16 + j * 256;
  const int avo = (64 * i + (l >> 3)) * 4096 + (l & 7) * 16 + m * 128;
  int sb[7];
#pragma unroll
  for (int s = 0; s < 7; ++s) sb[s] = mod7(s + t.rot1);
  auto ld_stage = [&](int s) {
    xr[2 * s] = ld16(rx, xvo, sb[s] * 128);
    xr[2 * s + 1] = ld16(rx, xvo, sb[s] * 128 + 16 * 896);
#pragma unroll
    for (int q = 0; q < 4; ++q) w2[4 * s + q] = ld16(rwin, wvo + 1792, (sb[s] * 4 + q) * 57344);
  };
  auto ld_next = [&](int n) {
    if (n < 28) w1[n] = ld16(rwin, wvo, (sb[n >> 2] * 4 + (n & 3)) * 57344);
    else if (n < 92) ar[n - 28] = ld16(rwsp, avo, ((((n - 28) >> 3) + t.rot2) & 7) * 512 + ((n - 28) & 7) * 32768);
  };
  ld_stage(0);
  ld_stage(1);
  SB();
  const int rsw = ((l & 7) ^ (l >> 3)) << 4;
  int xwo = L_XR + (16 * m + (l >> 3)) * 128 + rsw;
  int wwo = (4 * m + g) * 256 + ((((c >> 1) ^ (g | ((m & 2) << 1))) << 5) | ((c & 1) << 4)), wwh = wwo + 65536;
  PIN(xwo); PIN(wwo); PIN(wwh);
  const u32 one = (c & 1) ? 0x3f800000u : 0x3f80u;
  const v4u sel = {(c >> 1) == 0 ? one : 0u, (c >> 1) == 1 ? one : 0u, (c >> 1) == 2 ? one : 0u, (c >> 1) == 3 ? one : 0u};
  f4 cs = {0, 0, 0, 0};
  auto put_w = [&](int n, v4u v) {
    sts16(L, (n < 16 ? wwo : wwh) + (n & 15) * 4096, v);
    cs = mfma(sel, v, cs);
  };
  auto put_cs = [&](int off) {
    if (g < 2) {
#pragma unroll
      for (int e = 0; e < 4; ++e) *(LAS float *)(L + L_CS + off + (8 * c + 4 * g + e) * 16 + m * 4) = cs[e];
    }
    cs = f4{0, 0, 0, 0};
  };
#pragma unroll
  for (int s = 0; s < 7; ++s) {
    sts16(L, xwo + (s & 3) * 8192, xr[2 * s]);
    sts16(L, xwo + (s & 3) * 8192 + 1024, xr[2 * s + 1]);
#pragma unroll
    for (int q = 0; q < 4; ++q) put_w(4 * s + q, w2[4 * s + q]);
    SB();
    STAMPA(2 * s + 1);
    if (s + 2 < 7) ld_stage(s + 2);
    else {
#pragma unroll
      for (int q = 0; q < 6; ++q) ld_next(6 * (s - 5) + q);
    }
    STAMPA(2 * s + 2);
    BARW();
    if (s == 0) STAMP(1);
  }
  STAMP(2);
  put_cs(0);
#pragma unroll
  for (int q = 12; q < 16; ++q) ld_next(q);
  BARW();
  STAMP(3);
#pragma unroll
  for (int s = 0; s < 7; ++s) {
#pragma unroll
    for (int q = 0; q < 4; ++q) put_w(4 * s + q, w1[4 * s + q]);
    SB();
    if (s > 0) {
#pragma unroll
      for (int q = 0; q < 4; ++q) ld_next(12 + 4 * s + q);
    }
    SB();
  }
  put_cs(2048);
  STAMP(4);
  BARW();
  STAMP(5);
#pragma unroll
  for (int n = 40; n < 72; ++n) ld_next(n);
  SB();
  STAMP(6);
#pragma unroll 1
  for (u32 s = 0; s < SPIN_CAP; ++s) {
    if (__hip_atomic_load((LAS u32 *)(L + L_CNT), __ATOMIC_RELAXED, __HIP_MEMORY_SCOPE_WORKGROUP) >= 4) break;
    __builtin_amdgcn_s_sleep(1);
  }
  asm volatile("" ::: "memory");
  FRESH();
  int awo = m * 8192 + (l >> 3) * 128 + (((l & 7) ^ (l >> 3)) << 4), awh = awo + 65536;
  PIN(awo); PIN(awh);
  auto put_a = [&](int q) {
#pragma unroll
    for (int r = 0; r < 8; ++r) sts16(L, ((q & 2) ? awh : awo) + (q & 1) * 32768 + r * 1024, ar[8 * q + r]);
  };
#pragma unroll
  for (int q = 0; q < 4; ++q) {
    put_a(q);
    SB();
#pragma unroll
    for (int n = 0; n < 5; ++n) ld_next(72 + 5 * q + n);
    SB();
  }
  STAMP(7);
  BARW();
  STAMP(8);
#pragma unroll
  for (int q = 4; q < 8; ++q) {
    BARW();
    put_a(q);
  }
  BARW();
  BAR0();
  STAMP(9);
  {
    const rsrc_t rwout = mk(p.wout, 896 * D_ * 2);
    v4u wo[28];
    int lg[7];
#pragma unroll
    for (int q = 0; q < 7; ++q) lg[q] = mod7(q + 2 * (i & 3));
    const int ovo = (8 * m + (l >> 3)) * 896 + (l & 7) * 16 + j * 128;
    auto ldo = [&](int n) { wo[n] = ld16(rwout, ovo, (mod7(lg[n >> 2] + t.rot3) * 4 + (n & 3)) * 28672); };
#pragma unroll
    for (int n = 0; n < 12; ++n) ldo(n);
    BAR0();
    STAMP(10);
    const int owo = (8 * m + (l >> 3)) * 128 + (((((l & 7) >> 1) ^ ((g & 1) | ((m & 1) << 1))) << 5) | ((l & 1) << 4));
#pragma unroll
    for (int q = 0; q < 7; ++q) {
#pragma unroll
      for (int n = 0; n < 4; ++n) sts16(L, owo + lg[q] * 16384 + n * 4096, wo[4 * q + n]);
      SB();
      if (q < 4) {
#pragma unroll
        for (int n = 0; n < 4; ++n) ldo(12 + 4 * q + n);
      }
      SB();
    }
  }
  STAMP(11);
  BARW();
  wait_flags(p.ws, WS_YF + (((64 * j + l) / 14) * 4 + 2 * (l & 1) + (i >> 4)) * 8, tag);
  STAMP(12);
  {
    const rsrc_t rws = mk(p.ws, WS_BYTES);
#pragma unroll
    for (int q = 0; q < 2; ++q) {
      const int cc = 16 * m + 8 * q + (l >> 3);
      sts16(L, L_YT + cc * 144 + (l & 7) * 16, ld16c(rws, (64 * j + cc) * 4096 + i * 128 + (l & 7) * 16, WS_YB));
    }
  }
  BARW();
  STAMP_OUT(1);
}

constexpr int C_BUF = 37568, C_KV = 32992;
static __device__ __forceinline__ constexpr int copy_off(int s) { return s * 4112 + (s >> 1) * 32; }

static __device__ __forceinline__ void conv_compute(const Params &p, LAS unsigned char *L, int w) {
  STAMP_DECL;
  STAMP(0);
  const int l = threadIdx.x & 63, g = l >> 4, c = l & 15, ch = w >> 1, hf = w & 1;
  {
    const v4u z = {0, 0, 0, 0};
    if (l < 8) sts16(L, w * C_BUF + l * 4112 + (l >> 1) * 32, z);
    if (l < 30) sts16(L, w * C_BUF + C_KV + 4096 + l * 16, z);
  }
  const int hro = ch * C_BUF + (c & 7) * 4112 + ((c & 7) >> 1) * 32 + (g + (c >> 3)) * 16 + hf * 256;
  const int kro = ch * C_BUF + C_KV + (254 - 2 * c + g) * 16 + hf * 256 - 7 * 512;
  const int dwo = ch * C_BUF + copy_off(4) * hf + 16 + l * 16;
  v4u hfr[2][8], kfr[2][8];
  auto rd = [&](int set, int q) {
#pragma unroll
    for (int d = 0; d < 8; ++d) {
      hfr[q & 1][d] = lds16(L, hro + set + d * 512 + q * 64);
      kfr[q & 1][7 - d] = lds16(L, kro + set + d * 512 + q * 64);
    }
  };
  BARW();
  STAMP(2);
  rd(0, 0);
#pragma unroll 1
  for (int rho = 0; rho < 7; ++rho) {
    const int set = (rho & 1) * 2 * C_BUF, nset = 2 * C_BUF - set;
    f4 acc[8];
#pragma unroll
    for (int n = 0; n < 8; ++n) acc[n] = f4{0, 0, 0, 0};
#pragma unroll
    for (int q = 0; q < 4; ++q) {
      if (rho == 3) STAMP(7 + q);
      if (q == 3) BARW();
      if (q == 3 && rho == 3) STAMP(11);
      SB();
      if (q < 3) rd(set, q + 1); else rd(nset, 0);
#pragma unroll
      for (int P = 0; P < 8; ++P)
#pragma unroll
        for (int d = 7 - P; d >= 0; --d) acc[P + d] = mfma(hfr[q & 1][P], kfr[q & 1][d], acc[P + d]);
      for (int q_ = 0; q_ < 16; ++q_) { __builtin_amdgcn_sched_group_barrier(8, 2, 0); __builtin_amdgcn_sched_group_barrier(0x100, 1, 0); }
      SB();
    }
#pragma unroll
    for (int n = 0; n < 8; ++n) *(LAS f4 *)(L + dwo + set + copy_off(n & 3) + (n >> 2) * 2048) = acc[n];
    if (rho == 3) STAMP(12);
    BARW();
    if (rho == 0) STAMP(3);
    if (rho == 1) STAMP(4);
    if (rho == 3) STAMP(5);
    if (rho == 5) STAMP(6);
  }
  STAMP_OUT(0);
}

static __device__ __forceinline__ void conv_stage(const Params &p, LAS unsigned char *L, int v, int m, u64 tag) {
  STAMP_DECL;
  STAMP(0);
  const int l = threadIdx.x & 63, ch = m >> 1, hf = m & 1;
  const rsrc_t rws = mk(p.ws, WS_BYTES), rh = mk(p.h, D_ * S_ * 2);
  v4u hc[2], hp[2], kq[2];
  const int hvo = (128 * hf + l) * 16, hpo = hvo > 0 ? hvo - 16 : 0x40000000, kvo = (128 * (hf ^ 1) + l) * 16;
  auto load_h = [&](int rho) {
    const int so = (14 * v + 2 * rho + ch) * 4096;
    hc[0] = ld16(rh, hvo, so);
    hp[0] = ld16(rh, hpo, so);
    hc[1] = ld16(rh, hvo + 1024, so);
    hp[1] = ld16(rh, hvo + 1008, so);
  };
  auto load_kv = [&](int rho) {
    const int so = WS_KVR + (14 * v + 2 * rho + ch) * 4096;
    kq[0] = ld16c(rws, kvo, so);
    kq[1] = ld16c(rws, kvo + 1024, so);
  };
  auto stage_h = [&](int set) {
#pragma unroll
    for (int q = 0; q < 2; ++q) {
      const int o = set + ch * C_BUF + hvo + q * 1024 + 16;
      const u32 d[8] = {hp[q].x, hp[q].y, hp[q].z, hp[q].w, hc[q].x, hc[q].y, hc[q].z, hc[q].w};
      u32 e[7];
#pragma unroll
      for (int r = 0; r < 7; ++r) e[r] = __builtin_amdgcn_alignbit(d[r + 1], d[r], 16);
#pragma unroll
      for (int s = 0; s < 8; ++s) {
        const int r = s >> 1;
        const v4u val = (s & 1) ? v4u{d[r + 1], d[r + 2], d[r + 3], d[r + 4]} : v4u{e[r], e[r + 1], e[r + 2], e[r + 3]};
        sts16(L, o + copy_off(s), val);
      }
    }
  };
  auto stage_kv = [&](int set) {
    sts16(L, set + ch * C_BUF + C_KV + kvo, kq[0]);
    sts16(L, set + ch * C_BUF + C_KV + kvo + 1024, kq[1]);
  };
  const u32 dvl = p.dv[14 * v + (l < 14 ? l : 0)];
  load_h(0);
  stage_h(0);
  SB();
  load_h(1);
  stage_h(2 * C_BUF);
  SB();
  load_h(2);
  if (l == 0) stflag(p.ws, WS_HI + (v * 4 + m) * 8, tag);
  wait_flags(p.ws, WS_KVF + (((14 * v + (l < 32 ? 0 : 13)) >> 4) * 32 + (l & 31)) * 8, tag);
  STAMP(1);
  load_kv(0);
  v4u k1[2] = {kq[0], kq[1]};
  load_kv(1);
  sts16(L, ch * C_BUF + C_KV + kvo, k1[0]);
  sts16(L, ch * C_BUF + C_KV + kvo + 1024, k1[1]);
  BARW();
  STAMP(2);
  stage_kv(2 * C_BUF);
  load_kv(2);
  const int dro = ch * C_BUF + (l >> 5) * copy_off(1) + hf * 2048 + 16 + ((l & 1) * 32 + ((l >> 1) & 15)) * 16;
  const int kvr = ch * C_BUF + C_KV + (255 - 128 * hf - l) * 16;
#pragma unroll 1
  for (int rho = 0; rho < 7; ++rho) {
    const int set = (rho & 1) * 2 * C_BUF;
    if (rho == 3) STAMP(7);
    BARW();
    BAR0();
    if (rho == 2) STAMP(3);
    const float dv = lo16(__builtin_amdgcn_readlane(dvl, 2 * rho + ch));
    f4 s[2][4];
    v4u kv[2];
#pragma unroll
    for (int q = 0; q < 2; ++q) {
#pragma unroll
      for (int r = 0; r < 4; ++r) s[q][r] = ldsf4(L, dro + set + copy_off(4 * (r >> 1) + 2 * q) + (r & 1) * 256);
      kv[q] = lds16(L, kvr + set - q * 1024);
    }
#pragma unroll
    for (int q = 0; q < 2; ++q) {
      const f4 a = s[q][0] + s[q][2], b = s[q][1] + s[q][3];
      const v4u o = {pk(__builtin_fmaf(hi16(kv[q].w), dv, a.x), __builtin_fmaf(lo16(kv[q].w), dv, a.y)),
                     pk(__builtin_fmaf(hi16(kv[q].z), dv, a.z), __builtin_fmaf(lo16(kv[q].z), dv, a.w)),
                     pk(__builtin_fmaf(hi16(kv[q].y), dv, b.x), __builtin_fmaf(lo16(kv[q].y), dv, b.y)),
                     pk(__builtin_fmaf(hi16(kv[q].x), dv, b.z), __builtin_fmaf(lo16(kv[q].x), dv, b.w))};
      st16c(o, rws, hf * 2048 + q * 1024 + l * 16, WS_YB + (14 * v + 2 * rho + ch) * 4096);
    }
    SB();
    if (rho == 2) STAMP(4);
    if (rho < 5) {
      stage_h(set);
      stage_kv(set);
      SB();
      if (rho == 2) STAMP(5);
      if (rho < 4) { load_h(rho + 3); load_kv(rho + 3); }
    }
  }
  DRAIN();
  if (l == 0) stflag(p.ws, WS_YF + (v * 4 + m) * 8, tag);
  STAMP(10);
  if (v == 0 && m == 0) {
#pragma unroll 1
    for (u32 s = 0; s < SPIN_CAP; ++s) {
      bool ok = true;
#pragma unroll
      for (int q = 0; q < 8; ++q) {
        const v4u f = ld16c(rws, l * 16 + (q & 3) * 1024, WS_KVF + (q >> 2) * 4096);
        ok = ok && f.x == (u32)tag && f.y == (u32)(tag >> 32) && f.z == (u32)tag && f.w == (u32)(tag >> 32);
      }
      if (__all(ok)) break;
      asm volatile("" ::: "memory");
    }
    if (l == 0) stflag(p.ws, WS_EPOCH, tag);
  }
  STAMP_OUT(1);
}

__global__ void __launch_bounds__(512, 2) fused_kernel(Params p) {
  extern __shared__ __attribute__((aligned(16))) unsigned char smem[];
  LAS unsigned char *L = (LAS unsigned char *)smem;
  u32 pf;
  {
    u64 pc; asm volatile("s_getpc_b64 %0" : "=s"(pc));
    pc &= ~127ull;
    const u32 off = (threadIdx.x < 320 ? threadIdx.x : 0) * 128;
    asm volatile("global_load_dword %0, %1, %2" : "=v"(pf) : "v"(off), "s"(pc));
  }
  u64 tag = ldflag(p.ws, WS_EPOCH) + 1;
  asm volatile("" : "+v"(tag) : "v"(pf));
  const int b = blockIdx.x, x = b & 7, r = b >> 3, wv = __builtin_amdgcn_readfirstlane(threadIdx.x >> 6);
  if (r >= 28) {
    if (wv < 4) conv_compute(p, L, wv);
    else conv_stage(p, L, x * 4 + (r - 28), wv - 4, tag);
  } else {
    const int pg = x >> 1, o = x & 1;
    Tile t;
    if (r < 24) { t.i = 8 * pg + r / 3; t.j = 4 * o + r % 3; }
    else { t.i = 8 * pg + 4 * o + (r - 24); t.j = 3; }
    const int s = 2 * ((t.i + t.j) & 3);
    t.rot1 = s;
    t.rot2 = s;
    t.rot3 = 2 * (t.j & 3);
    if (wv < 4) gemm_compute(p, L, t, wv, tag);
    else gemm_move(p, L, t, wv - 4, tag);
  }
}

extern "C" void kernel_launch(void *const *d_in, const int *in_sizes, int n_in, void *d_out, int out_size, void *d_ws, size_t ws_size,
                              hipStream_t stream) {
  Params p;
  p.x = (const u16 *)d_in[0];
  p.k = (const u16 *)d_in[1];
  p.v = (const u16 *)d_in[2];
  p.shk = (const u16 *)d_in[3];
  p.h = (const u16 *)d_in[4];
  p.dv = (const u16 *)d_in[5];
  p.win = (const u16 *)d_in[7];
  p.wsp = (const u16 *)d_in[8];
  p.bsp = (const u16 *)d_in[9];
  p.wout = (const u16 *)d_in[10];
  p.out = (u16 *)d_out;
  p.ws = (unsigned char *)d_ws;
  (void)hipFuncSetAttribute((const void *)fused_kernel, hipFuncAttributeMaxDynamicSharedMemorySize, LDS_BYTES);
  fused_kernel<<<256, 512, LDS_BYTES, stream>>>(p);
}
```

```cpp
#include <hip/hip_runtime.h>
#include <stdint.h>
#include <type_traits>

typedef unsigned short u16;
typedef unsigned int u32;
typedef unsigned long long u64;
typedef u32 v4u __attribute__((ext_vector_type(4)));
typedef u32 v2u __attribute__((ext_vector_type(2)));
typedef float f4 __attribute__((ext_vector_type(4)));
typedef float f2 __attribute__((ext_vector_type(2)));
typedef short s4 __attribute__((ext_vector_type(4)));
typedef __bf16 bf8 __attribute__((ext_vector_type(8)));
typedef __bf16 bf2 __attribute__((ext_vector_type(2)));
#define LAS __attribute__((address_space(3)))
#define GAS __attribute__((address_space(1)))
typedef __amdgpu_buffer_rsrc_t rsrc_t;

constexpr int S_ = 2048, D_ = 448, F_ = 1792;
constexpr int WS_EPOCH = 0;
constexpr int WS_KVF = 4096;
constexpr int WS_HI = 11264;
constexpr int WS_ZF = 16384;
constexpr int WS_UF = 32768;
constexpr int WS_YF = 49152;
constexpr int WS_ST = 262144;
constexpr int WS_ZB = 1 << 20;
constexpr int WS_UB = 5 << 20;
constexpr int WS_KVR = 9 << 20;
constexpr int WS_YB = 11 << 20;
constexpr int WS_STAMP = 14 << 20;
constexpr int WS_BYTES = 16 << 20;
constexpr int LDS_BYTES = 163840;
constexpr u32 SPIN_CAP = 1u << 16;

struct Params {
  const u16 *x, *k, *v, *shk, *h, *dv, *win, *wsp, *bsp, *wout;
  u16 *out;
  unsigned char *ws;
};

#define SB() __builtin_amdgcn_sched_barrier(0)
#define BARW() do { SB(); asm volatile("s_waitcnt lgkmcnt(0)\n\ts_barrier" ::: "memory"); SB(); } while (0)
#define BAR0() do { SB(); asm volatile("s_barrier" ::: "memory"); SB(); } while (0)
#define FRESH() do { asm volatile("" : "+v"(l)); g = l >> 4; c = l & 15; } while (0)
#define DRAIN() asm volatile("s_waitcnt vmcnt(0)" ::: "memory")
#define PIN(x) asm volatile("" : "+v"(x))
#define STAMP(n) do { } while (0)
#define STAMPA(n) do { } while (0)
#define STAMP_DECL do { } while (0)
#define STAMP_OUT(slot) do { } while (0)

static __device__ __forceinline__ rsrc_t mk(const void *p, int bytes) { return __builtin_amdgcn_make_buffer_rsrc((void *)p, 0, bytes, 0x00020000); }
static __device__ __forceinline__ v4u ld16(rsrc_t r, int vo, int so) { return __builtin_amdgcn_raw_buffer_load_b128(r, vo, so, 0); }
static __device__ __forceinline__ v4u ld16c(rsrc_t r, int vo, int so) { return __builtin_amdgcn_raw_buffer_load_b128(r, vo, so, 16); }
static __device__ __forceinline__ u32 ld4(rsrc_t r, int vo, int so) { return __builtin_amdgcn_raw_buffer_load_b32(r, vo, so, 0); }
static __device__ __forceinline__ void st16c(v4u v, rsrc_t r, int vo, int so) { __builtin_amdgcn_raw_buffer_store_b128(v, r, vo + so, 0, 16); }
template <int N, typename Fn> static __device__ __forceinline__ void unr(Fn &&f) {
  if constexpr (N > 0) { unr<N - 1>(f); f(std::integral_constant<int, N - 1>{}); }
}
static __device__ __forceinline__ f4 mfma(v4u a, v4u b, f4 c) {
  return __builtin_amdgcn_mfma_f32_16x16x32_bf16(__builtin_bit_cast(bf8, a), __builtin_bit_cast(bf8, b), c, 0, 0, 0);
}
static __device__ __forceinline__ float asf(u32 u) { return __builtin_bit_cast(float, u); }
static __device__ __forceinline__ u32 asu(float f) { return __builtin_bit_cast(u32, f); }
static __device__ __forceinline__ float lo16(u32 u) { return asf(u << 16); }
static __device__ __forceinline__ float hi16(u32 u) { return asf(u & 0xffff0000u); }
static __device__ __forceinline__ u32 pk(float a, float b) { f2 t = {a, b}; return __builtin_bit_cast(u32, __builtin_convertvector(t, bf2)); }
static __device__ __forceinline__ float rbf(float a) { return lo16(pk(a, a)); }
static __device__ __forceinline__ v4u lds16(LAS unsigned char *L, int off) { return *(LAS v4u *)(L + off); }
static __device__ __forceinline__ f4 ldsf4(LAS unsigned char *L, int off) { return *(LAS f4 *)(L + off); }
static __device__ __forceinline__ void sts16(LAS unsigned char *L, int off, v4u v) { *(LAS v4u *)(L + off) = v; }
static __device__ __forceinline__ v2u trd(LAS unsigned char *L, int off) {
  return __builtin_bit_cast(v2u, __builtin_amdgcn_ds_read_tr16_b64_v4i16((LAS s4 *)(L + off)));
}
static __device__ __forceinline__ v4u trd2(LAS unsigned char *L, int off, int step) {
  const v2u a = trd(L, off), b = trd(L, off + step);
  return v4u{a.x, a.y, b.x, b.y};
}
static __device__ __forceinline__ float gelu(float x) {
  const float e = __builtin_amdgcn_exp2f(x * __builtin_fmaf(x * x, -0.1029432397f, -2.302208198f));
  return x * __builtin_amdgcn_rcpf(1.0f + e);
}
template <int CTRL> static __device__ __forceinline__ float dpp(float v) {
  return asf(__builtin_amdgcn_update_dpp(0, asu(v), CTRL, 0xf, 0xf, true));
}
static __device__ __forceinline__ float rowsum(float v) {
  v += dpp<0x128>(v);
  v += dpp<0x124>(v);
  v += dpp<0x122>(v);
  v += dpp<0x121>(v);
  return v;
}
static __device__ __forceinline__ u64 ldflag(unsigned char *ws, int off) { return __hip_atomic_load((GAS u64 *)(ws + off), __ATOMIC_RELAXED, __HIP_MEMORY_SCOPE_AGENT); }
static __device__ __forceinline__ void stflag(unsigned char *ws, int off, u64 v) { __hip_atomic_store((GAS u64 *)(ws + off), v, __ATOMIC_RELAXED, __HIP_MEMORY_SCOPE_AGENT); }
static __device__ __forceinline__ void wait_flags(unsigned char *ws, int off, u64 tag) {
#pragma unroll 1
  for (u32 s = 0; s < SPIN_CAP; ++s) {
    const u64 v = ldflag(ws, off);
    if (__all(v == tag)) break;
    __builtin_amdgcn_s_sleep(1);
  }
  asm volatile("" ::: "memory");
}
static __device__ __forceinline__ float inv_sigma(float var) {
  const float sd = rbf(__builtin_amdgcn_sqrtf(rbf(rbf(fmaxf(var, 0.f)) + 1e-5f)));
  return __builtin_amdgcn_rcpf(sd);
}
static __device__ __forceinline__ int mod7(int a) { return a >= 7 ? a - 7 : a; }

constexpr int L_XR = 114688;
constexpr int L_Z1 = 131072;
constexpr int L_YT = 147456;
constexpr int L_CS = 156672;
constexpr int L_XST = 160768;
constexpr int L_ZST = 161280;
constexpr int L_CNT = 161792;

#define MIX(cnt, n) do { for (int q_ = 0; q_ < (cnt); ++q_) { __builtin_amdgcn_sched_group_barrier(8, 1, 0); __builtin_amdgcn_sched_group_barrier(0x100, (n), 0); } } while (0)

struct Tile { int i, j, rot1, rot2, rot3; };

static __device__ __forceinline__ void gemm_compute(const Params &p, LAS unsigned char *L, const Tile t, int w, u64 tag) {
  STAMP_DECL;
  STAMP(0);
  const int i = t.i, j = t.j, rh = w >> 1, ch = w & 1;
  int l = threadIdx.x & 63, g = l >> 4, c = l & 15;
  const rsrc_t rws = mk(p.ws, WS_BYTES);

  {
    const rsrc_t rk = mk(p.k, S_ * D_ * 2), rv = mk(p.v, S_ * D_ * 2);
    const int c2 = l & 31, rg = 2 * w + (l >> 5), row0 = 64 * i + 8 * rg;
    const int vo = row0 * 896 + j * 128 + c2 * 4, vh = row0 > 0 ? vo - 3 * 896 : 0x40000000;
    u32 kr[11], vr[8];
#pragma unroll
    for (int r = 0; r < 3; ++r) kr[r] = ld4(rk, vh + r * 896, 0);
#pragma unroll
    for (int r = 0; r < 8; ++r) kr[3 + r] = ld4(rk, vo + (r & 3) * 896, (r >> 2) * 3584);
#pragma unroll
    for (int r = 0; r < 8; ++r) vr[r] = ld4(rv, vo + (r & 3) * 896, (r >> 2) * 3584);
    const v4u shk = *(const v4u *)(p.shk + (64 * j + 2 * c2) * 4);
    if (threadIdx.x == 0) *(LAS u32 *)(L + L_CNT) = 0;
    SB();
#pragma unroll
    for (int h = 0; h < 2; ++h) {
      float o[8];
      const u32 s01 = h ? shk.z : shk.x, s23 = h ? shk.w : shk.y;
      const float t0 = lo16(s01), t1 = hi16(s01), t2 = lo16(s23), t3 = hi16(s23);
#pragma unroll
      for (int r = 0; r < 8; ++r) {
        const float k3 = h ? hi16(kr[r]) : lo16(kr[r]), k2 = h ? hi16(kr[r + 1]) : lo16(kr[r + 1]);
        const float k1 = h ? hi16(kr[r + 2]) : lo16(kr[r + 2]), k0 = h ? hi16(kr[r + 3]) : lo16(kr[r + 3]);
        const float ks = __builtin_fmaf(k0, t0, __builtin_fmaf(k1, t1, __builtin_fmaf(k2, t2, k3 * t3)));
        o[r] = ks * (h ? hi16(vr[r]) : lo16(vr[r]));
      }
      sts16(L, L_YT + (2 * c2 + h) * 144 + (7 - rg) * 16, v4u{pk(o[7], o[6]), pk(o[5], o[4]), pk(o[3], o[2]), pk(o[1], o[0])});
    }
  }
  STAMP(1);

  v4u xf[2][14], wf[2][4];
  f4 a2[2][4], a1[2][4];
  int rb[4];
  const int rdoff = (8 * g + (c >> 2)) * 256 + ((((c >> 2) | ((g & 1) << 2)) ^ (4 * ch)) << 5) + (c & 3) * 8;
  auto bases = [&]() {
#pragma unroll
    for (int n = 0; n < 4; ++n) { rb[n] = rdoff ^ (n << 5); PIN(rb[n]); }
  };
  auto rdw = [&](int ks, v4u (&f)[4]) {
#pragma unroll
    for (int n = 0; n < 4; ++n) {
      if (ks == 7) { rb[n] += 57344; PIN(rb[n]); }
      f[n] = trd2(L, rb[n] + (ks % 7) * 8192, 1024);
    }
  };
  auto mma = [&](int ks, const v4u (&f)[4], f4 (&a)[2][4]) {
#pragma unroll
    for (int n = 0; n < 4; ++n)
#pragma unroll
      for (int m = 0; m < 2; ++m) a[m][n] = mfma(xf[m][ks], f[n], a[m][n]);
  };
#pragma unroll
  for (int n = 0; n < 4; ++n) a2[0][n] = a2[1][n] = a1[0][n] = a1[1][n] = f4{0, 0, 0, 0};
  bases();
  {
    const v4u ones = {0x3f803f80u, 0x3f803f80u, 0x3f803f80u, 0x3f803f80u};
    f4 sx = {0, 0, 0, 0}, gr = {0, 0, 0, 0};
    int xo0 = L_XR + (32 * rh + c) * 128 + ((g ^ (c & 7)) << 4), xo1 = xo0 ^ 64, xs0 = xo0 + ch * 2048, xs1 = xo1 + ch * 2048;
    PIN(xo0); PIN(xo1); PIN(xs0); PIN(xs1);
    v4u xs[2];
    auto step = [&](int ks) {
      mma(ks, wf[ks & 1], a2);
      sx = mfma(xs[ks & 1], ones, sx);
      gr = mfma(xs[ks & 1], xs[ks & 1], gr);
    };
    auto rd = [&](int ks) {
      const int so = ((ks >> 1) & 3) * 8192;
      xf[0][ks] = lds16(L, ((ks & 1) ? xo1 : xo0) + so);
      xf[1][ks] = lds16(L, ((ks & 1) ? xo1 : xo0) + so + 2048);
      rdw(ks, wf[ks & 1]);
      xs[ks & 1] = lds16(L, ((ks & 1) ? xs1 : xs0) + so);
    };
#pragma unroll
    for (int st = 0; st < 7; ++st) {
      STAMPA(2 * st + 1);
      if (st == 0) BARW(); else BAR0();
      STAMPA(2 * st + 2);
      if (st == 0) {
#pragma unroll
        for (int q = 0; q < 2; ++q) {
          const int cc = 16 * w + 8 * q + (l >> 3);
          st16c(lds16(L, L_YT + cc * 144 + (l & 7) * 16), rws, (64 * j + cc) * 4096 + (31 - i) * 128 + (l & 7) * 16, WS_KVR);
        }
        STAMP(2);
      }
      if (st == 2) {
        DRAIN();
        if (l == 0) stflag(p.ws, WS_KVF + ((4 * j + w) * 32 + i) * 8, tag);
        SB();
      }
      rd(2 * st);
      if (st > 0) { step(2 * st - 1); MIX(10, 2); }
      SB();
      rd(2 * st + 1);
      step(2 * st);
      MIX(10, 2);
      SB();
    }
    step(13);
    const int e = c & 3;
    const float s1 = e == 0 ? sx.x : e == 1 ? sx.y : e == 2 ? sx.z : sx.w;
    const float s2 = e == 0 ? gr.x : e == 1 ? gr.y : e == 2 ? gr.z : gr.w;
    const float m = s1 * (1.0f / 448.0f);
    const float var = __builtin_fmaf(-m, m, s2 * (1.0f / 448.0f));
    if ((c >> 2) == g) *(LAS f2 *)(L + L_XST + (32 * rh + 8 * g + 4 * ch + e) * 8) = f2{rbf(m), inv_sigma(var)};
  }
  BARW();
  STAMP(3);
  FRESH();
  const u32 tagA = (u32)tag, tagB = (u32)(tag >> 32) ^ 0x9e3779b9u;
  auto act = [&](f4 (&a)[2][4], int cso, int n0, int n1) {
    float mm[8], rr[8];
#pragma unroll
    for (int q = 0; q < 4; ++q) {
      const f4 s = ldsf4(L, L_XST + (32 * rh + 8 * g) * 8 + q * 16);
      mm[2 * q] = s.x; rr[2 * q] = s.y; mm[2 * q + 1] = s.z; rr[2 * q + 1] = s.w;
    }
#pragma unroll
    for (int n = n0; n < n1; ++n) {
      const f4 s = ldsf4(L, L_CS + cso + ((4 * ch + n) * 16 + c) * 16);
      const float cv = (s.x + s.y) + (s.z + s.w);
#pragma unroll
      for (int m = 0; m < 2; ++m)
#pragma unroll
        for (int e = 0; e < 4; ++e) a[m][n][e] = gelu(rr[4 * m + e] * __builtin_fmaf(-mm[4 * m + e], cv, a[m][n][e]));
    }
  };
  {
    act(a2, 0, 0, 4);
    float sa = 0, sq = 0;
#pragma unroll
    for (int q = 0; q < 8; ++q) {
      float s = 0, u = 0;
#pragma unroll
      for (int n = 0; n < 4; ++n) { const float z = a2[q >> 2][n][q & 3]; s += z; u = __builtin_fmaf(z, z, u); }
      s = rowsum(s);
      u = rowsum(u);
      if ((c & 7) == q) { sa = s; sq = u; }
    }
    if (c < 8) st16c(v4u{asu(sa), tagA, asu(sq), tagB}, rws, ((i * 2 + rh) * 16 + 2 * j + ch) * 512 + (8 * g + c) * 16, WS_ST);
  }
  STAMP(4);
  BAR0();
  STAMP(5);
  v4u sg[7];
  const int sto = (i * 2 + rh) * 8192 + l * 16;
  bases();
  rdw(0, wf[0]);
  SB();
#pragma unroll
  for (int ks = 0; ks < 14; ++ks) {
    if (ks < 13) rdw(ks + 1, wf[(ks + 1) & 1]);
    if (ks == 9) {
#pragma unroll
      for (int q = 0; q < 7; ++q) sg[q] = ld16c(rws, sto + (q & 3) * 1024, WS_ST + (q >> 2) * 4096);
    }
    mma(ks, wf[ks & 1], a1);
    MIX(8, 1);
    SB();
  }
  if (l == 0) __hip_atomic_fetch_add((LAS u32 *)(L + L_CNT), 1u, __ATOMIC_RELAXED, __HIP_MEMORY_SCOPE_WORKGROUP);
  STAMP(6);
  FRESH();
  {
#pragma unroll 1
    for (u32 s = 0; s < SPIN_CAP; ++s) {
      bool ok = true;
#pragma unroll
      for (int q = 0; q < 7; ++q) ok = ok && sg[q].y == tagA && sg[q].w == tagB;
      if (__all(ok)) break;
      asm volatile("" ::: "memory");
#pragma unroll
      for (int q = 0; q < 7; ++q) sg[q] = ld16c(rws, sto + (q & 3) * 1024, WS_ST + (q >> 2) * 4096);
    }
    float s = 0, u = 0;
#pragma unroll
    for (int q = 0; q < 7; ++q) { s += asf(sg[q].x); u += asf(sg[q].z); }
    s += __shfl_xor(s, 32);
    u += __shfl_xor(u, 32);
    const float mean = s * (1.0f / 896.0f);
    const float r = inv_sigma(__builtin_fmaf(-mean, mean, u * (1.0f / 896.0f)));
    *(LAS f2 *)(L + L_ZST + (32 * rh + (l & 31)) * 8) = f2{-rbf(mean) * r, r};
    f4 zs[4];
#pragma unroll
    for (int q = 0; q < 4; ++q) zs[q] = ldsf4(L, L_ZST + (32 * rh + 8 * g) * 8 + q * 16);
    STAMP(7);
    const int zo = (j * 64 + 2 * i + rh) * 8192 + (4 * ch + ((c >> 2) & 1)) * 1024 + (g * 16 + 4 * (c >> 3) + (c & 3)) * 16;
#pragma unroll
    for (int n = 0; n < 4; ++n) {
      u32 o[4];
#pragma unroll
      for (int q = 0; q < 4; ++q)
        o[q] = pk(__builtin_fmaf(a2[q >> 1][n][(2 * q) & 3], zs[q].y, zs[q].x), __builtin_fmaf(a2[q >> 1][n][(2 * q + 1) & 3], zs[q].w, zs[q].z));
      st16c(v4u{o[0], o[1], o[2], o[3]}, rws, zo + (n >> 1) * 2048 + (n & 1) * 128, WS_ZB);
    }
  }
  SB();
  {
    const int po = L_Z1 + (64 * ch + c) * 128 + ((((2 * rh + (g >> 1)) ^ (((c >> 1) & 1) | ((c >> 2) & 2))) << 5) | ((g & 1) << 4));
#pragma unroll
    for (int h = 0; h < 2; ++h) {
      act(a1, 2048, 2 * h, 2 * h + 2);
#pragma unroll
      for (int n = 2 * h; n < 2 * h + 2; ++n)
        sts16(L, po + n * 2048, v4u{pk(a1[0][n][0], a1[0][n][1]), pk(a1[0][n][2], a1[0][n][3]), pk(a1[1][n][0], a1[1][n][1]), pk(a1[1][n][2], a1[1][n][3])});
      SB();
      if (h == 0) {
        DRAIN();
        if (l == 0) stflag(p.ws, WS_ZF + ((j * 2 + ch) * 64 + 2 * i + rh) * 8, tag);
        SB();
      }
    }
  }
  STAMP(8);
  FRESH();
  v2u xe[4];
  u32 bias[4];
  {
    const rsrc_t rx = mk(p.x, S_ * D_ * 2);
#pragma unroll
    for (int n = 0; n < 4; ++n) {
      xe[n] = __builtin_amdgcn_raw_buffer_load_b64(rx, (64 * i + 16 * w + c) * 896 + j * 128 + g * 8 + n * 32, 0, 0);
      bias[n] = p.bsp[64 * i + 16 * n + c];
    }
  }
  wait_flags(p.ws, WS_ZF + ((j * 2 + rh) * 64 + l) * 8, tag);
  STAMP(9);
  f4 g2[4][2];
  {
#define G2_AH 6
#define G2_AD 1
    constexpr int AH = G2_AH, AD = G2_AD;
    v4u bf[64][2], af[AD + 1][4];
    const int bvo = w * 2048 + l * 16;
    int cso[8];
#pragma unroll
    for (int q = 0; q < 8; ++q) cso[q] = WS_ZB + (j * 8 + ((q + t.rot2) & 7)) * 65536;
    auto ldb = [&](int ks) {
      bf[ks][0] = ld16c(rws, bvo, cso[ks >> 3] + (ks & 7) * 8192);
      bf[ks][1] = ld16c(rws, bvo + 1024, cso[ks >> 3] + (ks & 7) * 8192);
    };
#pragma unroll
    for (int ks = 0; ks < AH; ++ks) ldb(ks);
    int ab[2][2];
    ab[0][0] = c * 128 + ((g ^ (c & 7)) << 4);
    ab[1][0] = ab[0][0] ^ 64;
    ab[0][1] = ab[0][0] + 65536;
    ab[1][1] = ab[1][0] + 65536;
    PIN(ab[0][0]); PIN(ab[1][0]); PIN(ab[0][1]); PIN(ab[1][1]);
    auto rda = [&](int ks) {
#pragma unroll
      for (int m = 0; m < 4; ++m) af[ks % (AD + 1)][m] = lds16(L, ab[ks & 1][(ks >> 4) & 1] + ((ks >> 1) & 7) * 8192 + m * 2048);
    };
#pragma unroll
    for (int m = 0; m < 4; ++m) g2[m][0] = g2[m][1] = f4{0, 0, 0, 0};
    BARW();
    STAMP(10);
#pragma unroll
    for (int ks = 0; ks < AD; ++ks) rda(ks);
    SB();
    unr<64>([&](auto ksc) {
      constexpr int ks = ksc;
      if constexpr (ks + AH < 64) ldb(ks + AH);
      if constexpr (ks + AD < 64) rda(ks + AD);
#pragma unroll
      for (int b = 0; b < 2; ++b)
#pragma unroll
        for (int m = 0; m < 4; ++m) g2[m][b] = mfma(bf[ks][b], af[ks % (AD + 1)][m], g2[m][b]);
      __builtin_amdgcn_sched_group_barrier(0x20, 2, 0);
      MIX(4, 1);
      SB();
      if constexpr ((ks & 7) == 7 && ks != 47) BAR0();
    });
  }
  STAMP(11);
  FRESH();
  {
    const int zr = L_Z1 + (32 * w + 8 * g + (c >> 2)) * 128 + (c & 3) * 8;
    const int sw = ((c >> 3) & 1) | ((g & 1) << 1);
#pragma unroll
    for (int m = 0; m < 4; ++m) {
      const v4u z = trd2(L, zr + ((m ^ sw) << 5), 512);
      const float b = lo16(bias[m]);
      const v4u o = {pk(lo16(z.x) * (g2[m][0].x + b), hi16(z.x) * (g2[m][0].y + b)), pk(lo16(z.y) * (g2[m][0].z + b), hi16(z.y) * (g2[m][0].w + b)),
                     pk(lo16(z.z) * (g2[m][1].x + b), hi16(z.z) * (g2[m][1].y + b)), pk(lo16(z.w) * (g2[m][1].z + b), hi16(z.w) * (g2[m][1].w + b))};
      st16c(o, rws, l * 16, WS_UB + ((i * 4 + m) * 28 + 4 * j + w) * 1024);
    }
  }
  DRAIN();
  if (l == 0) stflag(p.ws, WS_UF + (i * 32 + 4 * j + w) * 8, tag);
  wait_flags(p.ws, WS_UF + (i * 32 + (l < 28 ? l : 0)) * 8, tag);
  STAMP(12);
  FRESH();
  {
    constexpr int AH = 12;
    v4u uf[28], of[2][8];
    int uso[7];
#pragma unroll
    for (int q = 0; q < 7; ++q) uso[q] = WS_UB + (i * 4 + w) * 28672 + mod7(q + t.rot3) * 4096;
    auto ldu = [&](int ks) { uf[ks] = ld16c(rws, l * 16 + (ks & 3) * 1024, uso[ks >> 2]); };
#pragma unroll
    for (int ks = 0; ks < AH; ++ks) ldu(ks);
    f4 o[4];
    int ob[4];
    const int oro = (8 * g + (c >> 2)) * 128 + ((((c >> 3) & 1) | ((g & 1) << 1)) << 5) + (c & 3) * 8;
#pragma unroll
    for (int n = 0; n < 4; ++n) { o[n] = f4{0, 0, 0, 0}; ob[n] = oro ^ (n << 5); PIN(ob[n]); }
    auto rd = [&](int kp, v4u (&f)[8]) {
#pragma unroll
      for (int q = 0; q < 8; ++q) {
        const int n = q & 3, ks = 2 * kp + (q >> 2);
        if (ks == 14) { ob[n] += 57344; PIN(ob[n]); }
        f[q] = trd2(L, ob[n] + (ks % 14) * 4096, 512);
      }
    };
    BAR0();
    rd(0, of[0]);
    SB();
#pragma unroll
    for (int kp = 0; kp < 14; ++kp) {
      if (2 * kp + AH < 28) { ldu(2 * kp + AH); ldu(2 * kp + AH + 1); }
      if (kp < 13) rd(kp + 1, of[(kp + 1) & 1]);
#pragma unroll
      for (int q = 0; q < 8; ++q) o[q & 3] = mfma(of[kp & 1][q], uf[2 * kp + (q >> 2)], o[q & 3]);
      __builtin_amdgcn_sched_group_barrier(0x20, 2, 0);
      MIX(8, 2);
      SB();
    }
    STAMP(13);
    BAR0();
    FRESH();
    const f2 st = *(LAS f2 *)(L + L_XST + (16 * w + c) * 8);
#pragma unroll
    for (int n = 0; n < 4; ++n) {
      const v2u y = trd(L, L_YT + (16 * n + 4 * g + (c >> 2)) * 144 + w * 32 + (c & 3) * 8);
      const float xv[4] = {lo16(xe[n].x), hi16(xe[n].x), lo16(xe[n].y), hi16(xe[n].y)};
      const float yv[4] = {lo16(y.x), hi16(y.x), lo16(y.y), hi16(y.y)};
      float r[4];
#pragma unroll
      for (int e = 0; e < 4; ++e) {
        const float a = o[n][e];
        const float sg = a * __builtin_amdgcn_rcpf(1.0f + __builtin_amdgcn_exp2f(a * -1.442695041f));
        r[e] = __builtin_fmaf((xv[e] - st.x) * st.y * yv[e], sg, xv[e]);
      }
      __builtin_amdgcn_raw_buffer_store_b64(v2u{pk(r[0], r[1]), pk(r[2], r[3])}, mk(p.out, S_ * D_ * 2), (64 * i + 16 * w + c) * 896 + j * 128 + n * 32 + g * 8, 0, 17);
    }
  }
  STAMP_OUT(0);
}

static __device__ __forceinline__ void gemm_move(const Params &p, LAS unsigned char *L, const Tile t, int m, u64 tag) {
  STAMP_DECL;
  STAMP(0);
  const int i = t.i, j = t.j;
  int l = threadIdx.x & 63, g = l >> 4, c = l & 15;
  const rsrc_t rx = mk(p.x, S_ * D_ * 2), rwin = mk(p.win, D_ * F_ * 2), rwsp = mk(p.wsp, S_ * S_ * 2);
  v4u xr[14], w2[28], w1[28], ar[64];
  const int xvo = (64 * i + 32 * (m >> 1) + 4 * (m & 1) + 8 * (l >> 5) + ((l >> 3) & 3)) * 896 + (l & 7) * 16;
  const int wvo = (4 * m + g) * 3584 + c * 16 + j * 256;
  const int avo = (64 * i + (l >> 3)) * 4096 + (l & 7) * 16 + m * 128;
  int sb[7];
#pragma unroll
  for (int s = 0; s < 7; ++s) sb[s] = mod7(s + t.rot1);
  auto ld_stage = [&](int s) {
    xr[2 * s] = ld16(rx, xvo, sb[s] * 128);
    xr[2 * s + 1] = ld16(rx, xvo, sb[s] * 128 + 16 * 896);
#pragma unroll
    for (int q = 0; q < 4; ++q) w2[4 * s + q] = ld16(rwin, wvo + 1792, (sb[s] * 4 + q) * 57344);
  };
  auto ld_next = [&](int n) {
    if (n < 28) w1[n] = ld16(rwin, wvo, (sb[n >> 2] * 4 + (n & 3)) * 57344);
    else if (n < 92) ar[n - 28] = ld16(rwsp, avo, ((((n - 28) >> 3) + t.rot2) & 7) * 512 + ((n - 28) & 7) * 32768);
  };
  ld_stage(0);
  ld_stage(1);
  SB();
  const int rsw = ((l & 7) ^ (l >> 3)) << 4;
  int xwo = L_XR + (16 * m + (l >> 3)) * 128 + rsw;
  int wwo = (4 * m + g) * 256 + ((((c >> 1) ^ (g | ((m & 2) << 1))) << 5) | ((c & 1) << 4)), wwh = wwo + 65536;
  PIN(xwo); PIN(wwo); PIN(wwh);
  const u32 one = (c & 1) ? 0x3f800000u : 0x3f80u;
  const v4u sel = {(c >> 1) == 0 ? one : 0u, (c >> 1) == 1 ? one : 0u, (c >> 1) == 2 ? one : 0u, (c >> 1) == 3 ? one : 0u};
  f4 cs = {0, 0, 0, 0};
  auto put_w = [&](int n, v4u v) {
    sts16(L, (n < 16 ? wwo : wwh) + (n & 15) * 4096, v);
    cs = mfma(sel, v, cs);
  };
  auto put_cs = [&](int off) {
    if (g < 2) {
#pragma unroll
      for (int e = 0; e < 4; ++e) *(LAS float *)(L + L_CS + off + (8 * c + 4 * g + e) * 16 + m * 4) = cs[e];
    }
    cs = f4{0, 0, 0, 0};
  };
#pragma unroll
  for (int s = 0; s < 7; ++s) {
    sts16(L, xwo + (s & 3) * 8192, xr[2 * s]);
    sts16(L, xwo + (s & 3) * 8192 + 1024, xr[2 * s + 1]);
#pragma unroll
    for (int q = 0; q < 4; ++q) put_w(4 * s + q, w2[4 * s + q]);
    SB();
    STAMPA(2 * s + 1);
    if (s + 2 < 7) ld_stage(s + 2);
    else {
#pragma unroll
      for (int q = 0; q < 6; ++q) ld_next(6 * (s - 5) + q);
    }
    STAMPA(2 * s + 2);
    BARW();
    if (s == 0) STAMP(1);
  }
  STAMP(2);
  put_cs(0);
#pragma unroll
  for (int q = 12; q < 16; ++q) ld_next(q);
  BARW();
  STAMP(3);
#pragma unroll
  for (int s = 0; s < 7; ++s) {
#pragma unroll
    for (int q = 0; q < 4; ++q) put_w(4 * s + q, w1[4 * s + q]);
    SB();
    if (s > 0) {
#pragma unroll
      for (int q = 0; q < 4; ++q) ld_next(12 + 4 * s + q);
    }
    SB();
  }
  put_cs(2048);
  STAMP(4);
  BARW();
  STAMP(5);
#pragma unroll
  for (int n = 40; n < 72; ++n) ld_next(n);
  SB();
  STAMP(6);
#pragma unroll 1
  for (u32 s = 0; s < SPIN_CAP; ++s) {
    if (__hip_atomic_load((LAS u32 *)(L + L_CNT), __ATOMIC_RELAXED, __HIP_MEMORY_SCOPE_WORKGROUP) >= 4) break;
    __builtin_amdgcn_s_sleep(1);
  }
  asm volatile("" ::: "memory");
  FRESH();
  int awo = m * 8192 + (l >> 3) * 128 + (((l & 7) ^ (l >> 3)) << 4), awh = awo + 65536;
  PIN(awo); PIN(awh);
  auto put_a = [&](int q) {
#pragma unroll
    for (int r = 0; r < 8; ++r) sts16(L, ((q & 2) ? awh : awo) + (q & 1) * 32768 + r * 1024, ar[8 * q + r]);
  };
#pragma unroll
  for (int q = 0; q < 4; ++q) {
    put_a(q);
    SB();
#pragma unroll
    for (int n = 0; n < 5; ++n) ld_next(72 + 5 * q + n);
    SB();
  }
  STAMP(7);
  BARW();
  STAMP(8);
#pragma unroll
  for (int q = 4; q < 8; ++q) {
    BARW();
    put_a(q);
  }
  BARW();
  BAR0();
  STAMP(9);
  {
    const rsrc_t rwout = mk(p.wout, 896 * D_ * 2);
    v4u wo[28];
    int lg[7];
#pragma unroll
    for (int q = 0; q < 7; ++q) lg[q] = mod7(q + 2 * (i & 3));
    const int ovo = (8 * m + (l >> 3)) * 896 + (l & 7) * 16 + j * 128;
    auto ldo = [&](int n) { wo[n] = ld16(rwout, ovo, (mod7(lg[n >> 2] + t.rot3) * 4 + (n & 3)) * 28672); };
#pragma unroll
    for (int n = 0; n < 12; ++n) ldo(n);
    BAR0();
    STAMP(10);
    const int owo = (8 * m + (l >> 3)) * 128 + (((((l & 7) >> 1) ^ ((g & 1) | ((m & 1) << 1))) << 5) | ((l & 1) << 4));
#pragma unroll
    for (int q = 0; q < 7; ++q) {
#pragma unroll
      for (int n = 0; n < 4; ++n) sts16(L, owo + lg[q] * 16384 + n * 4096, wo[4 * q + n]);
      SB();
      if (q < 4) {
#pragma unroll
        for (int n = 0; n < 4; ++n) ldo(12 + 4 * q + n);
      }
      SB();
    }
  }
  STAMP(11);
  BARW();
  wait_flags(p.ws, WS_YF + (((64 * j + l) / 14) * 4 + 2 * (l & 1) + (i >> 4)) * 8, tag);
  STAMP(12);
  {
    const rsrc_t rws = mk(p.ws, WS_BYTES);
#pragma unroll
    for (int q = 0; q < 2; ++q) {
      const int cc = 16 * m + 8 * q + (l >> 3);
      sts16(L, L_YT + cc * 144 + (l & 7) * 16, ld16c(rws, (64 * j + cc) * 4096 + i * 128 + (l & 7) * 16, WS_YB));
    }
  }
  BARW();
  STAMP_OUT(1);
}

constexpr int C_BUF = 37568, C_KV = 32992;
static __device__ __forceinline__ constexpr int copy_off(int s) { return s * 4112 + (s >> 1) * 32; }

static __device__ __forceinline__ void conv_compute(const Params &p, LAS unsigned char *L, int w) {
  STAMP_DECL;
  STAMP(0);
  const int l = threadIdx.x & 63, g = l >> 4, c = l & 15, ch = w >> 1, hf = w & 1;
  {
    const v4u z = {0, 0, 0, 0};
    if (l < 8) sts16(L, w * C_BUF + l * 4112 + (l >> 1) * 32, z);
    if (l < 30) sts16(L, w * C_BUF + C_KV + 4096 + l * 16, z);
  }
  const int hro = ch * C_BUF + (c & 7) * 4112 + ((c & 7) >> 1) * 32 + (g + (c >> 3)) * 16 + hf * 256;
  const int kro = ch * C_BUF + C_KV + (254 - 2 * c + g) * 16 + hf * 256 - 7 * 512;
  const int dwo = ch * C_BUF + copy_off(4) * hf + 16 + l * 16;
  v4u hfr[2][8], kfr[2][8];
  auto rd = [&](int set, int q) {
#pragma unroll
    for (int d = 0; d < 8; ++d) {
      hfr[q & 1][d] = lds16(L, hro + set + d * 512 + q * 64);
      kfr[q & 1][7 - d] = lds16(L, kro + set + d * 512 + q * 64);
    }
  };
  BARW();
  STAMP(2);
  rd(0, 0);
#pragma unroll 1
  for (int rho = 0; rho < 7; ++rho) {
    const int set = (rho & 1) * 2 * C_BUF, nset = 2 * C_BUF - set;
    f4 acc[8];
#pragma unroll
    for (int n = 0; n < 8; ++n) acc[n] = f4{0, 0, 0, 0};
#pragma unroll
    for (int q = 0; q < 4; ++q) {
      if (rho == 3) STAMP(7 + q);
      if (q == 3) BARW();
      if (q == 3 && rho == 3) STAMP(11);
      SB();
      if (q < 3) rd(set, q + 1); else rd(nset, 0);
#pragma unroll
      for (int P = 0; P < 8; ++P)
#pragma unroll
        for (int d = 7 - P; d >= 0; --d) acc[P + d] = mfma(hfr[q & 1][P], kfr[q & 1][d], acc[P + d]);
      for (int q_ = 0; q_ < 16; ++q_) { __builtin_amdgcn_sched_group_barrier(8, 2, 0); __builtin_amdgcn_sched_group_barrier(0x100, 1, 0); }
      SB();
    }
#pragma unroll
    for (int n = 0; n < 8; ++n) *(LAS f4 *)(L + dwo + set + copy_off(n & 3) + (n >> 2) * 2048) = acc[n];
    if (rho == 3) STAMP(12);
    BARW();
    if (rho == 0) STAMP(3);
    if (rho == 1) STAMP(4);
    if (rho == 3) STAMP(5);
    if (rho == 5) STAMP(6);
  }
  STAMP_OUT(0);
}

static __device__ __forceinline__ void conv_stage(const Params &p, LAS unsigned char *L, int v, int m, u64 tag) {
  STAMP_DECL;
  STAMP(0);
  const int l = threadIdx.x & 63, ch = m >> 1, hf = m & 1;
  const rsrc_t rws = mk(p.ws, WS_BYTES), rh = mk(p.h, D_ * S_ * 2);
  v4u hc[2], hp[2], kq[2];
  const int hvo = (128 * hf + l) * 16, hpo = hvo > 0 ? hvo - 16 : 0x40000000, kvo = (128 * (hf ^ 1) + l) * 16;
  auto load_h = [&](int rho) {
    const int so = (14 * v + 2 * rho + ch) * 4096;
    hc[0] = ld16(rh, hvo, so);
    hp[0] = ld16(rh, hpo, so);
    hc[1] = ld16(rh, hvo + 1024, so);
    hp[1] = ld16(rh, hvo + 1008, so);
  };
  auto load_kv = [&](int rho) {
    const int so = WS_KVR + (14 * v + 2 * rho + ch) * 4096;
    kq[0] = ld16c(rws, kvo, so);
    kq[1] = ld16c(rws, kvo + 1024, so);
  };
  auto stage_h = [&](int set) {
#pragma unroll
    for (int q = 0; q < 2; ++q) {
      const int o = set + ch * C_BUF + hvo + q * 1024 + 16;
      const u32 d[8] = {hp[q].x, hp[q].y, hp[q].z, hp[q].w, hc[q].x, hc[q].y, hc[q].z, hc[q].w};
      u32 e[7];
#pragma unroll
      for (int r = 0; r < 7; ++r) e[r] = __builtin_amdgcn_alignbit(d[r + 1], d[r], 16);
#pragma unroll
      for (int s = 0; s < 8; ++s) {
        const int r = s >> 1;
        const v4u val = (s & 1) ? v4u{d[r + 1], d[r + 2], d[r + 3], d[r + 4]} : v4u{e[r], e[r + 1], e[r + 2], e[r + 3]};
        sts16(L, o + copy_off(s), val);
      }
    }
  };
  auto stage_kv = [&](int set) {
    sts16(L, set + ch * C_BUF + C_KV + kvo, kq[0]);
    sts16(L, set + ch * C_BUF + C_KV + kvo + 1024, kq[1]);
  };
  const u32 dvl = p.dv[14 * v + (l < 14 ? l : 0)];
  load_h(0);
  stage_h(0);
  SB();
  load_h(1);
  stage_h(2 * C_BUF);
  SB();
  load_h(2);
  if (l == 0) stflag(p.ws, WS_HI + (v * 4 + m) * 8, tag);
  wait_flags(p.ws, WS_KVF + (((14 * v + (l < 32 ? 0 : 13)) >> 4) * 32 + (l & 31)) * 8, tag);
  STAMP(1);
  load_kv(0);
  v4u k1[2] = {kq[0], kq[1]};
  load_kv(1);
  sts16(L, ch * C_BUF + C_KV + kvo, k1[0]);
  sts16(L, ch * C_BUF + C_KV + kvo + 1024, k1[1]);
  BARW();
  STAMP(2);
  stage_kv(2 * C_BUF);
  load_kv(2);
  const int dro = ch * C_BUF + (l >> 5) * copy_off(1) + hf * 2048 + 16 + ((l & 1) * 32 + ((l >> 1) & 15)) * 16;
  const int kvr = ch * C_BUF + C_KV + (255 - 128 * hf - l) * 16;
#pragma unroll 1
  for (int rho = 0; rho < 7; ++rho) {
    const int set = (rho & 1) * 2 * C_BUF;
    if (rho == 3) STAMP(7);
    BARW();
    BAR0();
    if (rho == 2) STAMP(3);
    const float dv = lo16(__builtin_amdgcn_readlane(dvl, 2 * rho + ch));
    f4 s[2][4];
    v4u kv[2];
#pragma unroll
    for (int q = 0; q < 2; ++q) {
#pragma unroll
      for (int r = 0; r < 4; ++r) s[q][r] = ldsf4(L, dro + set + copy_off(4 * (r >> 1) + 2 * q) + (r & 1) * 256);
      kv[q] = lds16(L, kvr + set - q * 1024);
    }
#pragma unroll
    for (int q = 0; q < 2; ++q) {
      const f4 a = s[q][0] + s[q][2], b = s[q][1] + s[q][3];
      const v4u o = {pk(__builtin_fmaf(hi16(kv[q].w), dv, a.x), __builtin_fmaf(lo16(kv[q].w), dv, a.y)),
                     pk(__builtin_fmaf(hi16(kv[q].z), dv, a.z), __builtin_fmaf(lo16(kv[q].z), dv, a.w)),
                     pk(__builtin_fmaf(hi16(kv[q].y), dv, b.x), __builtin_fmaf(lo16(kv[q].y), dv, b.y)),
                     pk(__builtin_fmaf(hi16(kv[q].x), dv, b.z), __builtin_fmaf(lo16(kv[q].x), dv, b.w))};
      st16c(o, rws, hf * 2048 + q * 1024 + l * 16, WS_YB + (14 * v + 2 * rho + ch) * 4096);
    }
    SB();
    if (rho == 2) STAMP(4);
    if (rho < 5) {
      stage_h(set);
      stage_kv(set);
      SB();
      if (rho == 2) STAMP(5);
      if (rho < 4) { load_h(rho + 3); load_kv(rho + 3); }
    }
  }
  DRAIN();
  if (l == 0) stflag(p.ws, WS_YF + (v * 4 + m) * 8, tag);
  STAMP(10);
  if (v == 0 && m == 0) {
#pragma unroll 1
    for (u32 s = 0; s < SPIN_CAP; ++s) {
      bool ok = true;
#pragma unroll
      for (int q = 0; q < 8; ++q) {
        const v4u f = ld16c(rws, l * 16 + (q & 3) * 1024, WS_KVF + (q >> 2) * 4096);
        ok = ok && f.x == (u32)tag && f.y == (u32)(tag >> 32) && f.z == (u32)tag && f.w == (u32)(tag >> 32);
      }
      if (__all(ok)) break;
      asm volatile("" ::: "memory");
    }
    if (l == 0) stflag(p.ws, WS_EPOCH, tag);
  }
  STAMP_OUT(1);
}

__global__ void __launch_bounds__(512, 2) fused_kernel(Params p) {
  extern __shared__ __attribute__((aligned(16))) unsigned char smem[];
  LAS unsigned char *L = (LAS unsigned char *)smem;
  const u64 tstart = __builtin_amdgcn_s_memrealtime();
  const u64 tag = ldflag(p.ws, WS_EPOCH) + 1;
  const int b = blockIdx.x, x = b & 7, r = b >> 3, wv = __builtin_amdgcn_readfirstlane(threadIdx.x >> 6);
  if (r >= 28) {
    if (wv < 4) conv_compute(p, L, wv);
    else conv_stage(p, L, x * 4 + (r - 28), wv - 4, tag);
  } else {
    const int pg = x >> 1, o = x & 1;
    Tile t;
    if (r < 24) { t.i = 8 * pg + r / 3; t.j = 4 * o + r % 3; }
    else { t.i = 8 * pg + 4 * o + (r - 24); t.j = 3; }
    const int s = 2 * ((t.i + t.j) & 3);
    t.rot1 = s;
    t.rot2 = s;
    t.rot3 = 2 * (t.j & 3);
    if (wv < 4) gemm_compute(p, L, t, wv, tag);
    else gemm_move(p, L, t, wv - 4, tag);
  }
  if (blockIdx.x == 0 && wv == 0) for (int q = 0; q < 100000; ++q) { if (__builtin_amdgcn_s_memrealtime() - tstart >= 5000) break; __builtin_amdgcn_s_sleep(1); }
}

extern "C" void kernel_launch(void *const *d_in, const int *in_sizes, int n_in, void *d_out, int out_size, void *d_ws, size_t ws_size,
                              hipStream_t stream) {
  Params p;
  p.x = (const u16 *)d_in[0];
  p.k = (const u16 *)d_in[1];
  p.v = (const u16 *)d_in[2];
  p.shk = (const u16 *)d_in[3];
  p.h = (const u16 *)d_in[4];
  p.dv = (const u16 *)d_in[5];
  p.win = (const u16 *)d_in[7];
  p.wsp = (const u16 *)d_in[8];
  p.bsp = (const u16 *)d_in[9];
  p.wout = (const u16 *)d_in[10];
  p.out = (u16 *)d_out;
  p.ws = (unsigned char *)d_ws;
  (void)hipFuncSetAttribute((const void *)fused_kernel, hipFuncAttributeMaxDynamicSharedMemorySize, LDS_BYTES);
  fused_kernel<<<256, 512, LDS_BYTES, stream>>>(p);
}
```

```cpp
#include <hip/hip_runtime.h>
#include <stdint.h>
#include <type_traits>

typedef unsigned short u16;
typedef unsigned int u32;
typedef unsigned long long u64;
typedef u32 v4u __attribute__((ext_vector_type(4)));
typedef u32 v2u __attribute__((ext_vector_type(2)));
typedef float f4 __attribute__((ext_vector_type(4)));
typedef float f2 __attribute__((ext_vector_type(2)));
typedef short s4 __attribute__((ext_vector_type(4)));
typedef __bf16 bf8 __attribute__((ext_vector_type(8)));
typedef __bf16 bf2 __attribute__((ext_vector_type(2)));
#define LAS __attribute__((address_space(3)))
#define GAS __attribute__((address_space(1)))
typedef __amdgpu_buffer_rsrc_t rsrc_t;
typedef int srd_t __attribute__((ext_vector_type(4)));

constexpr int S_ = 2048, D_ = 448, F_ = 1792;
constexpr int WS_EPOCH = 0;
constexpr int WS_KVF = 4096;
constexpr int WS_HI = 11264;
constexpr int WS_ZF = 16384;
constexpr int WS_UF = 32768;
constexpr int WS_YF = 49152;
constexpr int WS_ST = 262144;
constexpr int WS_ZB = 1 << 20;
constexpr int WS_UB = 5 << 20;
constexpr int WS_KVR = 9 << 20;
constexpr int WS_YB = 11 << 20;
constexpr int WS_STAMP = 14 << 20;
constexpr int WS_BYTES = 16 << 20;
constexpr int LDS_BYTES = 163840;
constexpr u32 SPIN_CAP = 1u << 16;

struct Params {
  const u16 *x, *k, *v, *shk, *h, *dv, *win, *wsp, *bsp, *wout;
  u16 *out;
  unsigned char *ws;
};

#define SB() __builtin_amdgcn_sched_barrier(0)
#define BARW() do { SB(); asm volatile("s_waitcnt lgkmcnt(0)\n\ts_barrier" ::: "memory"); SB(); } while (0)
#define BAR0() do { SB(); asm volatile("s_barrier" ::: "memory"); SB(); } while (0)
#define FRESH() do { asm volatile("" : "+v"(l)); g = l >> 4; c = l & 15; } while (0)
#define DRAIN() asm volatile("s_waitcnt vmcnt(0)" ::: "memory")
#define VM(n) asm volatile("s_waitcnt vmcnt(%0)" ::"n"(n) : "memory")
#define PIN(x) asm volatile("" : "+v"(x))
#define STAMP(n) do { } while (0)
#define STAMPA(n) do { } while (0)
#define STAMP_DECL do { } while (0)
#define STAMP_OUT(slot) do { } while (0)

static __device__ __forceinline__ rsrc_t mk(const void *p, int bytes) { return __builtin_amdgcn_make_buffer_rsrc((void *)p, 0, bytes, 0x00020000); }
static __device__ __forceinline__ v4u ld16(rsrc_t r, int vo, int so) { return __builtin_amdgcn_raw_buffer_load_b128(r, vo, so, 0); }
static __device__ __forceinline__ v4u ld16c(rsrc_t r, int vo, int so) { return __builtin_amdgcn_raw_buffer_load_b128(r, vo, so, 16); }
static __device__ __forceinline__ u32 ld4(rsrc_t r, int vo, int so) { return __builtin_amdgcn_raw_buffer_load_b32(r, vo, so, 0); }
static __device__ __forceinline__ void st16c(v4u v, rsrc_t r, int vo, int so) { __builtin_amdgcn_raw_buffer_store_b128(v, r, vo + so, 0, 16); }
static __device__ __forceinline__ srd_t mks(const void *p, int bytes) { return srd_t{(int)(u64)p, (int)((u64)p >> 32) & 0xffff, bytes, 0x00020000}; }
template <int IMM> static __device__ __forceinline__ void dma(srd_t r, int vo, int so, int base) {
  asm volatile("s_add_u32 m0, %0, %4\n\ts_nop 0\n\tbuffer_load_dwordx4 %1, %2, %3 offen lds" ::"s"(base), "v"(vo), "s"(r), "s"(so), "n"(IMM) : "m0", "scc", "memory");
}
template <int IMM> static __device__ __forceinline__ void dmac(srd_t r, int vo, int so, int base) {
  asm volatile("s_add_u32 m0, %0, %4\n\ts_nop 0\n\tbuffer_load_dwordx4 %1, %2, %3 offen sc1 lds" ::"s"(base), "v"(vo), "s"(r), "s"(so), "n"(IMM) : "m0", "scc", "memory");
}
static __device__ __forceinline__ void touch(srd_t r, int vo, int so) {
  asm volatile("s_mov_b32 m0, %3\n\ts_nop 0\n\tbuffer_load_dword %0, %1, %2 offen lds" ::"v"(vo), "s"(r), "s"(so), "n"(LDS_BYTES - 256) : "m0", "memory");
}
static __device__ __forceinline__ float one(float r) { asm("" : "+v"(r)); return r; }
static __device__ __forceinline__ float fadd(float a, float b) { return one(a + b); }
static __device__ __forceinline__ float fmul(float a, float b) { return one(a * b); }
static __device__ __forceinline__ float ffma(float a, float b, float c) { return one(__builtin_fmaf(a, b, c)); }
template <int N, typename Fn> static __device__ __forceinline__ void unr(Fn &&f) {
  if constexpr (N > 0) { unr<N - 1>(f); f(std::integral_constant<int, N - 1>{}); }
}
static __device__ __forceinline__ f4 mfma(v4u a, v4u b, f4 c) {
  return __builtin_amdgcn_mfma_f32_16x16x32_bf16(__builtin_bit_cast(bf8, a), __builtin_bit_cast(bf8, b), c, 0, 0, 0);
}
static __device__ __forceinline__ float asf(u32 u) { return __builtin_bit_cast(float, u); }
static __device__ __forceinline__ u32 asu(float f) { return __builtin_bit_cast(u32, f); }
static __device__ __forceinline__ float lo16(u32 u) { return asf(u << 16); }
static __device__ __forceinline__ float hi16(u32 u) { return asf(u & 0xffff0000u); }
static __device__ __forceinline__ u32 pk(float a, float b) { f2 t = {a, b}; return __builtin_bit_cast(u32, __builtin_convertvector(t, bf2)); }
static __device__ __forceinline__ float rbf(float a) { return lo16(pk(a, a)); }
static __device__ __forceinline__ v4u lds16(LAS unsigned char *L, int off) { return *(LAS v4u *)(L + off); }
static __device__ __forceinline__ f4 ldsf4(LAS unsigned char *L, int off) { return *(LAS f4 *)(L + off); }
static __device__ __forceinline__ void sts16(LAS unsigned char *L, int off, v4u v) { *(LAS v4u *)(L + off) = v; }
static __device__ __forceinline__ v2u trd(LAS unsigned char *L, int off) {
  return __builtin_bit_cast(v2u, __builtin_amdgcn_ds_read_tr16_b64_v4i16((LAS s4 *)(L + off)));
}
static __device__ __forceinline__ v4u trd2(LAS unsigned char *L, int off, int step) {
  const v2u a = trd(L, off), b = trd(L, off + step);
  return v4u{a.x, a.y, b.x, b.y};
}
static __device__ __forceinline__ f2 fma2(f2 a, f2 b, f2 c) { return __builtin_elementwise_fma(a, b, c); }
static __device__ __forceinline__ u32 pk2(f2 t) { return __builtin_bit_cast(u32, __builtin_convertvector(t, bf2)); }
static __device__ __forceinline__ f2 lo2(f4 v) { return __builtin_shufflevector(v, v, 0, 1); }
static __device__ __forceinline__ f2 hi2(f4 v) { return __builtin_shufflevector(v, v, 2, 3); }
static __device__ __forceinline__ void rowsum8(float (&a)[8], float (&b)[8]) {
#define DA(d, s, ctl, bm) "v_add_f32_dpp " d ", " s ", " s " " ctl " row_mask:0xf bank_mask:" bm "\n\t"
  asm volatile("s_nop 1\n\t"
               DA("%0", "%0", "row_mirror", "0x3") DA("%1", "%1", "row_mirror", "0x3") DA("%2", "%2", "row_mirror", "0x3") DA("%3", "%3", "row_mirror", "0x3")
               DA("%4", "%4", "row_mirror", "0x3") DA("%5", "%5", "row_mirror", "0x3") DA("%6", "%6", "row_mirror", "0x3") DA("%7", "%7", "row_mirror", "0x3")
               DA("%0", "%8", "row_mirror", "0xc") DA("%1", "%9", "row_mirror", "0xc") DA("%2", "%10", "row_mirror", "0xc") DA("%3", "%11", "row_mirror", "0xc")
               DA("%4", "%12", "row_mirror", "0xc") DA("%5", "%13", "row_mirror", "0xc") DA("%6", "%14", "row_mirror", "0xc") DA("%7", "%15", "row_mirror", "0xc")
               DA("%0", "%0", "row_half_mirror", "0x5") DA("%1", "%1", "row_half_mirror", "0x5") DA("%4", "%4", "row_half_mirror", "0x5") DA("%5", "%5", "row_half_mirror", "0x5")
               DA("%0", "%2", "row_half_mirror", "0xa") DA("%1", "%3", "row_half_mirror", "0xa") DA("%4", "%6", "row_half_mirror", "0xa") DA("%5", "%7", "row_half_mirror", "0xa")
               DA("%0", "%0", "quad_perm:[1,0,3,2]", "0xf") DA("%1", "%1", "quad_perm:[1,0,3,2]", "0xf") DA("%4", "%4", "quad_perm:[1,0,3,2]", "0xf") DA("%5", "%5", "quad_perm:[1,0,3,2]", "0xf")
               DA("%0", "%0", "quad_perm:[2,3,0,1]", "0xf") DA("%1", "%1", "quad_perm:[2,3,0,1]", "0xf") DA("%4", "%4", "quad_perm:[2,3,0,1]", "0xf") DA("%5", "%5", "quad_perm:[2,3,0,1]", "0xf")
               : "+v"(a[0]), "+v"(a[1]), "+v"(a[2]), "+v"(a[3]), "+v"(b[0]), "+v"(b[1]), "+v"(b[2]), "+v"(b[3])
               : "v"(a[4]), "v"(a[5]), "v"(a[6]), "v"(a[7]), "v"(b[4]), "v"(b[5]), "v"(b[6]), "v"(b[7]));
#undef DA
}
static __device__ __forceinline__ void lds_signal(LAS unsigned char *L, int off) {
  asm volatile("s_waitcnt lgkmcnt(0)" ::: "memory");
  if ((threadIdx.x & 63) == 0) __hip_atomic_fetch_add((LAS u32 *)(L + off), 1u, __ATOMIC_RELAXED, __HIP_MEMORY_SCOPE_WORKGROUP);
}
static __device__ __forceinline__ bool lds_last(LAS unsigned char *L, int off) {
  u32 n = 0;
  if ((threadIdx.x & 63) == 0) n = __hip_atomic_fetch_add((LAS u32 *)(L + off), 1u, __ATOMIC_RELAXED, __HIP_MEMORY_SCOPE_WORKGROUP);
  return __builtin_amdgcn_readfirstlane(n) == 3;
}
static __device__ __forceinline__ void lds_wait(LAS unsigned char *L, int off, u32 n) {
#pragma unroll 1
  for (u32 s = 0; s < (SPIN_CAP << 4); ++s)
  {
    if (__hip_atomic_load((LAS u32 *)(L + off), __ATOMIC_RELAXED, __HIP_MEMORY_SCOPE_WORKGROUP) >= n) break;
    __builtin_amdgcn_s_sleep(1);
  }
  asm volatile("" ::: "memory");
}
static __device__ __forceinline__ u64 ldflag(unsigned char *ws, int off) { return __hip_atomic_load((GAS u64 *)(ws + off), __ATOMIC_RELAXED, __HIP_MEMORY_SCOPE_AGENT); }
static __device__ __forceinline__ void stflag(unsigned char *ws, int off, u64 v) { __hip_atomic_store((GAS u64 *)(ws + off), v, __ATOMIC_RELAXED, __HIP_MEMORY_SCOPE_AGENT); }
static __device__ __forceinline__ void wait_flags(unsigned char *ws, int off, u64 tag) {
#pragma unroll 1
  for (u32 s = 0; s < SPIN_CAP; ++s) {
    const u64 v = ldflag(ws, off);
    if (__all(v == tag)) break;
    __builtin_amdgcn_s_sleep(1);
  }
  asm volatile("" ::: "memory");
}
static __device__ __forceinline__ float inv_sigma(float var) {
  const float sd = rbf(__builtin_amdgcn_sqrtf(rbf(rbf(fmaxf(var, 0.f)) + 1e-5f)));
  return __builtin_amdgcn_rcpf(sd);
}
static __device__ __forceinline__ int mod7(int a) { return a >= 7 ? a - 7 : a; }

constexpr int L_KR = 81920;
constexpr int L_VR = L_KR + 9216, L_RV = L_VR + 8192;
constexpr int L_XR = 114688;
constexpr int L_Z2 = 114688;
constexpr int L_Z1 = 131072;
constexpr int L_KD = 147456;
constexpr int L_CS = 155648;
constexpr int L_XST = 156672;
constexpr int L_ZST = 157184;
constexpr int L_CNT = 157696;

#define MIX(cnt, n) do { for (int q_ = 0; q_ < (cnt); ++q_) { __builtin_amdgcn_sched_group_barrier(8, 1, 0); __builtin_amdgcn_sched_group_barrier(0x100, (n), 0); } } while (0)

#define TOFF 0
#define ADEPTH 11
#define RD 4
#define NOTOUCH
#define NA1 6
struct Tile { int i, j, rot1, rot2, rot3, r, pg, o; };

static __device__ __forceinline__ u64 epoch_load(const unsigned char *ws) {
  u64 e;
  asm volatile("s_load_dwordx2 %0, %1, 0x0" : "=s"(e) : "s"(ws) : "memory");
  return e;
}
#define EPOCH_WAIT(e) asm volatile("s_waitcnt lgkmcnt(0)" : "+s"(e)::"memory")
static __device__ __forceinline__ v4u widen(v2u x, v2u y) {
  const v2u a = __builtin_amdgcn_permlane16_swap(x.x, y.x, false, false), b = __builtin_amdgcn_permlane16_swap(x.y, y.y, false, false);
  return v4u{a.x, b.x, a.y, b.y};
}

static __device__ __forceinline__ void gemm_compute(const Params &p, LAS unsigned char *L, const Tile t, int w) {
  STAMP_DECL;
  STAMP(0);
  u64 tag = epoch_load(p.ws);
  const int i = t.i, j = t.j, rh = w >> 1, ch = w & 1;
  int l = threadIdx.x & 63, g = l >> 4, c = l & 15;
  const rsrc_t rws = mk(p.ws, WS_BYTES);

  {
    const v2u shk = *(const v2u *)(p.shk + (64 * j + 16 * w + c) * 4);
    const u32 dvr = p.dv[64 * j + 16 * w + c];
    if (threadIdx.x < 8) *(LAS u32 *)(L + L_CNT + threadIdx.x * 4) = 0;
    EPOCH_WAIT(tag);
    tag += 1;
    BARW();
    STAMP(1);
    v2u kr[5], vr[4];
    const int r0 = c >> 2;
#pragma unroll
    for (int q = 0; q < 5; ++q) kr[q] = trd(L, L_KR + (16 * g + 4 + 4 * q + r0) * 128 + ((w ^ ((2 + 2 * q + (r0 >> 1)) & 3)) << 5) + (c & 3) * 8);
#pragma unroll
    for (int q = 0; q < 4; ++q) vr[q] = trd(L, L_VR + (16 * g + 4 * q + r0) * 128 + ((w ^ ((2 * q + (r0 >> 1)) & 3)) << 5) + (c & 3) * 8);
    float kf[20], o[16];
#pragma unroll
    for (int q = 0; q < 5; ++q) { kf[4 * q] = lo16(kr[q].x); kf[4 * q + 1] = hi16(kr[q].x); kf[4 * q + 2] = lo16(kr[q].y); kf[4 * q + 3] = hi16(kr[q].y); }
    const float t0 = lo16(shk.x), t1 = hi16(shk.x), t2 = lo16(shk.y), t3 = hi16(shk.y), dd = lo16(dvr);
#pragma unroll
    for (int r = 0; r < 16; ++r) {
      const u32 vv = (r & 2) ? vr[r >> 2].y : vr[r >> 2].x;
      o[r] = __builtin_fmaf(kf[r + 4], t0, __builtin_fmaf(kf[r + 3], t1, __builtin_fmaf(kf[r + 2], t2, kf[r + 1] * t3))) * ((r & 1) ? hi16(vv) : lo16(vv));
    }
    const int cc = 16 * w + c;
    u32 rv[8];
#pragma unroll
    for (int q = 0; q < 8; ++q) rv[q] = pk(o[15 - 2 * q], o[14 - 2 * q]);
    sts16(L, L_RV + cc * 144 + (3 - g) * 32, v4u{rv[0], rv[1], rv[2], rv[3]});
    sts16(L, L_RV + cc * 144 + (3 - g) * 32 + 16, v4u{rv[4], rv[5], rv[6], rv[7]});
    SB();
    v4u ln[2];
#pragma unroll
    for (int q = 0; q < 2; ++q) ln[q] = lds16(L, L_RV + (16 * w + 8 * q + (l >> 3)) * 144 + (l & 7) * 16);
    u32 kd[8];
#pragma unroll
    for (int q = 0; q < 8; ++q) kd[q] = pk(hi16(rv[7 - q]) * dd, lo16(rv[7 - q]) * dd);
#pragma unroll
    for (int q = 0; q < 2; ++q) st16c(ln[q], rws, (64 * j + 16 * w + 8 * q + (l >> 3)) * 4096 + (31 - i) * 128 + (l & 7) * 16, WS_KVR);
    const int ko = L_KD + cc * 128 + ((g ^ ((c >> 1) & 3)) << 5);
    sts16(L, ko, v4u{kd[0], kd[1], kd[2], kd[3]});
    sts16(L, ko + 16, v4u{kd[4], kd[5], kd[6], kd[7]});
  }
  STAMP(2);

  v4u xf[2][14], wf[2][4];
  f4 acc[2][4], cs[2];
  int rb[4];
  const v4u ones = {0x3f803f80u, 0x3f803f80u, 0x3f803f80u, 0x3f803f80u};
  auto start = [&]() {
    const int rdoff = (8 * g + (c >> 2)) * 256 + ((((c >> 2) | ((g & 1) << 2)) ^ (4 * ch + 2 * rh)) << 5) + (c & 3) * 8;
#pragma unroll
    for (int n = 0; n < 4; ++n) { rb[n] = rdoff ^ (n << 5); PIN(rb[n]); acc[0][n] = acc[1][n] = f4{0, 0, 0, 0}; }
    cs[0] = cs[1] = f4{0, 0, 0, 0};
  };
  auto rdw = [&](int ks, v4u (&f)[4]) {
#pragma unroll
    for (int n = 0; n < 4; ++n) {
      if (ks == 7) { rb[n] += 57344; PIN(rb[n]); }
      f[n] = trd2(L, rb[n] + (ks % 7) * 8192, 1024);
    }
  };
  auto sums = [&](int ks) { cs[0] = mfma(ones, wf[ks & 1][0], cs[0]); cs[1] = mfma(ones, wf[ks & 1][1], cs[1]); };
  auto mma = [&](int ks) {
#pragma unroll
    for (int n = 0; n < 4; ++n)
#pragma unroll
      for (int m = 0; m < 2; ++m) acc[m][n] = mfma(xf[m][ks], wf[ks & 1][n], acc[m][n]);
  };
  auto put_cs = [&](int off) {
    const int o = L_CS + off + ((4 * ch + 2 * rh) * 16 + c) * 4;
    *(LAS float *)(L + o) = cs[0].x;
    *(LAS float *)(L + o + 64) = cs[1].x;
  };
  start();
  {
    int xo0 = L_XR + (32 * rh + c) * 128 + ((g ^ (c & 7)) << 4), xo1 = xo0 ^ 64;
    PIN(xo0); PIN(xo1);
    auto rd = [&](int ks) {
      const int so = ((ks >> 1) & 3) * 8192;
      xf[0][ks] = lds16(L, ((ks & 1) ? xo1 : xo0) + so);
      xf[1][ks] = lds16(L, ((ks & 1) ? xo1 : xo0) + so + 2048);
      SB();
      rdw(ks, wf[ks & 1]);
    };
#pragma unroll
    for (int st = 0; st < 7; ++st) {
      STAMPA(2 * st + 1);
      if (st == 0) BARW(); else { SB(); asm volatile("s_waitcnt lgkmcnt(8)\n\ts_barrier" ::: "memory"); SB(); }
      STAMPA(2 * st + 2);
      if (st == 2) {
        DRAIN();
        if (l == 0) stflag(p.ws, WS_KVF + ((4 * j + w) * 32 + i) * 8, tag);
        SB();
      }
      rd(2 * st);
      if (st > 0) { mma(2 * st - 1); sums(2 * st - 1); MIX(8, 1); }
      SB();
      rd(2 * st + 1);
      mma(2 * st);
      sums(2 * st);
      MIX(8, 1);
      SB();
    }
    sums(13);
    put_cs(0);
    BARW();
    mma(13);
  }
  STAMP(3);
  FRESH();
  const u32 tagA = (u32)tag, tagB = (u32)(tag >> 32) ^ 0x9e3779b9u;
  f4 mm[2], rr[2];
  float cv[4];
  auto act_in = [&](int off) {
#pragma unroll
    for (int m = 0; m < 2; ++m) {
      mm[m] = ldsf4(L, L_XST + (32 * rh + 8 * g + 4 * m) * 4);
      rr[m] = ldsf4(L, L_XST + 256 + (32 * rh + 8 * g + 4 * m) * 4);
    }
    const int o = L_CS + off + ((4 * ch + (2 - 2 * rh)) * 16 + c) * 4;
    cv[0] = -cs[0].x;
    cv[1] = -cs[1].x;
    cv[2] = -*(LAS float *)(L + o);
    cv[3] = -*(LAS float *)(L + o + 64);
  };
  auto act = [&](int n, f2 (&z)[4]) {
    f2 x[4], a[4];
    const f2 c2 = {cv[n], cv[n]};
    SB();
#pragma unroll
    for (int m = 0; m < 2; ++m) {
      x[2 * m] = fma2(lo2(mm[m]), c2, lo2(acc[m][n]));
      x[2 * m + 1] = fma2(hi2(mm[m]), c2, hi2(acc[m][n]));
    }
    SB();
#pragma unroll
    for (int m = 0; m < 2; ++m) { x[2 * m] *= lo2(rr[m]); x[2 * m + 1] *= hi2(rr[m]); }
    SB();
#pragma unroll
    for (int q = 0; q < 4; ++q) a[q] = x[q] * x[q];
    SB();
#pragma unroll
    for (int q = 0; q < 4; ++q) a[q] = fma2(a[q], f2{-0.1029432397f, -0.1029432397f}, f2{-2.302208198f, -2.302208198f});
    SB();
#pragma unroll
    for (int q = 0; q < 4; ++q) a[q] *= x[q];
    SB();
#pragma unroll
    for (int q = 0; q < 4; ++q) a[q] = f2{__builtin_amdgcn_exp2f(a[q].x), __builtin_amdgcn_exp2f(a[q].y)};
    SB();
#pragma unroll
    for (int q = 0; q < 4; ++q) a[q] += 1.0f;
    SB();
#pragma unroll
    for (int q = 0; q < 4; ++q) a[q] = f2{__builtin_amdgcn_rcpf(a[q].x), __builtin_amdgcn_rcpf(a[q].y)};
    SB();
#pragma unroll
    for (int q = 0; q < 4; ++q) z[q] = x[q] * a[q];
    SB();
  };
  {
    act_in(0);
    f2 s[4], u[4];
    v4u zp[4];
#pragma unroll
    for (int n = 0; n < 4; ++n) {
      f2 z[4];
      act(n, z);
#pragma unroll
      for (int q = 0; q < 4; ++q) {
        s[q] = n ? s[q] + z[q] : z[q];
        u[q] = n ? fma2(z[q], z[q], u[q]) : z[q] * z[q];
      }
      zp[n] = v4u{pk2(z[0]), pk2(z[1]), pk2(z[2]), pk2(z[3])};
    }
    float sa[8], sq[8];
#pragma unroll
    for (int q = 0; q < 4; ++q) { sa[2 * q] = s[q].x; sa[2 * q + 1] = s[q].y; sq[2 * q] = u[q].x; sq[2 * q + 1] = u[q].y; }
    rowsum8(sa, sq);
    const int row = 8 * g + (c & 1) + ((c >> 1) & 6);
    if ((c & 2) == 0)
      st16c(v4u{asu((c & 1) ? sa[1] : sa[0]), tagA, asu((c & 1) ? sq[1] : sq[0]), tagB}, rws, ((i * 2 + rh) * 16 + 2 * j + ch) * 512 + row * 16, WS_ST);
    SB();
    const int zo = L_Z2 + (rh * 8 + 4 * ch) * 1024 + l * 16;
#pragma unroll
    for (int n = 0; n < 4; ++n) sts16(L, zo + ((n & 1) << 10) + (((n >> 1) ^ rh) << 11), zp[n]);
    lds_signal(L, L_CNT + 4);
  }
  STAMP(4);
  lds_wait(L, L_CNT, 4);
  STAMP(5);
  start();
  rdw(0, wf[0]);
  SB();
#pragma unroll
  for (int ks = 0; ks < 13; ++ks) {
    rdw(ks + 1, wf[(ks + 1) & 1]);
    mma(ks);
    sums(ks);
    MIX(8, 1);
    SB();
  }
  sums(13);
  put_cs(512);
  lds_signal(L, L_CNT + 8);
  SB();
  mma(13);
  SB();
  lds_wait(L, L_CNT + 8, 4);
  STAMP(6);
  FRESH();
  {
    act_in(512);
    const int po = L_Z1 + (64 * ch + c) * 128 + (((4 * rh + g) ^ (c & 7)) << 4);
#pragma unroll
    for (int n = 0; n < 4; ++n) {
      f2 z[4];
      act(n, z);
      sts16(L, po + ((n & 1) << 11) + (((n >> 1) ^ rh) << 12), v4u{pk2(z[0]), pk2(z[1]), pk2(z[2]), pk2(z[3])});
    }
  }
  STAMP(8);
  FRESH();
  v2u xe[4];
  u32 bias[4];
  {
    const rsrc_t rx = mk(p.x, S_ * D_ * 2);
#pragma unroll
    for (int n = 0; n < 4; ++n) {
      xe[n] = __builtin_amdgcn_raw_buffer_load_b64(rx, (64 * i + 16 * w + c) * 896 + j * 128 + g * 8 + n * 32, 0, 0);
      bias[n] = p.bsp[64 * i + 16 * n + c];
    }
  }
  STAMP(9);
  f4 g2[4][2];
  {
#define G2_AH 6
#define G2_AD 2
    constexpr int AH = G2_AH, AD = G2_AD;
    v4u bf[64][2], af[AD + 1][4];
    const int bvo = w * 2048 + l * 16;
    auto ldb = [&](int ks) {
      const int b = ks >> 1, so = WS_ZB + j * 524288 + ((((b >> 3) + (t.rot2 >> 3)) & 3) * 8 + ((b + t.rot2) & 7)) * 16384 + (ks & 1) * 8192;
      bf[ks][0] = ld16c(rws, bvo, so);
      bf[ks][1] = ld16c(rws, bvo + 1024, so);
    };
    int ab[2][2];
    ab[0][0] = c * 128 + ((g ^ (c & 7)) << 4);
    ab[1][0] = ab[0][0] ^ 64;
    ab[0][1] = ab[0][0] + 65536;
    ab[1][1] = ab[1][0] + 65536;
    PIN(ab[0][0]); PIN(ab[1][0]); PIN(ab[0][1]); PIN(ab[1][1]);
    auto rda = [&](int ks) {
#pragma unroll
      for (int m = 0; m < 4; ++m) af[ks % (AD + 1)][m] = lds16(L, ab[ks & 1][(ks >> 4) & 1] + ((ks >> 1) & 7) * 8192 + m * 2048);
    };
#pragma unroll
    for (int m = 0; m < 4; ++m) g2[m][0] = g2[m][1] = f4{0, 0, 0, 0};
    BARW();
    STAMP(10);
#pragma unroll
    for (int ks = 0; ks < AH; ++ks) ldb(ks);
#pragma unroll
    for (int ks = 0; ks < AD; ++ks) rda(ks);
    SB();
    unr<64>([&](auto ksc) {
      constexpr int ks = ksc;
      if constexpr (ks + AH < 64) ldb(ks + AH);
      if constexpr (ks + AD < 64) rda(ks + AD);
#pragma unroll
      for (int b = 0; b < 2; ++b)
#pragma unroll
        for (int m = 0; m < 4; ++m) g2[m][b] = mfma(bf[ks][b], af[ks % (AD + 1)][m], g2[m][b]);
      MIX(4, 1);
      for (int q_ = 0; q_ < 2; ++q_) { __builtin_amdgcn_sched_group_barrier(8, 1, 0); __builtin_amdgcn_sched_group_barrier(0x20, 1, 0); }
      SB();
      if constexpr ((ks & 7) == 7) BAR0();
    });
  }
  STAMP(11);
  FRESH();
  {
    const int f0 = 4 * g + (c >> 2);
    const int zr = L_Z1 + (32 * w + f0) * 128 + (c & 1) * 8;
    const int sw = ((c >> 1) & 1) ^ (f0 & 7);
#pragma unroll
    for (int m = 0; m < 4; ++m) {
      const v4u z = trd2(L, zr + (((2 * m) ^ sw) << 4), 2048);
      const float b = lo16(bias[m]);
      const v4u o = {pk(lo16(z.x) * (g2[m][0].x + b), hi16(z.x) * (g2[m][0].y + b)), pk(lo16(z.y) * (g2[m][0].z + b), hi16(z.y) * (g2[m][0].w + b)),
                     pk(lo16(z.z) * (g2[m][1].x + b), hi16(z.z) * (g2[m][1].y + b)), pk(lo16(z.w) * (g2[m][1].z + b), hi16(z.w) * (g2[m][1].w + b))};
      st16c(o, rws, l * 16, WS_UB + ((i * 4 + m) * 28 + 4 * j + w) * 1024);
    }
  }
  STAMP(7);
  DRAIN();
  if (lds_last(L, L_CNT + 16)) {
    if (l == 0) stflag(p.ws, WS_UF + (i * 16 + j) * 8, tag);
    wait_flags(p.ws, WS_UF + (i * 16 + (l < 7 ? l : 0)) * 8, tag);
    lds_signal(L, L_CNT + 20);
  } else lds_wait(L, L_CNT + 20, 1);
  STAMP(12);
  FRESH();
  {
    constexpr int AH = 12;
    v4u uf[28], of[2][8];
    int uso[7];
#pragma unroll
    for (int q = 0; q < 7; ++q) uso[q] = WS_UB + (i * 4 + w) * 28672 + mod7(q + t.rot3) * 4096;
    auto ldu = [&](int ks) { uf[ks] = ld16c(rws, l * 16 + (ks & 3) * 1024, uso[ks >> 2]); };
#pragma unroll
    for (int ks = 0; ks < AH; ++ks) ldu(ks);
    f4 o[4];
    int ob[4];
    const int oro = (4 * g + (c >> 2)) * 128 + ((((c >> 3) & 1) | ((g & 1) << 1)) << 5) + (c & 3) * 8;
#pragma unroll
    for (int n = 0; n < 4; ++n) { o[n] = f4{0, 0, 0, 0}; ob[n] = oro ^ (n << 5); PIN(ob[n]); }
    auto rd = [&](int kp, v4u (&f)[8]) {
#pragma unroll
      for (int q = 0; q < 8; ++q) {
        const int n = q & 3, ks = 2 * kp + (q >> 2);
        if (ks == 14) { ob[n] += 57344; PIN(ob[n]); }
        f[q] = trd2(L, ob[n] + (ks % 14) * 4096, 2048);
      }
    };
    BAR0();
    rd(0, of[0]);
    SB();
#pragma unroll
    for (int kp = 0; kp < 14; ++kp) {
      if (2 * kp + AH < 28) { ldu(2 * kp + AH); ldu(2 * kp + AH + 1); }
      if (kp < 13) rd(kp + 1, of[(kp + 1) & 1]);
#pragma unroll
      for (int q = 0; q < 8; ++q) o[q & 3] = mfma(of[kp & 1][q], uf[2 * kp + (q >> 2)], o[q & 3]);
      __builtin_amdgcn_sched_group_barrier(0x20, 2, 0);
      MIX(8, 2);
      SB();
    }
    STAMP(13);
    BAR0();
    FRESH();
    const f2 st = {*(LAS float *)(L + L_XST + (16 * w + c) * 4), *(LAS float *)(L + L_XST + 256 + (16 * w + c) * 4)};
    const int yo = (4 * g + (c >> 2)) * 128 + ((w ^ (((c >> 3) & 1) | ((g & 1) << 1))) << 5) + (c & 3) * 8;
    v2u res[4];
#pragma unroll
    for (int n = 0; n < 4; ++n) {
      const v2u y = trd(L, L_Z2 + yo + n * 2048), kd = trd(L, L_KD + yo + n * 2048);
      const float xv[4] = {lo16(xe[n].x), hi16(xe[n].x), lo16(xe[n].y), hi16(xe[n].y)};
      const float yv[4] = {lo16(y.x) + lo16(kd.x), hi16(y.x) + hi16(kd.x), lo16(y.y) + lo16(kd.y), hi16(y.y) + hi16(kd.y)};
      float r[4];
#pragma unroll
      for (int e = 0; e < 4; ++e) {
        const float a = o[n][e];
        const float sg = a * __builtin_amdgcn_rcpf(1.0f + __builtin_amdgcn_exp2f(a * -1.442695041f));
        r[e] = __builtin_fmaf((xv[e] - st.x) * st.y * yv[e], sg, xv[e]);
      }
      res[n] = v2u{pk(r[0], r[1]), pk(r[2], r[3])};
    }
    const rsrc_t ro = mk(p.out, S_ * D_ * 2);
    const int oo = (64 * i + 16 * w + c) * 896 + j * 128 + (g & 1) * 32 + (g >> 1) * 16;
    st16c(widen(res[0], res[1]), ro, oo, 0);
    st16c(widen(res[2], res[3]), ro, oo, 64);
  }
  STAMP_OUT(0);
}

static __device__ __forceinline__ void gemm_move(const Params &p, LAS unsigned char *L, const Tile t, int m) {
  STAMP_DECL;
  STAMP(0);
  const int i = t.i, j = t.j;
  int l = threadIdx.x & 63, g = l >> 4, c = l & 15;
  const srd_t sx_ = mks(p.x, S_ * D_ * 2), swin = mks(p.win, D_ * F_ * 2), swsp = mks(p.wsp, S_ * S_ * 2);
  const int lb = m * 1024, lb2 = m * 2048;
  const int pvo = (l >> 3) * 896 + j * 128 + (((((l & 7) >> 1) ^ g) << 5) | ((l & 1) << 4));
  {
    const srd_t sk = mks(p.k, S_ * D_ * 2), sv = mks(p.v, S_ * D_ * 2);
    const int so = (64 * i + 16 * m) * 896;
    if (m == 0) dma<L_KR>(sk, pvo, i > 0 ? so - 7168 : 0x40000000, 0);
    dma<L_KR + 1024>(sk, pvo, so, lb2);
    dma<L_KR + 2048>(sk, pvo, so + 7168, lb2);
    dma<L_VR>(sv, pvo, so, lb2);
    dma<L_VR + 1024>(sv, pvo, so + 7168, lb2);
  }
  const int rsw = ((l & 7) ^ (l >> 3)) << 4;
  const int xvo = (64 * i + 32 * (m >> 1) + 4 * (m & 1) + 8 * (l >> 5) + ((l >> 3) & 3)) * 896 + rsw;
  const int wvo = (4 * m + g) * 3584 + j * 256 + ((((c >> 1) ^ (g | ((m & 2) << 1))) << 5) | ((c & 1) << 4));
  int sb[7];
#pragma unroll
  for (int s = 0; s < 7; ++s) sb[s] = mod7(s + t.rot1);
  auto item = [&](auto nc) {
    constexpr int n = nc, s = n / 6, q = n % 6;
    if constexpr (n >= 42) dma<(n - 42) * 4096>(swin, wvo, (sb[(n - 42) >> 2] * 4 + ((n - 42) & 3)) * 57344, lb);
    else if constexpr (q < 2) dma<L_XR + (s & 3) * 8192 + q * 1024>(sx_, xvo, sb[s] * 128 + q * 14336, lb2);
    else dma<(4 * s + q - 2) * 4096>(swin, wvo, (sb[s] * 4 + q - 2) * 57344 + 1792, lb);
  };
  unr<12>(item);
  u64 tag = epoch_load(p.ws);
  VM(12);
  EPOCH_WAIT(tag);
  tag += 1;
  BAR0();
  unr<6>([&](auto q) { item(std::integral_constant<int, 12 + q>{}); });
  {
    const v4u ones = {0x3f803f80u, 0x3f803f80u, 0x3f803f80u, 0x3f803f80u};
    f4 sx = {0, 0, 0, 0}, gr = {0, 0, 0, 0};
    int xo0 = L_XR + (16 * m + c) * 128 + ((g ^ (c & 7)) << 4), xo1 = xo0 ^ 64;
    PIN(xo0); PIN(xo1);
    unr<7>([&](auto sc) {
      constexpr int s = sc;
      VM(12);
      v4u xs[2];
      xs[0] = lds16(L, xo0 + (s & 3) * 8192);
      xs[1] = lds16(L, xo1 + (s & 3) * 8192);
      STAMPA(2 * s + 1);
      BAR0();
      STAMPA(2 * s + 2);
      if (s == 0) STAMP(1);
#pragma unroll
      for (int q = 0; q < 2; ++q) { sx = mfma(xs[q], ones, sx); gr = mfma(xs[q], xs[q], gr); }
      SB();
      if constexpr (s < 6) unr<6>([&](auto q) { item(std::integral_constant<int, 18 + 6 * s + q>{}); });
    });
    STAMP(2);
    const int e = c & 3;
    const float s1 = e & 2 ? (e & 1 ? sx.w : sx.z) : (e & 1 ? sx.y : sx.x);
    const float s2 = e & 2 ? (e & 1 ? gr.w : gr.z) : (e & 1 ? gr.y : gr.x);
    const float mean = fmul(s1, 1.0f / 448.0f);
    const float r = inv_sigma(ffma(-mean, mean, fmul(s2, 1.0f / 448.0f)));
    if ((c >> 2) == g) {
      const int row = 32 * (m >> 1) + 8 * g + 4 * (m & 1) + e;
      *(LAS float *)(L + L_XST + row * 4) = rbf(mean);
      *(LAS float *)(L + L_XST + 256 + row * 4) = r;
    }
    SB();
    unr<6>([&](auto q) { item(std::integral_constant<int, 54 + q>{}); });
  }
  BARW();
  STAMP(3);
  unr<10>([&](auto q) { VM(9); item(std::integral_constant<int, 60 + q>{}); });
  const rsrc_t rwsp = mk(p.wsp, S_ * S_ * 2), rws = mk(p.ws, WS_BYTES);
  v4u ar[32];
  const int avo = (64 * i + 16 * m + (l >> 3)) * 4096 + rsw;
  auto aso = [&](int n) { const int b = n >> 1; return ((((b >> 3) + (t.rot2 >> 3)) & 3) * 8 + ((b + t.rot2) & 7)) * 128 + (n & 1) * 32768; };
  auto lda = [&](int n0, int n1) {
#pragma unroll
    for (int n = n0; n < n1; ++n) { VM(ADEPTH); ar[n] = ld16(rwsp, avo, aso(32 + n)); SB(); }
  };
  lda(0, NA1);
  VM(NA1);
  lds_signal(L, L_CNT);
  STAMP(4);
  {
    const u32 tagA = (u32)tag, tagB = (u32)(tag >> 32) ^ 0x9e3779b9u;
    const int so = WS_ST + (i * 2 + (m >> 1)) * 8192 + (m & 1) * 256;
    const int vo = g * 512 + c * 16, vo3 = (g & 1) * 512 + c * 16 + 2048;
    v4u sg[4], zt[4];
    auto ldst = [&]() {
      sg[0] = ld16c(rws, vo, so); sg[1] = ld16c(rws, vo + 2048, so); sg[2] = ld16c(rws, vo, so + 4096); sg[3] = ld16c(rws, vo3, so + 4096);
    };
    lda(NA1, 32);
    STAMP(12);
    lds_wait(L, L_CNT + 4, 4);
    ldst();
    SB();
    const int zo = ((m >> 1) * 8 + (l >> 5)) * 1024 + ((m & 1) * 32 + (l & 31)) * 16;
#pragma unroll
    for (int q = 0; q < 4; ++q) zt[q] = lds16(L, L_Z2 + zo + q * 2048);
    STAMP(5);
#pragma unroll 1
    for (u32 s = 0; s < SPIN_CAP; ++s) {
      bool ok = true;
#pragma unroll
      for (int q = 0; q < 4; ++q) ok = ok && sg[q].y == tagA && sg[q].w == tagB;
      if (__all(ok)) break;
      asm volatile("" ::: "memory");
      ldst();
    }
    STAMP(13);
    float s = fadd(fadd(asf(sg[0].x), asf(sg[1].x)), fadd(asf(sg[2].x), g < 2 ? asf(sg[3].x) : 0.f));
    float u = fadd(fadd(asf(sg[0].z), asf(sg[1].z)), fadd(asf(sg[2].z), g < 2 ? asf(sg[3].z) : 0.f));
    {
      v2u a = __builtin_amdgcn_permlane16_swap(asu(s), asu(u), false, false);
      const float h = fadd(asf(a.x), asf(a.y));
      a = __builtin_amdgcn_permlane32_swap(asu(h), asu(h), false, false);
      const float k = fadd(asf(a.x), asf(a.y));
      a = __builtin_amdgcn_permlane16_swap(asu(k), asu(k), false, false);
      s = asf(a.x);
      u = asf(a.y);
    }
    const float mean = fmul(s, 1.0f / 896.0f);
    const float r = inv_sigma(ffma(-mean, mean, fmul(u, 1.0f / 896.0f)));
    if (g < 2) *(LAS float *)(L + L_ZST + g * 256 + (16 * m + c) * 4) = g ? r : -rbf(mean) * r;
    SB();
    f4 zb[2], zr[2];
#pragma unroll
    for (int q = 0; q < 2; ++q) {
      zb[q] = ldsf4(L, L_ZST + (16 * m + 8 * (g & 1) + 4 * q) * 4);
      zr[q] = ldsf4(L, L_ZST + 256 + (16 * m + 8 * (g & 1) + 4 * q) * 4);
    }
#pragma unroll
    for (int q = 0; q < 4; ++q) {
      u32 o[4];
#pragma unroll
      for (int e = 0; e < 4; ++e) {
        const u32 z = zt[q][e];
        o[e] = pk(ffma(lo16(z), zr[e >> 1][(2 * e) & 3], zb[e >> 1][(2 * e) & 3]), ffma(hi16(z), zr[e >> 1][(2 * e + 1) & 3], zb[e >> 1][(2 * e + 1) & 3]));
      }
      st16c(v4u{o[0], o[1], o[2], o[3]}, rws, (j * 64 + 2 * i) * 8192 + zo + q * 2048, WS_ZB);
      SB();
    }
    STAMP(6);
  }
  lds_wait(L, L_CNT + 8, 4);
  auto ring = [&](auto nc) { constexpr int n = nc; dma<(n >> 1) * 8192 + (n & 1) * 1024>(swsp, avo, aso(n), lb2); };
  unr<RD>(ring);
  VM(RD);
  if (lds_last(L, L_CNT + 24) && l == 0) stflag(p.ws, WS_ZF + (j * 32 + i) * 8, tag);
  SB();
  STAMP(7);
  unr<28 - RD>([&](auto nc) { VM(RD - 1); ring(std::integral_constant<int, RD + nc>{}); });
  lds_wait(L, L_CNT + 24, 4);
  unr<4>([&](auto nc) { VM(RD - 1); ring(std::integral_constant<int, 28 + nc>{}); });
  STAMP(11);
  if (m == 0) wait_flags(p.ws, WS_ZF + j * 256 + (l & 31) * 8, tag);
  DRAIN();
  BAR0();
  STAMP(8);
  FRESH();
  int awo = m * 2048 + l * 16, awh = awo + 65536;
  PIN(awo); PIN(awh);
  {
    constexpr int cum[6] = {0, 6, 12, 19, 25, 32};
#pragma unroll
    for (int k = 0; k < 5; ++k) {
      BARW();
#pragma unroll
      for (int n = cum[k]; n < cum[k + 1]; ++n) {
        sts16(L, ((n & 16) ? awh : awo) + ((n >> 1) & 7) * 8192 + (n & 1) * 1024, ar[n]);
        SB();
        __builtin_amdgcn_s_sleep(2);
        SB();
      }
    }
  }
  BARW();
  BAR0();
  STAMP(9);
  {
    const srd_t swout = mks(p.wout, 896 * D_ * 2);
    auto ldo = [&](auto nc) { constexpr int n = nc; VM(5); dma<n * 4096>(swout, pvo, (mod7((n >> 2) + t.rot3) * 16 + (n & 3) * 4 + m) * 7168, lb); };
    unr<12>(ldo);
    BAR0();
    STAMP(10);
    unr<16>([&](auto q) { ldo(std::integral_constant<int, 12 + q>{}); });
  }
  DRAIN();
  BAR0();
  wait_flags(p.ws, WS_YF + (((64 * j + 16 * m + min(g * 7, 15)) / 14) * 4 + (l & 3)) * 8, tag);
  {
    const srd_t sws = mks(p.ws, WS_BYTES);
    const int yvo = (64 * j + 16 * m + (l >> 3)) * 4096 + i * 128 + (((((l & 7) >> 1) ^ g) << 5) | ((l & 1) << 4));
    dmac<L_Z2>(sws, yvo, WS_YB, lb2);
    dmac<L_Z2 + 1024>(sws, yvo, WS_YB + 32768, lb2);
  }
  DRAIN();
  BAR0();
  STAMP_OUT(1);
}

constexpr int C_CP = 4160, C_KV = 4 * C_CP, C_SLOT = C_KV + 4608;

template <int H> static __device__ __forceinline__ void conv_half(const Params &p, LAS unsigned char *L, int v, int w, u64 tag) {
  STAMP_DECL;
  STAMP(0);
  constexpr int NF = H ? 6 : 8, NT = H ? 5 : 3;
  constexpr int tile[5] = {H ? 5 : 7, H ? 4 : 6, H ? 3 : 2, 1, 0};
  const int l = threadIdx.x & 63, g = l >> 4, c = l & 15, ch = w >> 1;
  const rsrc_t rws = mk(p.ws, WS_BYTES);
  const int hro = ch * C_SLOT + (c & 3) * C_CP + g * 16 + (c >> 2) * 8;
  const int kro = ch * C_SLOT + C_KV + (254 - 2 * c + g) * 16 - 7 * 512;
  v4u hfr[2][NF], kfr[2][NF];
  auto rd = [&](int ho, int ko, int q) {
#pragma unroll
    for (int d = 0; d < NF; ++d) {
      const v2u a = *(LAS v2u *)(L + ho + d * 512 + q * 64);
      v2u b;
      asm volatile("" ::: "memory");
      b = *(LAS v2u *)(L + ho + d * 512 + q * 64 + 8);
      asm volatile("" ::: "memory");
      hfr[q & 1][d] = v4u{a.x, a.y, b.x, b.y};
      kfr[q & 1][NF - 1 - d] = lds16(L, ko + (8 - NF + d) * 512 + q * 64);
    }
  };
  BAR0();
  STAMP(2);
  rd(hro, kro, 0);
  int set = 0;
#pragma unroll 1
  for (int rho = 0; rho < 7; ++rho) {
    const int nset = set == 4 * C_SLOT ? 0 : set + 2 * C_SLOT;
    f4 acc[NT];
#pragma unroll
    for (int n = 0; n < NT; ++n) acc[n] = f4{0, 0, 0, 0};
#pragma unroll
    for (int q = 0; q < 8; ++q) {
      if (q == 7) BARW();
      SB();
      if (q < 7) rd(hro + set, kro + set, q + 1); else rd(hro + nset, kro + nset, 0);
#pragma unroll
      for (int d = 0; d < NF; ++d)
#pragma unroll
        for (int n = NT - 1; n >= 0; --n) {
          const int P = d - (NF - 1 - tile[n]);
          if (P >= 0) acc[n] = mfma(hfr[q & 1][P], kfr[q & 1][tile[n] - P], acc[n]);
        }
      if (H) MIX(18, 1);
      else { for (int q_ = 0; q_ < 6; ++q_) { MIX(2, 1); MIX(1, 2); } }
      SB();
    }
    v2u y[NT];
#pragma unroll
    for (int n = 0; n < NT; ++n) y[n] = v2u{pk(acc[n].x, acc[n].y), pk(acc[n].z, acc[n].w)};
    const int yo = WS_YB + (14 * v + 2 * rho + ch) * 4096 + c * 32;
    st16c(widen(y[1], y[0]), rws, yo + (tile[1] + (g & 1)) * 512 + (g >> 1) * 16, 0);
    __builtin_amdgcn_raw_buffer_store_b64(y[2], rws, yo + tile[2] * 512 + g * 8, 0, 16);
    if constexpr (H) st16c(widen(y[4], y[3]), rws, yo + (g & 1) * 512 + (g >> 1) * 16, 0);
    set = nset;
    if (rho == 0) STAMP(3);
    if (rho == 1) STAMP(4);
    if (rho == 3) STAMP(5);
    if (rho == 5) STAMP(6);
  }
  STAMP(7);
  DRAIN();
  if (l == 0) stflag(p.ws, WS_YF + (v * 4 + w) * 8, tag);
  STAMP_OUT(0);
}

static __device__ __forceinline__ void conv_stage(const Params &p, LAS unsigned char *L, int v, int m) {
  STAMP_DECL;
  STAMP(0);
  const int l = threadIdx.x & 63;
  const srd_t sh = mks(p.h, D_ * S_ * 2), sws = mks(p.ws, WS_BYTES);
  const rsrc_t rws = mk(p.ws, WS_BYTES);
  const int vo = m * 1024 + l * 16;
  auto stage_h = [&](int rho, int set) {
    const int so = (14 * v + 2 * rho) * 4096, lb = set + m * 1024;
    unr<8>([&](auto qc) { constexpr int q = qc; dma<(q >> 2) * C_SLOT + (q & 3) * C_CP + 32>(sh, vo, so + (q >> 2) * 4096 + (q & 3) * 2 + 2, lb); });
  };
  auto stage_kv = [&](int rho, int set) {
    const int so = WS_KVR + (14 * v + 2 * rho) * 4096, lb = set + m * 1024;
    dmac<C_KV>(sws, vo, so, lb);
    dmac<C_SLOT + C_KV>(sws, vo, so + 4096, lb);
  };
  u64 head = *(const u64 *)(p.h + (14 * v + (l < 56 ? l >> 2 : 0)) * 2048);
  stage_h(0, 0);
  u64 tag = epoch_load(p.ws);
  stage_h(1, 2 * C_SLOT);
  stage_h(2, 4 * C_SLOT);
  head <<= 48 - 16 * (l & 3);
  auto put_head = [&](int rho, int set) {
    if (m == 0 && (l >> 3) == rho) *(LAS u64 *)(L + set + ((l >> 2) & 1) * C_SLOT + (l & 3) * C_CP + 24) = head;
  };
  {
    const v4u z = {0, 0, 0, 0};
    const int t = m * 64 + l;
    if (t < 192) sts16(L, (t >> 5) * C_SLOT + C_KV + 4096 + (t & 31) * 16, z);
    if (t < 24) { sts16(L, (t >> 2) * C_SLOT + (t & 3) * C_CP, z); *(LAS u64 *)(L + (t >> 2) * C_SLOT + (t & 3) * C_CP + 16) = 0; }
  }
  put_head(0, 0);
  put_head(1, 2 * C_SLOT);
  put_head(2, 4 * C_SLOT);
  EPOCH_WAIT(tag);
  tag += 1;
  wait_flags(p.ws, WS_KVF + (((14 * v + (l < 32 ? 0 : 13)) >> 4) * 32 + (l & 31)) * 8, tag);
  STAMP(1);
  stage_kv(0, 0);
  stage_kv(1, 2 * C_SLOT);
  stage_kv(2, 4 * C_SLOT);
  VM(4);
  BARW();
  STAMP(2);
  if (l == 0) stflag(p.ws, WS_HI + (v * 4 + m) * 8, tag);
  int set = 0;
#pragma unroll 1
  for (int rho = 0; rho < 7; ++rho) {
    DRAIN();
    BARW();
    if (rho < 4) {
      stage_h(rho + 3, set);
      stage_kv(rho + 3, set);
      put_head(rho + 3, set);
    }
    set = set == 4 * C_SLOT ? 0 : set + 2 * C_SLOT;
    if (rho == 4 && v == 0 && m == 0) {
#pragma unroll 1
      for (u32 s = 0; s < SPIN_CAP; ++s) {
        bool ok = true;
#pragma unroll
        for (int q = 0; q < 8; ++q) {
          const v4u f = ld16c(rws, l * 16 + (q & 3) * 1024, WS_KVF + (q >> 2) * 4096);
          ok = ok && f.x == (u32)tag && f.y == (u32)(tag >> 32) && f.z == (u32)tag && f.w == (u32)(tag >> 32);
        }
        if (__all(ok)) break;
        asm volatile("" ::: "memory");
      }
      if (l == 0) stflag(p.ws, WS_EPOCH, tag);
    }
  }
  STAMP_OUT(1);
}

__global__ void __launch_bounds__(512, 2) fused_kernel(Params p) {
  extern __shared__ __attribute__((aligned(16))) unsigned char smem[];
  LAS unsigned char *L = (LAS unsigned char *)smem;
  const int b = blockIdx.x, x = b & 7, r = b >> 3, wv = __builtin_amdgcn_readfirstlane(threadIdx.x >> 6);
  if (wv >= 4) __builtin_amdgcn_s_setprio(2);
  if (r >= 28) {
    const int v = x * 4 + (r - 28);
    if (wv < 4) {
      u64 tag = epoch_load(p.ws);
      EPOCH_WAIT(tag);
      if (wv & 1) conv_half<1>(p, L, v, wv, tag + 1);
      else conv_half<0>(p, L, v, wv, tag + 1);
    } else conv_stage(p, L, v, wv - 4);
  } else {
    const int pg = x >> 1, o = x & 1;
    Tile t;
    if (r < 24) { t.i = 8 * pg + r / 3; t.j = 4 * o + r % 3; }
    else { t.i = 8 * pg + 4 * o + (r - 24); t.j = 3; }
    t.rot1 = 2 * ((t.i + t.j) & 3);
    t.rot2 = 8 * (pg + 2 * ((t.i + t.j) & 1)) + ((2 * (t.i >> 1) + 4 * (t.j >> 1)) & 7);
    t.rot3 = 2 * (t.j & 3);
    t.r = r; t.pg = pg; t.o = o;
    if (wv < 4) gemm_compute(p, L, t, wv);
    else gemm_move(p, L, t, wv - 4);
  }
}

extern "C" void kernel_launch(void *const *d_in, const int *in_sizes, int n_in, void *d_out, int out_size, void *d_ws, size_t ws_size,
                              hipStream_t stream) {
  Params p;
  p.x = (const u16 *)d_in[0];
  p.k = (const u16 *)d_in[1];
  p.v = (const u16 *)d_in[2];
  p.shk = (const u16 *)d_in[3];
  p.h = (const u16 *)d_in[4];
  p.dv = (const u16 *)d_in[5];
  p.win = (const u16 *)d_in[7];
  p.wsp = (const u16 *)d_in[8];
  p.bsp = (const u16 *)d_in[9];
  p.wout = (const u16 *)d_in[10];
  p.out = (u16 *)d_out;
  p.ws = (unsigned char *)d_ws;
  (void)hipFuncSetAttribute((const void *)fused_kernel, hipFuncAttributeMaxDynamicSharedMemorySize, LDS_BYTES);
  fused_kernel<<<256, 512, LDS_BYTES, stream>>>(p);
}
```

```cpp
#define HS 0
#define HK 0
#include <hip/hip_runtime.h>
#include <stdint.h>
#include <type_traits>

typedef unsigned short u16;
typedef unsigned int u32;
typedef unsigned long long u64;
typedef u32 v4u __attribute__((ext_vector_type(4)));
typedef u32 v2u __attribute__((ext_vector_type(2)));
typedef float f4 __attribute__((ext_vector_type(4)));
typedef float f2 __attribute__((ext_vector_type(2)));
typedef short s4 __attribute__((ext_vector_type(4)));
typedef __bf16 bf8 __attribute__((ext_vector_type(8)));
typedef __bf16 bf2 __attribute__((ext_vector_type(2)));
#define LAS __attribute__((address_space(3)))
#define GAS __attribute__((address_space(1)))
typedef __amdgpu_buffer_rsrc_t rsrc_t;
typedef int srd_t __attribute__((ext_vector_type(4)));

constexpr int S_ = 2048, D_ = 448, F_ = 1792;
constexpr int WS_EPOCH = 0;
constexpr int WS_KVF = 4096;
constexpr int WS_HI = 11264;
constexpr int WS_ZF = 16384;
constexpr int WS_UF = 32768;
constexpr int WS_YF = 49152;
constexpr int WS_ST = 262144;
constexpr int WS_ZB = 1 << 20;
constexpr int WS_UB = 5 << 20;
constexpr int WS_KVR = 9 << 20;
constexpr int WS_YB = 11 << 20;
constexpr int WS_STAMP = 14 << 20;
constexpr int WS_BYTES = 16 << 20;
constexpr int LDS_BYTES = 163840;
constexpr u32 SPIN_CAP = 1u << 16;

struct Params {
  const u16 *x, *k, *v, *shk, *h, *dv, *win, *wsp, *bsp, *wout;
  u16 *out;
  unsigned char *ws;
};

#define SB() __builtin_amdgcn_sched_barrier(0)
#define BARW() do { SB(); asm volatile("s_waitcnt lgkmcnt(0)\n\ts_barrier" ::: "memory"); SB(); } while (0)
#define BAR0() do { SB(); asm volatile("s_barrier" ::: "memory"); SB(); } while (0)
#define FRESH() do { asm volatile("" : "+v"(l)); g = l >> 4; c = l & 15; } while (0)
#define DRAIN() asm volatile("s_waitcnt vmcnt(0)" ::: "memory")
#define VM(n) asm volatile("s_waitcnt vmcnt(%0)" ::"n"(n) : "memory")
#define PIN(x) asm volatile("" : "+v"(x))
#define STAMP(n) do { if constexpr ((n) == HS) { SB(); hs_ = __builtin_amdgcn_s_memrealtime(); SB(); } } while (0)
#define STAMPA(n) do { } while (0)
#define STAMP_DECL u64 hs_ = 0
#define STAMP_OUT(slot) do { DRAIN(); STAMP(14); if ((slot) + 2 * ((blockIdx.x >> 3) >= 28) == HK) for (int q_ = 0; q_ < 100000; ++q_) { if (__builtin_amdgcn_s_memrealtime() - hs_ >= 4000) break; __builtin_amdgcn_s_sleep(1); } } while (0)

static __device__ __forceinline__ rsrc_t mk(const void *p, int bytes) { return __builtin_amdgcn_make_buffer_rsrc((void *)p, 0, bytes, 0x00020000); }
static __device__ __forceinline__ v4u ld16(rsrc_t r, int vo, int so) { return __builtin_amdgcn_raw_buffer_load_b128(r, vo, so, 0); }
static __device__ __forceinline__ v4u ld16c(rsrc_t r, int vo, int so) { return __builtin_amdgcn_raw_buffer_load_b128(r, vo, so, 16); }
static __device__ __forceinline__ u32 ld4(rsrc_t r, int vo, int so) { return __builtin_amdgcn_raw_buffer_load_b32(r, vo, so, 0); }
static __device__ __forceinline__ void st16c(v4u v, rsrc_t r, int vo, int so) { __builtin_amdgcn_raw_buffer_store_b128(v, r, vo + so, 0, 16); }
static __device__ __forceinline__ srd_t mks(const void *p, int bytes) { return srd_t{(int)(u64)p, (int)((u64)p >> 32) & 0xffff, bytes, 0x00020000}; }
template <int IMM> static __device__ __forceinline__ void dma(srd_t r, int vo, int so, int base) {
  asm volatile("s_add_u32 m0, %0, %4\n\ts_nop 0\n\tbuffer_load_dwordx4 %1, %2, %3 offen lds" ::"s"(base), "v"(vo), "s"(r), "s"(so), "n"(IMM) : "m0", "scc", "memory");
}
template <int IMM> static __device__ __forceinline__ void dmac(srd_t r, int vo, int so, int base) {
  asm volatile("s_add_u32 m0, %0, %4\n\ts_nop 0\n\tbuffer_load_dwordx4 %1, %2, %3 offen sc1 lds" ::"s"(base), "v"(vo), "s"(r), "s"(so), "n"(IMM) : "m0", "scc", "memory");
}
static __device__ __forceinline__ void touch(srd_t r, int vo, int so) {
  asm volatile("s_mov_b32 m0, %3\n\ts_nop 0\n\tbuffer_load_dword %0, %1, %2 offen lds" ::"v"(vo), "s"(r), "s"(so), "n"(LDS_BYTES - 256) : "m0", "memory");
}
static __device__ __forceinline__ float one(float r) { asm("" : "+v"(r)); return r; }
static __device__ __forceinline__ float fadd(float a, float b) { return one(a + b); }
static __device__ __forceinline__ float fmul(float a, float b) { return one(a * b); }
static __device__ __forceinline__ float ffma(float a, float b, float c) { return one(__builtin_fmaf(a, b, c)); }
template <int N, typename Fn> static __device__ __forceinline__ void unr(Fn &&f) {
  if constexpr (N > 0) { unr<N - 1>(f); f(std::integral_constant<int, N - 1>{}); }
}
static __device__ __forceinline__ f4 mfma(v4u a, v4u b, f4 c) {
  return __builtin_amdgcn_mfma_f32_16x16x32_bf16(__builtin_bit_cast(bf8, a), __builtin_bit_cast(bf8, b), c, 0, 0, 0);
}
static __device__ __forceinline__ float asf(u32 u) { return __builtin_bit_cast(float, u); }
static __device__ __forceinline__ u32 asu(float f) { return __builtin_bit_cast(u32, f); }
static __device__ __forceinline__ float lo16(u32 u) { return asf(u << 16); }
static __device__ __forceinline__ float hi16(u32 u) { return asf(u & 0xffff0000u); }
static __device__ __forceinline__ u32 pk(float a, float b) { f2 t = {a, b}; return __builtin_bit_cast(u32, __builtin_convertvector(t, bf2)); }
static __device__ __forceinline__ float rbf(float a) { return lo16(pk(a, a)); }
static __device__ __forceinline__ v4u lds16(LAS unsigned char *L, int off) { return *(LAS v4u *)(L + off); }
static __device__ __forceinline__ f4 ldsf4(LAS unsigned char *L, int off) { return *(LAS f4 *)(L + off); }
static __device__ __forceinline__ void sts16(LAS unsigned char *L, int off, v4u v) { *(LAS v4u *)(L + off) = v; }
static __device__ __forceinline__ v2u trd(LAS unsigned char *L, int off) {
  return __builtin_bit_cast(v2u, __builtin_amdgcn_ds_read_tr16_b64_v4i16((LAS s4 *)(L + off)));
}
static __device__ __forceinline__ v4u trd2(LAS unsigned char *L, int off, int step) {
  const v2u a = trd(L, off), b = trd(L, off + step);
  return v4u{a.x, a.y, b.x, b.y};
}
static __device__ __forceinline__ f2 fma2(f2 a, f2 b, f2 c) { return __builtin_elementwise_fma(a, b, c); }
static __device__ __forceinline__ u32 pk2(f2 t) { return __builtin_bit_cast(u32, __builtin_convertvector(t, bf2)); }
static __device__ __forceinline__ f2 lo2(f4 v) { return __builtin_shufflevector(v, v, 0, 1); }
static __device__ __forceinline__ f2 hi2(f4 v) { return __builtin_shufflevector(v, v, 2, 3); }
static __device__ __forceinline__ void rowsum8(float (&a)[8], float (&b)[8]) {
#define DA(d, s, ctl, bm) "v_add_f32_dpp " d ", " s ", " s " " ctl " row_mask:0xf bank_mask:" bm "\n\t"
  asm volatile("s_nop 1\n\t"
               DA("%0", "%0", "row_mirror", "0x3") DA("%1", "%1", "row_mirror", "0x3") DA("%2", "%2", "row_mirror", "0x3") DA("%3", "%3", "row_mirror", "0x3")
               DA("%4", "%4", "row_mirror", "0x3") DA("%5", "%5", "row_mirror", "0x3") DA("%6", "%6", "row_mirror", "0x3") DA("%7", "%7", "row_mirror", "0x3")
               DA("%0", "%8", "row_mirror", "0xc") DA("%1", "%9", "row_mirror", "0xc") DA("%2", "%10", "row_mirror", "0xc") DA("%3", "%11", "row_mirror", "0xc")
               DA("%4", "%12", "row_mirror", "0xc") DA("%5", "%13", "row_mirror", "0xc") DA("%6", "%14", "row_mirror", "0xc") DA("%7", "%15", "row_mirror", "0xc")
               DA("%0", "%0", "row_half_mirror", "0x5") DA("%1", "%1", "row_half_mirror", "0x5") DA("%4", "%4", "row_half_mirror", "0x5") DA("%5", "%5", "row_half_mirror", "0x5")
               DA("%0", "%2", "row_half_mirror", "0xa") DA("%1", "%3", "row_half_mirror", "0xa") DA("%4", "%6", "row_half_mirror", "0xa") DA("%5", "%7", "row_half_mirror", "0xa")
               DA("%0", "%0", "quad_perm:[1,0,3,2]", "0xf") DA("%1", "%1", "quad_perm:[1,0,3,2]", "0xf") DA("%4", "%4", "quad_perm:[1,0,3,2]", "0xf") DA("%5", "%5", "quad_perm:[1,0,3,2]", "0xf")
               DA("%0", "%0", "quad_perm:[2,3,0,1]", "0xf") DA("%1", "%1", "quad_perm:[2,3,0,1]", "0xf") DA("%4", "%4", "quad_perm:[2,3,0,1]", "0xf") DA("%5", "%5", "quad_perm:[2,3,0,1]", "0xf")
               : "+v"(a[0]), "+v"(a[1]), "+v"(a[2]), "+v"(a[3]), "+v"(b[0]), "+v"(b[1]), "+v"(b[2]), "+v"(b[3])
               : "v"(a[4]), "v"(a[5]), "v"(a[6]), "v"(a[7]), "v"(b[4]), "v"(b[5]), "v"(b[6]), "v"(b[7]));
#undef DA
}
static __device__ __forceinline__ void lds_signal(LAS unsigned char *L, int off) {
  asm volatile("s_waitcnt lgkmcnt(0)" ::: "memory");
  if ((threadIdx.x & 63) == 0) __hip_atomic_fetch_add((LAS u32 *)(L + off), 1u, __ATOMIC_RELAXED, __HIP_MEMORY_SCOPE_WORKGROUP);
}
static __device__ __forceinline__ bool lds_last(LAS unsigned char *L, int off) {
  u32 n = 0;
  if ((threadIdx.x & 63) == 0) n = __hip_atomic_fetch_add((LAS u32 *)(L + off), 1u, __ATOMIC_RELAXED, __HIP_MEMORY_SCOPE_WORKGROUP);
  return __builtin_amdgcn_readfirstlane(n) == 3;
}
static __device__ __forceinline__ void lds_wait(LAS unsigned char *L, int off, u32 n) {
#pragma unroll 1
  for (u32 s = 0; s < (SPIN_CAP << 4); ++s)
  {
    if (__hip_atomic_load((LAS u32 *)(L + off), __ATOMIC_RELAXED, __HIP_MEMORY_SCOPE_WORKGROUP) >= n) break;
    __builtin_amdgcn_s_sleep(1);
  }
  asm volatile("" ::: "memory");
}
static __device__ __forceinline__ u64 ldflag(unsigned char *ws, int off) { return __hip_atomic_load((GAS u64 *)(ws + off), __ATOMIC_RELAXED, __HIP_MEMORY_SCOPE_AGENT); }
static __device__ __forceinline__ void stflag(unsigned char *ws, int off, u64 v) { __hip_atomic_store((GAS u64 *)(ws + off), v, __ATOMIC_RELAXED, __HIP_MEMORY_SCOPE_AGENT); }
static __device__ __forceinline__ void wait_flags(unsigned char *ws, int off, u64 tag) {
#pragma unroll 1
  for (u32 s = 0; s < SPIN_CAP; ++s) {
    const u64 v = ldflag(ws, off);
    if (__all(v == tag)) break;
    __builtin_amdgcn_s_sleep(1);
  }
  asm volatile("" ::: "memory");
}
static __device__ __forceinline__ float inv_sigma(float var) {
  const float sd = rbf(__builtin_amdgcn_sqrtf(rbf(rbf(fmaxf(var, 0.f)) + 1e-5f)));
  return __builtin_amdgcn_rcpf(sd);
}
static __device__ __forceinline__ int mod7(int a) { return a >= 7 ? a - 7 : a; }

constexpr int L_KR = 81920;
constexpr int L_VR = L_KR + 9216, L_RV = L_VR + 8192;
constexpr int L_XR = 114688;
constexpr int L_Z2 = 114688;
constexpr int L_Z1 = 131072;
constexpr int L_KD = 147456;
constexpr int L_CS = 155648;
constexpr int L_XST = 156672;
constexpr int L_ZST = 157184;
constexpr int L_CNT = 157696;

#define MIX(cnt, n) do { for (int q_ = 0; q_ < (cnt); ++q_) { __builtin_amdgcn_sched_group_barrier(8, 1, 0); __builtin_amdgcn_sched_group_barrier(0x100, (n), 0); } } while (0)

#define TOFF 0
#define ADEPTH 11
#define RD 4
#define NOTOUCH
#define NA1 6
struct Tile { int i, j, rot1, rot2, rot3, r, pg, o; };

static __device__ __forceinline__ u64 epoch_load(const unsigned char *ws) {
  u64 e;
  asm volatile("s_load_dwordx2 %0, %1, 0x0" : "=s"(e) : "s"(ws) : "memory");
  return e;
}
#define EPOCH_WAIT(e) asm volatile("s_waitcnt lgkmcnt(0)" : "+s"(e)::"memory")
static __device__ __forceinline__ v4u widen(v2u x, v2u y) {
  const v2u a = __builtin_amdgcn_permlane16_swap(x.x, y.x, false, false), b = __builtin_amdgcn_permlane16_swap(x.y, y.y, false, false);
  return v4u{a.x, b.x, a.y, b.y};
}

static __device__ __forceinline__ void gemm_compute(const Params &p, LAS unsigned char *L, const Tile t, int w) {
  STAMP_DECL;
  STAMP(0);
  u64 tag = epoch_load(p.ws);
  const int i = t.i, j = t.j, rh = w >> 1, ch = w & 1;
  int l = threadIdx.x & 63, g = l >> 4, c = l & 15;
  const rsrc_t rws = mk(p.ws, WS_BYTES);

  {
    const v2u shk = *(const v2u *)(p.shk + (64 * j + 16 * w + c) * 4);
    const u32 dvr = p.dv[64 * j + 16 * w + c];
    if (threadIdx.x < 8) *(LAS u32 *)(L + L_CNT + threadIdx.x * 4) = 0;
    EPOCH_WAIT(tag);
    tag += 1;
    BARW();
    STAMP(1);
    v2u kr[5], vr[4];
    const int r0 = c >> 2;
#pragma unroll
    for (int q = 0; q < 5; ++q) kr[q] = trd(L, L_KR + (16 * g + 4 + 4 * q + r0) * 128 + ((w ^ ((2 + 2 * q + (r0 >> 1)) & 3)) << 5) + (c & 3) * 8);
#pragma unroll
    for (int q = 0; q < 4; ++q) vr[q] = trd(L, L_VR + (16 * g + 4 * q + r0) * 128 + ((w ^ ((2 * q + (r0 >> 1)) & 3)) << 5) + (c & 3) * 8);
    float kf[20], o[16];
#pragma unroll
    for (int q = 0; q < 5; ++q) { kf[4 * q] = lo16(kr[q].x); kf[4 * q + 1] = hi16(kr[q].x); kf[4 * q + 2] = lo16(kr[q].y); kf[4 * q + 3] = hi16(kr[q].y); }
    const float t0 = lo16(shk.x), t1 = hi16(shk.x), t2 = lo16(shk.y), t3 = hi16(shk.y), dd = lo16(dvr);
#pragma unroll
    for (int r = 0; r < 16; ++r) {
      const u32 vv = (r & 2) ? vr[r >> 2].y : vr[r >> 2].x;
      o[r] = __builtin_fmaf(kf[r + 4], t0, __builtin_fmaf(kf[r + 3], t1, __builtin_fmaf(kf[r + 2], t2, kf[r + 1] * t3))) * ((r & 1) ? hi16(vv) : lo16(vv));
    }
    const int cc = 16 * w + c;
    u32 rv[8];
#pragma unroll
    for (int q = 0; q < 8; ++q) rv[q] = pk(o[15 - 2 * q], o[14 - 2 * q]);
    sts16(L, L_RV + cc * 144 + (3 - g) * 32, v4u{rv[0], rv[1], rv[2], rv[3]});
    sts16(L, L_RV + cc * 144 + (3 - g) * 32 + 16, v4u{rv[4], rv[5], rv[6], rv[7]});
    SB();
    v4u ln[2];
#pragma unroll
    for (int q = 0; q < 2; ++q) ln[q] = lds16(L, L_RV + (16 * w + 8 * q + (l >> 3)) * 144 + (l & 7) * 16);
    u32 kd[8];
#pragma unroll
    for (int q = 0; q < 8; ++q) kd[q] = pk(hi16(rv[7 - q]) * dd, lo16(rv[7 - q]) * dd);
#pragma unroll
    for (int q = 0; q < 2; ++q) st16c(ln[q], rws, (64 * j + 16 * w + 8 * q + (l >> 3)) * 4096 + (31 - i) * 128 + (l & 7) * 16, WS_KVR);
    const int ko = L_KD + cc * 128 + ((g ^ ((c >> 1) & 3)) << 5);
    sts16(L, ko, v4u{kd[0], kd[1], kd[2], kd[3]});
    sts16(L, ko + 16, v4u{kd[4], kd[5], kd[6], kd[7]});
  }
  STAMP(2);

  v4u xf[2][14], wf[2][4];
  f4 acc[2][4], cs[2];
  int rb[4];
  const v4u ones = {0x3f803f80u, 0x3f803f80u, 0x3f803f80u, 0x3f803f80u};
  auto start = [&]() {
    const int rdoff = (8 * g + (c >> 2)) * 256 + ((((c >> 2) | ((g & 1) << 2)) ^ (4 * ch + 2 * rh)) << 5) + (c & 3) * 8;
#pragma unroll
    for (int n = 0; n < 4; ++n) { rb[n] = rdoff ^ (n << 5); PIN(rb[n]); acc[0][n] = acc[1][n] = f4{0, 0, 0, 0}; }
    cs[0] = cs[1] = f4{0, 0, 0, 0};
  };
  auto rdw = [&](int ks, v4u (&f)[4]) {
#pragma unroll
    for (int n = 0; n < 4; ++n) {
      if (ks == 7) { rb[n] += 57344; PIN(rb[n]); }
      f[n] = trd2(L, rb[n] + (ks % 7) * 8192, 1024);
    }
  };
  auto sums = [&](int ks) { cs[0] = mfma(ones, wf[ks & 1][0], cs[0]); cs[1] = mfma(ones, wf[ks & 1][1], cs[1]); };
  auto mma = [&](int ks) {
#pragma unroll
    for (int n = 0; n < 4; ++n)
#pragma unroll
      for (int m = 0; m < 2; ++m) acc[m][n] = mfma(xf[m][ks], wf[ks & 1][n], acc[m][n]);
  };
  auto put_cs = [&](int off) {
    const int o = L_CS + off + ((4 * ch + 2 * rh) * 16 + c) * 4;
    *(LAS float *)(L + o) = cs[0].x;
    *(LAS float *)(L + o + 64) = cs[1].x;
  };
  start();
  {
    int xo0 = L_XR + (32 * rh + c) * 128 + ((g ^ (c & 7)) << 4), xo1 = xo0 ^ 64;
    PIN(xo0); PIN(xo1);
    auto rd = [&](int ks) {
      const int so = ((ks >> 1) & 3) * 8192;
      xf[0][ks] = lds16(L, ((ks & 1) ? xo1 : xo0) + so);
      xf[1][ks] = lds16(L, ((ks & 1) ? xo1 : xo0) + so + 2048);
      SB();
      rdw(ks, wf[ks & 1]);
    };
#pragma unroll
    for (int st = 0; st < 7; ++st) {
      STAMPA(2 * st + 1);
      if (st == 0) BARW(); else { SB(); asm volatile("s_waitcnt lgkmcnt(8)\n\ts_barrier" ::: "memory"); SB(); }
      STAMPA(2 * st + 2);
      if (st == 2) {
        DRAIN();
        if (l == 0) stflag(p.ws, WS_KVF + ((4 * j + w) * 32 + i) * 8, tag);
        SB();
      }
      rd(2 * st);
      if (st > 0) { mma(2 * st - 1); sums(2 * st - 1); MIX(8, 1); }
      SB();
      rd(2 * st + 1);
      mma(2 * st);
      sums(2 * st);
      MIX(8, 1);
      SB();
    }
    sums(13);
    put_cs(0);
    BARW();
    mma(13);
  }
  STAMP(3);
  FRESH();
  const u32 tagA = (u32)tag, tagB = (u32)(tag >> 32) ^ 0x9e3779b9u;
  f4 mm[2], rr[2];
  float cv[4];
  auto act_in = [&](int off) {
#pragma unroll
    for (int m = 0; m < 2; ++m) {
      mm[m] = ldsf4(L, L_XST + (32 * rh + 8 * g + 4 * m) * 4);
      rr[m] = ldsf4(L, L_XST + 256 + (32 * rh + 8 * g + 4 * m) * 4);
    }
    const int o = L_CS + off + ((4 * ch + (2 - 2 * rh)) * 16 + c) * 4;
    cv[0] = -cs[0].x;
    cv[1] = -cs[1].x;
    cv[2] = -*(LAS float *)(L + o);
    cv[3] = -*(LAS float *)(L + o + 64);
  };
  auto act = [&](int n, f2 (&z)[4]) {
    f2 x[4], a[4];
    const f2 c2 = {cv[n], cv[n]};
    SB();
#pragma unroll
    for (int m = 0; m < 2; ++m) {
      x[2 * m] = fma2(lo2(mm[m]), c2, lo2(acc[m][n]));
      x[2 * m + 1] = fma2(hi2(mm[m]), c2, hi2(acc[m][n]));
    }
    SB();
#pragma unroll
    for (int m = 0; m < 2; ++m) { x[2 * m] *= lo2(rr[m]); x[2 * m + 1] *= hi2(rr[m]); }
    SB();
#pragma unroll
    for (int q = 0; q < 4; ++q) a[q] = x[q] * x[q];
    SB();
#pragma unroll
    for (int q = 0; q < 4; ++q) a[q] = fma2(a[q], f2{-0.1029432397f, -0.1029432397f}, f2{-2.302208198f, -2.302208198f});
    SB();
#pragma unroll
    for (int q = 0; q < 4; ++q) a[q] *= x[q];
    SB();
#pragma unroll
    for (int q = 0; q < 4; ++q) a[q] = f2{__builtin_amdgcn_exp2f(a[q].x), __builtin_amdgcn_exp2f(a[q].y)};
    SB();
#pragma unroll
    for (int q = 0; q < 4; ++q) a[q] += 1.0f;
    SB();
#pragma unroll
    for (int q = 0; q < 4; ++q) a[q] = f2{__builtin_amdgcn_rcpf(a[q].x), __builtin_amdgcn_rcpf(a[q].y)};
    SB();
#pragma unroll
    for (int q = 0; q < 4; ++q) z[q] = x[q] * a[q];
    SB();
  };
  {
    act_in(0);
    f2 s[4], u[4];
    v4u zp[4];
#pragma unroll
    for (int n = 0; n < 4; ++n) {
      f2 z[4];
      act(n, z);
#pragma unroll
      for (int q = 0; q < 4; ++q) {
        s[q] = n ? s[q] + z[q] : z[q];
        u[q] = n ? fma2(z[q], z[q], u[q]) : z[q] * z[q];
      }
      zp[n] = v4u{pk2(z[0]), pk2(z[1]), pk2(z[2]), pk2(z[3])};
    }
    float sa[8], sq[8];
#pragma unroll
    for (int q = 0; q < 4; ++q) { sa[2 * q] = s[q].x; sa[2 * q + 1] = s[q].y; sq[2 * q] = u[q].x; sq[2 * q + 1] = u[q].y; }
    rowsum8(sa, sq);
    const int row = 8 * g + (c & 1) + ((c >> 1) & 6);
    if ((c & 2) == 0)
      st16c(v4u{asu((c & 1) ? sa[1] : sa[0]), tagA, asu((c & 1) ? sq[1] : sq[0]), tagB}, rws, ((i * 2 + rh) * 16 + 2 * j + ch) * 512 + row * 16, WS_ST);
    SB();
    const int zo = L_Z2 + (rh * 8 + 4 * ch) * 1024 + l * 16;
#pragma unroll
    for (int n = 0; n < 4; ++n) sts16(L, zo + ((n & 1) << 10) + (((n >> 1) ^ rh) << 11), zp[n]);
    lds_signal(L, L_CNT + 4);
  }
  STAMP(4);
  lds_wait(L, L_CNT, 4);
  STAMP(5);
  start();
  rdw(0, wf[0]);
  SB();
#pragma unroll
  for (int ks = 0; ks < 13; ++ks) {
    rdw(ks + 1, wf[(ks + 1) & 1]);
    mma(ks);
    sums(ks);
    MIX(8, 1);
    SB();
  }
  sums(13);
  put_cs(512);
  lds_signal(L, L_CNT + 8);
  SB();
  mma(13);
  SB();
  lds_wait(L, L_CNT + 8, 4);
  STAMP(6);
  FRESH();
  {
    act_in(512);
    const int po = L_Z1 + (64 * ch + c) * 128 + (((4 * rh + g) ^ (c & 7)) << 4);
#pragma unroll
    for (int n = 0; n < 4; ++n) {
      f2 z[4];
      act(n, z);
      sts16(L, po + ((n & 1) << 11) + (((n >> 1) ^ rh) << 12), v4u{pk2(z[0]), pk2(z[1]), pk2(z[2]), pk2(z[3])});
    }
  }
  STAMP(8);
  FRESH();
  v2u xe[4];
  u32 bias[4];
  {
    const rsrc_t rx = mk(p.x, S_ * D_ * 2);
#pragma unroll
    for (int n = 0; n < 4; ++n) {
      xe[n] = __builtin_amdgcn_raw_buffer_load_b64(rx, (64 * i + 16 * w + c) * 896 + j * 128 + g * 8 + n * 32, 0, 0);
      bias[n] = p.bsp[64 * i + 16 * n + c];
    }
  }
  STAMP(9);
  f4 g2[4][2];
  {
#define G2_AH 6
#define G2_AD 2
    constexpr int AH = G2_AH, AD = G2_AD;
    v4u bf[64][2], af[AD + 1][4];
    const int bvo = w * 2048 + l * 16;
    auto ldb = [&](int ks) {
      const int b = ks >> 1, so = WS_ZB + j * 524288 + ((((b >> 3) + (t.rot2 >> 3)) & 3) * 8 + ((b + t.rot2) & 7)) * 16384 + (ks & 1) * 8192;
      bf[ks][0] = ld16c(rws, bvo, so);
      bf[ks][1] = ld16c(rws, bvo + 1024, so);
    };
    int ab[2][2];
    ab[0][0] = c * 128 + ((g ^ (c & 7)) << 4);
    ab[1][0] = ab[0][0] ^ 64;
    ab[0][1] = ab[0][0] + 65536;
    ab[1][1] = ab[1][0] + 65536;
    PIN(ab[0][0]); PIN(ab[1][0]); PIN(ab[0][1]); PIN(ab[1][1]);
    auto rda = [&](int ks) {
#pragma unroll
      for (int m = 0; m < 4; ++m) af[ks % (AD + 1)][m] = lds16(L, ab[ks & 1][(ks >> 4) & 1] + ((ks >> 1) & 7) * 8192 + m * 2048);
    };
#pragma unroll
    for (int m = 0; m < 4; ++m) g2[m][0] = g2[m][1] = f4{0, 0, 0, 0};
    BARW();
    STAMP(10);
#pragma unroll
    for (int ks = 0; ks < AH; ++ks) ldb(ks);
#pragma unroll
    for (int ks = 0; ks < AD; ++ks) rda(ks);
    SB();
    unr<64>([&](auto ksc) {
      constexpr int ks = ksc;
      if constexpr (ks + AH < 64) ldb(ks + AH);
      if constexpr (ks + AD < 64) rda(ks + AD);
#pragma unroll
      for (int b = 0; b < 2; ++b)
#pragma unroll
        for (int m = 0; m < 4; ++m) g2[m][b] = mfma(bf[ks][b], af[ks % (AD + 1)][m], g2[m][b]);
      MIX(4, 1);
      for (int q_ = 0; q_ < 2; ++q_) { __builtin_amdgcn_sched_group_barrier(8, 1, 0); __builtin_amdgcn_sched_group_barrier(0x20, 1, 0); }
      SB();
      if constexpr ((ks & 7) == 7) BAR0();
    });
  }
  STAMP(11);
  FRESH();
  {
    const int f0 = 4 * g + (c >> 2);
    const int zr = L_Z1 + (32 * w + f0) * 128 + (c & 1) * 8;
    const int sw = ((c >> 1) & 1) ^ (f0 & 7);
#pragma unroll
    for (int m = 0; m < 4; ++m) {
      const v4u z = trd2(L, zr + (((2 * m) ^ sw) << 4), 2048);
      const float b = lo16(bias[m]);
      const v4u o = {pk(lo16(z.x) * (g2[m][0].x + b), hi16(z.x) * (g2[m][0].y + b)), pk(lo16(z.y) * (g2[m][0].z + b), hi16(z.y) * (g2[m][0].w + b)),
                     pk(lo16(z.z) * (g2[m][1].x + b), hi16(z.z) * (g2[m][1].y + b)), pk(lo16(z.w) * (g2[m][1].z + b), hi16(z.w) * (g2[m][1].w + b))};
      st16c(o, rws, l * 16, WS_UB + ((i * 4 + m) * 28 + 4 * j + w) * 1024);
    }
  }
  STAMP(7);
  DRAIN();
  if (lds_last(L, L_CNT + 16)) {
    if (l == 0) stflag(p.ws, WS_UF + (i * 16 + j) * 8, tag);
    wait_flags(p.ws, WS_UF + (i * 16 + (l < 7 ? l : 0)) * 8, tag);
    lds_signal(L, L_CNT + 20);
  } else lds_wait(L, L_CNT + 20, 1);
  STAMP(12);
  FRESH();
  {
    constexpr int AH = 12;
    v4u uf[28], of[2][8];
    int uso[7];
#pragma unroll
    for (int q = 0; q < 7; ++q) uso[q] = WS_UB + (i * 4 + w) * 28672 + mod7(q + t.rot3) * 4096;
    auto ldu = [&](int ks) { uf[ks] = ld16c(rws, l * 16 + (ks & 3) * 1024, uso[ks >> 2]); };
#pragma unroll
    for (int ks = 0; ks < AH; ++ks) ldu(ks);
    f4 o[4];
    int ob[4];
    const int oro = (4 * g + (c >> 2)) * 128 + ((((c >> 3) & 1) | ((g & 1) << 1)) << 5) + (c & 3) * 8;
#pragma unroll
    for (int n = 0; n < 4; ++n) { o[n] = f4{0, 0, 0, 0}; ob[n] = oro ^ (n << 5); PIN(ob[n]); }
    auto rd = [&](int kp, v4u (&f)[8]) {
#pragma unroll
      for (int q = 0; q < 8; ++q) {
        const int n = q & 3, ks = 2 * kp + (q >> 2);
        if (ks == 14) { ob[n] += 57344; PIN(ob[n]); }
        f[q] = trd2(L, ob[n] + (ks % 14) * 4096, 2048);
      }
    };
    BAR0();
    rd(0, of[0]);
    SB();
#pragma unroll
    for (int kp = 0; kp < 14; ++kp) {
      if (2 * kp + AH < 28) { ldu(2 * kp + AH); ldu(2 * kp + AH + 1); }
      if (kp < 13) rd(kp + 1, of[(kp + 1) & 1]);
#pragma unroll
      for (int q = 0; q < 8; ++q) o[q & 3] = mfma(of[kp & 1][q], uf[2 * kp + (q >> 2)], o[q & 3]);
      __builtin_amdgcn_sched_group_barrier(0x20, 2, 0);
      MIX(8, 2);
      SB();
    }
    STAMP(13);
    BAR0();
    FRESH();
    const f2 st = {*(LAS float *)(L + L_XST + (16 * w + c) * 4), *(LAS float *)(L + L_XST + 256 + (16 * w + c) * 4)};
    const int yo = (4 * g + (c >> 2)) * 128 + ((w ^ (((c >> 3) & 1) | ((g & 1) << 1))) << 5) + (c & 3) * 8;
    v2u res[4];
#pragma unroll
    for (int n = 0; n < 4; ++n) {
      const v2u y = trd(L, L_Z2 + yo + n * 2048), kd = trd(L, L_KD + yo + n * 2048);
      const float xv[4] = {lo16(xe[n].x), hi16(xe[n].x), lo16(xe[n].y), hi16(xe[n].y)};
      const float yv[4] = {lo16(y.x) + lo16(kd.x), hi16(y.x) + hi16(kd.x), lo16(y.y) + lo16(kd.y), hi16(y.y) + hi16(kd.y)};
      float r[4];
#pragma unroll
      for (int e = 0; e < 4; ++e) {
        const float a = o[n][e];
        const float sg = a * __builtin_amdgcn_rcpf(1.0f + __builtin_amdgcn_exp2f(a * -1.442695041f));
        r[e] = __builtin_fmaf((xv[e] - st.x) * st.y * yv[e], sg, xv[e]);
      }
      res[n] = v2u{pk(r[0], r[1]), pk(r[2], r[3])};
    }
    const rsrc_t ro = mk(p.out, S_ * D_ * 2);
    const int oo = (64 * i + 16 * w + c) * 896 + j * 128 + (g & 1) * 32 + (g >> 1) * 16;
    st16c(widen(res[0], res[1]), ro, oo, 0);
    st16c(widen(res[2], res[3]), ro, oo, 64);
  }
  STAMP_OUT(0);
}

static __device__ __forceinline__ void gemm_move(const Params &p, LAS unsigned char *L, const Tile t, int m) {
  STAMP_DECL;
  STAMP(0);
  const int i = t.i, j = t.j;
  int l = threadIdx.x & 63, g = l >> 4, c = l & 15;
  const srd_t sx_ = mks(p.x, S_ * D_ * 2), swin = mks(p.win, D_ * F_ * 2), swsp = mks(p.wsp, S_ * S_ * 2);
  const int lb = m * 1024, lb2 = m * 2048;
  const int pvo = (l >> 3) * 896 + j * 128 + (((((l & 7) >> 1) ^ g) << 5) | ((l & 1) << 4));
  {
    const srd_t sk = mks(p.k, S_ * D_ * 2), sv = mks(p.v, S_ * D_ * 2);
    const int so = (64 * i + 16 * m) * 896;
    if (m == 0) dma<L_KR>(sk, pvo, i > 0 ? so - 7168 : 0x40000000, 0);
    dma<L_KR + 1024>(sk, pvo, so, lb2);
    dma<L_KR + 2048>(sk, pvo, so + 7168, lb2);
    dma<L_VR>(sv, pvo, so, lb2);
    dma<L_VR + 1024>(sv, pvo, so + 7168, lb2);
  }
  const int rsw = ((l & 7) ^ (l >> 3)) << 4;
  const int xvo = (64 * i + 32 * (m >> 1) + 4 * (m & 1) + 8 * (l >> 5) + ((l >> 3) & 3)) * 896 + rsw;
  const int wvo = (4 * m + g) * 3584 + j * 256 + ((((c >> 1) ^ (g | ((m & 2) << 1))) << 5) | ((c & 1) << 4));
  int sb[7];
#pragma unroll
  for (int s = 0; s < 7; ++s) sb[s] = mod7(s + t.rot1);
  auto item = [&](auto nc) {
    constexpr int n = nc, s = n / 6, q = n % 6;
    if constexpr (n >= 42) dma<(n - 42) * 4096>(swin, wvo, (sb[(n - 42) >> 2] * 4 + ((n - 42) & 3)) * 57344, lb);
    else if constexpr (q < 2) dma<L_XR + (s & 3) * 8192 + q * 1024>(sx_, xvo, sb[s] * 128 + q * 14336, lb2);
    else dma<(4 * s + q - 2) * 4096>(swin, wvo, (sb[s] * 4 + q - 2) * 57344 + 1792, lb);
  };
  unr<12>(item);
  u64 tag = epoch_load(p.ws);
  VM(12);
  EPOCH_WAIT(tag);
  tag += 1;
  BAR0();
  unr<6>([&](auto q) { item(std::integral_constant<int, 12 + q>{}); });
  {
    const v4u ones = {0x3f803f80u, 0x3f803f80u, 0x3f803f80u, 0x3f803f80u};
    f4 sx = {0, 0, 0, 0}, gr = {0, 0, 0, 0};
    int xo0 = L_XR + (16 * m + c) * 128 + ((g ^ (c & 7)) << 4), xo1 = xo0 ^ 64;
    PIN(xo0); PIN(xo1);
    unr<7>([&](auto sc) {
      constexpr int s = sc;
      VM(12);
      v4u xs[2];
      xs[0] = lds16(L, xo0 + (s & 3) * 8192);
      xs[1] = lds16(L, xo1 + (s & 3) * 8192);
      STAMPA(2 * s + 1);
      BAR0();
      STAMPA(2 * s + 2);
      if (s == 0) STAMP(1);
#pragma unroll
      for (int q = 0; q < 2; ++q) { sx = mfma(xs[q], ones, sx); gr = mfma(xs[q], xs[q], gr); }
      SB();
      if constexpr (s < 6) unr<6>([&](auto q) { item(std::integral_constant<int, 18 + 6 * s + q>{}); });
    });
    STAMP(2);
    const int e = c & 3;
    const float s1 = e & 2 ? (e & 1 ? sx.w : sx.z) : (e & 1 ? sx.y : sx.x);
    const float s2 = e & 2 ? (e & 1 ? gr.w : gr.z) : (e & 1 ? gr.y : gr.x);
    const float mean = fmul(s1, 1.0f / 448.0f);
    const float r = inv_sigma(ffma(-mean, mean, fmul(s2, 1.0f / 448.0f)));
    if ((c >> 2) == g) {
      const int row = 32 * (m >> 1) + 8 * g + 4 * (m & 1) + e;
      *(LAS float *)(L + L_XST + row * 4) = rbf(mean);
      *(LAS float *)(L + L_XST + 256 + row * 4) = r;
    }
    SB();
    unr<6>([&](auto q) { item(std::integral_constant<int, 54 + q>{}); });
  }
  BARW();
  STAMP(3);
  unr<10>([&](auto q) { VM(9); item(std::integral_constant<int, 60 + q>{}); });
  const rsrc_t rwsp = mk(p.wsp, S_ * S_ * 2), rws = mk(p.ws, WS_BYTES);
  v4u ar[32];
  const int avo = (64 * i + 16 * m + (l >> 3)) * 4096 + rsw;
  auto aso = [&](int n) { const int b = n >> 1; return ((((b >> 3) + (t.rot2 >> 3)) & 3) * 8 + ((b + t.rot2) & 7)) * 128 + (n & 1) * 32768; };
  auto lda = [&](int n0, int n1) {
#pragma unroll
    for (int n = n0; n < n1; ++n) { VM(ADEPTH); ar[n] = ld16(rwsp, avo, aso(32 + n)); SB(); }
  };
  lda(0, NA1);
  VM(NA1);
  lds_signal(L, L_CNT);
  STAMP(4);
  {
    const u32 tagA = (u32)tag, tagB = (u32)(tag >> 32) ^ 0x9e3779b9u;
    const int so = WS_ST + (i * 2 + (m >> 1)) * 8192 + (m & 1) * 256;
    const int vo = g * 512 + c * 16, vo3 = (g & 1) * 512 + c * 16 + 2048;
    v4u sg[4], zt[4];
    auto ldst = [&]() {
      sg[0] = ld16c(rws, vo, so); sg[1] = ld16c(rws, vo + 2048, so); sg[2] = ld16c(rws, vo, so + 4096); sg[3] = ld16c(rws, vo3, so + 4096);
    };
    lda(NA1, 32);
    STAMP(12);
    lds_wait(L, L_CNT + 4, 4);
    ldst();
    SB();
    const int zo = ((m >> 1) * 8 + (l >> 5)) * 1024 + ((m & 1) * 32 + (l & 31)) * 16;
#pragma unroll
    for (int q = 0; q < 4; ++q) zt[q] = lds16(L, L_Z2 + zo + q * 2048);
    STAMP(5);
#pragma unroll 1
    for (u32 s = 0; s < SPIN_CAP; ++s) {
      bool ok = true;
#pragma unroll
      for (int q = 0; q < 4; ++q) ok = ok && sg[q].y == tagA && sg[q].w == tagB;
      if (__all(ok)) break;
      asm volatile("" ::: "memory");
      ldst();
    }
    STAMP(13);
    float s = fadd(fadd(asf(sg[0].x), asf(sg[1].x)), fadd(asf(sg[2].x), g < 2 ? asf(sg[3].x) : 0.f));
    float u = fadd(fadd(asf(sg[0].z), asf(sg[1].z)), fadd(asf(sg[2].z), g < 2 ? asf(sg[3].z) : 0.f));
    {
      v2u a = __builtin_amdgcn_permlane16_swap(asu(s), asu(u), false, false);
      const float h = fadd(asf(a.x), asf(a.y));
      a = __builtin_amdgcn_permlane32_swap(asu(h), asu(h), false, false);
      const float k = fadd(asf(a.x), asf(a.y));
      a = __builtin_amdgcn_permlane16_swap(asu(k), asu(k), false, false);
      s = asf(a.x);
      u = asf(a.y);
    }
    const float mean = fmul(s, 1.0f / 896.0f);
    const float r = inv_sigma(ffma(-mean, mean, fmul(u, 1.0f / 896.0f)));
    if (g < 2) *(LAS float *)(L + L_ZST + g * 256 + (16 * m + c) * 4) = g ? r : -rbf(mean) * r;
    SB();
    f4 zb[2], zr[2];
#pragma unroll
    for (int q = 0; q < 2; ++q) {
      zb[q] = ldsf4(L, L_ZST + (16 * m + 8 * (g & 1) + 4 * q) * 4);
      zr[q] = ldsf4(L, L_ZST + 256 + (16 * m + 8 * (g & 1) + 4 * q) * 4);
    }
#pragma unroll
    for (int q = 0; q < 4; ++q) {
      u32 o[4];
#pragma unroll
      for (int e = 0; e < 4; ++e) {
        const u32 z = zt[q][e];
        o[e] = pk(ffma(lo16(z), zr[e >> 1][(2 * e) & 3], zb[e >> 1][(2 * e) & 3]), ffma(hi16(z), zr[e >> 1][(2 * e + 1) & 3], zb[e >> 1][(2 * e + 1) & 3]));
      }
      st16c(v4u{o[0], o[1], o[2], o[3]}, rws, (j * 64 + 2 * i) * 8192 + zo + q * 2048, WS_ZB);
      SB();
    }
    STAMP(6);
  }
  lds_wait(L, L_CNT + 8, 4);
  auto ring = [&](auto nc) { constexpr int n = nc; dma<(n >> 1) * 8192 + (n & 1) * 1024>(swsp, avo, aso(n), lb2); };
  unr<RD>(ring);
  VM(RD);
  if (lds_last(L, L_CNT + 24) && l == 0) stflag(p.ws, WS_ZF + (j * 32 + i) * 8, tag);
  SB();
  STAMP(7);
  unr<28 - RD>([&](auto nc) { VM(RD - 1); ring(std::integral_constant<int, RD + nc>{}); });
  lds_wait(L, L_CNT + 24, 4);
  unr<4>([&](auto nc) { VM(RD - 1); ring(std::integral_constant<int, 28 + nc>{}); });
  STAMP(11);
  if (m == 0) wait_flags(p.ws, WS_ZF + j * 256 + (l & 31) * 8, tag);
  DRAIN();
  BAR0();
  STAMP(8);
  FRESH();
  int awo = m * 2048 + l * 16, awh = awo + 65536;
  PIN(awo); PIN(awh);
  {
    constexpr int cum[6] = {0, 6, 12, 19, 25, 32};
#pragma unroll
    for (int k = 0; k < 5; ++k) {
      BARW();
#pragma unroll
      for (int n = cum[k]; n < cum[k + 1]; ++n) {
        sts16(L, ((n & 16) ? awh : awo) + ((n >> 1) & 7) * 8192 + (n & 1) * 1024, ar[n]);
        SB();
        __builtin_amdgcn_s_sleep(2);
        SB();
      }
    }
  }
  BARW();
  BAR0();
  STAMP(9);
  {
    const srd_t swout = mks(p.wout, 896 * D_ * 2);
    auto ldo = [&](auto nc) { constexpr int n = nc; VM(5); dma<n * 4096>(swout, pvo, (mod7((n >> 2) + t.rot3) * 16 + (n & 3) * 4 + m) * 7168, lb); };
    unr<12>(ldo);
    BAR0();
    STAMP(10);
    unr<16>([&](auto q) { ldo(std::integral_constant<int, 12 + q>{}); });
  }
  DRAIN();
  BAR0();
  wait_flags(p.ws, WS_YF + (((64 * j + 16 * m + min(g * 7, 15)) / 14) * 4 + (l & 3)) * 8, tag);
  {
    const srd_t sws = mks(p.ws, WS_BYTES);
    const int yvo = (64 * j + 16 * m + (l >> 3)) * 4096 + i * 128 + (((((l & 7) >> 1) ^ g) << 5) | ((l & 1) << 4));
    dmac<L_Z2>(sws, yvo, WS_YB, lb2);
    dmac<L_Z2 + 1024>(sws, yvo, WS_YB + 32768, lb2);
  }
  DRAIN();
  BAR0();
  STAMP_OUT(1);
}

constexpr int C_CP = 4160, C_KV = 4 * C_CP, C_SLOT = C_KV + 4608;

template <int H> static __device__ __forceinline__ void conv_half(const Params &p, LAS unsigned char *L, int v, int w, u64 tag) {
  STAMP_DECL;
  STAMP(0);
  constexpr int NF = H ? 6 : 8, NT = H ? 5 : 3;
  constexpr int tile[5] = {H ? 5 : 7, H ? 4 : 6, H ? 3 : 2, 1, 0};
  const int l = threadIdx.x & 63, g = l >> 4, c = l & 15, ch = w >> 1;
  const rsrc_t rws = mk(p.ws, WS_BYTES);
  const int hro = ch * C_SLOT + (c & 3) * C_CP + g * 16 + (c >> 2) * 8;
  const int kro = ch * C_SLOT + C_KV + (254 - 2 * c + g) * 16 - 7 * 512;
  v4u hfr[2][NF], kfr[2][NF];
  auto rd = [&](int ho, int ko, int q) {
#pragma unroll
    for (int d = 0; d < NF; ++d) {
      const v2u a = *(LAS v2u *)(L + ho + d * 512 + q * 64);
      v2u b;
      asm volatile("" ::: "memory");
      b = *(LAS v2u *)(L + ho + d * 512 + q * 64 + 8);
      asm volatile("" ::: "memory");
      hfr[q & 1][d] = v4u{a.x, a.y, b.x, b.y};
      kfr[q & 1][NF - 1 - d] = lds16(L, ko + (8 - NF + d) * 512 + q * 64);
    }
  };
  BAR0();
  STAMP(2);
  rd(hro, kro, 0);
  int set = 0;
#pragma unroll 1
  for (int rho = 0; rho < 7; ++rho) {
    const int nset = set == 4 * C_SLOT ? 0 : set + 2 * C_SLOT;
    f4 acc[NT];
#pragma unroll
    for (int n = 0; n < NT; ++n) acc[n] = f4{0, 0, 0, 0};
#pragma unroll
    for (int q = 0; q < 8; ++q) {
      if (q == 7) BARW();
      SB();
      if (q < 7) rd(hro + set, kro + set, q + 1); else rd(hro + nset, kro + nset, 0);
#pragma unroll
      for (int d = 0; d < NF; ++d)
#pragma unroll
        for (int n = NT - 1; n >= 0; --n) {
          const int P = d - (NF - 1 - tile[n]);
          if (P >= 0) acc[n] = mfma(hfr[q & 1][P], kfr[q & 1][tile[n] - P], acc[n]);
        }
      if (H) MIX(18, 1);
      else { for (int q_ = 0; q_ < 6; ++q_) { MIX(2, 1); MIX(1, 2); } }
      SB();
    }
    v2u y[NT];
#pragma unroll
    for (int n = 0; n < NT; ++n) y[n] = v2u{pk(acc[n].x, acc[n].y), pk(acc[n].z, acc[n].w)};
    const int yo = WS_YB + (14 * v + 2 * rho + ch) * 4096 + c * 32;
    st16c(widen(y[1], y[0]), rws, yo + (tile[1] + (g & 1)) * 512 + (g >> 1) * 16, 0);
    __builtin_amdgcn_raw_buffer_store_b64(y[2], rws, yo + tile[2] * 512 + g * 8, 0, 16);
    if constexpr (H) st16c(widen(y[4], y[3]), rws, yo + (g & 1) * 512 + (g >> 1) * 16, 0);
    set = nset;
    if (rho == 0) STAMP(3);
    if (rho == 1) STAMP(4);
    if (rho == 3) STAMP(5);
    if (rho == 5) STAMP(6);
  }
  STAMP(7);
  DRAIN();
  if (l == 0) stflag(p.ws, WS_YF + (v * 4 + w) * 8, tag);
  STAMP_OUT(0);
}

static __device__ __forceinline__ void conv_stage(const Params &p, LAS unsigned char *L, int v, int m) {
  STAMP_DECL;
  STAMP(0);
  const int l = threadIdx.x & 63;
  const srd_t sh = mks(p.h, D_ * S_ * 2), sws = mks(p.ws, WS_BYTES);
  const rsrc_t rws = mk(p.ws, WS_BYTES);
  const int vo = m * 1024 + l * 16;
  auto stage_h = [&](int rho, int set) {
    const int so = (14 * v + 2 * rho) * 4096, lb = set + m * 1024;
    unr<8>([&](auto qc) { constexpr int q = qc; dma<(q >> 2) * C_SLOT + (q & 3) * C_CP + 32>(sh, vo, so + (q >> 2) * 4096 + (q & 3) * 2 + 2, lb); });
  };
  auto stage_kv = [&](int rho, int set) {
    const int so = WS_KVR + (14 * v + 2 * rho) * 4096, lb = set + m * 1024;
    dmac<C_KV>(sws, vo, so, lb);
    dmac<C_SLOT + C_KV>(sws, vo, so + 4096, lb);
  };
  u64 head = *(const u64 *)(p.h + (14 * v + (l < 56 ? l >> 2 : 0)) * 2048);
  stage_h(0, 0);
  u64 tag = epoch_load(p.ws);
  stage_h(1, 2 * C_SLOT);
  stage_h(2, 4 * C_SLOT);
  head <<= 48 - 16 * (l & 3);
  auto put_head = [&](int rho, int set) {
    if (m == 0 && (l >> 3) == rho) *(LAS u64 *)(L + set + ((l >> 2) & 1) * C_SLOT + (l & 3) * C_CP + 24) = head;
  };
  {
    const v4u z = {0, 0, 0, 0};
    const int t = m * 64 + l;
    if (t < 192) sts16(L, (t >> 5) * C_SLOT + C_KV + 4096 + (t & 31) * 16, z);
    if (t < 24) { sts16(L, (t >> 2) * C_SLOT + (t & 3) * C_CP, z); *(LAS u64 *)(L + (t >> 2) * C_SLOT + (t & 3) * C_CP + 16) = 0; }
  }
  put_head(0, 0);
  put_head(1, 2 * C_SLOT);
  put_head(2, 4 * C_SLOT);
  EPOCH_WAIT(tag);
  tag += 1;
  wait_flags(p.ws, WS_KVF + (((14 * v + (l < 32 ? 0 : 13)) >> 4) * 32 + (l & 31)) * 8, tag);
  STAMP(1);
  stage_kv(0, 0);
  stage_kv(1, 2 * C_SLOT);
  stage_kv(2, 4 * C_SLOT);
  VM(4);
  BARW();
  STAMP(2);
  if (l == 0) stflag(p.ws, WS_HI + (v * 4 + m) * 8, tag);
  int set = 0;
#pragma unroll 1
  for (int rho = 0; rho < 7; ++rho) {
    DRAIN();
    BARW();
    if (rho < 4) {
      stage_h(rho + 3, set);
      stage_kv(rho + 3, set);
      put_head(rho + 3, set);
    }
    set = set == 4 * C_SLOT ? 0 : set + 2 * C_SLOT;
    if (rho == 4 && v == 0 && m == 0) {
#pragma unroll 1
      for (u32 s = 0; s < SPIN_CAP; ++s) {
        bool ok = true;
#pragma unroll
        for (int q = 0; q < 8; ++q) {
          const v4u f = ld16c(rws, l * 16 + (q & 3) * 1024, WS_KVF + (q >> 2) * 4096);
          ok = ok && f.x == (u32)tag && f.y == (u32)(tag >> 32) && f.z == (u32)tag && f.w == (u32)(tag >> 32);
        }
        if (__all(ok)) break;
        asm volatile("" ::: "memory");
      }
      if (l == 0) stflag(p.ws, WS_EPOCH, tag);
    }
  }
  STAMP_OUT(1);
}

__global__ void __launch_bounds__(512, 2) fused_kernel(Params p) {
  extern __shared__ __attribute__((aligned(16))) unsigned char smem[];
  LAS unsigned char *L = (LAS unsigned char *)smem;
  const int b = blockIdx.x, x = b & 7, r = b >> 3, wv = __builtin_amdgcn_readfirstlane(threadIdx.x >> 6);
  if (wv >= 4) __builtin_amdgcn_s_setprio(2);
  if (r >= 28) {
    const int v = x * 4 + (r - 28);
    if (wv < 4) {
      u64 tag = epoch_load(p.ws);
      EPOCH_WAIT(tag);
      if (wv & 1) conv_half<1>(p, L, v, wv, tag + 1);
      else conv_half<0>(p, L, v, wv, tag + 1);
    } else conv_stage(p, L, v, wv - 4);
  } else {
    const int pg = x >> 1, o = x & 1;
    Tile t;
    if (r < 24) { t.i = 8 * pg + r / 3; t.j = 4 * o + r % 3; }
    else { t.i = 8 * pg + 4 * o + (r - 24); t.j = 3; }
    t.rot1 = 2 * ((t.i + t.j) & 3);
    t.rot2 = 8 * (pg + 2 * ((t.i + t.j) & 1)) + ((2 * (t.i >> 1) + 4 * (t.j >> 1)) & 7);
    t.rot3 = 2 * (t.j & 3);
    t.r = r; t.pg = pg; t.o = o;
    if (wv < 4) gemm_compute(p, L, t, wv);
    else gemm_move(p, L, t, wv - 4);
  }
}

extern "C" void kernel_launch(void *const *d_in, const int *in_sizes, int n_in, void *d_out, int out_size, void *d_ws, size_t ws_size,
                              hipStream_t stream) {
  Params p;
  p.x = (const u16 *)d_in[0];
  p.k = (const u16 *)d_in[1];
  p.v = (const u16 *)d_in[2];
  p.shk = (const u16 *)d_in[3];
  p.h = (const u16 *)d_in[4];
  p.dv = (const u16 *)d_in[5];
  p.win = (const u16 *)d_in[7];
  p.wsp = (const u16 *)d_in[8];
  p.bsp = (const u16 *)d_in[9];
  p.wout = (const u16 *)d_in[10];
  p.out = (u16 *)d_out;
  p.ws = (unsigned char *)d_ws;
  (void)hipFuncSetAttribute((const void *)fused_kernel, hipFuncAttributeMaxDynamicSharedMemorySize, LDS_BYTES);
  fused_kernel<<<256, 512, LDS_BYTES, stream>>>(p);
}
```

```cpp
#include <hip/hip_runtime.h>
#include <stdint.h>
#include <type_traits>

typedef unsigned short u16;
typedef unsigned int u32;
typedef unsigned long long u64;
typedef u32 v4u __attribute__((ext_vector_type(4)));
typedef u32 v2u __attribute__((ext_vector_type(2)));
typedef float f4 __attribute__((ext_vector_type(4)));
typedef float f2 __attribute__((ext_vector_type(2)));
typedef short s4 __attribute__((ext_vector_type(4)));
typedef __bf16 bf8 __attribute__((ext_vector_type(8)));
typedef __bf16 bf2 __attribute__((ext_vector_type(2)));
#define LAS __attribute__((address_space(3)))
#define GAS __attribute__((address_space(1)))
typedef __amdgpu_buffer_rsrc_t rsrc_t;
typedef int srd_t __attribute__((ext_vector_type(4)));

constexpr int S_ = 2048, D_ = 448, F_ = 1792;
constexpr int WS_EPOCH = 0;
constexpr int WS_KVF = 4096;
constexpr int WS_HI = 11264;
constexpr int WS_ZF = 16384;
constexpr int WS_UF = 32768;
constexpr int WS_YF = 49152;
constexpr int WS_ST = 262144;
constexpr int WS_ZB = 1 << 20;
constexpr int WS_UB = 5 << 20;
constexpr int WS_KVR = 9 << 20;
constexpr int WS_YB = 11 << 20;
constexpr int WS_STAMP = 14 << 20;
constexpr int WS_BYTES = 16 << 20;
constexpr int LDS_BYTES = 163840;
constexpr u32 SPIN_CAP = 1u << 16;

struct Params {
  const u16 *x, *k, *v, *shk, *h, *dv, *win, *wsp, *bsp, *wout;
  u16 *out;
  unsigned char *ws;
};

#define SB() __builtin_amdgcn_sched_barrier(0)
#define BARW() do { SB(); asm volatile("s_waitcnt lgkmcnt(0)\n\ts_barrier" ::: "memory"); SB(); } while (0)
#define BAR0() do { SB(); asm volatile("s_barrier" ::: "memory"); SB(); } while (0)
#define FRESH() do { asm volatile("" : "+v"(l)); g = l >> 4; c = l & 15; } while (0)
#define DRAIN() asm volatile("s_waitcnt vmcnt(0)" ::: "memory")
#define VM(n) asm volatile("s_waitcnt vmcnt(%0)" ::"n"(n) : "memory")
#define PIN(x) asm volatile("" : "+v"(x))
#define STAMP(n) do { } while (0)
#define STAMPA(n) do { } while (0)
#define STAMP_DECL do { } while (0)
#define STAMP_OUT(slot) do { } while (0)

static __device__ __forceinline__ rsrc_t mk(const void *p, int bytes) { return __builtin_amdgcn_make_buffer_rsrc((void *)p, 0, bytes, 0x00020000); }
static __device__ __forceinline__ v4u ld16(rsrc_t r, int vo, int so) { return __builtin_amdgcn_raw_buffer_load_b128(r, vo, so, 0); }
static __device__ __forceinline__ v4u ld16c(rsrc_t r, int vo, int so) { return __builtin_amdgcn_raw_buffer_load_b128(r, vo, so, 16); }
static __device__ __forceinline__ u32 ld4(rsrc_t r, int vo, int so) { return __builtin_amdgcn_raw_buffer_load_b32(r, vo, so, 0); }
static __device__ __forceinline__ void st16c(v4u v, rsrc_t r, int vo, int so) { __builtin_amdgcn_raw_buffer_store_b128(v, r, vo + so, 0, 16); }
static __device__ __forceinline__ srd_t mks(const void *p, int bytes) { return srd_t{(int)(u64)p, (int)((u64)p >> 32) & 0xffff, bytes, 0x00020000}; }
template <int IMM> static __device__ __forceinline__ void dma(srd_t r, int vo, int so, int base) {
  asm volatile("s_add_u32 m0, %0, %4\n\ts_nop 0\n\tbuffer_load_dwordx4 %1, %2, %3 offen lds" ::"s"(base), "v"(vo), "s"(r), "s"(so), "n"(IMM) : "m0", "scc", "memory");
}
template <int IMM> static __device__ __forceinline__ void dmac(srd_t r, int vo, int so, int base) {
  asm volatile("s_add_u32 m0, %0, %4\n\ts_nop 0\n\tbuffer_load_dwordx4 %1, %2, %3 offen sc1 lds" ::"s"(base), "v"(vo), "s"(r), "s"(so), "n"(IMM) : "m0", "scc", "memory");
}
static __device__ __forceinline__ void touch(srd_t r, int vo, int so) {
  asm volatile("s_mov_b32 m0, %3\n\ts_nop 0\n\tbuffer_load_dword %0, %1, %2 offen lds" ::"v"(vo), "s"(r), "s"(so), "n"(LDS_BYTES - 256) : "m0", "memory");
}
static __device__ __forceinline__ float one(float r) { asm("" : "+v"(r)); return r; }
static __device__ __forceinline__ float fadd(float a, float b) { return one(a + b); }
static __device__ __forceinline__ float fmul(float a, float b) { return one(a * b); }
static __device__ __forceinline__ float ffma(float a, float b, float c) { return one(__builtin_fmaf(a, b, c)); }
template <int N, typename Fn> static __device__ __forceinline__ void unr(Fn &&f) {
  if constexpr (N > 0) { unr<N - 1>(f); f(std::integral_constant<int, N - 1>{}); }
}
static __device__ __forceinline__ f4 mfma(v4u a, v4u b, f4 c) {
  return __builtin_amdgcn_mfma_f32_16x16x32_bf16(__builtin_bit_cast(bf8, a), __builtin_bit_cast(bf8, b), c, 0, 0, 0);
}
static __device__ __forceinline__ float asf(u32 u) { return __builtin_bit_cast(float, u); }
static __device__ __forceinline__ u32 asu(float f) { return __builtin_bit_cast(u32, f); }
static __device__ __forceinline__ float lo16(u32 u) { return asf(u << 16); }
static __device__ __forceinline__ float hi16(u32 u) { return asf(u & 0xffff0000u); }
static __device__ __forceinline__ u32 pk(float a, float b) { f2 t = {a, b}; return __builtin_bit_cast(u32, __builtin_convertvector(t, bf2)); }
static __device__ __forceinline__ float rbf(float a) { return lo16(pk(a, a)); }
static __device__ __forceinline__ v4u lds16(LAS unsigned char *L, int off) { return *(LAS v4u *)(L + off); }
static __device__ __forceinline__ f4 ldsf4(LAS unsigned char *L, int off) { return *(LAS f4 *)(L + off); }
static __device__ __forceinline__ void sts16(LAS unsigned char *L, int off, v4u v) { *(LAS v4u *)(L + off) = v; }
static __device__ __forceinline__ v2u trd(LAS unsigned char *L, int off) {
  return __builtin_bit_cast(v2u, __builtin_amdgcn_ds_read_tr16_b64_v4i16((LAS s4 *)(L + off)));
}
static __device__ __forceinline__ v4u trd2(LAS unsigned char *L, int off, int step) {
  const v2u a = trd(L, off), b = trd(L, off + step);
  return v4u{a.x, a.y, b.x, b.y};
}
static __device__ __forceinline__ f2 fma2(f2 a, f2 b, f2 c) { return __builtin_elementwise_fma(a, b, c); }
static __device__ __forceinline__ u32 pk2(f2 t) { return __builtin_bit_cast(u32, __builtin_convertvector(t, bf2)); }
static __device__ __forceinline__ f2 lo2(f4 v) { return __builtin_shufflevector(v, v, 0, 1); }
static __device__ __forceinline__ f2 hi2(f4 v) { return __builtin_shufflevector(v, v, 2, 3); }
static __device__ __forceinline__ void rowsum8(float (&a)[8], float (&b)[8]) {
#define DA(d, s, ctl, bm) "v_add_f32_dpp " d ", " s ", " s " " ctl " row_mask:0xf bank_mask:" bm "\n\t"
  asm volatile("s_nop 1\n\t"
               DA("%0", "%0", "row_mirror", "0x3") DA("%1", "%1", "row_mirror", "0x3") DA("%2", "%2", "row_mirror", "0x3") DA("%3", "%3", "row_mirror", "0x3")
               DA("%4", "%4", "row_mirror", "0x3") DA("%5", "%5", "row_mirror", "0x3") DA("%6", "%6", "row_mirror", "0x3") DA("%7", "%7", "row_mirror", "0x3")
               DA("%0", "%8", "row_mirror", "0xc") DA("%1", "%9", "row_mirror", "0xc") DA("%2", "%10", "row_mirror", "0xc") DA("%3", "%11", "row_mirror", "0xc")
               DA("%4", "%12", "row_mirror", "0xc") DA("%5", "%13", "row_mirror", "0xc") DA("%6", "%14", "row_mirror", "0xc") DA("%7", "%15", "row_mirror", "0xc")
               DA("%0", "%0", "row_half_mirror", "0x5") DA("%1", "%1", "row_half_mirror", "0x5") DA("%4", "%4", "row_half_mirror", "0x5") DA("%5", "%5", "row_half_mirror", "0x5")
               DA("%0", "%2", "row_half_mirror", "0xa") DA("%1", "%3", "row_half_mirror", "0xa") DA("%4", "%6", "row_half_mirror", "0xa") DA("%5", "%7", "row_half_mirror", "0xa")
               DA("%0", "%0", "quad_perm:[1,0,3,2]", "0xf") DA("%1", "%1", "quad_perm:[1,0,3,2]", "0xf") DA("%4", "%4", "quad_perm:[1,0,3,2]", "0xf") DA("%5", "%5", "quad_perm:[1,0,3,2]", "0xf")
               DA("%0", "%0", "quad_perm:[2,3,0,1]", "0xf") DA("%1", "%1", "quad_perm:[2,3,0,1]", "0xf") DA("%4", "%4", "quad_perm:[2,3,0,1]", "0xf") DA("%5", "%5", "quad_perm:[2,3,0,1]", "0xf")
               : "+v"(a[0]), "+v"(a[1]), "+v"(a[2]), "+v"(a[3]), "+v"(b[0]), "+v"(b[1]), "+v"(b[2]), "+v"(b[3])
               : "v"(a[4]), "v"(a[5]), "v"(a[6]), "v"(a[7]), "v"(b[4]), "v"(b[5]), "v"(b[6]), "v"(b[7]));
#undef DA
}
static __device__ __forceinline__ void lds_signal(LAS unsigned char *L, int off) {
  asm volatile("s_waitcnt lgkmcnt(0)" ::: "memory");
  if ((threadIdx.x & 63) == 0) __hip_atomic_fetch_add((LAS u32 *)(L + off), 1u, __ATOMIC_RELAXED, __HIP_MEMORY_SCOPE_WORKGROUP);
}
static __device__ __forceinline__ bool lds_last(LAS unsigned char *L, int off) {
  u32 n = 0;
  if ((threadIdx.x & 63) == 0) n = __hip_atomic_fetch_add((LAS u32 *)(L + off), 1u, __ATOMIC_RELAXED, __HIP_MEMORY_SCOPE_WORKGROUP);
  return __builtin_amdgcn_readfirstlane(n) == 3;
}
static __device__ __forceinline__ void lds_wait(LAS unsigned char *L, int off, u32 n) {
#pragma unroll 1
  for (u32 s = 0; s < (SPIN_CAP << 4); ++s)
  {
    if (__hip_atomic_load((LAS u32 *)(L + off), __ATOMIC_RELAXED, __HIP_MEMORY_SCOPE_WORKGROUP) >= n) break;
    __builtin_amdgcn_s_sleep(1);
  }
  asm volatile("" ::: "memory");
}
static __device__ __forceinline__ u64 ldflag(unsigned char *ws, int off) { return __hip_atomic_load((GAS u64 *)(ws + off), __ATOMIC_RELAXED, __HIP_MEMORY_SCOPE_AGENT); }
static __device__ __forceinline__ void stflag(unsigned char *ws, int off, u64 v) { __hip_atomic_store((GAS u64 *)(ws + off), v, __ATOMIC_RELAXED, __HIP_MEMORY_SCOPE_AGENT); }
static __device__ __forceinline__ void wait_flags(unsigned char *ws, int off, u64 tag) {
#pragma unroll 1
  for (u32 s = 0; s < SPIN_CAP; ++s) {
    const u64 v = ldflag(ws, off);
    if (__all(v == tag)) break;
    __builtin_amdgcn_s_sleep(1);
  }
  asm volatile("" ::: "memory");
}
static __device__ __forceinline__ float inv_sigma(float var) {
  const float sd = rbf(__builtin_amdgcn_sqrtf(rbf(rbf(fmaxf(var, 0.f)) + 1e-5f)));
  return __builtin_amdgcn_rcpf(sd);
}
static __device__ __forceinline__ int mod7(int a) { return a >= 7 ? a - 7 : a; }

constexpr int L_KR = 81920;
constexpr int L_VR = L_KR + 9216, L_RV = L_VR + 8192;
constexpr int L_XR = 114688;
constexpr int L_Z2 = 114688;
constexpr int L_Z1 = 131072;
constexpr int L_KD = 147456;
constexpr int L_CS = 155648;
constexpr int L_XST = 156672;
constexpr int L_ZST = 157184;
constexpr int L_CNT = 157696;

#define MIX(cnt, n) do { for (int q_ = 0; q_ < (cnt); ++q_) { __builtin_amdgcn_sched_group_barrier(8, 1, 0); __builtin_amdgcn_sched_group_barrier(0x100, (n), 0); } } while (0)

#define TOFF 0
#define ADEPTH 11
#define RD 4
#define NOTOUCH
#define NA1 6
struct Tile { int i, j, rot1, rot2, rot3, r, pg, o; };

static __device__ __forceinline__ u64 epoch_load(const unsigned char *ws) {
  u64 e;
  asm volatile("s_load_dwordx2 %0, %1, 0x0" : "=s"(e) : "s"(ws) : "memory");
  return e;
}
#define EPOCH_WAIT(e) asm volatile("s_waitcnt lgkmcnt(0)" : "+s"(e)::"memory")
static __device__ __forceinline__ v4u widen(v2u x, v2u y) {
  const v2u a = __builtin_amdgcn_permlane16_swap(x.x, y.x, false, false), b = __builtin_amdgcn_permlane16_swap(x.y, y.y, false, false);
  return v4u{a.x, b.x, a.y, b.y};
}

static __device__ __forceinline__ void gemm_compute(const Params &p, LAS unsigned char *L, const Tile t, int w) {
  STAMP_DECL;
  STAMP(0);
  u64 tag = epoch_load(p.ws);
  const int i = t.i, j = t.j, rh = w >> 1, ch = w & 1;
  int l = threadIdx.x & 63, g = l >> 4, c = l & 15;
  const rsrc_t rws = mk(p.ws, WS_BYTES);

  {
    const v2u shk = *(const v2u *)(p.shk + (64 * j + 16 * w + c) * 4);
    const u32 dvr = p.dv[64 * j + 16 * w + c];
    if (threadIdx.x < 8) *(LAS u32 *)(L + L_CNT + threadIdx.x * 4) = 0;
    EPOCH_WAIT(tag);
    tag += 1;
    BARW();
    STAMP(1);
    v2u kr[5], vr[4];
    const int r0 = c >> 2;
#pragma unroll
    for (int q = 0; q < 5; ++q) kr[q] = trd(L, L_KR + (16 * g + 4 + 4 * q + r0) * 128 + ((w ^ ((2 + 2 * q + (r0 >> 1)) & 3)) << 5) + (c & 3) * 8);
#pragma unroll
    for (int q = 0; q < 4; ++q) vr[q] = trd(L, L_VR + (16 * g + 4 * q + r0) * 128 + ((w ^ ((2 * q + (r0 >> 1)) & 3)) << 5) + (c & 3) * 8);
    float kf[20], o[16];
#pragma unroll
    for (int q = 0; q < 5; ++q) { kf[4 * q] = lo16(kr[q].x); kf[4 * q + 1] = hi16(kr[q].x); kf[4 * q + 2] = lo16(kr[q].y); kf[4 * q + 3] = hi16(kr[q].y); }
    const float t0 = lo16(shk.x), t1 = hi16(shk.x), t2 = lo16(shk.y), t3 = hi16(shk.y), dd = lo16(dvr);
#pragma unroll
    for (int r = 0; r < 16; ++r) {
      const u32 vv = (r & 2) ? vr[r >> 2].y : vr[r >> 2].x;
      o[r] = __builtin_fmaf(kf[r + 4], t0, __builtin_fmaf(kf[r + 3], t1, __builtin_fmaf(kf[r + 2], t2, kf[r + 1] * t3))) * ((r & 1) ? hi16(vv) : lo16(vv));
    }
    const int cc = 16 * w + c;
    u32 rv[8];
#pragma unroll
    for (int q = 0; q < 8; ++q) rv[q] = pk(o[15 - 2 * q], o[14 - 2 * q]);
    sts16(L, L_RV + cc * 144 + (3 - g) * 32, v4u{rv[0], rv[1], rv[2], rv[3]});
    sts16(L, L_RV + cc * 144 + (3 - g) * 32 + 16, v4u{rv[4], rv[5], rv[6], rv[7]});
    SB();
    v4u ln[2];
#pragma unroll
    for (int q = 0; q < 2; ++q) ln[q] = lds16(L, L_RV + (16 * w + 8 * q + (l >> 3)) * 144 + (l & 7) * 16);
    u32 kd[8];
#pragma unroll
    for (int q = 0; q < 8; ++q) kd[q] = pk(hi16(rv[7 - q]) * dd, lo16(rv[7 - q]) * dd);
#pragma unroll
    for (int q = 0; q < 2; ++q) st16c(ln[q], rws, (64 * j + 16 * w + 8 * q + (l >> 3)) * 4096 + (31 - i) * 128 + (l & 7) * 16, WS_KVR);
    const int ko = L_KD + cc * 128 + ((g ^ ((c >> 1) & 3)) << 5);
    sts16(L, ko, v4u{kd[0], kd[1], kd[2], kd[3]});
    sts16(L, ko + 16, v4u{kd[4], kd[5], kd[6], kd[7]});
  }
  STAMP(2);

  v4u xf[2][14], wf[2][4];
  f4 acc[2][4], cs[2];
  int rb[4];
  const v4u ones = {0x3f803f80u, 0x3f803f80u, 0x3f803f80u, 0x3f803f80u};
  auto start = [&]() {
    const int rdoff = (8 * g + (c >> 2)) * 256 + ((((c >> 2) | ((g & 1) << 2)) ^ (4 * ch + 2 * rh)) << 5) + (c & 3) * 8;
#pragma unroll
    for (int n = 0; n < 4; ++n) { rb[n] = rdoff ^ (n << 5); PIN(rb[n]); acc[0][n] = acc[1][n] = f4{0, 0, 0, 0}; }
    cs[0] = cs[1] = f4{0, 0, 0, 0};
  };
  auto rdw = [&](int ks, v4u (&f)[4]) {
#pragma unroll
    for (int n = 0; n < 4; ++n) {
      if (ks == 7) { rb[n] += 57344; PIN(rb[n]); }
      f[n] = trd2(L, rb[n] + (ks % 7) * 8192, 1024);
    }
  };
  auto sums = [&](int ks) { cs[0] = mfma(ones, wf[ks & 1][0], cs[0]); cs[1] = mfma(ones, wf[ks & 1][1], cs[1]); };
  auto mma = [&](int ks) {
#pragma unroll
    for (int n = 0; n < 4; ++n)
#pragma unroll
      for (int m = 0; m < 2; ++m) acc[m][n] = mfma(xf[m][ks], wf[ks & 1][n], acc[m][n]);
  };
  auto put_cs = [&](int off) {
    const int o = L_CS + off + ((4 * ch + 2 * rh) * 16 + c) * 4;
    *(LAS float *)(L + o) = cs[0].x;
    *(LAS float *)(L + o + 64) = cs[1].x;
  };
  start();
  {
    int xo0 = L_XR + (32 * rh + c) * 128 + ((g ^ (c & 7)) << 4), xo1 = xo0 ^ 64;
    PIN(xo0); PIN(xo1);
    auto rd = [&](int ks) {
      const int so = ((ks >> 1) & 3) * 8192;
      xf[0][ks] = lds16(L, ((ks & 1) ? xo1 : xo0) + so);
      xf[1][ks] = lds16(L, ((ks & 1) ? xo1 : xo0) + so + 2048);
      SB();
      rdw(ks, wf[ks & 1]);
    };
#pragma unroll
    for (int st = 0; st < 7; ++st) {
      STAMPA(2 * st + 1);
      if (st == 0) BARW(); else { SB(); asm volatile("s_waitcnt lgkmcnt(8)\n\ts_barrier" ::: "memory"); SB(); }
      STAMPA(2 * st + 2);
      if (st == 2) {
        DRAIN();
        if (l == 0) stflag(p.ws, WS_KVF + ((4 * j + w) * 32 + i) * 8, tag);
        SB();
      }
      rd(2 * st);
      if (st > 0) { mma(2 * st - 1); sums(2 * st - 1); MIX(8, 1); }
      SB();
      rd(2 * st + 1);
      mma(2 * st);
      sums(2 * st);
      MIX(8, 1);
      SB();
    }
    sums(13);
    put_cs(0);
    BARW();
    mma(13);
  }
  STAMP(3);
  FRESH();
  const u32 tagA = (u32)tag, tagB = (u32)(tag >> 32) ^ 0x9e3779b9u;
  f4 mm[2], rr[2];
  float cv[4];
  auto act_in = [&](int off) {
#pragma unroll
    for (int m = 0; m < 2; ++m) {
      mm[m] = ldsf4(L, L_XST + (32 * rh + 8 * g + 4 * m) * 4);
      rr[m] = ldsf4(L, L_XST + 256 + (32 * rh + 8 * g + 4 * m) * 4);
    }
    const int o = L_CS + off + ((4 * ch + (2 - 2 * rh)) * 16 + c) * 4;
    cv[0] = -cs[0].x;
    cv[1] = -cs[1].x;
    cv[2] = -*(LAS float *)(L + o);
    cv[3] = -*(LAS float *)(L + o + 64);
  };
  auto act = [&](int n, f2 (&z)[4]) {
    f2 x[4], a[4];
    const f2 c2 = {cv[n], cv[n]};
    SB();
#pragma unroll
    for (int m = 0; m < 2; ++m) {
      x[2 * m] = fma2(lo2(mm[m]), c2, lo2(acc[m][n]));
      x[2 * m + 1] = fma2(hi2(mm[m]), c2, hi2(acc[m][n]));
    }
    SB();
#pragma unroll
    for (int m = 0; m < 2; ++m) { x[2 * m] *= lo2(rr[m]); x[2 * m + 1] *= hi2(rr[m]); }
    SB();
#pragma unroll
    for (int q = 0; q < 4; ++q) a[q] = x[q] * x[q];
    SB();
#pragma unroll
    for (int q = 0; q < 4; ++q) a[q] = fma2(a[q], f2{-0.1029432397f, -0.1029432397f}, f2{-2.302208198f, -2.302208198f});
    SB();
#pragma unroll
    for (int q = 0; q < 4; ++q) a[q] *= x[q];
    SB();
#pragma unroll
    for (int q = 0; q < 4; ++q) a[q] = f2{__builtin_amdgcn_exp2f(a[q].x), __builtin_amdgcn_exp2f(a[q].y)};
    SB();
#pragma unroll
    for (int q = 0; q < 4; ++q) a[q] += 1.0f;
    SB();
#pragma unroll
    for (int q = 0; q < 4; ++q) a[q] = f2{__builtin_amdgcn_rcpf(a[q].x), __builtin_amdgcn_rcpf(a[q].y)};
    SB();
#pragma unroll
    for (int q = 0; q < 4; ++q) z[q] = x[q] * a[q];
    SB();
  };
  {
    act_in(0);
    f2 s[4], u[4];
    v4u zp[4];
#pragma unroll
    for (int n = 0; n < 4; ++n) {
      f2 z[4];
      act(n, z);
#pragma unroll
      for (int q = 0; q < 4; ++q) {
        s[q] = n ? s[q] + z[q] : z[q];
        u[q] = n ? fma2(z[q], z[q], u[q]) : z[q] * z[q];
      }
      zp[n] = v4u{pk2(z[0]), pk2(z[1]), pk2(z[2]), pk2(z[3])};
    }
    float sa[8], sq[8];
#pragma unroll
    for (int q = 0; q < 4; ++q) { sa[2 * q] = s[q].x; sa[2 * q + 1] = s[q].y; sq[2 * q] = u[q].x; sq[2 * q + 1] = u[q].y; }
    rowsum8(sa, sq);
    const int row = 8 * g + (c & 1) + ((c >> 1) & 6);
    if ((c & 2) == 0)
      st16c(v4u{asu((c & 1) ? sa[1] : sa[0]), tagA, asu((c & 1) ? sq[1] : sq[0]), tagB}, rws, ((i * 2 + rh) * 16 + 2 * j + ch) * 512 + row * 16, WS_ST);
    SB();
    const int zo = L_Z2 + (rh * 8 + 4 * ch) * 1024 + l * 16;
#pragma unroll
    for (int n = 0; n < 4; ++n) sts16(L, zo + ((n & 1) << 10) + (((n >> 1) ^ rh) << 11), zp[n]);
    lds_signal(L, L_CNT + 4);
  }
  STAMP(4);
  lds_wait(L, L_CNT, 4);
  STAMP(5);
  start();
  rdw(0, wf[0]);
  SB();
#pragma unroll
  for (int ks = 0; ks < 13; ++ks) {
    rdw(ks + 1, wf[(ks + 1) & 1]);
    mma(ks);
    sums(ks);
    MIX(8, 1);
    SB();
  }
  sums(13);
  put_cs(512);
  lds_signal(L, L_CNT + 8);
  SB();
  mma(13);
  SB();
  lds_wait(L, L_CNT + 8, 4);
  STAMP(6);
  FRESH();
  {
    act_in(512);
    const int po = L_Z1 + (64 * ch + c) * 128 + (((4 * rh + g) ^ (c & 7)) << 4);
#pragma unroll
    for (int n = 0; n < 4; ++n) {
      f2 z[4];
      act(n, z);
      sts16(L, po + ((n & 1) << 11) + (((n >> 1) ^ rh) << 12), v4u{pk2(z[0]), pk2(z[1]), pk2(z[2]), pk2(z[3])});
    }
  }
  STAMP(8);
  FRESH();
  v2u xe[4];
  u32 bias[4];
  {
    const rsrc_t rx = mk(p.x, S_ * D_ * 2);
#pragma unroll
    for (int n = 0; n < 4; ++n) {
      xe[n] = __builtin_amdgcn_raw_buffer_load_b64(rx, (64 * i + 16 * w + c) * 896 + j * 128 + g * 8 + n * 32, 0, 0);
      bias[n] = p.bsp[64 * i + 16 * n + c];
    }
  }
  STAMP(9);
  f4 g2[4][2];
  {
#define G2_AH 6
#define G2_AD 2
    constexpr int AH = G2_AH, AD = G2_AD;
    v4u bf[64][2], af[AD + 1][4];
    const int bvo = w * 2048 + l * 16;
    auto ldb = [&](int ks) {
      const int b = ks >> 1, so = WS_ZB + j * 524288 + ((((b >> 3) + (t.rot2 >> 3)) & 3) * 8 + ((b + t.rot2) & 7)) * 16384 + (ks & 1) * 8192;
      bf[ks][0] = ld16c(rws, bvo, so);
      bf[ks][1] = ld16c(rws, bvo + 1024, so);
    };
    int ab[2][2];
    ab[0][0] = c * 128 + ((g ^ (c & 7)) << 4);
    ab[1][0] = ab[0][0] ^ 64;
    ab[0][1] = ab[0][0] + 65536;
    ab[1][1] = ab[1][0] + 65536;
    PIN(ab[0][0]); PIN(ab[1][0]); PIN(ab[0][1]); PIN(ab[1][1]);
    auto rda = [&](int ks) {
#pragma unroll
      for (int m = 0; m < 4; ++m) af[ks % (AD + 1)][m] = lds16(L, ab[ks & 1][(ks >> 4) & 1] + ((ks >> 1) & 7) * 8192 + m * 2048);
    };
#pragma unroll
    for (int m = 0; m < 4; ++m) g2[m][0] = g2[m][1] = f4{0, 0, 0, 0};
    BARW();
    STAMP(10);
#pragma unroll
    for (int ks = 0; ks < AH; ++ks) ldb(ks);
#pragma unroll
    for (int ks = 0; ks < AD; ++ks) rda(ks);
    SB();
    unr<64>([&](auto ksc) {
      constexpr int ks = ksc;
      if constexpr (ks + AH < 64) ldb(ks + AH);
      if constexpr (ks + AD < 64) rda(ks + AD);
#pragma unroll
      for (int b = 0; b < 2; ++b)
#pragma unroll
        for (int m = 0; m < 4; ++m) g2[m][b] = mfma(bf[ks][b], af[ks % (AD + 1)][m], g2[m][b]);
      MIX(4, 1);
      for (int q_ = 0; q_ < 2; ++q_) { __builtin_amdgcn_sched_group_barrier(8, 1, 0); __builtin_amdgcn_sched_group_barrier(0x20, 1, 0); }
      SB();
      if constexpr ((ks & 7) == 7) BAR0();
    });
  }
  STAMP(11);
  FRESH();
  {
    const int f0 = 4 * g + (c >> 2);
    const int zr = L_Z1 + (32 * w + f0) * 128 + (c & 1) * 8;
    const int sw = ((c >> 1) & 1) ^ (f0 & 7);
#pragma unroll
    for (int m = 0; m < 4; ++m) {
      const v4u z = trd2(L, zr + (((2 * m) ^ sw) << 4), 2048);
      const float b = lo16(bias[m]);
      const v4u o = {pk(lo16(z.x) * (g2[m][0].x + b), hi16(z.x) * (g2[m][0].y + b)), pk(lo16(z.y) * (g2[m][0].z + b), hi16(z.y) * (g2[m][0].w + b)),
                     pk(lo16(z.z) * (g2[m][1].x + b), hi16(z.z) * (g2[m][1].y + b)), pk(lo16(z.w) * (g2[m][1].z + b), hi16(z.w) * (g2[m][1].w + b))};
      st16c(o, rws, l * 16, WS_UB + ((i * 4 + m) * 28 + 4 * j + w) * 1024);
    }
  }
  STAMP(7);
  DRAIN();
  if (lds_last(L, L_CNT + 16)) {
    if (l == 0) stflag(p.ws, WS_UF + (i * 16 + j) * 8, tag);
    wait_flags(p.ws, WS_UF + (i * 16 + (l < 7 ? l : 0)) * 8, tag);
    lds_signal(L, L_CNT + 20);
  } else lds_wait(L, L_CNT + 20, 1);
  STAMP(12);
  FRESH();
  {
    constexpr int AH = 12;
    v4u uf[28], of[2][8];
    int uso[7];
#pragma unroll
    for (int q = 0; q < 7; ++q) uso[q] = WS_UB + (i * 4 + w) * 28672 + mod7(q + t.rot3) * 4096;
    auto ldu = [&](int ks) { uf[ks] = ld16c(rws, l * 16 + (ks & 3) * 1024, uso[ks >> 2]); };
#pragma unroll
    for (int ks = 0; ks < AH; ++ks) ldu(ks);
    f4 o[4];
    int ob[4];
    const int oro = (4 * g + (c >> 2)) * 128 + ((((c >> 3) & 1) | ((g & 1) << 1)) << 5) + (c & 3) * 8;
#pragma unroll
    for (int n = 0; n < 4; ++n) { o[n] = f4{0, 0, 0, 0}; ob[n] = oro ^ (n << 5); PIN(ob[n]); }
    auto rd = [&](int kp, v4u (&f)[8]) {
#pragma unroll
      for (int q = 0; q < 8; ++q) {
        const int n = q & 3, ks = 2 * kp + (q >> 2);
        if (ks == 14) { ob[n] += 57344; PIN(ob[n]); }
        f[q] = trd2(L, ob[n] + (ks % 14) * 4096, 2048);
      }
    };
    BAR0();
    rd(0, of[0]);
    SB();
#pragma unroll
    for (int kp = 0; kp < 14; ++kp) {
      if (2 * kp + AH < 28) { ldu(2 * kp + AH); ldu(2 * kp + AH + 1); }
      if (kp < 13) rd(kp + 1, of[(kp + 1) & 1]);
#pragma unroll
      for (int q = 0; q < 8; ++q) o[q & 3] = mfma(of[kp & 1][q], uf[2 * kp + (q >> 2)], o[q & 3]);
      __builtin_amdgcn_sched_group_barrier(0x20, 2, 0);
      MIX(8, 2);
      SB();
    }
    STAMP(13);
    BAR0();
    FRESH();
    const f2 st = {*(LAS float *)(L + L_XST + (16 * w + c) * 4), *(LAS float *)(L + L_XST + 256 + (16 * w + c) * 4)};
    const int yo = (4 * g + (c >> 2)) * 128 + ((w ^ (((c >> 3) & 1) | ((g & 1) << 1))) << 5) + (c & 3) * 8;
    v2u res[4];
#pragma unroll
    for (int n = 0; n < 4; ++n) {
      const v2u y = trd(L, L_Z2 + yo + n * 2048), kd = trd(L, L_KD + yo + n * 2048);
      const float xv[4] = {lo16(xe[n].x), hi16(xe[n].x), lo16(xe[n].y), hi16(xe[n].y)};
      const float yv[4] = {lo16(y.x) + lo16(kd.x), hi16(y.x) + hi16(kd.x), lo16(y.y) + lo16(kd.y), hi16(y.y) + hi16(kd.y)};
      float r[4];
#pragma unroll
      for (int e = 0; e < 4; ++e) {
        const float a = o[n][e];
        const float sg = a * __builtin_amdgcn_rcpf(1.0f + __builtin_amdgcn_exp2f(a * -1.442695041f));
        r[e] = __builtin_fmaf((xv[e] - st.x) * st.y * yv[e], sg, xv[e]);
      }
      res[n] = v2u{pk(r[0], r[1]), pk(r[2], r[3])};
    }
    const rsrc_t ro = mk(p.out, S_ * D_ * 2);
    const int oo = (64 * i + 16 * w + c) * 896 + j * 128 + (g & 1) * 32 + (g >> 1) * 16;
    st16c(widen(res[0], res[1]), ro, oo, 0);
    st16c(widen(res[2], res[3]), ro, oo, 64);
  }
  STAMP_OUT(0);
}

static __device__ __forceinline__ void gemm_move(const Params &p, LAS unsigned char *L, const Tile t, int m) {
  STAMP_DECL;
  STAMP(0);
  const int i = t.i, j = t.j;
  int l = threadIdx.x & 63, g = l >> 4, c = l & 15;
  const srd_t sx_ = mks(p.x, S_ * D_ * 2), swin = mks(p.win, D_ * F_ * 2), swsp = mks(p.wsp, S_ * S_ * 2);
  const int lb = m * 1024, lb2 = m * 2048;
  const int pvo = (l >> 3) * 896 + j * 128 + (((((l & 7) >> 1) ^ g) << 5) | ((l & 1) << 4));
  {
    const srd_t sk = mks(p.k, S_ * D_ * 2), sv = mks(p.v, S_ * D_ * 2);
    const int so = (64 * i + 16 * m) * 896;
    if (m == 0) dma<L_KR>(sk, pvo, i > 0 ? so - 7168 : 0x40000000, 0);
    dma<L_KR + 1024>(sk, pvo, so, lb2);
    dma<L_KR + 2048>(sk, pvo, so + 7168, lb2);
    dma<L_VR>(sv, pvo, so, lb2);
    dma<L_VR + 1024>(sv, pvo, so + 7168, lb2);
  }
  const int rsw = ((l & 7) ^ (l >> 3)) << 4;
  const int xvo = (64 * i + 32 * (m >> 1) + 4 * (m & 1) + 8 * (l >> 5) + ((l >> 3) & 3)) * 896 + rsw;
  const int wvo = (4 * m + g) * 3584 + j * 256 + ((((c >> 1) ^ (g | ((m & 2) << 1))) << 5) | ((c & 1) << 4));
  int sb[7];
#pragma unroll
  for (int s = 0; s < 7; ++s) sb[s] = mod7(s + t.rot1);
  auto item = [&](auto nc) {
    constexpr int n = nc, s = n / 6, q = n % 6;
    if constexpr (n >= 42) dma<(n - 42) * 4096>(swin, wvo, (sb[(n - 42) >> 2] * 4 + ((n - 42) & 3)) * 57344, lb);
    else if constexpr (q < 2) dma<L_XR + (s & 3) * 8192 + q * 1024>(sx_, xvo, sb[s] * 128 + q * 14336, lb2);
    else dma<(4 * s + q - 2) * 4096>(swin, wvo, (sb[s] * 4 + q - 2) * 57344 + 1792, lb);
  };
  unr<12>(item);
  u64 tag = epoch_load(p.ws);
  VM(12);
  EPOCH_WAIT(tag);
  tag += 1;
  BAR0();
  unr<6>([&](auto q) { item(std::integral_constant<int, 12 + q>{}); });
  {
    const v4u ones = {0x3f803f80u, 0x3f803f80u, 0x3f803f80u, 0x3f803f80u};
    f4 sx = {0, 0, 0, 0}, gr = {0, 0, 0, 0};
    int xo0 = L_XR + (16 * m + c) * 128 + ((g ^ (c & 7)) << 4), xo1 = xo0 ^ 64;
    PIN(xo0); PIN(xo1);
    unr<7>([&](auto sc) {
      constexpr int s = sc;
      VM(12);
      v4u xs[2];
      xs[0] = lds16(L, xo0 + (s & 3) * 8192);
      xs[1] = lds16(L, xo1 + (s & 3) * 8192);
      STAMPA(2 * s + 1);
      BAR0();
      STAMPA(2 * s + 2);
      if (s == 0) STAMP(1);
#pragma unroll
      for (int q = 0; q < 2; ++q) { sx = mfma(xs[q], ones, sx); gr = mfma(xs[q], xs[q], gr); }
      SB();
      if constexpr (s < 6) unr<6>([&](auto q) { item(std::integral_constant<int, 18 + 6 * s + q>{}); });
    });
    STAMP(2);
    const int e = c & 3;
    const float s1 = e & 2 ? (e & 1 ? sx.w : sx.z) : (e & 1 ? sx.y : sx.x);
    const float s2 = e & 2 ? (e & 1 ? gr.w : gr.z) : (e & 1 ? gr.y : gr.x);
    const float mean = fmul(s1, 1.0f / 448.0f);
    const float r = inv_sigma(ffma(-mean, mean, fmul(s2, 1.0f / 448.0f)));
    if ((c >> 2) == g) {
      const int row = 32 * (m >> 1) + 8 * g + 4 * (m & 1) + e;
      *(LAS float *)(L + L_XST + row * 4) = rbf(mean);
      *(LAS float *)(L + L_XST + 256 + row * 4) = r;
    }
    SB();
    unr<6>([&](auto q) { item(std::integral_constant<int, 54 + q>{}); });
  }
  BARW();
  STAMP(3);
  unr<10>([&](auto q) { VM(9); item(std::integral_constant<int, 60 + q>{}); });
  const rsrc_t rwsp = mk(p.wsp, S_ * S_ * 2), rws = mk(p.ws, WS_BYTES);
  v4u ar[32];
  const int avo = (64 * i + 16 * m + (l >> 3)) * 4096 + rsw;
  auto aso = [&](int n) { const int b = n >> 1; return ((((b >> 3) + (t.rot2 >> 3)) & 3) * 8 + ((b + t.rot2) & 7)) * 128 + (n & 1) * 32768; };
  auto lda = [&](int n0, int n1) {
#pragma unroll
    for (int n = n0; n < n1; ++n) { VM(ADEPTH); ar[n] = ld16(rwsp, avo, aso(32 + n)); SB(); }
  };
  lda(0, NA1);
  VM(NA1);
  lds_signal(L, L_CNT);
  STAMP(4);
  {
    const u32 tagA = (u32)tag, tagB = (u32)(tag >> 32) ^ 0x9e3779b9u;
    const int so = WS_ST + (i * 2 + (m >> 1)) * 8192 + (m & 1) * 256;
    const int vo = g * 512 + c * 16, vo3 = (g & 1) * 512 + c * 16 + 2048;
    v4u sg[4], zt[4];
    auto ldst = [&]() {
      sg[0] = ld16c(rws, vo, so); sg[1] = ld16c(rws, vo + 2048, so); sg[2] = ld16c(rws, vo, so + 4096); sg[3] = ld16c(rws, vo3, so + 4096);
    };
    lda(NA1, 32);
    STAMP(12);
    lds_wait(L, L_CNT + 4, 4);
    ldst();
    SB();
    const int zo = ((m >> 1) * 8 + (l >> 5)) * 1024 + ((m & 1) * 32 + (l & 31)) * 16;
#pragma unroll
    for (int q = 0; q < 4; ++q) zt[q] = lds16(L, L_Z2 + zo + q * 2048);
    STAMP(5);
#pragma unroll 1
    for (u32 s = 0; s < SPIN_CAP; ++s) {
      bool ok = true;
#pragma unroll
      for (int q = 0; q < 4; ++q) ok = ok && sg[q].y == tagA && sg[q].w == tagB;
      if (__all(ok)) break;
      asm volatile("" ::: "memory");
      ldst();
    }
    STAMP(13);
    float s = fadd(fadd(asf(sg[0].x), asf(sg[1].x)), fadd(asf(sg[2].x), g < 2 ? asf(sg[3].x) : 0.f));
    float u = fadd(fadd(asf(sg[0].z), asf(sg[1].z)), fadd(asf(sg[2].z), g < 2 ? asf(sg[3].z) : 0.f));
    {
      v2u a = __builtin_amdgcn_permlane16_swap(asu(s), asu(u), false, false);
      const float h = fadd(asf(a.x), asf(a.y));
      a = __builtin_amdgcn_permlane32_swap(asu(h), asu(h), false, false);
      const float k = fadd(asf(a.x), asf(a.y));
      a = __builtin_amdgcn_permlane16_swap(asu(k), asu(k), false, false);
      s = asf(a.x);
      u = asf(a.y);
    }
    const float mean = fmul(s, 1.0f / 896.0f);
    const float r = inv_sigma(ffma(-mean, mean, fmul(u, 1.0f / 896.0f)));
    if (g < 2) *(LAS float *)(L + L_ZST + g * 256 + (16 * m + c) * 4) = g ? r : -rbf(mean) * r;
    SB();
    f4 zb[2], zr[2];
#pragma unroll
    for (int q = 0; q < 2; ++q) {
      zb[q] = ldsf4(L, L_ZST + (16 * m + 8 * (g & 1) + 4 * q) * 4);
      zr[q] = ldsf4(L, L_ZST + 256 + (16 * m + 8 * (g & 1) + 4 * q) * 4);
    }
#pragma unroll
    for (int q = 0; q < 4; ++q) {
      u32 o[4];
#pragma unroll
      for (int e = 0; e < 4; ++e) {
        const u32 z = zt[q][e];
        o[e] = pk(ffma(lo16(z), zr[e >> 1][(2 * e) & 3], zb[e >> 1][(2 * e) & 3]), ffma(hi16(z), zr[e >> 1][(2 * e + 1) & 3], zb[e >> 1][(2 * e + 1) & 3]));
      }
      st16c(v4u{o[0], o[1], o[2], o[3]}, rws, (j * 64 + 2 * i) * 8192 + zo + q * 2048, WS_ZB);
      SB();
    }
    STAMP(6);
  }
  lds_wait(L, L_CNT + 8, 4);
  auto ring = [&](auto nc) { constexpr int n = nc; dma<(n >> 1) * 8192 + (n & 1) * 1024>(swsp, avo, aso(n), lb2); };
  unr<RD>(ring);
  VM(RD);
  if (lds_last(L, L_CNT + 24) && l == 0) stflag(p.ws, WS_ZF + (j * 32 + i) * 8, tag);
  SB();
  STAMP(7);
  unr<28 - RD>([&](auto nc) { VM(RD - 1); ring(std::integral_constant<int, RD + nc>{}); });
  lds_wait(L, L_CNT + 24, 4);
  unr<4>([&](auto nc) { VM(RD - 1); ring(std::integral_constant<int, 28 + nc>{}); });
  STAMP(11);
  if (m == 0) wait_flags(p.ws, WS_ZF + j * 256 + (l & 31) * 8, tag);
  DRAIN();
  BAR0();
  STAMP(8);
  FRESH();
  int awo = m * 2048 + l * 16, awh = awo + 65536;
  PIN(awo); PIN(awh);
  {
    constexpr int cum[6] = {0, 6, 12, 19, 25, 32};
#pragma unroll
    for (int k = 0; k < 5; ++k) {
      BARW();
#pragma unroll
      for (int n = cum[k]; n < cum[k + 1]; ++n) {
        sts16(L, ((n & 16) ? awh : awo) + ((n >> 1) & 7) * 8192 + (n & 1) * 1024, ar[n]);
        SB();
        __builtin_amdgcn_s_sleep(2);
        SB();
      }
    }
  }
  BARW();
  BAR0();
  STAMP(9);
  {
    const srd_t swout = mks(p.wout, 896 * D_ * 2);
    auto ldo = [&](auto nc) { constexpr int n = nc; VM(5); dma<n * 4096>(swout, pvo, (mod7((n >> 2) + t.rot3) * 16 + (n & 3) * 4 + m) * 7168, lb); };
    unr<12>(ldo);
    BAR0();
    STAMP(10);
    unr<16>([&](auto q) { ldo(std::integral_constant<int, 12 + q>{}); });
  }
  DRAIN();
  BAR0();
  wait_flags(p.ws, WS_YF + (((64 * j + 16 * m + min(g * 7, 15)) / 14) * 4 + (l & 3)) * 8, tag);
  {
    const srd_t sws = mks(p.ws, WS_BYTES);
    const int yvo = (64 * j + 16 * m + (l >> 3)) * 4096 + i * 128 + (((((l & 7) >> 1) ^ g) << 5) | ((l & 1) << 4));
    dmac<L_Z2>(sws, yvo, WS_YB, lb2);
    dmac<L_Z2 + 1024>(sws, yvo, WS_YB + 32768, lb2);
  }
  DRAIN();
  BAR0();
  STAMP_OUT(1);
}

constexpr int C_CP = 4160, C_KV = 4 * C_CP, C_SLOT = C_KV + 4608;

template <int H> static __device__ __forceinline__ void conv_half(const Params &p, LAS unsigned char *L, int v, int w, u64 tag) {
  STAMP_DECL;
  STAMP(0);
  constexpr int NF = H ? 6 : 8, NT = H ? 5 : 3;
  constexpr int tile[5] = {H ? 5 : 7, H ? 4 : 6, H ? 3 : 2, 1, 0};
  const int l = threadIdx.x & 63, g = l >> 4, c = l & 15, ch = w >> 1;
  const rsrc_t rws = mk(p.ws, WS_BYTES);
  const int hro = ch * C_SLOT + (c & 3) * C_CP + g * 16 + (c >> 2) * 8;
  const int kro = ch * C_SLOT + C_KV + (254 - 2 * c + g) * 16 - 7 * 512;
  v4u hfr[2][NF], kfr[2][NF];
  auto rd = [&](int ho, int ko, int q) {
#pragma unroll
    for (int d = 0; d < NF; ++d) {
      const v2u a = *(LAS v2u *)(L + ho + d * 512 + q * 64);
      v2u b;
      asm volatile("" ::: "memory");
      b = *(LAS v2u *)(L + ho + d * 512 + q * 64 + 8);
      asm volatile("" ::: "memory");
      hfr[q & 1][d] = v4u{a.x, a.y, b.x, b.y};
      kfr[q & 1][NF - 1 - d] = lds16(L, ko + (8 - NF + d) * 512 + q * 64);
    }
  };
  BAR0();
  STAMP(2);
  rd(hro, kro, 0);
  int set = 0;
#pragma unroll 1
  for (int rho = 0; rho < 7; ++rho) {
    const int nset = set == 4 * C_SLOT ? 0 : set + 2 * C_SLOT;
    f4 acc[NT];
#pragma unroll
    for (int n = 0; n < NT; ++n) acc[n] = f4{0, 0, 0, 0};
#pragma unroll
    for (int q = 0; q < 8; ++q) {
      if (q == 7) BARW();
      SB();
      if (q < 7) rd(hro + set, kro + set, q + 1); else rd(hro + nset, kro + nset, 0);
#pragma unroll
      for (int d = 0; d < NF; ++d)
#pragma unroll
        for (int n = NT - 1; n >= 0; --n) {
          const int P = d - (NF - 1 - tile[n]);
          if (P >= 0) acc[n] = mfma(hfr[q & 1][P], kfr[q & 1][tile[n] - P], acc[n]);
        }
      if (H) MIX(18, 1);
      else { for (int q_ = 0; q_ < 6; ++q_) { MIX(2, 1); MIX(1, 2); } }
      SB();
    }
    v2u y[NT];
#pragma unroll
    for (int n = 0; n < NT; ++n) y[n] = v2u{pk(acc[n].x, acc[n].y), pk(acc[n].z, acc[n].w)};
    const int yo = WS_YB + (14 * v + 2 * rho + ch) * 4096 + c * 32;
    st16c(widen(y[1], y[0]), rws, yo + (tile[1] + (g & 1)) * 512 + (g >> 1) * 16, 0);
    __builtin_amdgcn_raw_buffer_store_b64(y[2], rws, yo + tile[2] * 512 + g * 8, 0, 16);
    if constexpr (H) st16c(widen(y[4], y[3]), rws, yo + (g & 1) * 512 + (g >> 1) * 16, 0);
    set = nset;
    if (rho == 0) STAMP(3);
    if (rho == 1) STAMP(4);
    if (rho == 3) STAMP(5);
    if (rho == 5) STAMP(6);
  }
  STAMP(7);
  DRAIN();
  if (l == 0) stflag(p.ws, WS_YF + (v * 4 + w) * 8, tag);
  STAMP_OUT(0);
}

static __device__ __forceinline__ void conv_stage(const Params &p, LAS unsigned char *L, int v, int m) {
  STAMP_DECL;
  STAMP(0);
  const int l = threadIdx.x & 63;
  const srd_t sh = mks(p.h, D_ * S_ * 2), sws = mks(p.ws, WS_BYTES);
  const rsrc_t rws = mk(p.ws, WS_BYTES);
  const int vo = m * 1024 + l * 16;
  auto stage_h = [&](int rho, int set) {
    const int so = (14 * v + 2 * rho) * 4096, lb = set + m * 1024;
    unr<8>([&](auto qc) { constexpr int q = qc; dma<(q >> 2) * C_SLOT + (q & 3) * C_CP + 32>(sh, vo, so + (q >> 2) * 4096 + (q & 3) * 2 + 2, lb); });
  };
  auto stage_kv = [&](int rho, int set) {
    const int so = WS_KVR + (14 * v + 2 * rho) * 4096, lb = set + m * 1024;
    dmac<C_KV>(sws, vo, so, lb);
    dmac<C_SLOT + C_KV>(sws, vo, so + 4096, lb);
  };
  u64 head = *(const u64 *)(p.h + (14 * v + (l < 56 ? l >> 2 : 0)) * 2048);
  stage_h(0, 0);
  u64 tag = epoch_load(p.ws);
  stage_h(1, 2 * C_SLOT);
  stage_h(2, 4 * C_SLOT);
  head <<= 48 - 16 * (l & 3);
  auto put_head = [&](int rho, int set) {
    if (m == 0 && (l >> 3) == rho) *(LAS u64 *)(L + set + ((l >> 2) & 1) * C_SLOT + (l & 3) * C_CP + 24) = head;
  };
  {
    const v4u z = {0, 0, 0, 0};
    const int t = m * 64 + l;
    if (t < 192) sts16(L, (t >> 5) * C_SLOT + C_KV + 4096 + (t & 31) * 16, z);
    if (t < 24) { sts16(L, (t >> 2) * C_SLOT + (t & 3) * C_CP, z); *(LAS u64 *)(L + (t >> 2) * C_SLOT + (t & 3) * C_CP + 16) = 0; }
  }
  put_head(0, 0);
  put_head(1, 2 * C_SLOT);
  put_head(2, 4 * C_SLOT);
  EPOCH_WAIT(tag);
  tag += 1;
  wait_flags(p.ws, WS_KVF + (((14 * v + (l < 32 ? 0 : 13)) >> 4) * 32 + (l & 31)) * 8, tag);
  STAMP(1);
  stage_kv(0, 0);
  stage_kv(1, 2 * C_SLOT);
  stage_kv(2, 4 * C_SLOT);
  VM(4);
  BARW();
  STAMP(2);
  if (l == 0) stflag(p.ws, WS_HI + (v * 4 + m) * 8, tag);
  int set = 0;
#pragma unroll 1
  for (int rho = 0; rho < 7; ++rho) {
    DRAIN();
    BARW();
    if (rho < 4) {
      stage_h(rho + 3, set);
      stage_kv(rho + 3, set);
      put_head(rho + 3, set);
    }
    set = set == 4 * C_SLOT ? 0 : set + 2 * C_SLOT;
    if (rho == 4 && v == 0 && m == 0) {
#pragma unroll 1
      for (u32 s = 0; s < SPIN_CAP; ++s) {
        bool ok = true;
#pragma unroll
        for (int q = 0; q < 8; ++q) {
          const v4u f = ld16c(rws, l * 16 + (q & 3) * 1024, WS_KVF + (q >> 2) * 4096);
          ok = ok && f.x == (u32)tag && f.y == (u32)(tag >> 32) && f.z == (u32)tag && f.w == (u32)(tag >> 32);
        }
        if (__all(ok)) break;
        asm volatile("" ::: "memory");
      }
      if (l == 0) stflag(p.ws, WS_EPOCH, tag);
    }
  }
  STAMP_OUT(1);
}

__global__ void __launch_bounds__(512, 2) fused_kernel(Params p) {
  extern __shared__ __attribute__((aligned(16))) unsigned char smem[];
  u64 ts_; asm volatile("s_memrealtime %0" : "=s"(ts_) :: "memory"); SB();
  LAS unsigned char *L = (LAS unsigned char *)smem;
  const int b = blockIdx.x, x = b & 7, r = b >> 3, wv = __builtin_amdgcn_readfirstlane(threadIdx.x >> 6);
  if (wv >= 4) __builtin_amdgcn_s_setprio(2);
  if (r >= 28) {
    const int v = x * 4 + (r - 28);
    if (wv < 4) {
      u64 tag = epoch_load(p.ws);
      EPOCH_WAIT(tag);
      if (wv & 1) conv_half<1>(p, L, v, wv, tag + 1);
      else conv_half<0>(p, L, v, wv, tag + 1);
    } else conv_stage(p, L, v, wv - 4);
  } else {
    const int pg = x >> 1, o = x & 1;
    Tile t;
    if (r < 24) { t.i = 8 * pg + r / 3; t.j = 4 * o + r % 3; }
    else { t.i = 8 * pg + 4 * o + (r - 24); t.j = 3; }
    t.rot1 = 2 * ((t.i + t.j) & 3);
    t.rot2 = 8 * (pg + 2 * ((t.i + t.j) & 1)) + ((2 * (t.i >> 1) + 4 * (t.j >> 1)) & 7);
    t.rot3 = 2 * (t.j & 3);
    t.r = r; t.pg = pg; t.o = o;
    if (wv < 4) gemm_compute(p, L, t, wv);
    else gemm_move(p, L, t, wv - 4);
  }
  DRAIN();
  if (blockIdx.x == 0 && wv == 4) for (int q_ = 0; q_ < 100000; ++q_) { if (__builtin_amdgcn_s_memrealtime() - ts_ >= 4000) break; __builtin_amdgcn_s_sleep(1); }
}

extern "C" void kernel_launch(void *const *d_in, const int *in_sizes, int n_in, void *d_out, int out_size, void *d_ws, size_t ws_size,
                              hipStream_t stream) {
  Params p;
  p.x = (const u16 *)d_in[0];
  p.k = (const u16 *)d_in[1];
  p.v = (const u16 *)d_in[2];
  p.shk = (const u16 *)d_in[3];
  p.h = (const u16 *)d_in[4];
  p.dv = (const u16 *)d_in[5];
  p.win = (const u16 *)d_in[7];
  p.wsp = (const u16 *)d_in[8];
  p.bsp = (const u16 *)d_in[9];
  p.wout = (const u16 *)d_in[10];
  p.out = (u16 *)d_out;
  p.ws = (unsigned char *)d_ws;
  (void)hipFuncSetAttribute((const void *)fused_kernel, hipFuncAttributeMaxDynamicSharedMemorySize, LDS_BYTES);
  fused_kernel<<<256, 512, LDS_BYTES, stream>>>(p);
}
```

```cpp
#include <hip/hip_runtime.h>
#include <stdint.h>
#include <type_traits>

typedef unsigned short u16;
typedef unsigned int u32;
typedef unsigned long long u64;
typedef u32 v4u __attribute__((ext_vector_type(4)));
typedef u32 v2u __attribute__((ext_vector_type(2)));
typedef float f4 __attribute__((ext_vector_type(4)));
typedef float f2 __attribute__((ext_vector_type(2)));
typedef short s4 __attribute__((ext_vector_type(4)));
typedef __bf16 bf8 __attribute__((ext_vector_type(8)));
typedef __bf16 bf2 __attribute__((ext_vector_type(2)));
#define LAS __attribute__((address_space(3)))
#define GAS __attribute__((address_space(1)))
typedef __amdgpu_buffer_rsrc_t rsrc_t;
typedef int srd_t __attribute__((ext_vector_type(4)));

constexpr int S_ = 2048, D_ = 448, F_ = 1792;
constexpr int WS_EPOCH = 0;
#define FS 128
#define FULL 1
constexpr int WS_FL = 4096;
constexpr int F_KV = 0;
constexpr int F_HI = 896;
constexpr int F_Z = 1024;
constexpr int F_U = 1280;
constexpr int F_Y = 1536;
constexpr int WS_ST = 262144;
constexpr int WS_ZB = 1 << 20;
constexpr int WS_UB = 5 << 20;
constexpr int WS_KVR = 9 << 20;
constexpr int WS_YB = 11 << 20;
constexpr int WS_STAMP = 14 << 20;
constexpr int WS_BYTES = 16 << 20;
constexpr int LDS_BYTES = 163840;
constexpr u32 SPIN_CAP = 1u << 16;

struct Params {
  const u16 *x, *k, *v, *shk, *h, *dv, *win, *wsp, *bsp, *wout;
  u16 *out;
  unsigned char *ws;
};

#define SB() __builtin_amdgcn_sched_barrier(0)
#define BARW() do { SB(); asm volatile("s_waitcnt lgkmcnt(0)\n\ts_barrier" ::: "memory"); SB(); } while (0)
#define BAR0() do { SB(); asm volatile("s_barrier" ::: "memory"); SB(); } while (0)
#define FRESH() do { asm volatile("" : "+v"(l)); g = l >> 4; c = l & 15; } while (0)
#define DRAIN() asm volatile("s_waitcnt vmcnt(0)" ::: "memory")
#define VM(n) asm volatile("s_waitcnt vmcnt(%0)" ::"n"(n) : "memory")
#define PIN(x) asm volatile("" : "+v"(x))
#define STAMP(n) do { } while (0)
#define STAMPA(n) do { } while (0)
#define STAMP_DECL do { } while (0)
#define STAMP_OUT(slot) do { } while (0)

static __device__ __forceinline__ rsrc_t mk(const void *p, int bytes) { return __builtin_amdgcn_make_buffer_rsrc((void *)p, 0, bytes, 0x00020000); }
static __device__ __forceinline__ v4u ld16(rsrc_t r, int vo, int so) { return __builtin_amdgcn_raw_buffer_load_b128(r, vo, so, 0); }
static __device__ __forceinline__ v4u ld16c(rsrc_t r, int vo, int so) { return __builtin_amdgcn_raw_buffer_load_b128(r, vo, so, 16); }
static __device__ __forceinline__ u32 ld4(rsrc_t r, int vo, int so) { return __builtin_amdgcn_raw_buffer_load_b32(r, vo, so, 0); }
static __device__ __forceinline__ void st16c(v4u v, rsrc_t r, int vo, int so) { __builtin_amdgcn_raw_buffer_store_b128(v, r, vo + so, 0, 16); }
static __device__ __forceinline__ srd_t mks(const void *p, int bytes) { return srd_t{(int)(u64)p, (int)((u64)p >> 32) & 0xffff, bytes, 0x00020000}; }
template <int IMM> static __device__ __forceinline__ void dma(srd_t r, int vo, int so, int base) {
  asm volatile("s_add_u32 m0, %0, %4\n\ts_nop 0\n\tbuffer_load_dwordx4 %1, %2, %3 offen lds" ::"s"(base), "v"(vo), "s"(r), "s"(so), "n"(IMM) : "m0", "scc", "memory");
}
template <int IMM, int NT> static __device__ __forceinline__ void dman(srd_t r, int vo, int so, int base) {
  if constexpr (NT) asm volatile("s_add_u32 m0, %0, %4\n\ts_nop 0\n\tbuffer_load_dwordx4 %1, %2, %3 offen nt lds" ::"s"(base), "v"(vo), "s"(r), "s"(so), "n"(IMM) : "m0", "scc", "memory");
  else dma<IMM>(r, vo, so, base);
}
#define NT_KV 0
#define NT_H 0
#define NT_SP 0
#define NT_X 0
#define NT_RING 0
template <int IMM> static __device__ __forceinline__ void dmac(srd_t r, int vo, int so, int base) {
  asm volatile("s_add_u32 m0, %0, %4\n\ts_nop 0\n\tbuffer_load_dwordx4 %1, %2, %3 offen sc1 lds" ::"s"(base), "v"(vo), "s"(r), "s"(so), "n"(IMM) : "m0", "scc", "memory");
}
static __device__ __forceinline__ void touch(srd_t r, int vo, int so) {
  asm volatile("s_mov_b32 m0, %3\n\ts_nop 0\n\tbuffer_load_dword %0, %1, %2 offen lds" ::"v"(vo), "s"(r), "s"(so), "n"(LDS_BYTES - 256) : "m0", "memory");
}
static __device__ __forceinline__ float one(float r) { asm("" : "+v"(r)); return r; }
static __device__ __forceinline__ float fadd(float a, float b) { return one(a + b); }
static __device__ __forceinline__ float fmul(float a, float b) { return one(a * b); }
static __device__ __forceinline__ float ffma(float a, float b, float c) { return one(__builtin_fmaf(a, b, c)); }
template <int N, typename Fn> static __device__ __forceinline__ void unr(Fn &&f) {
  if constexpr (N > 0) { unr<N - 1>(f); f(std::integral_constant<int, N - 1>{}); }
}
static __device__ __forceinline__ f4 mfma(v4u a, v4u b, f4 c) {
  return __builtin_amdgcn_mfma_f32_16x16x32_bf16(__builtin_bit_cast(bf8, a), __builtin_bit_cast(bf8, b), c, 0, 0, 0);
}
static __device__ __forceinline__ void mma(f4 &c, v4u a, v4u b) { asm volatile("v_mfma_f32_16x16x32_bf16 %0, %1, %2, %0" : "+v"(c) : "v"(a), "v"(b)); }
static __device__ __forceinline__ void mma0(f4 &c, v4u a, v4u b) { asm volatile("v_mfma_f32_16x16x32_bf16 %0, %1, %2, 0" : "=v"(c) : "v"(a), "v"(b)); }
typedef float f16v __attribute__((ext_vector_type(16)));
static __device__ __forceinline__ void mmb(f16v &c, v4u a, v4u b) { asm volatile("v_mfma_f32_32x32x16_bf16 %0, %1, %2, %0" : "+v"(c) : "v"(a), "v"(b)); }
template <bool FIRST> static __device__ __forceinline__ void mmaf(f4 &c, v4u a, v4u b) { if constexpr (FIRST) mma0(c, a, b); else mma(c, a, b); }
#define SETTLE() do { SB(); asm volatile("s_nop 7" ::: "memory"); SB(); } while (0)
#define WAITC(vm, lg) __builtin_amdgcn_s_waitcnt(((vm) & 15) | 0x70 | ((lg) << 8) | (((vm) >> 4) << 14))
static __device__ __forceinline__ float dot2(u32 a, u32 b, float c) { return __builtin_amdgcn_fdot2_f32_bf16(__builtin_bit_cast(bf2, a), __builtin_bit_cast(bf2, b), c, false); }
static __device__ __forceinline__ float asf(u32 u) { return __builtin_bit_cast(float, u); }
static __device__ __forceinline__ u32 asu(float f) { return __builtin_bit_cast(u32, f); }
static __device__ __forceinline__ float lo16(u32 u) { return asf(u << 16); }
static __device__ __forceinline__ float hi16(u32 u) { return asf(u & 0xffff0000u); }
static __device__ __forceinline__ u32 pk(float a, float b) { f2 t = {a, b}; return __builtin_bit_cast(u32, __builtin_convertvector(t, bf2)); }
static __device__ __forceinline__ float rbf(float a) { return lo16(pk(a, a)); }
static __device__ __forceinline__ v4u lds16(LAS unsigned char *L, int off) { return *(LAS v4u *)(L + off); }
static __device__ __forceinline__ f4 ldsf4(LAS unsigned char *L, int off) { return *(LAS f4 *)(L + off); }
static __device__ __forceinline__ void sts16(LAS unsigned char *L, int off, v4u v) { *(LAS v4u *)(L + off) = v; }
static __device__ __forceinline__ v2u trd(LAS unsigned char *L, int off) {
  return __builtin_bit_cast(v2u, __builtin_amdgcn_ds_read_tr16_b64_v4i16((LAS s4 *)(L + off)));
}
static __device__ __forceinline__ v4u trd2(LAS unsigned char *L, int off, int step) {
  const v2u a = trd(L, off), b = trd(L, off + step);
  return v4u{a.x, a.y, b.x, b.y};
}
static __device__ __forceinline__ f2 fma2(f2 a, f2 b, f2 c) { return __builtin_elementwise_fma(a, b, c); }
static __device__ __forceinline__ u32 pk2(f2 t) { return __builtin_bit_cast(u32, __builtin_convertvector(t, bf2)); }
static __device__ __forceinline__ f2 lo2(f4 v) { return __builtin_shufflevector(v, v, 0, 1); }
static __device__ __forceinline__ f2 hi2(f4 v) { return __builtin_shufflevector(v, v, 2, 3); }
static __device__ __forceinline__ void rowsum8(float (&a)[8], float (&b)[8]) {
#define DA(d, s, ctl, bm) "v_add_f32_dpp " d ", " s ", " s " " ctl " row_mask:0xf bank_mask:" bm "\n\t"
  asm volatile("s_nop 1\n\t"
               DA("%0", "%0", "row_mirror", "0x3") DA("%1", "%1", "row_mirror", "0x3") DA("%2", "%2", "row_mirror", "0x3") DA("%3", "%3", "row_mirror", "0x3")
               DA("%4", "%4", "row_mirror", "0x3") DA("%5", "%5", "row_mirror", "0x3") DA("%6", "%6", "row_mirror", "0x3") DA("%7", "%7", "row_mirror", "0x3")
               DA("%0", "%8", "row_mirror", "0xc") DA("%1", "%9", "row_mirror", "0xc") DA("%2", "%10", "row_mirror", "0xc") DA("%3", "%11", "row_mirror", "0xc")
               DA("%4", "%12", "row_mirror", "0xc") DA("%5", "%13", "row_mirror", "0xc") DA("%6", "%14", "row_mirror", "0xc") DA("%7", "%15", "row_mirror", "0xc")
               DA("%0", "%0", "row_half_mirror", "0x5") DA("%1", "%1", "row_half_mirror", "0x5") DA("%4", "%4", "row_half_mirror", "0x5") DA("%5", "%5", "row_half_mirror", "0x5")
               DA("%0", "%2", "row_half_mirror", "0xa") DA("%1", "%3", "row_half_mirror", "0xa") DA("%4", "%6", "row_half_mirror", "0xa") DA("%5", "%7", "row_half_mirror", "0xa")
               DA("%0", "%0", "quad_perm:[1,0,3,2]", "0xf") DA("%1", "%1", "quad_perm:[1,0,3,2]", "0xf") DA("%4", "%4", "quad_perm:[1,0,3,2]", "0xf") DA("%5", "%5", "quad_perm:[1,0,3,2]", "0xf")
               DA("%0", "%0", "quad_perm:[2,3,0,1]", "0xf") DA("%1", "%1", "quad_perm:[2,3,0,1]", "0xf") DA("%4", "%4", "quad_perm:[2,3,0,1]", "0xf") DA("%5", "%5", "quad_perm:[2,3,0,1]", "0xf")
               : "+v"(a[0]), "+v"(a[1]), "+v"(a[2]), "+v"(a[3]), "+v"(b[0]), "+v"(b[1]), "+v"(b[2]), "+v"(b[3])
               : "v"(a[4]), "v"(a[5]), "v"(a[6]), "v"(a[7]), "v"(b[4]), "v"(b[5]), "v"(b[6]), "v"(b[7]));
#undef DA
}
static __device__ __forceinline__ void lds_signal(LAS unsigned char *L, int off) {
  asm volatile("s_waitcnt lgkmcnt(0)" ::: "memory");
  if ((threadIdx.x & 63) == 0) __hip_atomic_fetch_add((LAS u32 *)(L + off), 1u, __ATOMIC_RELAXED, __HIP_MEMORY_SCOPE_WORKGROUP);
}
static __device__ __forceinline__ bool lds_last(LAS unsigned char *L, int off) {
  u32 n = 0;
  if ((threadIdx.x & 63) == 0) n = __hip_atomic_fetch_add((LAS u32 *)(L + off), 1u, __ATOMIC_RELAXED, __HIP_MEMORY_SCOPE_WORKGROUP);
  return __builtin_amdgcn_readfirstlane(n) == 3;
}
static __device__ __forceinline__ void lds_wait(LAS unsigned char *L, int off, u32 n) {
#pragma unroll 1
  for (u32 s = 0; s < (SPIN_CAP << 4); ++s)
  {
    if (__hip_atomic_load((LAS u32 *)(L + off), __ATOMIC_RELAXED, __HIP_MEMORY_SCOPE_WORKGROUP) >= n) break;
    __builtin_amdgcn_s_sleep(1);
  }
  asm volatile("" ::: "memory");
}
static __device__ __forceinline__ u64 ldflag(unsigned char *ws, int off) { return __hip_atomic_load((GAS u64 *)(ws + off), __ATOMIC_RELAXED, __HIP_MEMORY_SCOPE_AGENT); }
static __device__ __forceinline__ void stflag(unsigned char *ws, int off, u64 v) { __hip_atomic_store((GAS u64 *)(ws + off), v, __ATOMIC_RELAXED, __HIP_MEMORY_SCOPE_AGENT); }
static __device__ __forceinline__ int fl(int n) { return WS_FL + n * FS; }
#define FSK 8
static __device__ __forceinline__ int flk(int n) { return WS_FL + n * FSK; }
static __device__ __forceinline__ void setflagk(unsigned char *ws, int n, u64 tag) {
  const int l = threadIdx.x & 63;
  if (FULL && FSK >= 16) { if (l < FSK / 16) st16c(v4u{(u32)tag, (u32)(tag >> 32), (u32)tag, (u32)(tag >> 32)}, mk(ws, WS_BYTES), flk(n) + l * 16, 0); }
  else if (l == 0) stflag(ws, flk(n), tag);
}
static __device__ __forceinline__ void setflag(unsigned char *ws, int n, u64 tag) {
  const int l = threadIdx.x & 63;
  if (FULL) { if (l < FS / 16) st16c(v4u{(u32)tag, (u32)(tag >> 32), (u32)tag, (u32)(tag >> 32)}, mk(ws, WS_BYTES), fl(n) + l * 16, 0); }
  else if (l == 0) stflag(ws, fl(n), tag);
}
static __device__ __forceinline__ void wait_flags(unsigned char *ws, int off, u64 tag) {
#pragma unroll 1
  for (u32 s = 0; s < SPIN_CAP; ++s) {
    const u64 v = ldflag(ws, off);
    if (__all(v == tag)) break;
    __builtin_amdgcn_s_sleep(1);
  }
  asm volatile("" ::: "memory");
}
static __device__ __forceinline__ void hint_flags(const unsigned char *at, u64 tag) {
  typedef u64 v8q __attribute__((ext_vector_type(8)));
#pragma unroll 1
  for (u32 s = 0; s < 4096; ++s) {
    v8q f;
    asm volatile("s_load_dwordx16 %0, %1, 0x0 glc\n\ts_waitcnt lgkmcnt(0)" : "=s"(f) : "s"(at) : "memory");
    u64 d = 0;
#pragma unroll
    for (int q = 0; q < 8; ++q) d |= f[q] ^ tag;
    if (d == 0) break;
    __builtin_amdgcn_s_sleep(1);
  }
}
static __device__ __forceinline__ u32 lds_peek(LAS unsigned char *L, int off) { return __hip_atomic_load((LAS u32 *)(L + off), __ATOMIC_RELAXED, __HIP_MEMORY_SCOPE_WORKGROUP); }
static __device__ __forceinline__ void lds_check(LAS unsigned char *L, int off, u32 n, u32 seen) {
  if (__builtin_expect(__builtin_amdgcn_readfirstlane(seen) < n, 0)) lds_wait(L, off, n);
  asm volatile("" ::: "memory");
}
static __device__ __forceinline__ float inv_sigma(float var) {
  const float sd = rbf(__builtin_amdgcn_sqrtf(rbf(rbf(fmaxf(var, 0.f)) + 1e-5f)));
  return __builtin_amdgcn_rcpf(sd);
}
static __device__ __forceinline__ int mod7(int a) { return a >= 7 ? a - 7 : a; }

constexpr int L_KR = 81920;
constexpr int L_RV = L_KR + 20480;
constexpr int L_XR = 114688;
constexpr int L_Z2 = 114688;
constexpr int L_Z1 = 131072;
constexpr int L_KD = 147456;
constexpr int L_CS = 155648;
constexpr int L_XST = 156672;
constexpr int L_ZST = 157184;
constexpr int L_CNT = 157696;
constexpr int L_BS = 157952;

#define PMS 1
#define PTW 1
#define TZ1 0
#define NISS {18, 5, 5, 5, 5, 4, 0, 0}
#define NIZ {0, 0, 0, 0, 0, 1, 5, 5}
#define NIW {0, 0, 0, 0, 0, 0, 0, 0}
#define MIXZ 30
#define LASTA 2
#define W1 7
#define W2 5
#define KV1 1
#define KSLEEP 16
#define ZP 32
#define WOW 5
#define NO1 12
constexpr int VLIM = 63;
#define RD 5
#define KVF_AT 3
#define SH_AT 6
#define TSLEEP 32
#define NS 16
#define PRE 0
#define DELTA 1
#define WOT 2
#define CLR_EARLY 1
#define CLR_SLEEP 8
#define PW 4
#define WSL 0
#define PC0 0
#define PC1 2
#define PM2 3
#define PM3 1
#define PCV 2
#define NB 6
#define NB2 4
struct Order {
  int seq[102], bar[8];
  constexpr int last(int a, int b) const { int r = 0; for (int q = 0; q < 102; ++q) if (seq[q] >= a && seq[q] < b) r = q; return r; }
};
constexpr Order make_order() {
  Order o{};
  constexpr int a[8] = NISS, z[8] = NIZ, w[8] = NIW;
  int n = 0, ia = 0, iz = 42, iw = 70;
  for (int s = 0; s < 8; ++s) {
    int ca = a[s], cz = z[s], cw = w[s];
    while (ca + cz + cw > 0) {
      for (int q = 0; q < 2 && ca > 0; ++q) { o.seq[n++] = ia++; --ca; }
      if (cz > 0) { o.seq[n++] = iz++; --cz; } else if (cw > 0) { o.seq[n++] = iw++; --cw; }
    }
    o.bar[s] = n;
  }
  while (n < 70 + NS) {
    for (int q = 0; q < MIXZ && iz < 70; ++q) o.seq[n++] = iz++;
    if (iw < 70 + NS && (iz == 70 || iw < 80)) o.seq[n++] = iw++;
  }
  while (n < 102) o.seq[n++] = 1000;
  return o;
}
constexpr Order ORD = make_order();
constexpr bool order_ok() {
  for (int q = 0; q < ORD.bar[7]; ++q) {
    int s = 0;
    while (q >= ORD.bar[s]) ++s;
    const int n = ORD.seq[q], st = n / 6;
    if (n < 42 && s > st) return false;
    if (n < 42 && n % 6 < 2 && st >= 4 && s < st - 2) return false;
    if (n < 42 && n % 6 >= 2 && st >= 5 && s < 2) return false;
    if (n >= 42 && n < 70 && s < (n - 42) / 4 + 3) return false;
  }
  return ORD.last(0, 42) < ORD.bar[6];
}
static_assert(order_ok(), "");
struct Tile { int i, j, rot1, rot2; };

static __device__ __forceinline__ u64 epoch_load(const unsigned char *ws) {
  u64 e;
  asm volatile("s_load_dwordx2 %0, %1, 0x0" : "=s"(e) : "s"(ws) : "memory");
  return e;
}
#define EPOCH_WAIT(e) asm volatile("s_waitcnt lgkmcnt(0)" : "+s"(e)::"memory")
static __device__ __forceinline__ v4u widen(v2u x, v2u y) {
  const v2u a = __builtin_amdgcn_permlane16_swap(x.x, y.x, false, false), b = __builtin_amdgcn_permlane16_swap(x.y, y.y, false, false);
  return v4u{a.x, b.x, a.y, b.y};
}

static __device__ __forceinline__ void kv_fetch(const Params &p, const Tile t, int w, v2u &shk, u32 &dvr) {
  const int i = t.i, j = t.j, l = threadIdx.x & 63;
  const srd_t sk = mks(p.k, S_ * D_ * 2), sv = mks(p.v, S_ * D_ * 2);
  const int vo = (l >> 3) * 896 + j * 128 + (((l & 7) ^ ((l >> 2) & 4)) << 4), so = (64 * i + 16 * w) * 896, lb = w * 5120;
  dman<L_KR + 1024, NT_KV>(sk, vo, so, lb);
  dman<L_KR + 2048, NT_KV>(sk, vo, so + 7168, lb);
  dman<L_KR, NT_KV>(sk, vo, so > 0 ? so - 7168 : 0x40000000, lb);
  dman<L_KR + 3072, NT_KV>(sv, vo, so, lb);
  dman<L_KR + 4096, NT_KV>(sv, vo, so + 7168, lb);
  asm volatile("global_load_dwordx2 %0, %2, %3\n\tglobal_load_ushort %1, %4, %5" : "=&v"(shk), "=&v"(dvr) : "v"((64 * j + l) * 8), "s"(p.shk), "v"((64 * j + l) * 2), "s"(p.dv) : "memory");
}

static __device__ __forceinline__ void gemm_compute(const Params &p, LAS unsigned char *L, const Tile t, int w, v2u shk, u32 dvr) {
  STAMP_DECL;
  STAMP(0);
  const int i = t.i, j = t.j, rh = w >> 1, ch = w & 1;
  int l = threadIdx.x & 63, g = l >> 4, c = l & 15;
  u64 tag = epoch_load(p.ws);
  const rsrc_t rws = mk(p.ws, WS_BYTES);
  v4u xf[2][14];
  f4 acc[2][4], cs[2];
  int rb[4];
  v4u ones = {0x3f803f80u, 0x3f803f80u, 0x3f803f80u, 0x3f803f80u};
  auto start = [&]() {
    const int rdoff = (8 * g + (c >> 2)) * 256 + ((((c >> 2) | ((g & 1) << 2)) ^ (4 * ch + 2 * rh)) << 5) + (c & 3) * 8;
#pragma unroll
    for (int n = 0; n < 4; ++n) { rb[n] = rdoff ^ (n << 5); PIN(rb[n]); }
    PIN(ones);
  };
  int xo0 = L_XR + (32 * rh + c) * 128 + ((g ^ (c & 7)) << 4), xo1 = xo0 ^ 64;
  int lno = L_RV + (16 * w + (l >> 3)) * 144 + (l & 7) * 16, lso = WS_KVR + (64 * j + 16 * w + (l >> 3)) * 4096 + (31 - i) * 128 + (l & 7) * 16;
  {
    __builtin_amdgcn_s_setprio(PC0);
    SB();
    if (threadIdx.x < 16) *(LAS u32 *)(L + L_CNT + threadIdx.x * 4) = 0;
    int ro = L_KR + w * 5120 + (c >> 2) * 128 + ((g ^ ((c >> 2) & 2)) << 5) + (c & 3) * 8;
    int wo = L_RV + l * 144 + (3 - w) * 32, ko = L_KD + l * 128 + ((w ^ ((c >> 1) & 3)) << 5);
    start();
    PIN(xo0); PIN(xo1); PIN(lno); PIN(lso); PIN(ro); PIN(wo); PIN(ko);
    f2 o[8];
#pragma unroll
    for (int q = 0; q < 8; ++q) { o[q] = f2{0, 0}; PIN(o[q]); }
    EPOCH_WAIT(tag);
    tag += 1;
    asm volatile("s_waitcnt vmcnt(0)" : "+v"(shk), "+v"(dvr)::"memory");
    STAMP(1);
    v2u kr[5], vr[4];
#pragma unroll
    for (int q = 0; q < 5; ++q) kr[q] = trd(L, ro + 512 + q * 512);
#pragma unroll
    for (int q = 0; q < 4; ++q) vr[q] = trd(L, ro + 3072 + q * 512);
    SB();
    const u32 ta = __builtin_amdgcn_alignbit(shk.y, shk.y, 16), tb = __builtin_amdgcn_alignbit(shk.x, shk.x, 16);
    const float dd = lo16(dvr);
    u32 ke[10], kq[9];
#pragma unroll
    for (int q = 0; q < 5; ++q) { ke[2 * q] = kr[q].x; ke[2 * q + 1] = kr[q].y; }
#pragma unroll
    for (int q = 0; q < 9; ++q) kq[q] = __builtin_amdgcn_alignbit(ke[q + 1], ke[q], 16);
#pragma unroll
    for (int q = 0; q < 8; ++q) o[q] = f2{dot2(kq[q], ta, o[q].x), dot2(ke[q + 1], ta, o[q].y)};
    SB();
#pragma unroll
    for (int q = 0; q < 8; ++q) o[q] = f2{dot2(kq[q + 1], tb, o[q].x), dot2(ke[q + 2], tb, o[q].y)};
    SB();
#pragma unroll
    for (int q = 0; q < 8; ++q) { const u32 vv = (q & 1) ? vr[q >> 1].y : vr[q >> 1].x; o[q] *= f2{lo16(vv), hi16(vv)}; }
    u32 rv[8];
#pragma unroll
    for (int q = 0; q < 8; ++q) asm("v_cvt_pk_bf16_f32 %0, %1, %2" : "=v"(rv[q]) : "v"(o[7 - q].y), "v"(o[7 - q].x));
    sts16(L, wo, v4u{rv[0], rv[1], rv[2], rv[3]});
    sts16(L, wo + 16, v4u{rv[4], rv[5], rv[6], rv[7]});
    SB();
#pragma unroll
    for (int q = 0; q < 8; ++q) rv[q] = pk2(o[q] * dd);
    sts16(L, ko, v4u{rv[0], rv[1], rv[2], rv[3]});
    sts16(L, ko + 16, v4u{rv[4], rv[5], rv[6], rv[7]});
  }
  STAMP(2);

  v2u wh[2][8];
  auto rd1 = [&](int ks, int q) {
    if (q < 2) xf[q][ks] = lds16(L, ((ks & 1) ? xo1 : xo0) + ((ks >> 1) & 3) * 8192 + q * 2048);
    else {
      const int n = (q - 2) >> 1;
      if (ks == 7 && !(q & 1)) { rb[n] += 57344; PIN(rb[n]); }
      wh[ks & 1][q - 2] = trd(L, rb[n] + (ks % 7) * 8192 + (q & 1) * 1024);
    }
  };
  auto wfr = [&](int ks, int n) { const v2u a = wh[ks & 1][2 * n], b = wh[ks & 1][2 * n + 1]; return v4u{a.x, a.y, b.x, b.y}; };
  auto mm1 = [&](auto ksc, int q) {
    constexpr int ks = ksc;
    if (q < 8) mmaf<ks == 0>(acc[q & 1][q >> 1], xf[q & 1][ks], wfr(ks, q >> 1));
    else mmaf<ks == 0>(cs[q - 8], ones, wfr(ks, q - 8));
  };
  auto step = [&](auto ksc, int q0) {
#pragma unroll
    for (int q = 0; q < 10; ++q) { mm1(ksc, q); SB(); if (q + q0 < 10) rd1(ksc + 1, q + q0); SB(); }
  };
  auto put_cs = [&](int off) {
    const int o = L_CS + off + ((4 * ch + 2 * rh) * 16 + c) * 4;
    *(LAS float *)(L + o) = cs[0].x;
    *(LAS float *)(L + o + 64) = cs[1].x;
  };
  auto last = [&](int off) {
    mm1(std::integral_constant<int, 13>{}, 8);
    mm1(std::integral_constant<int, 13>{}, 9);
#pragma unroll
    for (int q = 0; q < 4; ++q) mm1(std::integral_constant<int, 13>{}, q);
    SB();
    asm volatile("s_nop 3" ::: "memory");
    SB();
    put_cs(off);
    SB();
#pragma unroll
    for (int q = 4; q < 8; ++q) mm1(std::integral_constant<int, 13>{}, q);
    SB();
  };
  STAMPA(1);
  __builtin_amdgcn_s_setprio(PC1);
  BARW();
  STAMPA(2);
  {
    v4u ln[2];
    ln[0] = lds16(L, lno);
    ln[1] = lds16(L, lno + 1152);
#pragma unroll
    for (int q = 0; q < 10; ++q) rd1(0, q);
    SB();
    st16c(ln[0], rws, lso, 0);
    st16c(ln[1], rws, lso, 32768);
    SB();
  }
  unr<13>([&](auto ksc) {
    constexpr int ks = ksc;
    if constexpr (ks & 1) {
      STAMPA(ks + 2);
      SB(); asm volatile("s_waitcnt lgkmcnt(8)\n\ts_barrier" ::: "memory"); SB();
      STAMPA(ks + 3);
      if (ks == KVF_AT) {
        DRAIN();
        setflagk(p.ws, F_KV + (4 * j + w) * 32 + i, tag);
        SB();
      }
    }
    step(ksc, 0);
  });
  last(0);
  BARW();
  SETTLE();
  STAMP(3);
  FRESH();
  const u32 tagA = (u32)tag, tagB = (u32)(tag >> 32) ^ 0x9e3779b9u;
  f4 mm[2], rr[2];
  float cv[4];
  auto act_in = [&]() {
#pragma unroll
    for (int m = 0; m < 2; ++m) {
      mm[m] = ldsf4(L, L_XST + (32 * rh + 8 * g + 4 * m) * 4);
      rr[m] = ldsf4(L, L_XST + 256 + (32 * rh + 8 * g + 4 * m) * 4);
    }
    cv[0] = -cs[0].x;
    cv[1] = -cs[1].x;
  };
  auto act_in2 = [&](int off) {
    const int o = L_CS + off + ((4 * ch + (2 - 2 * rh)) * 16 + c) * 4;
    cv[2] = -*(LAS float *)(L + o);
    cv[3] = -*(LAS float *)(L + o + 64);
  };
  auto act = [&](int n, f2 (&z)[4]) {
    f2 x[4], a[4];
    const f2 c2 = {cv[n], cv[n]};
    SB();
#pragma unroll
    for (int m = 0; m < 2; ++m) {
      x[2 * m] = fma2(lo2(mm[m]), c2, lo2(acc[m][n]));
      x[2 * m + 1] = fma2(hi2(mm[m]), c2, hi2(acc[m][n]));
    }
    SB();
#pragma unroll
    for (int m = 0; m < 2; ++m) { x[2 * m] *= lo2(rr[m]); x[2 * m + 1] *= hi2(rr[m]); }
    SB();
#pragma unroll
    for (int q = 0; q < 4; ++q) a[q] = x[q] * x[q];
    SB();
#pragma unroll
    for (int q = 0; q < 4; ++q) a[q] = fma2(a[q], f2{-0.1029432397f, -0.1029432397f}, f2{-2.302208198f, -2.302208198f});
    SB();
#pragma unroll
    for (int q = 0; q < 4; ++q) a[q] *= x[q];
    SB();
#pragma unroll
    for (int q = 0; q < 4; ++q) a[q] = f2{__builtin_amdgcn_exp2f(a[q].x), __builtin_amdgcn_exp2f(a[q].y)};
    SB();
#pragma unroll
    for (int q = 0; q < 4; ++q) a[q] += 1.0f;
    SB();
#pragma unroll
    for (int q = 0; q < 4; ++q) a[q] = f2{__builtin_amdgcn_rcpf(a[q].x), __builtin_amdgcn_rcpf(a[q].y)};
    SB();
#pragma unroll
    for (int q = 0; q < 4; ++q) z[q] = x[q] * a[q];
    SB();
  };
  u32 seen;
  {
    act_in();
    act_in2(0);
    f2 s[4], u[4];
    v4u zp[4];
#pragma unroll
    for (int n = 0; n < 4; ++n) {
      f2 z[4];
      act(n, z);
#pragma unroll
      for (int q = 0; q < 4; ++q) {
        s[q] = n ? s[q] + z[q] : z[q];
        u[q] = n ? fma2(z[q], z[q], u[q]) : z[q] * z[q];
      }
      zp[n] = v4u{pk2(z[0]), pk2(z[1]), pk2(z[2]), pk2(z[3])};
    }
    seen = lds_peek(L, L_CNT);
    float sa[8], sq[8];
#pragma unroll
    for (int q = 0; q < 4; ++q) { sa[2 * q] = s[q].x; sa[2 * q + 1] = s[q].y; sq[2 * q] = u[q].x; sq[2 * q + 1] = u[q].y; }
    rowsum8(sa, sq);
    const int row = 8 * g + (c & 1) + ((c >> 1) & 6);
    if ((c & 2) == 0)
      st16c(v4u{asu((c & 1) ? sa[1] : sa[0]), tagA, asu((c & 1) ? sq[1] : sq[0]), tagB}, rws, ((i * 2 + rh) * 16 + 2 * j + ch) * 512 + row * 16, WS_ST);
    SB();
    STAMP(1);
    const int zo = L_Z2 + (rh * 8 + 4 * ch) * 1024 + l * 16;
#pragma unroll
    for (int n = 0; n < 4; ++n) sts16(L, zo + ((n & 1) << 10) + (((n >> 1) ^ rh) << 11), zp[n]);
    SB();
    if (l == 0) __hip_atomic_fetch_add((LAS u32 *)(L + L_CNT + 4), 1u, __ATOMIC_RELAXED, __HIP_MEMORY_SCOPE_WORKGROUP);
    SB();
    lds_check(L, L_CNT, 4, seen);
#pragma unroll
    for (int n = 0; n < 4; ++n) { rb[n] -= 57344; PIN(rb[n]); }
#pragma unroll
    for (int q = 2; q < 10; ++q) rd1(0, q);
    SB();
  }
  STAMP(4);
  unr<13>([&](auto ksc) {
    if constexpr (ksc == 6) seen = lds_peek(L, L_CNT + 28);
    if constexpr (ksc == 7) lds_check(L, L_CNT + 28, 4, seen);
    SB();
    step(ksc, 2);
  });
  last(512);
  lds_signal(L, L_CNT + 8);
  SETTLE();
  STAMP(6);
  FRESH();
  u32 bias[2];
  {
    act_in();
    const int po = L_Z1 + (64 * ch + c) * 128 + (((4 * rh + g) ^ (c & 7)) << 4);
#pragma unroll
    for (int n = 0; n < 4; ++n) {
      f2 z[4];
      if (n == 1) seen = lds_peek(L, L_CNT + 8);
      if (n == 2) {
        lds_check(L, L_CNT + 8, 4, seen);
        act_in2(512);
#pragma unroll
        for (int q = 0; q < 2; ++q) bias[q] = *(LAS u16 *)(L + L_BS + (32 * q + (l & 31)) * 2);
      }
      if (n == 3) seen = lds_peek(L, L_CNT + 16);
      act(n, z);
      sts16(L, po + ((n & 1) << 11) + (((n >> 1) ^ rh) << 12), v4u{pk2(z[0]), pk2(z[1]), pk2(z[2]), pk2(z[3])});
    }
  }
  STAMP(8);
  FRESH();
  f16v g2[2];
  float z1[32];
  int uo = WS_UB + (i * 112 + 4 * j + w) * 1024 + (g & 1) * 28672 + (g >> 1) * 256 + c * 16;
  {
    v4u bf[8][2], af[4][4];
    const rsrc_t rz = mk(p.ws + WS_ZB + j * 524288, 524288);
    int bvo[2], bso = t.rot2 * 16384;
    bvo[0] = w * 2048 + (g & 1) * 1024 + (g >> 1) * 256 + c * 16;
    bvo[1] = bvo[0] + 8192;
    PIN(bvo[0]); PIN(bvo[1]);
    auto ldb = [&](int ks, int b, int so) { bf[ks & 7][b] = ld16c(rz, bvo[ks & 1] + b * 512, so); };
    auto next = [&](int &so) { asm volatile("s_add_u32 %0, %0, 0x4000\n\ts_and_b32 %0, %0, 0x7ffff" : "+s"(so)::"scc"); };
#pragma unroll
    for (int m = 0; m < 2; ++m) { g2[m] = lo16(bias[m]); PIN(g2[m]); }
    int ab[4];
#pragma unroll
    for (int q = 0; q < 4; ++q) { ab[q] = ((l & 31) * 128 + (((g >> 1) ^ ((l >> 1) & 7)) << 4)) ^ (q << 5); PIN(ab[q]); }
    auto rda = [&](int ks, int q) { af[ks & 3][q] = *(LAS v4u *)((LAS unsigned char *)(u32)ab[2 * (ks & 1) + (q >> 1)] + ((ks >> 1) & 3) * 8192 + (q & 1) * 4096); };
    auto turn = [&](int q) { ab[q] = (ab[q] + 32768) & 0x1ffff; PIN(ab[q]); };
    const int f0 = 4 * (g >> 1) + (c >> 2);
    int zr = L_Z1 + (32 * w + f0) * 128 + (c & 1) * 8 + (((2 * (g & 1) + ((c >> 1) & 1)) ^ f0) << 4);
    v2u zq[8];
    PIN(zr); PIN(uo);
    if (ZP < 0 && w == 0) { wait_flags(p.ws, fl(F_Z + j * 32 + (l & 31)), tag); lds_signal(L, L_CNT + 16); }
    else lds_check(L, L_CNT + 16, 1, seen);
    STAMP(9);
#pragma unroll
    for (int ks = 0; ks < 7; ++ks) { ldb(ks, 0, bso); SB(); ldb(ks, 1, bso); SB(); if (ks & 1) next(bso); SB(); }
    lds_wait(L, L_CNT + 12, 4);
    __builtin_amdgcn_s_setprio(3);
    STAMP(10);
#pragma unroll
    for (int ks = 0; ks < 2; ++ks)
#pragma unroll
      for (int m = 0; m < 4; ++m) rda(ks, m);
    SB();
    WAITC(12, 15);
    STAMP(5);
    auto chunk = [&](auto lastc) {
      constexpr bool LAST = lastc;
      unr<8>([&](auto sc) {
        constexpr int s = sc;
        constexpr bool CHK = !LAST || s < 7;
        WAITC(63, LAST && s == 4 ? 12 : LAST && s == 7 ? 0 : 4);
        SB();
#pragma unroll
        for (int q = 0; q < 4; ++q) {
          mmb(g2[q & 1], bf[s][q >> 1], af[s & 3][q]);
          SB();
          if (!LAST || s < 6) rda(s + 2, q);
          SB();
          if (q < 2 && (!LAST || s == 0)) ldb(s + 7, q, bso);
          SB();
          if (q == 1 && !(s & 1) && (!LAST || s == 0)) next(bso);
          if (!LAST && s == 5 && q < 2) turn(q);
          if (!LAST && s == 6 && q == 0) turn(2);
          if (!LAST && s == 7 && q == 0) turn(3);
          if constexpr (CHK) if (q == 3) WAITC(LAST ? 12 - 2 * s : 12, 15);
          if (LAST && s == 3) { const int a = zr ^ ((q >> 1) << 6); zq[2 * q] = trd(L, a + (q & 1) * 2048); zq[2 * q + 1] = trd(L, a + (q & 1) * 2048 + 1024); }
          if (LAST && s >= 6) {
            const int e = (s - 6) * 4 + q;
            z1[4 * e] = lo16(zq[e].x); z1[4 * e + 1] = hi16(zq[e].x); z1[4 * e + 2] = lo16(zq[e].y); z1[4 * e + 3] = hi16(zq[e].y);
            PIN(z1[4 * e]); PIN(z1[4 * e + 1]); PIN(z1[4 * e + 2]); PIN(z1[4 * e + 3]);
          }
          SB();
        }
      });
      BAR0();
    };
#pragma unroll 1
    for (int ck = 0; ck < 7; ++ck) {
      chunk(std::false_type{});
    }
    chunk(std::true_type{});
    SB(); asm volatile("s_nop 11" ::: "memory"); SB();
  }
  STAMP(11);
#pragma unroll
  for (int m = 0; m < 4; ++m) {
    u32 o[4];
#pragma unroll
    for (int e = 0; e < 4; ++e) {
      const int r = 8 * (m & 1) + 2 * e;
      o[e] = pk2(f2{z1[8 * m + 2 * e], z1[8 * m + 2 * e + 1]} * f2{g2[m >> 1][r], g2[m >> 1][r + 1]});
    }
    st16c(v4u{o[0], o[1], o[2], o[3]}, rws, uo, (m >> 1) * 57344 + (m & 1) * 512);
  }
  FRESH();
  STAMP(7);
  v2u xe[4];
  {
    const rsrc_t rx = mk(p.x, S_ * D_ * 2);
#pragma unroll
    for (int q = 0; q < 4; ++q) xe[q] = __builtin_amdgcn_raw_buffer_load_b64(rx, (64 * i + 16 * w + c) * 896 + j * 128 + g * 8 + q * 32, 0, 0);
  }
  {
    v4u uf[28], of[3][4];
    int uso[7];
#pragma unroll
    for (int q = 0; q < 7; ++q) uso[q] = WS_UB + (i * 4 + w) * 28672 + mod7(q + j) * 4096;
    int uvo = l * 16;
    auto ldu = [&](int ks) { uf[ks] = ld16c(rws, uvo + (ks & 3) * 1024, uso[ks >> 2]); };
    f4 o[4];
    int ob[4];
    const int oro = (16 * (g >> 1) + 4 * (g & 1) + (c >> 2)) * 128 + ((((c >> 3) & 1) | ((g & 1) << 1)) << 5) + (c & 3) * 8;
#pragma unroll
    for (int n = 0; n < 4; ++n) { ob[n] = oro ^ (n << 5); PIN(ob[n]); }
    PIN(uvo);
    auto rd = [&](int ks, int n) {
      if (ks == 14) { ob[n] += 57344; PIN(ob[n]); }
      of[ks % 3][n] = trd2(L, ob[n] + (ks % 14) * 4096, 1024);
    };
    VM(4);
    if (lds_last(L, L_CNT + 20)) {
      setflag(p.ws, F_U + i * 8 + j, tag);
      wait_flags(p.ws, fl(F_U + i * 8 + (l < 7 ? l : 0)), tag);
      lds_signal(L, L_CNT + 32);
    } else lds_wait(L, L_CNT + 32, 1);
    STAMP(12);
#pragma unroll
    for (int ks = 0; ks < 16; ++ks) { ldu(ks); SB(); }
    BAR0();
#pragma unroll
    for (int q = 0; q < 8; ++q) rd(q >> 2, q & 3);
    SB();
    unr<28>([&](auto ksc) {
      constexpr int ks = ksc;
      WAITC(ks < 12 ? 15 : 27 - ks, ks < 27 ? 8 : 0);
      SB();
#pragma unroll
      for (int n = 0; n < 4; ++n) {
        mmaf<ks == 0>(o[n], of[ks % 3][n], uf[ks]);
        SB();
        if (ks + 2 < 28) rd(ks + 2, n);
        SB();
      }
      if (ks + 16 < 28) ldu(ks + 16);
      SB();
    });
    SETTLE();
    STAMP(13);
    BAR0();
    FRESH();
    const f2 st = {*(LAS float *)(L + L_XST + (16 * w + c) * 4), *(LAS float *)(L + L_XST + 256 + (16 * w + c) * 4)};
    const int yo = (4 * g + (c >> 2)) * 128 + ((w ^ (((c >> 3) & 1) | ((g & 1) << 1))) << 5) + (c & 3) * 8;
    v2u res[4];
#pragma unroll
    for (int n = 0; n < 4; ++n) {
      const v2u y = trd(L, L_Z2 + yo + n * 2048), kd = trd(L, L_KD + yo + n * 2048);
      const float xv[4] = {lo16(xe[n].x), hi16(xe[n].x), lo16(xe[n].y), hi16(xe[n].y)};
      const float yv[4] = {lo16(y.x) + lo16(kd.x), hi16(y.x) + hi16(kd.x), lo16(y.y) + lo16(kd.y), hi16(y.y) + hi16(kd.y)};
      float r[4];
#pragma unroll
      for (int e = 0; e < 4; ++e) {
        const float a = o[n][e];
        const float sg = a * __builtin_amdgcn_rcpf(1.0f + __builtin_amdgcn_exp2f(a * -1.442695041f));
        r[e] = __builtin_fmaf((xv[e] - st.x) * st.y * yv[e], sg, xv[e]);
      }
      res[n] = v2u{pk(r[0], r[1]), pk(r[2], r[3])};
    }
    const rsrc_t ro = mk(p.out, S_ * D_ * 2);
    const int oo = (64 * i + 16 * w + c) * 896 + j * 128 + (g & 1) * 32 + (g >> 1) * 16;
    st16c(widen(res[0], res[1]), ro, oo, 0);
    st16c(widen(res[2], res[3]), ro, oo, 64);
  }
  STAMP_OUT(0);
}

static __device__ __forceinline__ void gemm_move(const Params &p, LAS unsigned char *L, const Tile t, int m) {
  STAMP_DECL;
  STAMP(0);
  const int i = t.i, j = t.j;
  int l = threadIdx.x & 63, g = l >> 4, c = l & 15;
  const srd_t sx_ = mks(p.x, S_ * D_ * 2), swin = mks(p.win, D_ * F_ * 2), swsp = mks(p.wsp, S_ * S_ * 2);
  const int lb = m * 1024, lb2 = m * 2048;
  const int pvo = (l >> 3) * 896 + j * 128 + (((((l & 7) >> 1) ^ g) << 5) | ((l & 1) << 4));
  const int rsw = ((l & 7) ^ (l >> 3)) << 4;
  const int xvo = (64 * i + 32 * (m >> 1) + 4 * (m & 1) + 8 * (l >> 5) + ((l >> 3) & 3)) * 896 + rsw;
  const int wvo = (4 * m + g) * 3584 + j * 256 + ((((c >> 1) ^ (g | ((m & 2) << 1))) << 5) | ((c & 1) << 4));
  int sb[7];
#pragma unroll
  for (int s = 0; s < 7; ++s) sb[s] = mod7(s + t.rot1);
  auto item = [&](auto nc) {
    constexpr int n = nc, s = n / 6, q = n % 6;
    if constexpr (n >= 42) dma<(n - 42) * 4096>(swin, wvo, (sb[(n - 42) >> 2] * 4 + ((n - 42) & 3)) * 57344, lb);
    else if constexpr (q < 2) dman<L_XR + (s & 3) * 8192 + q * 1024, NT_X>(sx_, xvo, sb[s] * 128 + q * 14336, lb2);
    else dma<(4 * s + q - 2) * 4096>(swin, wvo, (sb[s] * 4 + q - 2) * 57344 + 1792, lb);
  };
#define IC(n) std::integral_constant<int, (n)>{}
  const rsrc_t rwsp = mk(p.wsp, S_ * S_ * 2), rws = mk(p.ws, WS_BYTES);
  v4u ar[32];
  const int avo0 = (64 * i + 16 * m + (l >> 3)) * 4096 + (((l & 7) ^ g) << 4), avo1 = avo0 ^ 64;
  auto aso = [&](int n) { return (((n >> 1) + t.rot2) & 31) * 128 + (n & 1) * 32768; };
  auto lda = [&](int n) { const int avo = (n & 1) ? avo1 : avo0; ar[n] = n < 10 ? __builtin_amdgcn_raw_buffer_load_b128(rwsp, avo, aso(32 + n), NT_SP * 2) : ld16(rwsp, avo, aso(32 + n)); SB(); };
  auto items = [&](auto n0, auto cnt) { unr<cnt>([&](auto q) { constexpr int n = ORD.seq[n0 + q]; if constexpr (n < 70) item(IC(n)); else lda(n - 70); }); };
  constexpr const int *NI = ORD.bar;
  items(IC(0), IC(NI[0]));
  u64 tag = epoch_load(p.ws);
  EPOCH_WAIT(tag);
  tag += 1;
  {
    const v4u ones = {0x3f803f80u, 0x3f803f80u, 0x3f803f80u, 0x3f803f80u};
    f4 sx = {0, 0, 0, 0}, gr = {0, 0, 0, 0};
    int xo0 = L_XR + (16 * m + c) * 128 + ((g ^ (c & 7)) << 4), xo1 = xo0 ^ 64;
    PIN(xo0); PIN(xo1);
    unr<7>([&](auto sc) {
      constexpr int s = sc;
      VM(NI[s] - 1 - ORD.last(6 * s, 6 * s + 6));
      v4u xs[2];
      xs[0] = lds16(L, xo0 + (s & 3) * 8192);
      xs[1] = lds16(L, xo1 + (s & 3) * 8192);
      STAMPA(2 * s + 1);
      BAR0();
      STAMPA(2 * s + 2);
      if (s == 0) STAMP(1);
      if constexpr (s < 6) items(IC(NI[s]), IC(NI[s + 1] - NI[s]));
      SB();
      if (PMS) __builtin_amdgcn_s_setprio(3);
#pragma unroll
      for (int q = 0; q < 2; ++q) { sx = mfma(xs[q], ones, sx); gr = mfma(xs[q], xs[q], gr); }
      SB();
      if (PMS) __builtin_amdgcn_s_setprio(1);
      SB();
      if constexpr (s == 6) items(IC(NI[6]), IC(LASTA));
    });
    STAMP(2);
    const int e = c & 3;
    const float s1 = e & 2 ? (e & 1 ? sx.w : sx.z) : (e & 1 ? sx.y : sx.x);
    const float s2 = e & 2 ? (e & 1 ? gr.w : gr.z) : (e & 1 ? gr.y : gr.x);
    const float mean = fmul(s1, 1.0f / 448.0f);
    const float r = inv_sigma(ffma(-mean, mean, fmul(s2, 1.0f / 448.0f)));
    if ((c >> 2) == g) {
      const int row = 32 * (m >> 1) + 8 * g + 4 * (m & 1) + e;
      *(LAS float *)(L + L_XST + row * 4) = rbf(mean);
      *(LAS float *)(L + L_XST + 256 + row * 4) = r;
    }
    SB();
    items(IC(NI[6] + LASTA), IC(NI[7] - NI[6] - LASTA));
  }
  BARW();
  STAMP(3);
  asm volatile("s_mov_b32 m0, %3\n\ts_nop 0\n\tbuffer_load_dword %0, %1, %2 offen lds" ::"v"(l * 4), "s"(mks(p.bsp, S_ * 2)), "s"(i * 128), "n"(L_BS) : "m0", "memory");
  unr<70 + NS - NI[7]>([&](auto q) {
    constexpr int n = NI[7] + q;
    VM(W1);
    if constexpr (n == ORD.last(42, 58) + 1 + W1) lds_signal(L, L_CNT);
    if constexpr (n == ORD.last(42, 70) + 1 + W1) lds_signal(L, L_CNT + 28);
    items(IC(n), IC(1));
  });
  static_assert(ORD.last(42, 70) + 1 + W1 < 70 + NS && NI[7] <= ORD.last(42, 58) + 1 + W1, "");
  STAMP(4);
  {
    const u32 tagA = (u32)tag, tagB = (u32)(tag >> 32) ^ 0x9e3779b9u;
    const int so = WS_ST + (i * 2 + (m >> 1)) * 8192 + (m & 1) * 256;
    const int vo = g * 512 + c * 16, vo3 = (g & 1) * 512 + c * 16 + 2048;
    v4u sg[4], zt[4];
    auto ldst = [&]() {
      sg[0] = ld16c(rws, vo, so); sg[1] = ld16c(rws, vo + 2048, so); sg[2] = ld16c(rws, vo, so + 4096); sg[3] = ld16c(rws, vo3, so + 4096);
    };
    lds_wait(L, L_CNT + 4, 4);
    __builtin_amdgcn_s_setprio(PM2);
    const int zo = ((m >> 1) * 8 + (l >> 5)) * 1024 + ((m & 1) * 32 + (l & 31)) * 16;
#pragma unroll
    for (int q = 0; q < 4; ++q) zt[q] = lds16(L, L_Z2 + zo + q * 2048);
    SB();
    VM(PW);
    ldst();
    SB();
#pragma unroll
    for (int n = 0; n < NB; ++n) lda(NS + n);
    STAMP(12);
    float zu[32];
#pragma unroll
    for (int q = 0; q < 16; ++q) { const u32 z = zt[q >> 2][q & 3]; zu[2 * q] = lo16(z); zu[2 * q + 1] = hi16(z); PIN(zu[2 * q]); PIN(zu[2 * q + 1]); }
    int zso = WS_ZB + (j * 64 + 2 * i) * 8192 + zo;
    PIN(zso);
    STAMP(5);
#pragma unroll 1
    for (u32 s = 0; s < SPIN_CAP; ++s) {
      bool ok = true;
#pragma unroll
      for (int q = 0; q < 4; ++q) ok = ok && sg[q].y == tagA && sg[q].w == tagB;
      if (__all(ok)) break;
      asm volatile("" ::: "memory");
      ldst();
    }
    STAMP(13);
    float s = fadd(fadd(asf(sg[0].x), asf(sg[1].x)), fadd(asf(sg[2].x), g < 2 ? asf(sg[3].x) : 0.f));
    float u = fadd(fadd(asf(sg[0].z), asf(sg[1].z)), fadd(asf(sg[2].z), g < 2 ? asf(sg[3].z) : 0.f));
    {
      v2u a = __builtin_amdgcn_permlane16_swap(asu(s), asu(u), false, false);
      const float h = fadd(asf(a.x), asf(a.y));
      a = __builtin_amdgcn_permlane32_swap(asu(h), asu(h), false, false);
      const float k = fadd(asf(a.x), asf(a.y));
      a = __builtin_amdgcn_permlane16_swap(asu(k), asu(k), false, false);
      s = asf(a.x);
      u = asf(a.y);
    }
    const float mean = fmul(s, 1.0f / 896.0f);
    const float r = inv_sigma(ffma(-mean, mean, fmul(u, 1.0f / 896.0f)));
    if (g < 2) *(LAS float *)(L + L_ZST + g * 256 + (16 * m + c) * 4) = g ? r : -rbf(mean) * r;
    SB();
    f4 zb[2], zr[2];
#pragma unroll
    for (int q = 0; q < 2; ++q) {
      zb[q] = ldsf4(L, L_ZST + (16 * m + 8 * (g & 1) + 4 * q) * 4);
      zr[q] = ldsf4(L, L_ZST + 256 + (16 * m + 8 * (g & 1) + 4 * q) * 4);
    }
    SB();
#pragma unroll
    for (int n = 0; n < NB2; ++n) lda(NS + NB + n);
#pragma unroll
    for (int q = 0; q < 4; ++q) {
      u32 o[4];
#pragma unroll
      for (int e = 0; e < 4; ++e) {
        o[e] = pk(ffma(zu[8 * q + 2 * e], zr[e >> 1][(2 * e) & 3], zb[e >> 1][(2 * e) & 3]), ffma(zu[8 * q + 2 * e + 1], zr[e >> 1][(2 * e + 1) & 3], zb[e >> 1][(2 * e + 1) & 3]));
      }
      st16c(v4u{o[0], o[1], o[2], o[3]}, rws, zso, q * 2048);
      SB();
    }
    STAMP(6);
  }
  __builtin_amdgcn_s_setprio(PM3);
  constexpr int NR = 32 - NS - NB - NB2;
  auto zflag = [&]() { if (lds_last(L, L_CNT + 24)) setflag(p.ws, F_Z + j * 32 + i, tag); SB(); STAMP(7); };
#pragma unroll
  for (int n = 0; n < NR; ++n) { if (n == W2) { VM(W2); zflag(); } if (n > W2) VM(W2); lda(NS + NB + NB2 + n); }
  if (NR >= 4 && NR <= W2) { VM(NR); zflag(); }
  lds_wait(L, L_CNT + 8, 4);
  STAMP(8);
  auto ring = [&](auto nc) { constexpr int n = nc; dman<(n >> 1) * 8192 + (n & 1) * 1024, NT_RING>(swsp, (n & 1) ? avo1 : avo0, aso(n), lb2); };
  unr<RD>(ring);
  if (NR < 4) { VM(RD + NR); zflag(); }
  unr<32 - RD>([&](auto qc) {
    constexpr int n = RD + qc;
    VM(RD - 1);
    if constexpr (n == 9 + RD) lds_signal(L, L_CNT + 12);
    if constexpr (n == 28) lds_wait(L, L_CNT + 24, 4);
    if constexpr (n == ZP) if (m == 0) { wait_flags(p.ws, fl(F_Z + j * 32 + (l & 31)), tag); lds_signal(L, L_CNT + 16); }
    ring(IC(n));
  });
  if (ZP >= 32 && m == 0) { wait_flags(p.ws, fl(F_Z + j * 32 + (l & 31)), tag); lds_signal(L, L_CNT + 16); }
  STAMP(11);
  FRESH();
  __builtin_amdgcn_s_setprio(0);
  int awo = m * 2048 + l * 16, awh = awo + 65536;
  PIN(awo); PIN(awh);
  {
    constexpr int cum[6] = {0, 6, 12, 19, 25, 32};
#pragma unroll
    for (int k = 0; k < 5; ++k) {
      if (k == 0) DRAIN();
      BARW();
#pragma unroll
      for (int n = cum[k]; n < cum[k + 1]; ++n) {
        sts16(L, ((n & 16) ? awh : awo) + ((n >> 1) & 7) * 8192 + (n & 1) * 1024, ar[n]);
        SB();
        __builtin_amdgcn_s_sleep(WSL);
        SB();
      }
    }
  }
  BARW();
  BAR0();
  STAMP(9);
  {
    const srd_t swout = mks(p.wout, 896 * D_ * 2);
    auto ldo = [&](auto nc) { constexpr int n = nc; VM(WOW); dma<n * 4096>(swout, pvo, (mod7((n >> 2) + j) * 16 + (n & 3) * 4 + m) * 7168, lb); };
    const srd_t sws = mks(p.ws, WS_BYTES);
    const int yfo = fl(F_Y + ((64 * j + 16 * m + min(g * 7, 15)) / 14) * 4 + (l & 3));
    const int yvo = (64 * j + 16 * m + (l >> 3)) * 4096 + i * 128 + (((((l & 7) >> 1) ^ g) << 5) | ((l & 1) << 4));
    auto ldy = [&]() { dmac<L_Z2>(sws, yvo, WS_YB, lb2); dmac<L_Z2 + 1024>(sws, yvo, WS_YB + 32768, lb2); };
    unr<NO1>(ldo);
    const bool early = __all(ldflag(p.ws, yfo) == tag);
    BAR0();
    STAMP(10);
    unr<28 - NO1>([&](auto q) { ldo(std::integral_constant<int, NO1 + q>{}); });
    if (early) { ldy(); VM(2); } else DRAIN();
    BAR0();
    if (!early) { wait_flags(p.ws, yfo, tag); ldy(); }
  }
  DRAIN();
  BAR0();
  STAMP_OUT(1);
}

constexpr int C_CP = 4160, C_KV = 4 * C_CP, C_SLOT = C_KV + 4608;

constexpr int C_EX = 131072;

template <int H> static __device__ __forceinline__ void conv_compute(const Params &p, LAS unsigned char *L, int v, int w) {
  STAMP_DECL;
  STAMP(0);
  __builtin_amdgcn_s_setprio(PCV);
  u64 tag = epoch_load(p.ws);
  constexpr int ord[8] = {H ? 0 : 2, H ? 1 : 3, H ? 6 : 4, H ? 7 : 5, H ? 2 : 0, H ? 3 : 1, H ? 4 : 6, H ? 5 : 7};
  const int l = threadIdx.x & 63, g = l >> 4, c = l & 15, ch = w >> 1;
  const rsrc_t rws = mk(p.ws, WS_BYTES);
  {
    const int t = threadIdx.x;
    const srd_t sws = mks(p.ws, WS_BYTES);
    touch(sws, t < 128 ? WS_ST + v * 16384 + t * 128 : WS_FL + (v * 52 + min(t & 63, 51)) * 128, 0);
  }
  const int hro = ch * C_SLOT + (c & 3) * C_CP + g * 16 + (c >> 2) * 8 + H * 64;
  const int kro = ch * C_SLOT + C_KV + (254 - 2 * c + g) * 16 - 7 * 512 + H * 64;
  v2u hh[2][16];
  v4u kf[2][8];
  auto rd = [&](int ho, int ko, int q, int n) {
    const int d = n / 3;
    if (n % 3 == 2) kf[q & 1][7 - d] = lds16(L, ko + d * 512 + q * 128);
    else hh[q & 1][2 * d + n % 3] = *(LAS v2u *)(L + ho + d * 512 + q * 128 + (n % 3) * 8);
  };
  f4 acc[8], in[4];
#pragma unroll
  for (int n = 0; n < 8; ++n) acc[n] = f4{0, 0, 0, 0};
  int xw = C_EX + w * 4096 + l * 16, xr = xw ^ 4096;
  int yo = WS_YB + (14 * v + ch) * 4096 + c * 32 + (g & 1) * 512 + (g >> 1) * 16 + 0x40000000;
  PIN(xw); PIN(xr);
  v2u y[4];
  auto fin = [&](int k) {
    if (k < 2) { in[2 * k] = ldsf4(L, xr + k * 2048); in[2 * k + 1] = ldsf4(L, xr + k * 2048 + 1024); }
    else if (k < 10) { const int n = (k - 2) >> 1, e = (k & 1) * 2; in[n][e] = fadd(in[n][e], acc[ord[4 + n]][e]); in[n][e + 1] = fadd(in[n][e + 1], acc[ord[4 + n]][e + 1]); }
    else if (k < 14) { const int n = k - 10; y[n] = v2u{pk(in[n].x, in[n].y), pk(in[n].z, in[n].w)}; PIN(y[n]); }
    else if (k < 16) st16c(widen(y[2 * k - 28], y[2 * k - 27]), rws, yo, ord[2 * k - 24] * 512);
  };
  EPOCH_WAIT(tag);
  tag += 1;
  if (KV1) BAR0();
  BAR0();
  STAMP(2);
#pragma unroll
  for (int n = 0; n < 24; ++n) { rd(hro, kro, 0, n); SB(); }
  WAITC(63, 0);
  int set = 0;
#pragma unroll 1
  for (int rho = 0; rho < 7; ++rho) {
    const int nset = set == 4 * C_SLOT ? 0 : set + 2 * C_SLOT;
    int ho = hro + set, ko = kro + set;
    unr<4>([&](auto qc) {
      constexpr int q = qc;
      if (q == 3) { ho = hro + nset - 512; ko = kro + nset - 512; PIN(ho); PIN(ko); }
      if (q > 0) WAITC(63, 0);
      if (q == 3) BAR0();
      SB();
      int k = 0;
#pragma unroll
      for (int hf = 0; hf < 2; ++hf)
#pragma unroll
        for (int P = 0; P < 8; ++P)
#pragma unroll
          for (int n = 0; n < 4; ++n) {
            const int tl = ord[4 * hf + n];
            if (P > tl) continue;
            const v2u a = hh[q & 1][2 * P], b = hh[q & 1][2 * P + 1];
            if (q == 0 && P == 0) mma0(acc[tl], v4u{a.x, a.y, b.x, b.y}, kf[q & 1][tl - P]);
            else mma(acc[tl], v4u{a.x, a.y, b.x, b.y}, kf[q & 1][tl - P]);
            SB();
            if (q == 0) {
              if (k < 2) fin(k);
              else if (k < 9) { rd(ho, ko, 1, 2 * k - 4); SB(); rd(ho, ko, 1, 2 * k - 3); }
              else if (k < 17) fin(k - 7);
              else if (k < 22) { rd(ho, ko, 1, 2 * k - 20); SB(); rd(ho, ko, 1, 2 * k - 19); }
              else if (k < 26) fin(k - 12);
            } else if (k < 24) rd(ho, ko, q + 1, k);
            else if (q == 2 && (k == 27 || k == 30)) fin(k / 3 + 5);
            else if (q == 3 && k < 28) sts16(L, xw + (k - 24) * 1024, __builtin_bit_cast(v4u, acc[ord[k - 24]]));
            SB();
            ++k;
          }
    });
    yo += rho ? 8192 : -0x40000000;
    set = nset;
    WAITC(63, 0);
    BAR0();
    if (rho == 0) STAMP(3);
    if (rho == 1) STAMP(4);
    if (rho == 3) STAMP(5);
    if (rho == 5) STAMP(6);
  }
  SETTLE();
#pragma unroll
  for (int k = 0; k < 16; ++k) fin(k);
  STAMP(7);
  DRAIN();
  setflag(p.ws, F_Y + v * 4 + w, tag);
  STAMP_OUT(0);
}

static __device__ __forceinline__ void conv_stage(const Params &p, LAS unsigned char *L, int v, int m) {
  STAMP_DECL;
  STAMP(0);
  const int l = threadIdx.x & 63;
  const srd_t sh = mks(p.h, D_ * S_ * 2), sws = mks(p.ws, WS_BYTES), swout = mks(p.wout, 896 * D_ * 2);
  const rsrc_t rws = mk(p.ws, WS_BYTES);
  const int vo = m * 1024 + l * 16;
  auto stage_h = [&](int rho, int set) {
    const int so = (14 * v + 2 * rho) * 4096, lb = set + m * 1024;
    unr<8>([&](auto qc) { constexpr int q = qc; dman<(q >> 2) * C_SLOT + (q & 3) * C_CP + 32, NT_H>(sh, vo, so + (q >> 2) * 4096 + (q & 3) * 2 + 2, lb); });
  };
  auto stage_kv = [&](int rho, int set) {
    const int so = WS_KVR + (14 * v + 2 * rho) * 4096, lb = set + m * 1024;
    dmac<C_KV>(sws, vo, so, lb);
    dmac<C_SLOT + C_KV>(sws, vo, so + 4096, lb);
  };
  u64 head;
  asm volatile("global_load_dwordx2 %0, %1, %2" : "=&v"(head) : "v"((14 * v + (l < 56 ? l >> 2 : 0)) * 4096), "s"(p.h) : "memory");
  stage_h(0, 0);
  u64 tag = epoch_load(p.ws);
  stage_h(1, 2 * C_SLOT);
  asm volatile("s_waitcnt vmcnt(16)" : "+v"(head)::"memory");
  head <<= 48 - 16 * (l & 3);
  auto put_head = [&](int rho, int set) {
    if (m == 0 && (l >> 3) == rho) *(LAS u64 *)(L + set + ((l >> 2) & 1) * C_SLOT + (l & 3) * C_CP + 24) = head;
  };
  {
    const v4u z = {0, 0, 0, 0};
    const int t = m * 64 + l;
    if (t < 192) sts16(L, (t >> 5) * C_SLOT + C_KV + 4096 + (t & 31) * 16, z);
    if (t < 24) { sts16(L, (t >> 2) * C_SLOT + (t & 3) * C_CP, z); *(LAS u64 *)(L + (t >> 2) * C_SLOT + (t & 3) * C_CP + 16) = 0; }
  }
  put_head(0, 0);
  put_head(1, 2 * C_SLOT);
  put_head(2, 4 * C_SLOT);
  EPOCH_WAIT(tag);
  tag += 1;
  if (m == 0) {
    __builtin_amdgcn_s_sleep(KSLEEP);
    wait_flags(p.ws, flk(F_KV + ((14 * v + (l < 32 ? 0 : 13)) >> 4) * 32 + (l & 31)), tag);
  }
  BAR0();
  STAMP(1);
  stage_kv(0, 0);
  stage_kv(1, 2 * C_SLOT);
  VM(2);
  BARW();
  stage_h(2, 4 * C_SLOT);
  stage_kv(2, 4 * C_SLOT);
  STAMP(2);
  setflagk(p.ws, F_HI + v * 4 + m, tag);
  int set = 0;
#pragma unroll 1
  for (int rho = 0; rho < 7; ++rho) {
    if (rho == 2) STAMP(3);
    if (rho == 3) STAMP(8);
    if (rho == 0) VM(11); else if (rho < 5) VM(WOT ? 12 : 10); else DRAIN();
    if (rho == 2) STAMP(4);
    if (rho == 3) STAMP(9);
    BARW();
    if (rho == 2) STAMP(5);
    if (rho == 3) STAMP(10);
    BAR0();
    if (rho == 2) STAMP(6);
    if (rho == 3) STAMP(11);
    if (rho < 4) {
      const int n = ((v & 3) * 4 + rho) * 392 + m * 64 + l;
      if (WOT == 1) {
      touch(swout, n * 128, 0);
      touch(swout, min(n + 256, 6271) * 128, 0);
      }
      stage_h(rho + 3, set);
      if (rho == 2) STAMP(7);
      if (rho == 3) STAMP(12);
      stage_kv(rho + 3, set);
      put_head(rho + 3, set);
      if (WOT == 2) {
      touch(swout, n * 128, 0);
      touch(swout, min(n + 256, 6271) * 128, 0);
      }
    }
    set = set == 4 * C_SLOT ? 0 : set + 2 * C_SLOT;
    if (rho == 4 && v == 0 && m == 0) {
#pragma unroll 1
      for (u32 s = 0; s < SPIN_CAP; ++s) {
        u64 f[16], d = 0;
#pragma unroll
        for (int q = 0; q < 16; ++q) f[q] = ldflag(p.ws, flk(q * 64 + l));
#pragma unroll
        for (int q = 0; q < 16; ++q) d |= f[q] ^ tag;
        if (__all(d == 0)) break;
        asm volatile("" ::: "memory");
      }
      if (l == 0) stflag(p.ws, WS_EPOCH, tag);
    }
  }
  STAMP_OUT(1);
}

#define CODE_LINES 390
#define CPFW 4
__global__ void __launch_bounds__(512, 2) fused_kernel(Params p) {
  extern __shared__ __attribute__((aligned(16))) unsigned char smem[];
  LAS unsigned char *L = (LAS unsigned char *)smem;
  const int b = blockIdx.x, x = b & 7, r = b >> 3, wv = __builtin_amdgcn_readfirstlane(threadIdx.x >> 6);
  {
    u64 pc;
    const u32 line = min((u32)(r * 16 + (threadIdx.x & 15)), (u32)CODE_LINES);
    asm volatile("s_cmp_lg_u32 %2, %4\n\ts_cbranch_scc1 1f\n\ts_getpc_b64 %0\n\ts_mov_b32 m0, %3\n\ts_nop 0\n\tglobal_load_lds_dword %1, %0\n1:\n\t"
                 "s_setprio 3\n\ts_cmp_lt_u32 %2, 4\n\ts_cbranch_scc1 2f\n\ts_setprio 1\n2:"
                 : "=&s"(pc) : "v"(line * 128), "s"(wv), "n"(LDS_BYTES - 256), "n"(CPFW) : "m0", "scc", "memory");
  }
  if (r >= 28) {
    const int v = x * 4 + (r - 28);
    if (wv < 4) { if (wv & 1) conv_compute<1>(p, L, v, wv); else conv_compute<0>(p, L, v, wv); }
    else conv_stage(p, L, v, wv - 4);
  } else {
    Tile t;
    t.i = 4 * x + r / 7;
    t.j = r % 7;
    t.rot1 = mod7(2 * (t.i & 3) + t.j);
    t.rot2 = (4 * x + 4 + (32 * t.j) / 7 + DELTA * (t.i & 3)) & 31;
    v2u shk = {0, 0};
    u32 dvr = 0;
    if (wv < 4) kv_fetch(p, t, wv, shk, dvr);
    SB();
    asm volatile("" ::: "memory");
    if (wv < 4) gemm_compute(p, L, t, wv, shk, dvr);
    else gemm_move(p, L, t, wv - 4);
  }
}

extern "C" void kernel_launch(void *const *d_in, const int *in_sizes, int n_in, void *d_out, int out_size, void *d_ws, size_t ws_size,
                              hipStream_t stream) {
  Params p;
  p.x = (const u16 *)d_in[0];
  p.k = (const u16 *)d_in[1];
  p.v = (const u16 *)d_in[2];
  p.shk = (const u16 *)d_in[3];
  p.h = (const u16 *)d_in[4];
  p.dv = (const u16 *)d_in[5];
  p.win = (const u16 *)d_in[7];
  p.wsp = (const u16 *)d_in[8];
  p.bsp = (const u16 *)d_in[9];
  p.wout = (const u16 *)d_in[10];
  p.out = (u16 *)d_out;
  p.ws = (unsigned char *)d_ws;
  (void)hipFuncSetAttribute((const void *)fused_kernel, hipFuncAttributeMaxDynamicSharedMemorySize, LDS_BYTES);
  fused_kernel<<<256, 512, LDS_BYTES, stream>>>(p);
}
```

```cpp
#include <hip/hip_runtime.h>
#include <stdint.h>
#include <type_traits>

typedef unsigned short u16;
typedef unsigned int u32;
typedef unsigned long long u64;
typedef u32 v4u __attribute__((ext_vector_type(4)));
typedef u32 v2u __attribute__((ext_vector_type(2)));
typedef float f4 __attribute__((ext_vector_type(4)));
typedef float f2 __attribute__((ext_vector_type(2)));
typedef short s4 __attribute__((ext_vector_type(4)));
typedef __bf16 bf8 __attribute__((ext_vector_type(8)));
typedef __bf16 bf2 __attribute__((ext_vector_type(2)));
#define LAS __attribute__((address_space(3)))
#define GAS __attribute__((address_space(1)))
typedef __amdgpu_buffer_rsrc_t rsrc_t;
typedef int srd_t __attribute__((ext_vector_type(4)));

constexpr int S_ = 2048, D_ = 448, F_ = 1792;
constexpr int WS_EPOCH = 0;
#define FS 128
#define FULL 1
constexpr int WS_FL = 4096;
constexpr int F_KV = 0;
constexpr int F_HI = 896;
constexpr int F_Z = 1024;
constexpr int F_U = 1280;
constexpr int F_Y = 1536;
constexpr int WS_ST = 262144;
constexpr int WS_ZB = 1 << 20;
constexpr int WS_UB = 5 << 20;
constexpr int WS_KVR = 9 << 20;
constexpr int WS_YB = 11 << 20;
constexpr int WS_STAMP = 14 << 20;
constexpr int WS_BYTES = 16 << 20;
constexpr int LDS_BYTES = 163840;
constexpr u32 SPIN_CAP = 1u << 16;

struct Params {
  const u16 *x, *k, *v, *shk, *h, *dv, *win, *wsp, *bsp, *wout;
  u16 *out;
  unsigned char *ws;
};

#define SB() __builtin_amdgcn_sched_barrier(0)
#define BARW() do { SB(); asm volatile("s_waitcnt lgkmcnt(0)\n\ts_barrier" ::: "memory"); SB(); } while (0)
#define BAR0() do { SB(); asm volatile("s_barrier" ::: "memory"); SB(); } while (0)
#define FRESH() do { asm volatile("" : "+v"(l)); g = l >> 4; c = l & 15; } while (0)
#define DRAIN() asm volatile("s_waitcnt vmcnt(0)" ::: "memory")
#define VM(n) asm volatile("s_waitcnt vmcnt(%0)" ::"n"(n) : "memory")
#define PIN(x) asm volatile("" : "+v"(x))
#define STAMP(n) SB()
#define STAMPA(n) do { } while (0)
#define STAMP_DECL do { } while (0)
#define STAMP_OUT(slot) do { } while (0)

static __device__ __forceinline__ rsrc_t mk(const void *p, int bytes) { return __builtin_amdgcn_make_buffer_rsrc((void *)p, 0, bytes, 0x00020000); }
static __device__ __forceinline__ v4u ld16(rsrc_t r, int vo, int so) { return __builtin_amdgcn_raw_buffer_load_b128(r, vo, so, 0); }
static __device__ __forceinline__ v4u ld16c(rsrc_t r, int vo, int so) { return __builtin_amdgcn_raw_buffer_load_b128(r, vo, so, 16); }
static __device__ __forceinline__ u32 ld4(rsrc_t r, int vo, int so) { return __builtin_amdgcn_raw_buffer_load_b32(r, vo, so, 0); }
static __device__ __forceinline__ void st16c(v4u v, rsrc_t r, int vo, int so) { __builtin_amdgcn_raw_buffer_store_b128(v, r, vo + so, 0, 16); }
static __device__ __forceinline__ srd_t mks(const void *p, int bytes) { return srd_t{(int)(u64)p, (int)((u64)p >> 32) & 0xffff, bytes, 0x00020000}; }
template <int IMM> static __device__ __forceinline__ void dma(srd_t r, int vo, int so, int base) {
  asm volatile("s_add_u32 m0, %0, %4\n\ts_nop 0\n\tbuffer_load_dwordx4 %1, %2, %3 offen lds" ::"s"(base), "v"(vo), "s"(r), "s"(so), "n"(IMM) : "m0", "scc", "memory");
}
template <int IMM, int NT> static __device__ __forceinline__ void dman(srd_t r, int vo, int so, int base) {
  if constexpr (NT) asm volatile("s_add_u32 m0, %0, %4\n\ts_nop 0\n\tbuffer_load_dwordx4 %1, %2, %3 offen nt lds" ::"s"(base), "v"(vo), "s"(r), "s"(so), "n"(IMM) : "m0", "scc", "memory");
  else dma<IMM>(r, vo, so, base);
}
#define NT_KV 0
#define NT_H 0
#define NT_SP 0
#define NT_X 0
#define NT_RING 0
template <int IMM> static __device__ __forceinline__ void dmac(srd_t r, int vo, int so, int base) {
  asm volatile("s_add_u32 m0, %0, %4\n\ts_nop 0\n\tbuffer_load_dwordx4 %1, %2, %3 offen sc1 lds" ::"s"(base), "v"(vo), "s"(r), "s"(so), "n"(IMM) : "m0", "scc", "memory");
}
static __device__ __forceinline__ void touch(srd_t r, int vo, int so) {
  asm volatile("s_mov_b32 m0, %3\n\ts_nop 0\n\tbuffer_load_dword %0, %1, %2 offen lds" ::"v"(vo), "s"(r), "s"(so), "n"(LDS_BYTES - 256) : "m0", "memory");
}
static __device__ __forceinline__ float one(float r) { asm("" : "+v"(r)); return r; }
static __device__ __forceinline__ float fadd(float a, float b) { return one(a + b); }
static __device__ __forceinline__ float fmul(float a, float b) { return one(a * b); }
static __device__ __forceinline__ float ffma(float a, float b, float c) { return one(__builtin_fmaf(a, b, c)); }
template <int N, typename Fn> static __device__ __forceinline__ void unr(Fn &&f) {
  if constexpr (N > 0) { unr<N - 1>(f); f(std::integral_constant<int, N - 1>{}); }
}
static __device__ __forceinline__ f4 mfma(v4u a, v4u b, f4 c) {
  return __builtin_amdgcn_mfma_f32_16x16x32_bf16(__builtin_bit_cast(bf8, a), __builtin_bit_cast(bf8, b), c, 0, 0, 0);
}
static __device__ __forceinline__ void mma(f4 &c, v4u a, v4u b) { asm volatile("v_mfma_f32_16x16x32_bf16 %0, %1, %2, %0" : "+v"(c) : "v"(a), "v"(b)); }
static __device__ __forceinline__ void mma0(f4 &c, v4u a, v4u b) { asm volatile("v_mfma_f32_16x16x32_bf16 %0, %1, %2, 0" : "=v"(c) : "v"(a), "v"(b)); }
typedef float f16v __attribute__((ext_vector_type(16)));
static __device__ __forceinline__ void mmb(f16v &c, v4u a, v4u b) { asm volatile("v_mfma_f32_32x32x16_bf16 %0, %1, %2, %0" : "+v"(c) : "v"(a), "v"(b)); }
template <bool FIRST> static __device__ __forceinline__ void mmaf(f4 &c, v4u a, v4u b) { if constexpr (FIRST) mma0(c, a, b); else mma(c, a, b); }
#define SETTLE() do { SB(); asm volatile("s_nop 7" ::: "memory"); SB(); } while (0)
#define WAITC(vm, lg) __builtin_amdgcn_s_waitcnt(((vm) & 15) | 0x70 | ((lg) << 8) | (((vm) >> 4) << 14))
static __device__ __forceinline__ float dot2(u32 a, u32 b, float c) { return __builtin_amdgcn_fdot2_f32_bf16(__builtin_bit_cast(bf2, a), __builtin_bit_cast(bf2, b), c, false); }
static __device__ __forceinline__ float asf(u32 u) { return __builtin_bit_cast(float, u); }
static __device__ __forceinline__ u32 asu(float f) { return __builtin_bit_cast(u32, f); }
static __device__ __forceinline__ float lo16(u32 u) { return asf(u << 16); }
static __device__ __forceinline__ float hi16(u32 u) { return asf(u & 0xffff0000u); }
static __device__ __forceinline__ u32 pk(float a, float b) { f2 t = {a, b}; return __builtin_bit_cast(u32, __builtin_convertvector(t, bf2)); }
static __device__ __forceinline__ float rbf(float a) { return lo16(pk(a, a)); }
static __device__ __forceinline__ v4u lds16(LAS unsigned char *L, int off) { return *(LAS v4u *)(L + off); }
static __device__ __forceinline__ f4 ldsf4(LAS unsigned char *L, int off) { return *(LAS f4 *)(L + off); }
static __device__ __forceinline__ void sts16(LAS unsigned char *L, int off, v4u v) { *(LAS v4u *)(L + off) = v; }
static __device__ __forceinline__ v2u trd(LAS unsigned char *L, int off) {
  return __builtin_bit_cast(v2u, __builtin_amdgcn_ds_read_tr16_b64_v4i16((LAS s4 *)(L + off)));
}
static __device__ __forceinline__ v4u trd2(LAS unsigned char *L, int off, int step) {
  const v2u a = trd(L, off), b = trd(L, off + step);
  return v4u{a.x, a.y, b.x, b.y};
}
static __device__ __forceinline__ f2 fma2(f2 a, f2 b, f2 c) { return __builtin_elementwise_fma(a, b, c); }
static __device__ __forceinline__ u32 pk2(f2 t) { return __builtin_bit_cast(u32, __builtin_convertvector(t, bf2)); }
static __device__ __forceinline__ f2 lo2(f4 v) { return __builtin_shufflevector(v, v, 0, 1); }
static __device__ __forceinline__ f2 hi2(f4 v) { return __builtin_shufflevector(v, v, 2, 3); }
static __device__ __forceinline__ void rowsum8(float (&a)[8], float (&b)[8]) {
#define DA(d, s, ctl, bm) "v_add_f32_dpp " d ", " s ", " s " " ctl " row_mask:0xf bank_mask:" bm "\n\t"
  asm volatile("s_nop 1\n\t"
               DA("%0", "%0", "row_mirror", "0x3") DA("%1", "%1", "row_mirror", "0x3") DA("%2", "%2", "row_mirror", "0x3") DA("%3", "%3", "row_mirror", "0x3")
               DA("%4", "%4", "row_mirror", "0x3") DA("%5", "%5", "row_mirror", "0x3") DA("%6", "%6", "row_mirror", "0x3") DA("%7", "%7", "row_mirror", "0x3")
               DA("%0", "%8", "row_mirror", "0xc") DA("%1", "%9", "row_mirror", "0xc") DA("%2", "%10", "row_mirror", "0xc") DA("%3", "%11", "row_mirror", "0xc")
               DA("%4", "%12", "row_mirror", "0xc") DA("%5", "%13", "row_mirror", "0xc") DA("%6", "%14", "row_mirror", "0xc") DA("%7", "%15", "row_mirror", "0xc")
               DA("%0", "%0", "row_half_mirror", "0x5") DA("%1", "%1", "row_half_mirror", "0x5") DA("%4", "%4", "row_half_mirror", "0x5") DA("%5", "%5", "row_half_mirror", "0x5")
               DA("%0", "%2", "row_half_mirror", "0xa") DA("%1", "%3", "row_half_mirror", "0xa") DA("%4", "%6", "row_half_mirror", "0xa") DA("%5", "%7", "row_half_mirror", "0xa")
               DA("%0", "%0", "quad_perm:[1,0,3,2]", "0xf") DA("%1", "%1", "quad_perm:[1,0,3,2]", "0xf") DA("%4", "%4", "quad_perm:[1,0,3,2]", "0xf") DA("%5", "%5", "quad_perm:[1,0,3,2]", "0xf")
               DA("%0", "%0", "quad_perm:[2,3,0,1]", "0xf") DA("%1", "%1", "quad_perm:[2,3,0,1]", "0xf") DA("%4", "%4", "quad_perm:[2,3,0,1]", "0xf") DA("%5", "%5", "quad_perm:[2,3,0,1]", "0xf")
               : "+v"(a[0]), "+v"(a[1]), "+v"(a[2]), "+v"(a[3]), "+v"(b[0]), "+v"(b[1]), "+v"(b[2]), "+v"(b[3])
               : "v"(a[4]), "v"(a[5]), "v"(a[6]), "v"(a[7]), "v"(b[4]), "v"(b[5]), "v"(b[6]), "v"(b[7]));
#undef DA
}
static __device__ __forceinline__ void lds_signal(LAS unsigned char *L, int off) {
  asm volatile("s_waitcnt lgkmcnt(0)" ::: "memory");
  if ((threadIdx.x & 63) == 0) __hip_atomic_fetch_add((LAS u32 *)(L + off), 1u, __ATOMIC_RELAXED, __HIP_MEMORY_SCOPE_WORKGROUP);
}
static __device__ __forceinline__ bool lds_last(LAS unsigned char *L, int off) {
  u32 n = 0;
  if ((threadIdx.x & 63) == 0) n = __hip_atomic_fetch_add((LAS u32 *)(L + off), 1u, __ATOMIC_RELAXED, __HIP_MEMORY_SCOPE_WORKGROUP);
  return __builtin_amdgcn_readfirstlane(n) == 3;
}
static __device__ __forceinline__ void lds_wait(LAS unsigned char *L, int off, u32 n) {
#pragma unroll 1
  for (u32 s = 0; s < (SPIN_CAP << 4); ++s)
  {
    if (__hip_atomic_load((LAS u32 *)(L + off), __ATOMIC_RELAXED, __HIP_MEMORY_SCOPE_WORKGROUP) >= n) break;
    __builtin_amdgcn_s_sleep(1);
  }
  asm volatile("" ::: "memory");
}
static __device__ __forceinline__ u64 ldflag(unsigned char *ws, int off) { return __hip_atomic_load((GAS u64 *)(ws + off), __ATOMIC_RELAXED, __HIP_MEMORY_SCOPE_AGENT); }
static __device__ __forceinline__ void stflag(unsigned char *ws, int off, u64 v) { __hip_atomic_store((GAS u64 *)(ws + off), v, __ATOMIC_RELAXED, __HIP_MEMORY_SCOPE_AGENT); }
static __device__ __forceinline__ int fl(int n) { return WS_FL + n * FS; }
#define FSK 8
static __device__ __forceinline__ int flk(int n) { return WS_FL + n * FSK; }
static __device__ __forceinline__ void setflagk(unsigned char *ws, int n, u64 tag) {
  const int l = threadIdx.x & 63;
  if (FULL && FSK >= 16) { if (l < FSK / 16) st16c(v4u{(u32)tag, (u32)(tag >> 32), (u32)tag, (u32)(tag >> 32)}, mk(ws, WS_BYTES), flk(n) + l * 16, 0); }
  else if (l == 0) stflag(ws, flk(n), tag);
}
static __device__ __forceinline__ void setflag(unsigned char *ws, int n, u64 tag) {
  const int l = threadIdx.x & 63;
  if (FULL) { if (l < FS / 16) st16c(v4u{(u32)tag, (u32)(tag >> 32), (u32)tag, (u32)(tag >> 32)}, mk(ws, WS_BYTES), fl(n) + l * 16, 0); }
  else if (l == 0) stflag(ws, fl(n), tag);
}
static __device__ __forceinline__ void wait_flags(unsigned char *ws, int off, u64 tag) {
#pragma unroll 1
  for (u32 s = 0; s < SPIN_CAP; ++s) {
    const u64 v = ldflag(ws, off);
    if (__all(v == tag)) break;
    __builtin_amdgcn_s_sleep(1);
  }
  asm volatile("" ::: "memory");
}
static __device__ __forceinline__ void hint_flags(const unsigned char *at, u64 tag) {
  typedef u64 v8q __attribute__((ext_vector_type(8)));
#pragma unroll 1
  for (u32 s = 0; s < 4096; ++s) {
    v8q f;
    asm volatile("s_load_dwordx16 %0, %1, 0x0 glc\n\ts_waitcnt lgkmcnt(0)" : "=s"(f) : "s"(at) : "memory");
    u64 d = 0;
#pragma unroll
    for (int q = 0; q < 8; ++q) d |= f[q] ^ tag;
    if (d == 0) break;
    __builtin_amdgcn_s_sleep(1);
  }
}
static __device__ __forceinline__ u32 lds_peek(LAS unsigned char *L, int off) { return __hip_atomic_load((LAS u32 *)(L + off), __ATOMIC_RELAXED, __HIP_MEMORY_SCOPE_WORKGROUP); }
static __device__ __forceinline__ void lds_check(LAS unsigned char *L, int off, u32 n, u32 seen) {
  if (__builtin_expect(__builtin_amdgcn_readfirstlane(seen) < n, 0)) lds_wait(L, off, n);
  asm volatile("" ::: "memory");
}
static __device__ __forceinline__ float inv_sigma(float var) {
  const float sd = rbf(__builtin_amdgcn_sqrtf(rbf(rbf(fmaxf(var, 0.f)) + 1e-5f)));
  return __builtin_amdgcn_rcpf(sd);
}
static __device__ __forceinline__ int mod7(int a) { return a >= 7 ? a - 7 : a; }

constexpr int L_KR = 81920;
constexpr int L_RV = L_KR + 20480;
constexpr int L_XR = 114688;
constexpr int L_Z2 = 114688;
constexpr int L_Z1 = 131072;
constexpr int L_KD = 147456;
constexpr int L_CS = 155648;
constexpr int L_XST = 156672;
constexpr int L_ZST = 157184;
constexpr int L_CNT = 157696;
constexpr int L_BS = 157952;

#define PMS 1
#define PTW 1
#define TSP 0
#define TZ1 0
#define NISS {18, 5, 5, 5, 5, 4, 0, 0}
#define NIZ {0, 0, 0, 0, 0, 1, 5, 5}
#define NIW {0, 0, 0, 0, 0, 0, 0, 0}
#define MIXZ 30
#define LASTA 2
#define W1 7
#define W2 12
#define KV1 1
#define KSLEEP 16
#define ZP -100
#define WOW 12
#define WSLP 4
#define AD 2
#define RREG 1
#define OP 8
#define UW 14
#define WOR 2
#define WOK 1
#define YLATE 1
#define WSO(n) ((mod7(((n) >> 2) + j) * 16 + ((n) & 3) * 4 + m) * 7168)
#define SIGE 6
#define TLC 0
#define TLG 0
#define NR1 12
#define X0 26
#define X1 32
#define X2 38
#define ZSL 16
#define ZSL2 2
#define NO1 12
constexpr int VLIM = 63;
#define RD 5
#define KVF_AT 3
#define SH_AT 6
#define TSLEEP 32
#define NS 10
#define PRE 0
#define DELTA 1
#define WOT 2
#define CLR_EARLY 1
#define CLR_SLEEP 8
#define PW 63
#define WSL 0
#define PC0 0
#define PC1 2
#define PM2 3
#define PM3 1
#define PCV 2
#define NB 6
#define NB2 4
struct Order {
  int seq[102], bar[8];
  constexpr int last(int a, int b) const { int r = 0; for (int q = 0; q < 102; ++q) if (seq[q] >= a && seq[q] < b) r = q; return r; }
};
constexpr Order make_order() {
  Order o{};
  constexpr int a[8] = NISS, z[8] = NIZ, w[8] = NIW;
  int n = 0, ia = 0, iz = 42, iw = 70;
  for (int s = 0; s < 8; ++s) {
    int ca = a[s], cz = z[s], cw = w[s];
    while (ca + cz + cw > 0) {
      for (int q = 0; q < 2 && ca > 0; ++q) { o.seq[n++] = ia++; --ca; }
      if (cz > 0) { o.seq[n++] = iz++; --cz; } else if (cw > 0) { o.seq[n++] = iw++; --cw; }
    }
    o.bar[s] = n;
  }
  while (n < 70 + NS) {
    for (int q = 0; q < MIXZ && iz < 70; ++q) o.seq[n++] = iz++;
    if (iw < 70 + NS && (iz == 70 || iw < 80)) o.seq[n++] = iw++;
  }
  while (n < 102) o.seq[n++] = 1000;
  return o;
}
constexpr Order ORD = make_order();
constexpr bool order_ok() {
  for (int q = 0; q < ORD.bar[7]; ++q) {
    int s = 0;
    while (q >= ORD.bar[s]) ++s;
    const int n = ORD.seq[q], st = n / 6;
    if (n < 42 && s > st) return false;
    if (n < 42 && n % 6 < 2 && st >= 4 && s < st - 2) return false;
    if (n < 42 && n % 6 >= 2 && st >= 5 && s < 2) return false;
    if (n >= 42 && n < 70 && s < (n - 42) / 4 + 3) return false;
  }
  return ORD.last(0, 42) < ORD.bar[6];
}
static_assert(order_ok(), "");
static_assert(WOR + 4 / WOK >= 4, "");
struct Tile { int i, j, rot1, rot2; };

static __device__ __forceinline__ u64 epoch_load(const unsigned char *ws) {
  u64 e;
  asm volatile("s_load_dwordx2 %0, %1, 0x0" : "=s"(e) : "s"(ws) : "memory");
  return e;
}
#define EPOCH_WAIT(e) asm volatile("s_waitcnt lgkmcnt(0)" : "+s"(e)::"memory")
static __device__ __forceinline__ v4u widen(v2u x, v2u y) {
  const v2u a = __builtin_amdgcn_permlane16_swap(x.x, y.x, false, false), b = __builtin_amdgcn_permlane16_swap(x.y, y.y, false, false);
  return v4u{a.x, b.x, a.y, b.y};
}

static __device__ __forceinline__ void kv_fetch(const Params &p, const Tile t, int w, v2u &shk, u32 &dvr) {
  const int i = t.i, j = t.j, l = threadIdx.x & 63;
  const srd_t sk = mks(p.k, S_ * D_ * 2), sv = mks(p.v, S_ * D_ * 2);
  const int vo = (l >> 3) * 896 + j * 128 + (((l & 7) ^ ((l >> 2) & 4)) << 4), so = (64 * i + 16 * w) * 896, lb = w * 5120;
  dman<L_KR + 1024, NT_KV>(sk, vo, so, lb);
  dman<L_KR + 2048, NT_KV>(sk, vo, so + 7168, lb);
  dman<L_KR, NT_KV>(sk, vo, so > 0 ? so - 7168 : 0x40000000, lb);
  dman<L_KR + 3072, NT_KV>(sv, vo, so, lb);
  dman<L_KR + 4096, NT_KV>(sv, vo, so + 7168, lb);
  asm volatile("global_load_dwordx2 %0, %2, %3\n\tglobal_load_ushort %1, %4, %5" : "=&v"(shk), "=&v"(dvr) : "v"((64 * j + l) * 8), "s"(p.shk), "v"((64 * j + l) * 2), "s"(p.dv) : "memory");
}

static __device__ __forceinline__ void gemm_compute(const Params &p, LAS unsigned char *L, const Tile t, int w, v2u shk, u32 dvr) {
  STAMP_DECL;
  STAMP(0);
  const int i = t.i, j = t.j, rh = w >> 1, ch = w & 1;
  int l = threadIdx.x & 63, g = l >> 4, c = l & 15;
  u64 tag = epoch_load(p.ws);
  const rsrc_t rws = mk(p.ws, WS_BYTES);
  v4u xf[2][14];
  f4 acc[2][4], cs[2];
  int rb[4];
  v4u ones = {0x3f803f80u, 0x3f803f80u, 0x3f803f80u, 0x3f803f80u};
  auto start = [&]() {
    const int rdoff = (8 * g + (c >> 2)) * 256 + ((((c >> 2) | ((g & 1) << 2)) ^ (4 * ch + 2 * rh)) << 5) + (c & 3) * 8;
#pragma unroll
    for (int n = 0; n < 4; ++n) { rb[n] = rdoff ^ (n << 5); PIN(rb[n]); }
    PIN(ones);
  };
  int xo0 = L_XR + (32 * rh + c) * 128 + ((g ^ (c & 7)) << 4), xo1 = xo0 ^ 64;
  int lno = L_RV + (16 * w + (l >> 3)) * 144 + (l & 7) * 16, lso = WS_KVR + (64 * j + 16 * w + (l >> 3)) * 4096 + (31 - i) * 128 + (l & 7) * 16;
  {
    __builtin_amdgcn_s_setprio(PC0);
    SB();
    if (threadIdx.x < 16) *(LAS u32 *)(L + L_CNT + threadIdx.x * 4) = 0;
    int ro = L_KR + w * 5120 + (c >> 2) * 128 + ((g ^ ((c >> 2) & 2)) << 5) + (c & 3) * 8;
    int wo = L_RV + l * 144 + (3 - w) * 32, ko = L_KD + l * 128 + ((w ^ ((c >> 1) & 3)) << 5);
    start();
    PIN(xo0); PIN(xo1); PIN(lno); PIN(lso); PIN(ro); PIN(wo); PIN(ko);
    f2 o[8];
#pragma unroll
    for (int q = 0; q < 8; ++q) { o[q] = f2{0, 0}; PIN(o[q]); }
    EPOCH_WAIT(tag);
    tag += 1;
    asm volatile("s_waitcnt vmcnt(0)" : "+v"(shk), "+v"(dvr)::"memory");
    STAMP(1);
    v2u kr[5], vr[4];
#pragma unroll
    for (int q = 0; q < 5; ++q) kr[q] = trd(L, ro + 512 + q * 512);
#pragma unroll
    for (int q = 0; q < 4; ++q) vr[q] = trd(L, ro + 3072 + q * 512);
    SB();
    const u32 ta = __builtin_amdgcn_alignbit(shk.y, shk.y, 16), tb = __builtin_amdgcn_alignbit(shk.x, shk.x, 16);
    const float dd = lo16(dvr);
    u32 ke[10], kq[9];
#pragma unroll
    for (int q = 0; q < 5; ++q) { ke[2 * q] = kr[q].x; ke[2 * q + 1] = kr[q].y; }
#pragma unroll
    for (int q = 0; q < 9; ++q) kq[q] = __builtin_amdgcn_alignbit(ke[q + 1], ke[q], 16);
#pragma unroll
    for (int q = 0; q < 8; ++q) o[q] = f2{dot2(kq[q], ta, o[q].x), dot2(ke[q + 1], ta, o[q].y)};
    SB();
#pragma unroll
    for (int q = 0; q < 8; ++q) o[q] = f2{dot2(kq[q + 1], tb, o[q].x), dot2(ke[q + 2], tb, o[q].y)};
    SB();
#pragma unroll
    for (int q = 0; q < 8; ++q) { const u32 vv = (q & 1) ? vr[q >> 1].y : vr[q >> 1].x; o[q] *= f2{lo16(vv), hi16(vv)}; }
    u32 rv[8];
#pragma unroll
    for (int q = 0; q < 8; ++q) asm("v_cvt_pk_bf16_f32 %0, %1, %2" : "=v"(rv[q]) : "v"(o[7 - q].y), "v"(o[7 - q].x));
    sts16(L, wo, v4u{rv[0], rv[1], rv[2], rv[3]});
    sts16(L, wo + 16, v4u{rv[4], rv[5], rv[6], rv[7]});
    SB();
#pragma unroll
    for (int q = 0; q < 8; ++q) rv[q] = pk2(o[q] * dd);
    sts16(L, ko, v4u{rv[0], rv[1], rv[2], rv[3]});
    sts16(L, ko + 16, v4u{rv[4], rv[5], rv[6], rv[7]});
  }
  STAMP(2);

  v2u wh[2][8];
  auto rd1 = [&](int ks, int q) {
    if (q < 2) xf[q][ks] = lds16(L, ((ks & 1) ? xo1 : xo0) + ((ks >> 1) & 3) * 8192 + q * 2048);
    else {
      const int n = (q - 2) >> 1;
      if (ks == 7 && !(q & 1)) { rb[n] += 57344; PIN(rb[n]); }
      wh[ks & 1][q - 2] = trd(L, rb[n] + (ks % 7) * 8192 + (q & 1) * 1024);
    }
  };
  auto wfr = [&](int ks, int n) { const v2u a = wh[ks & 1][2 * n], b = wh[ks & 1][2 * n + 1]; return v4u{a.x, a.y, b.x, b.y}; };
  auto mm1 = [&](auto ksc, int q) {
    constexpr int ks = ksc;
    if (q < 8) mmaf<ks == 0>(acc[q & 1][q >> 1], xf[q & 1][ks], wfr(ks, q >> 1));
    else mmaf<ks == 0>(cs[q - 8], ones, wfr(ks, q - 8));
  };
  auto step = [&](auto ksc, int q0) {
#pragma unroll
    for (int q = 0; q < 10; ++q) { mm1(ksc, q); SB(); if (q + q0 < 10) rd1(ksc + 1, q + q0); SB(); }
  };
  auto put_cs = [&](int off) {
    const int o = L_CS + off + ((4 * ch + 2 * rh) * 16 + c) * 4;
    *(LAS float *)(L + o) = cs[0].x;
    *(LAS float *)(L + o + 64) = cs[1].x;
  };
  auto last = [&](int off) {
    mm1(std::integral_constant<int, 13>{}, 8);
    mm1(std::integral_constant<int, 13>{}, 9);
#pragma unroll
    for (int q = 0; q < 4; ++q) mm1(std::integral_constant<int, 13>{}, q);
    SB();
    asm volatile("s_nop 3" ::: "memory");
    SB();
    put_cs(off);
    SB();
#pragma unroll
    for (int q = 4; q < 8; ++q) mm1(std::integral_constant<int, 13>{}, q);
    SB();
  };
  STAMPA(1);
  __builtin_amdgcn_s_setprio(PC1);
  BARW();
  STAMPA(2);
  {
    v4u ln[2];
    ln[0] = lds16(L, lno);
    ln[1] = lds16(L, lno + 1152);
#pragma unroll
    for (int q = 0; q < 10; ++q) rd1(0, q);
    SB();
    st16c(ln[0], rws, lso, 0);
    st16c(ln[1], rws, lso, 32768);
    SB();
  }
  unr<13>([&](auto ksc) {
    constexpr int ks = ksc;
    if constexpr (ks & 1) {
      STAMPA(ks + 2);
      SB(); asm volatile("s_waitcnt lgkmcnt(8)\n\ts_barrier" ::: "memory"); SB();
      STAMPA(ks + 3);
      if (ks == KVF_AT) {
        DRAIN();
        setflagk(p.ws, F_KV + (4 * j + w) * 32 + i, tag);
        SB();
      }
    }
    step(ksc, 0);
  });
  last(0);
  BARW();
  SETTLE();
  STAMP(3);
  FRESH();
  const u32 tagA = (u32)tag, tagB = (u32)(tag >> 32) ^ 0x9e3779b9u;
  f4 mm[2], rr[2];
  float cv[4];
  auto act_in = [&]() {
#pragma unroll
    for (int m = 0; m < 2; ++m) {
      mm[m] = ldsf4(L, L_XST + (32 * rh + 8 * g + 4 * m) * 4);
      rr[m] = ldsf4(L, L_XST + 256 + (32 * rh + 8 * g + 4 * m) * 4);
    }
    cv[0] = -cs[0].x;
    cv[1] = -cs[1].x;
  };
  auto act_in2 = [&](int off) {
    const int o = L_CS + off + ((4 * ch + (2 - 2 * rh)) * 16 + c) * 4;
    cv[2] = -*(LAS float *)(L + o);
    cv[3] = -*(LAS float *)(L + o + 64);
  };
  auto act = [&](int n, f2 (&z)[4]) {
    f2 x[4], a[4];
    const f2 c2 = {cv[n], cv[n]};
    SB();
#pragma unroll
    for (int m = 0; m < 2; ++m) {
      x[2 * m] = fma2(lo2(mm[m]), c2, lo2(acc[m][n]));
      x[2 * m + 1] = fma2(hi2(mm[m]), c2, hi2(acc[m][n]));
    }
    SB();
#pragma unroll
    for (int m = 0; m < 2; ++m) { x[2 * m] *= lo2(rr[m]); x[2 * m + 1] *= hi2(rr[m]); }
    SB();
#pragma unroll
    for (int q = 0; q < 4; ++q) a[q] = x[q] * x[q];
    SB();
#pragma unroll
    for (int q = 0; q < 4; ++q) a[q] = fma2(a[q], f2{-0.1029432397f, -0.1029432397f}, f2{-2.302208198f, -2.302208198f});
    SB();
#pragma unroll
    for (int q = 0; q < 4; ++q) a[q] *= x[q];
    SB();
#pragma unroll
    for (int q = 0; q < 4; ++q) a[q] = f2{__builtin_amdgcn_exp2f(a[q].x), __builtin_amdgcn_exp2f(a[q].y)};
    SB();
#pragma unroll
    for (int q = 0; q < 4; ++q) a[q] += 1.0f;
    SB();
#pragma unroll
    for (int q = 0; q < 4; ++q) a[q] = f2{__builtin_amdgcn_rcpf(a[q].x), __builtin_amdgcn_rcpf(a[q].y)};
    SB();
#pragma unroll
    for (int q = 0; q < 4; ++q) z[q] = x[q] * a[q];
    SB();
  };
  u32 seen;
  {
    act_in();
    act_in2(0);
    f2 s[4], u[4];
    v4u zp[4];
#pragma unroll
    for (int n = 0; n < 4; ++n) {
      f2 z[4];
      act(n, z);
#pragma unroll
      for (int q = 0; q < 4; ++q) {
        s[q] = n ? s[q] + z[q] : z[q];
        u[q] = n ? fma2(z[q], z[q], u[q]) : z[q] * z[q];
      }
      zp[n] = v4u{pk2(z[0]), pk2(z[1]), pk2(z[2]), pk2(z[3])};
    }
    seen = lds_peek(L, L_CNT);
    float sa[8], sq[8];
#pragma unroll
    for (int q = 0; q < 4; ++q) { sa[2 * q] = s[q].x; sa[2 * q + 1] = s[q].y; sq[2 * q] = u[q].x; sq[2 * q + 1] = u[q].y; }
    rowsum8(sa, sq);
    const int row = 8 * g + (c & 1) + ((c >> 1) & 6);
    if ((c & 2) == 0)
      st16c(v4u{asu((c & 1) ? sa[1] : sa[0]), tagA, asu((c & 1) ? sq[1] : sq[0]), tagB}, rws, ((i * 2 + rh) * 16 + 2 * j + ch) * 512 + row * 16, WS_ST);
    SB();
    STAMP(1);
    const int zo = L_Z2 + (rh * 8 + 4 * ch) * 1024 + l * 16;
#pragma unroll
    for (int n = 0; n < 4; ++n) sts16(L, zo + ((n & 1) << 10) + (((n >> 1) ^ rh) << 11), zp[n]);
    SB();
    if (l == 0) __hip_atomic_fetch_add((LAS u32 *)(L + L_CNT + 4), 1u, __ATOMIC_RELAXED, __HIP_MEMORY_SCOPE_WORKGROUP);
    SB();
    lds_check(L, L_CNT, 4, seen);
#pragma unroll
    for (int n = 0; n < 4; ++n) { rb[n] -= 57344; PIN(rb[n]); }
#pragma unroll
    for (int q = 2; q < 10; ++q) rd1(0, q);
    SB();
  }
  STAMP(4);
  unr<13>([&](auto ksc) {
    if constexpr (ksc == 6) seen = lds_peek(L, L_CNT + 28);
    if constexpr (ksc == 7) lds_check(L, L_CNT + 28, 4, seen);
    SB();
    step(ksc, 2);
  });
  last(512);
  lds_signal(L, L_CNT + 8);
  SETTLE();
  STAMP(6);
  FRESH();
  u32 bias[4];
  {
    act_in();
    const int po = L_Z1 + (64 * ch + c) * 128 + (((4 * rh + g) ^ (c & 7)) << 4);
#pragma unroll
    for (int n = 0; n < 4; ++n) {
      f2 z[4];
      if (n == 1) seen = lds_peek(L, L_CNT + 8);
      if (n == 2) {
        lds_check(L, L_CNT + 8, 4, seen);
        act_in2(512);
#pragma unroll
        for (int q = 0; q < 4; ++q) bias[q] = *(LAS u16 *)(L + L_BS + (16 * q + c) * 2);
      }
      if (n == 3) seen = lds_peek(L, L_CNT + 16);
      act(n, z);
      sts16(L, po + ((n & 1) << 11) + (((n >> 1) ^ rh) << 12), v4u{pk2(z[0]), pk2(z[1]), pk2(z[2]), pk2(z[3])});
    }
  }
  STAMP(8);
  FRESH();
  f4 g2[4][2];
  float z1[32];
  int uo = WS_UB + (i * 112 + 4 * j + w) * 1024 + l * 16, uw = L_Z1 + w * 4096 + l * 16;
  {
    v4u bf[8][2], af[4][4];
    const rsrc_t rz = mk(p.ws + WS_ZB + j * 524288, 524288);
    int bvo[2], bso = t.rot2 * 16384;
    bvo[0] = w * 2048 + l * 16;
    bvo[1] = bvo[0] + 8192;
    PIN(bvo[0]); PIN(bvo[1]);
    auto ldb = [&](int ks, int b, int so) { bf[ks & 7][b] = ld16c(rz, bvo[ks & 1] + b * 1024, so); };
    auto next = [&](int &so) { asm volatile("s_add_u32 %0, %0, 0x4000\n\ts_and_b32 %0, %0, 0x7ffff" : "+s"(so)::"scc"); };
#pragma unroll
    for (int m = 0; m < 4; ++m) { const float b = lo16(bias[m]); g2[m][0] = g2[m][1] = f4{b, b, b, b}; PIN(g2[m][0]); PIN(g2[m][1]); }
    int ab[2];
    ab[0] = c * 128 + ((g ^ (c >> 1)) << 4);
    ab[1] = ab[0] ^ 64;
    PIN(ab[0]); PIN(ab[1]);
    auto rda = [&](int ks, int m) { af[ks & 3][m] = *(LAS v4u *)((LAS unsigned char *)(u32)ab[ks & 1] + ((ks >> 1) & 3) * 8192 + m * 2048); };
    auto turn = [&](int q) { ab[q] = (ab[q] + 32768) & 0x1ffff; PIN(ab[q]); };
    const int f0 = 4 * g + (c >> 2);
    int zr = L_Z1 + (32 * w + f0) * 128 + (c & 1) * 8 + ((((c >> 1) & 1) ^ (f0 & 7)) << 4);
    v2u zq[8];
    PIN(zr); PIN(uo); PIN(uw);
    if (ZP < 0 && w == 0) {
      lds_wait(L, L_CNT + 24, 4);
      __builtin_amdgcn_s_sleep(ZSL);
      const int fo = fl(F_Z + j * 32 + (l & 31));
#pragma unroll 1
      for (u32 s = 0; s < SPIN_CAP; ++s) {
        if (__all(ldflag(p.ws, fo) == tag)) break;
        __builtin_amdgcn_s_sleep(ZSL2);
      }
      asm volatile("" ::: "memory");
      lds_signal(L, L_CNT + 16);
    } else lds_check(L, L_CNT + 16, 1, seen);
    STAMP(9);
#pragma unroll
    for (int ks = 0; ks < 7; ++ks) { ldb(ks, 0, bso); SB(); ldb(ks, 1, bso); SB(); if (ks & 1) next(bso); SB(); }
    lds_wait(L, L_CNT + 12, 4);
    __builtin_amdgcn_s_setprio(3);
    STAMP(10);
#pragma unroll
    for (int ks = 0; ks < AD; ++ks)
#pragma unroll
      for (int m = 0; m < 4; ++m) rda(ks, m);
    SB();
    STAMP(5);
    auto chunk = [&](auto lastc) {
      constexpr bool LAST = lastc;
      unr<8>([&](auto sc) {
        constexpr int s = sc;
        if (AD == 2) WAITC(LAST && s ? 14 - 2 * s : 12, LAST && s == 4 ? 12 : LAST && s == 7 ? 0 : 4);
        else WAITC(LAST && s ? 14 - 2 * s : 12, LAST && (s == 4 || s == 5) ? 15 : LAST && s == 6 ? 4 : LAST && s == 7 ? 0 : 8);
        SB();
#pragma unroll
        for (int q = 0; q < 8; ++q) {
          mma(g2[q >> 1][q & 1], bf[s][q & 1], af[s & 3][q >> 1]);
          SB();
          if (!(q & 1) && (!LAST || s < 8 - AD)) rda(s + AD, q >> 1);
          if ((q == 1 || q == 3) && (!LAST || s == 0)) ldb(s + 7, q >> 1, bso);
          if (q == 5 && !(s & 1) && !LAST) next(bso);
          if (AD == 2 && !LAST && q == (s == 5 ? 5 : 7) && (s == 5 || s == 6)) turn(~s & 1);
          if (AD == 3 && !LAST && q == 7 && (s == 4 || s == 5)) turn(s & 1);
          if (LAST && s == 3 && q == 7)
#pragma unroll
            for (int e = 0; e < 4; ++e) { const int a = zr ^ (e << 5); zq[2 * e] = trd(L, a); zq[2 * e + 1] = trd(L, a + 2048); }
          if (LAST && s >= 6) {
            const int e = (s - 6) * 8 + q;
            const u32 z = (e & 1) ? zq[e >> 1].y : zq[e >> 1].x;
            z1[2 * e] = lo16(z); z1[2 * e + 1] = hi16(z);
            PIN(z1[2 * e]); PIN(z1[2 * e + 1]);
          }
          SB();
        }
      });
      if (!LAST) BAR0();
    };
#pragma unroll 1
    for (int ck = 0; ck < 7; ++ck) {
      chunk(std::false_type{});
    }
    chunk(std::true_type{});
    SETTLE();
  }
  STAMP(11);
#pragma unroll
  for (int m = 0; m < 4; ++m) {
    u32 o[4];
#pragma unroll
    for (int e = 0; e < 4; ++e) o[e] = pk2(f2{z1[8 * m + 2 * e], z1[8 * m + 2 * e + 1]} * ((e & 1) ? hi2(g2[m][e >> 1]) : lo2(g2[m][e >> 1])));
    st16c(v4u{o[0], o[1], o[2], o[3]}, rws, uo, m * 28672);
    sts16(L, uw + m * 1024, v4u{o[0], o[1], o[2], o[3]});
  }
  lds_signal(L, L_CNT + 36);
  FRESH();
  STAMP(7);
  v2u xe[4];
  {
    const rsrc_t rx = mk(p.x, S_ * D_ * 2);
#pragma unroll
    for (int q = 0; q < 4; ++q) xe[q] = __builtin_amdgcn_raw_buffer_load_b64(rx, (64 * i + 16 * w + c) * 896 + j * 128 + g * 8 + q * 32, 0, 0);
  }
  {
    v4u uf[28], of[OP][4];
    int uso[7];
#pragma unroll
    for (int q = 1; q < 7; ++q) uso[q] = WS_UB + (i * 4 + w) * 28672 + mod7(q + j) * 4096;
    int uvo = l * 16;
    auto ldu = [&](int ks) { uf[ks] = ld16c(rws, uvo + (ks & 3) * 1024, uso[ks >> 2]); };
    f4 o[4];
    int ob[4];
    const int oro = (4 * g + (c >> 2)) * 128 + ((((c >> 3) & 1) | ((g & 1) << 1)) << 5) + (c & 3) * 8;
#pragma unroll
    for (int n = 0; n < 4; ++n) { ob[n] = oro ^ (n << 5); PIN(ob[n]); }
    int yo = L_Z2 + (oro ^ (w << 5)), xo = L_XST + (16 * w + c) * 4, ur = L_Z1 + w * 1024 + l * 16;
    int oo = (64 * i + 16 * w + c) * 896 + j * 128 + (g & 1) * 32 + (g >> 1) * 16;
    PIN(uvo); PIN(yo); PIN(xo); PIN(oo); PIN(ur);
    auto rd = [&](int ks, int n) {
      if (ks == 14) { ob[n] += 57344; PIN(ob[n]); }
      of[ks % OP][n] = trd2(L, ob[n] + (ks % 14) * 4096, 2048);
    };
    float xv[16], xn[16], kn[16], mr, rs;
    v2u kd[4];
    auto prep = [&](int q) {
      const int e = q >> 1;
      const u32 a = (e & 2) ? xe[e >> 2].y : xe[e >> 2].x, b = (e & 2) ? kd[e >> 2].y : kd[e >> 2].x;
      if (q & 1) asm volatile("v_fma_f32 %0, %2, %3, %4\n\tv_mul_f32 %1, %1, %0" : "=&v"(xn[e]), "+v"(kn[e]) : "v"(xv[e]), "v"(rs), "v"(mr));
      else if (e & 1) asm volatile("v_and_b32 %0, 0xffff0000, %2\n\tv_and_b32 %1, 0xffff0000, %3" : "=&v"(xv[e]), "=&v"(kn[e]) : "v"(a), "v"(b));
      else asm volatile("v_lshlrev_b32 %0, 16, %2\n\tv_lshlrev_b32 %1, 16, %3" : "=&v"(xv[e]), "=&v"(kn[e]) : "v"(a), "v"(b));
    };
    auto step = [&](auto ksc) {
      constexpr int ks = ksc;
#pragma unroll
      for (int n = 0; n < 4; ++n) {
        mmaf<ks == 0>(o[n], of[ks % OP][n], uf[ks]);
        SB();
        if (ks + OP < 28) rd(ks + OP, n);
        if (ks == 17) kd[n] = trd(L, yo + (L_KD - L_Z2) + n * 2048);
        if (ks == 17 && n == 3) { mr = *(LAS float *)(L + xo); rs = *(LAS float *)(L + xo + 256); }
        if (ks == 19 && n == 2) { mr = -mr * rs; PIN(mr); }
        if (ks >= 19 && ks < 27 && n < 2) prep(4 * (ks - 19) + 2 * n);
        if (ks >= 20 && n >= 2) prep(4 * (ks - 20) + 2 * n - 3);
        SB();
      }
    };
    VM(4);
    const bool last = lds_last(L, L_CNT + 20);
    if (last) setflag(p.ws, F_U + i * 8 + j, tag);
    SB();
    STAMP(1);
    lds_wait(L, L_CNT + 36, 4);
    lds_wait(L, L_CNT + 40, 4);
#pragma unroll
    for (int ks = 0; ks < 4; ++ks) uf[ks] = lds16(L, ur + ks * 4096);
#pragma unroll
    for (int q = 0; q < 4 * OP; ++q) rd(q >> 2, q & 3);
    SB();
    unr<4>(step);
    STAMP(2);
    if (last) {
      wait_flags(p.ws, fl(F_U + i * 8 + (l < 7 ? l : 0)), tag);
      lds_signal(L, L_CNT + 32);
    } else lds_wait(L, L_CNT + 32, 1);
    STAMP(12);
#pragma unroll
    for (int ks = 4; ks < 4 + UW; ++ks) { ldu(ks); SB(); }
    BAR0();
    unr<24>([&](auto qc) {
      constexpr int ks = qc + 4;
      WAITC(ks < 28 - UW ? UW - 1 : 27 - ks, ks == 19 ? 8 : ks == 26 ? 0 : 15);
      SB();
      step(std::integral_constant<int, ks>{});
      if (ks + UW < 28) ldu(ks + UW);
      SB();
    });
    STAMP(13);
    SETTLE();
    v2u y[4], res[4];
    f2 a[8], e[8];
#pragma unroll
    for (int q = 0; q < 8; ++q) { a[q] = (q & 1) ? hi2(o[q >> 1]) : lo2(o[q >> 1]); e[q] = a[q] * -1.442695041f; }
#pragma unroll
    for (int q = 0; q < 8; ++q) e[q] = f2{__builtin_amdgcn_exp2f(e[q].x), __builtin_amdgcn_exp2f(e[q].y)} + 1.0f;
    asm volatile("s_barrier" : "+v"(e[0]), "+v"(e[1]), "+v"(e[2]), "+v"(e[3]), "+v"(e[4]), "+v"(e[5]), "+v"(e[6]), "+v"(e[7])::"memory");
    SB();
#pragma unroll
    for (int n = 0; n < 4; ++n) y[n] = trd(L, yo + n * 2048);
    SB();
#pragma unroll
    for (int q = 0; q < 8; ++q) e[q] = a[q] * f2{__builtin_amdgcn_rcpf(e[q].x), __builtin_amdgcn_rcpf(e[q].y)};
    asm volatile("" : "+v"(e[0]), "+v"(e[1]), "+v"(e[2]), "+v"(e[3]), "+v"(e[4]), "+v"(e[5]), "+v"(e[6]), "+v"(e[7]));
    SB();
    asm volatile("" : "+v"(y[0]), "+v"(y[1]), "+v"(y[2]), "+v"(y[3]));
#pragma unroll
    for (int q = 0; q < 8; ++q) {
      const u32 yy = (q & 1) ? y[q >> 1].y : y[q >> 1].x;
      const f2 tt = fma2(f2{lo16(yy), hi16(yy)}, f2{xn[2 * q], xn[2 * q + 1]}, f2{kn[2 * q], kn[2 * q + 1]});
      const u32 r = pk2(fma2(tt, e[q], f2{xv[2 * q], xv[2 * q + 1]}));
      if (q & 1) res[q >> 1].y = r; else res[q >> 1].x = r;
    }
    const rsrc_t ro = mk(p.out, S_ * D_ * 2);
    st16c(widen(res[0], res[1]), ro, oo, 0);
    st16c(widen(res[2], res[3]), ro, oo, 64);
  }
  STAMP_OUT(0);
}

static __device__ __forceinline__ void gemm_move(const Params &p, LAS unsigned char *L, const Tile t, int m) {
  STAMP_DECL;
  STAMP(0);
  const int i = t.i, j = t.j;
  int l = threadIdx.x & 63, g = l >> 4, c = l & 15;
  const srd_t sx_ = mks(p.x, S_ * D_ * 2), swin = mks(p.win, D_ * F_ * 2), swsp = mks(p.wsp, S_ * S_ * 2);
  const int lb = m * 1024, lb2 = m * 2048;
  const int pvo = (l >> 3) * 896 + j * 128 + (((((l & 7) >> 1) ^ g) << 5) | ((l & 1) << 4));
  const int rsw = ((l & 7) ^ (l >> 3)) << 4;
  const int xvo = (64 * i + 32 * (m >> 1) + 4 * (m & 1) + 8 * (l >> 5) + ((l >> 3) & 3)) * 896 + rsw;
  const int wvo = (4 * m + g) * 3584 + j * 256 + ((((c >> 1) ^ (g | ((m & 2) << 1))) << 5) | ((c & 1) << 4));
  int sb[7];
#pragma unroll
  for (int s = 0; s < 7; ++s) sb[s] = mod7(s + t.rot1);
  auto item = [&](auto nc) {
    constexpr int n = nc, s = n / 6, q = n % 6;
    if constexpr (n >= 42) dma<(n - 42) * 4096>(swin, wvo, (sb[(n - 42) >> 2] * 4 + ((n - 42) & 3)) * 57344, lb);
    else if constexpr (q < 2) dman<L_XR + (s & 3) * 8192 + q * 1024, NT_X>(sx_, xvo, sb[s] * 128 + q * 14336, lb2);
    else dma<(4 * s + q - 2) * 4096>(swin, wvo, (sb[s] * 4 + q - 2) * 57344 + 1792, lb);
  };
#define IC(n) std::integral_constant<int, (n)>{}
  const rsrc_t rwsp = mk(p.wsp, S_ * S_ * 2), rws = mk(p.ws, WS_BYTES);
  v4u ar[32];
  const int avo0 = (64 * i + 16 * m + (l >> 3)) * 4096 + (((l & 7) ^ g) << 4), avo1 = avo0 ^ 64;
  auto aso = [&](int n) { return (((n >> 1) + t.rot2) & 31) * 128 + (n & 1) * 32768; };
  auto lda = [&](int n) { const int avo = (n & 1) ? avo1 : avo0; ar[n] = n < 10 ? __builtin_amdgcn_raw_buffer_load_b128(rwsp, avo, aso(32 + n), NT_SP * 2) : ld16(rwsp, avo, aso(32 + n)); SB(); };
  auto items = [&](auto n0, auto cnt) { unr<cnt>([&](auto q) { constexpr int n = ORD.seq[n0 + q]; if constexpr (n < 70) item(IC(n)); else lda(n - 70); }); };
  constexpr const int *NI = ORD.bar;
  items(IC(0), IC(NI[0]));
  u64 tag = epoch_load(p.ws);
  EPOCH_WAIT(tag);
  tag += 1;
  {
    const v4u ones = {0x3f803f80u, 0x3f803f80u, 0x3f803f80u, 0x3f803f80u};
    f4 sx = {0, 0, 0, 0}, gr = {0, 0, 0, 0};
    int xo0 = L_XR + (16 * m + c) * 128 + ((g ^ (c & 7)) << 4), xo1 = xo0 ^ 64;
    PIN(xo0); PIN(xo1);
    unr<7>([&](auto sc) {
      constexpr int s = sc;
      VM(NI[s] - 1 - ORD.last(6 * s, 6 * s + 6) + (TLG && ORD.last(6 * s, 6 * s + 6) < NI[0]));
      v4u xs[2];
      xs[0] = lds16(L, xo0 + (s & 3) * 8192);
      xs[1] = lds16(L, xo1 + (s & 3) * 8192);
      STAMPA(2 * s + 1);
      BAR0();
      STAMPA(2 * s + 2);
      if (s == 0) STAMP(1);
      if constexpr (s < 6) items(IC(NI[s]), IC(NI[s + 1] - NI[s]));
      SB();
      if (PMS) __builtin_amdgcn_s_setprio(3);
#pragma unroll
      for (int q = 0; q < 2; ++q) { sx = mfma(xs[q], ones, sx); gr = mfma(xs[q], xs[q], gr); }
      SB();
      if (PMS) __builtin_amdgcn_s_setprio(1);
      SB();
      if constexpr (s == 6) items(IC(NI[6]), IC(LASTA));
    });
    SB();
    const int e = c & 3;
    const float s1 = e & 2 ? (e & 1 ? sx.w : sx.z) : (e & 1 ? sx.y : sx.x);
    const float s2 = e & 2 ? (e & 1 ? gr.w : gr.z) : (e & 1 ? gr.y : gr.x);
    const float mean = fmul(s1, 1.0f / 448.0f);
    const float r = inv_sigma(ffma(-mean, mean, fmul(s2, 1.0f / 448.0f)));
    if ((c >> 2) == g) {
      const int row = 32 * (m >> 1) + 8 * g + 4 * (m & 1) + e;
      *(LAS float *)(L + L_XST + row * 4) = rbf(mean);
      *(LAS float *)(L + L_XST + 256 + row * 4) = r;
    }
    SB();
    items(IC(NI[6] + LASTA), IC(NI[7] - NI[6] - LASTA));
  }
  BARW();
  STAMP(3);
  asm volatile("s_mov_b32 m0, %3\n\ts_nop 0\n\tbuffer_load_dword %0, %1, %2 offen lds" ::"v"(l * 4), "s"(mks(p.bsp, S_ * 2)), "s"(i * 128), "n"(L_BS) : "m0", "memory");
  unr<70 + NS - NI[7]>([&](auto q) {
    constexpr int n = NI[7] + q;
    VM(W1);
    if constexpr (n == ORD.last(42, 58) + 1 + W1) lds_signal(L, L_CNT);
    if constexpr (n == ORD.last(42, 70) + 1 + W1) lds_signal(L, L_CNT + 28);
    items(IC(n), IC(1));
  });
  static_assert(ORD.last(42, 70) + 1 + W1 < 70 + NS && NI[7] <= ORD.last(42, 58) + 1 + W1, "");
  STAMP(4);
  {
    const u32 tagA = (u32)tag, tagB = (u32)(tag >> 32) ^ 0x9e3779b9u;
    const int so = WS_ST + (i * 2 + (m >> 1)) * 8192 + (m & 1) * 256;
    const int vo = g * 512 + c * 16, vo3 = (g & 1) * 512 + c * 16 + 2048;
    v4u sg[4], zt[4];
    auto ldst = [&]() {
      sg[0] = ld16c(rws, vo, so); sg[1] = ld16c(rws, vo + 2048, so); sg[2] = ld16c(rws, vo, so + 4096); sg[3] = ld16c(rws, vo3, so + 4096);
    };
    lds_wait(L, L_CNT + 4, 4);
    __builtin_amdgcn_s_setprio(PM2);
    const int zo = ((m >> 1) * 8 + (l >> 5)) * 1024 + ((m & 1) * 32 + (l & 31)) * 16;
#pragma unroll
    for (int q = 0; q < 4; ++q) zt[q] = lds16(L, L_Z2 + zo + q * 2048);
    SB();
    if (PW < 60) VM(PW);
    ldst();
    SB();
#pragma unroll
    for (int n = 0; n < NB; ++n) lda(NS + n);
    SB();
    float zu[32];
#pragma unroll
    for (int q = 0; q < 16; ++q) { const u32 z = zt[q >> 2][q & 3]; zu[2 * q] = lo16(z); zu[2 * q + 1] = hi16(z); PIN(zu[2 * q]); PIN(zu[2 * q + 1]); }
    int zso = WS_ZB + (j * 64 + 2 * i) * 8192 + zo;
    PIN(zso);
    STAMP(5);
    auto late = [&]() {
      u32 d = 0;
#pragma unroll
      for (int q = 0; q < 4; ++q) d |= (sg[q].y ^ tagA) | (sg[q].w ^ tagB);
      return __builtin_amdgcn_ballot_w64(d != 0) != 0;
    };
    if (__builtin_expect(late(), 0)) {
#pragma unroll 1
      for (u32 s = 0; s < SPIN_CAP; ++s) {
        asm volatile("" ::: "memory");
        ldst();
        if (!late()) break;
      }
    }
    STAMP(2);
    float s = fadd(fadd(asf(sg[0].x), asf(sg[1].x)), fadd(asf(sg[2].x), g < 2 ? asf(sg[3].x) : 0.f));
    float u = fadd(fadd(asf(sg[0].z), asf(sg[1].z)), fadd(asf(sg[2].z), g < 2 ? asf(sg[3].z) : 0.f));
    {
      v2u a = __builtin_amdgcn_permlane16_swap(asu(s), asu(u), false, false);
      const float h = fadd(asf(a.x), asf(a.y));
      a = __builtin_amdgcn_permlane32_swap(asu(h), asu(h), false, false);
      const float k = fadd(asf(a.x), asf(a.y));
      a = __builtin_amdgcn_permlane16_swap(asu(k), asu(k), false, false);
      s = asf(a.x);
      u = asf(a.y);
    }
    const float mean = fmul(s, 1.0f / 896.0f);
    const float r = inv_sigma(ffma(-mean, mean, fmul(u, 1.0f / 896.0f)));
    if (g < 2) *(LAS float *)(L + L_ZST + g * 256 + (16 * m + c) * 4) = g ? r : -rbf(mean) * r;
    SB();
    f4 zb[2], zr[2];
#pragma unroll
    for (int q = 0; q < 2; ++q) {
      zb[q] = ldsf4(L, L_ZST + (16 * m + 8 * (g & 1) + 4 * q) * 4);
      zr[q] = ldsf4(L, L_ZST + 256 + (16 * m + 8 * (g & 1) + 4 * q) * 4);
    }
    SB();
#pragma unroll
    for (int n = 0; n < NB2; ++n) lda(NS + NB + n);
#pragma unroll
    for (int q = 0; q < 4; ++q) {
      u32 o[4];
#pragma unroll
      for (int e = 0; e < 4; ++e) {
        o[e] = pk(ffma(zu[8 * q + 2 * e], zr[e >> 1][(2 * e) & 3], zb[e >> 1][(2 * e) & 3]), ffma(zu[8 * q + 2 * e + 1], zr[e >> 1][(2 * e + 1) & 3], zb[e >> 1][(2 * e + 1) & 3]));
      }
      st16c(v4u{o[0], o[1], o[2], o[3]}, rws, zso, q * 2048);
      SB();
    }
    STAMP(6);
  }
  __builtin_amdgcn_s_setprio(PM3);
  constexpr int NA = NS + NB + NB2, NQ = 64 - NA;
  auto zflag = [&]() { if (lds_last(L, L_CNT + 24)) setflag(p.ws, F_Z + j * 32 + i, tag); SB(); STAMP(7); };
  auto ring = [&](auto nc) { constexpr int n = nc; dman<(n >> 1) * 8192 + (n & 1) * 1024, NT_RING>(swsp, (n & 1) ? avo1 : avo0, aso(n), lb2); };
  auto piece = [&](auto qc) {
    constexpr int n = qc - NR1;
    if constexpr (n < 0) lda(NA + qc); else if constexpr (n < 32) ring(IC(n < 0 || n > 31 ? 0 : n)); else lda(NA + qc - 32);
  };
  constexpr int XE[4] = {NR1 + X0 < NQ ? NR1 + X0 : NQ, NR1 + X1 < NQ ? NR1 + X1 : NQ, NR1 + X2 < NQ ? NR1 + X2 : NQ, NQ};
  static_assert(X0 >= 14 + 2 * AD && X1 >= 22 + 2 * AD && X2 >= 32 && NA + NR1 >= 19 && NA + NR1 <= 32 && W2 <= NR1 + 9, "");
  unr<XE[0]>([&](auto qc) {
    constexpr int n = qc - NR1;
    if constexpr (qc == W2) { VM(W2); zflag(); }
    if constexpr (qc > W2 && n < 14 + 2 * AD + RD) VM(RD - 1);
    if constexpr (n == 0) { lds_wait(L, L_CNT + 8, 4); STAMP(8); }
    if constexpr (n == 5 + 2 * AD + RD) lds_signal(L, L_CNT + 12);
    if constexpr (n == 28) lds_wait(L, L_CNT + 24, 4);
    if constexpr (n == ZP) if (m == 0) { wait_flags(p.ws, fl(F_Z + j * 32 + (l & 31)), tag); lds_signal(L, L_CNT + 16); }
    piece(qc);
  });
  STAMP(11);
  __builtin_amdgcn_s_setprio(0);
  int awo = m * 2048 + l * 16, awh = awo + 65536;
  PIN(awo); PIN(awh);
  {
    constexpr int cum[6] = {0, 6, 12, 19, 25, 32};
    unr<5>([&](auto kc) {
      constexpr int k = kc;
      if constexpr (k < 2) VM(XE[k] - NR1 - 14 - 2 * AD - 8 * k);
      if constexpr (k == 2) VM(XE[2] - NR1 - 32);
      BARW();
      constexpr int nq = k < 3 ? XE[k + 1] - XE[k] : 0, nw = cum[k + 1] - cum[k];
      unr<(nq > nw ? nq : nw)>([&](auto qc) {
        constexpr int n = cum[k] + qc;
        if constexpr (qc < nq) piece(IC(XE[k < 3 ? k : 0] + qc));
        if constexpr (qc < nw) sts16(L, ((n & 16) ? awh : awo) + ((n >> 1) & 7) * 8192 + (n & 1) * 1024, ar[n]);
        SB();
        __builtin_amdgcn_s_sleep(WSL);
        SB();
      });
    });
  }
  BARW();
  BAR0();
  STAMP(9);
  {
    const srd_t swout = mks(p.wout, 896 * D_ * 2);
    auto ldo = [&](auto nc) { constexpr int n = nc; VM(WOW); dma<n * 4096>(swout, pvo, WSO(n), lb); };
    const srd_t sws = mks(p.ws, WS_BYTES);
    const int yfo = fl(F_Y + ((64 * j + 16 * m + min(g * 7, 15)) / 14) * 4 + (l & 3));
    const int yvo = (64 * j + 16 * m + (l >> 3)) * 4096 + i * 128 + (((((l & 7) >> 1) ^ g) << 5) | ((l & 1) << 4));
    auto ldy = [&]() { dmac<L_Z2>(sws, yvo, WS_YB, lb2); dmac<L_Z2 + 1024>(sws, yvo, WS_YB + 32768, lb2); };
    __builtin_amdgcn_s_sleep(WSLP);
    static_assert(NO1 >= 4 + OP && NO1 <= 24, "");
    unr<NO1>(ldo);
    lds_wait(L, L_CNT + 36, 4);
    const rsrc_t rwout = mk(p.wout, 896 * D_ * 2);
#pragma unroll
    for (int n = NO1; n < 28; ++n) {
      if (n == NO1 + SIGE) { VM(SIGE); lds_signal(L, L_CNT + 40); STAMP(10); }
      ar[n] = ld16(rwout, pvo, WSO(n));
      SB();
    }
    STAMP(13);
    int wwo = m * 1024 + l * 16 + 49152;
    PIN(wwo);
#pragma unroll
    for (int n = NO1; n < 28; ++n) { sts16(L, wwo + (n - 12) * 4096, ar[n]); SB(); }
    STAMP(12);
    const bool early = !YLATE && __all(ldflag(p.ws, yfo) == tag);
    if (early) ldy();
    if (YLATE && !RREG) DRAIN();
    BARW();
    if (!early) { wait_flags(p.ws, yfo, tag); ldy(); }
  }
  DRAIN();
  BAR0();
  STAMP_OUT(1);
}

constexpr int C_CP = 4160, C_KV = 4 * C_CP, C_SLOT = C_KV + 4608;

constexpr int C_EX = 131072;

template <int H> static __device__ __forceinline__ void conv_compute(const Params &p, LAS unsigned char *L, int v, int w) {
  STAMP_DECL;
  STAMP(0);
  __builtin_amdgcn_s_setprio(PCV);
  u64 tag = epoch_load(p.ws);
  constexpr int ord[8] = {H ? 0 : 2, H ? 1 : 3, H ? 6 : 4, H ? 7 : 5, H ? 2 : 0, H ? 3 : 1, H ? 4 : 6, H ? 5 : 7};
  const int l = threadIdx.x & 63, g = l >> 4, c = l & 15, ch = w >> 1;
  const rsrc_t rws = mk(p.ws, WS_BYTES);
  {
    const int t = threadIdx.x;
    const srd_t sws = mks(p.ws, WS_BYTES);
    touch(sws, t < 128 ? WS_ST + v * 16384 + t * 128 : WS_FL + (v * 52 + min(t & 63, 51)) * 128, 0);
  }
  const int hro = ch * C_SLOT + (c & 3) * C_CP + g * 16 + (c >> 2) * 8 + H * 64;
  const int kro = ch * C_SLOT + C_KV + (254 - 2 * c + g) * 16 - 7 * 512 + H * 64;
  v2u hh[2][16];
  v4u kf[2][8];
  auto rd = [&](int ho, int ko, int q, int n) {
    const int d = n / 3;
    if (n % 3 == 2) kf[q & 1][7 - d] = lds16(L, ko + d * 512 + q * 128);
    else hh[q & 1][2 * d + n % 3] = *(LAS v2u *)(L + ho + d * 512 + q * 128 + (n % 3) * 8);
  };
  f4 acc[8], in[4];
#pragma unroll
  for (int n = 0; n < 8; ++n) acc[n] = f4{0, 0, 0, 0};
  int xw = C_EX + w * 4096 + l * 16, xr = xw ^ 4096;
  int yo = WS_YB + (14 * v + ch) * 4096 + c * 32 + (g & 1) * 512 + (g >> 1) * 16 + 0x40000000;
  PIN(xw); PIN(xr);
  v2u y[4];
  auto fin = [&](int k) {
    if (k < 2) { in[2 * k] = ldsf4(L, xr + k * 2048); in[2 * k + 1] = ldsf4(L, xr + k * 2048 + 1024); }
    else if (k < 10) { const int n = (k - 2) >> 1, e = (k & 1) * 2; in[n][e] = fadd(in[n][e], acc[ord[4 + n]][e]); in[n][e + 1] = fadd(in[n][e + 1], acc[ord[4 + n]][e + 1]); }
    else if (k < 14) { const int n = k - 10; y[n] = v2u{pk(in[n].x, in[n].y), pk(in[n].z, in[n].w)}; PIN(y[n]); }
    else if (k < 16) st16c(widen(y[2 * k - 28], y[2 * k - 27]), rws, yo, ord[2 * k - 24] * 512);
  };
  EPOCH_WAIT(tag);
  tag += 1;
  if (KV1) BAR0();
  BAR0();
  STAMP(2);
#pragma unroll
  for (int n = 0; n < 24; ++n) { rd(hro, kro, 0, n); SB(); }
  WAITC(63, 0);
  int set = 0;
#pragma unroll 1
  for (int rho = 0; rho < 7; ++rho) {
    const int nset = set == 4 * C_SLOT ? 0 : set + 2 * C_SLOT;
    int ho = hro + set, ko = kro + set;
    unr<4>([&](auto qc) {
      constexpr int q = qc;
      if (q == 3) { ho = hro + nset - 512; ko = kro + nset - 512; PIN(ho); PIN(ko); }
      if (q > 0) WAITC(63, 0);
      if (q == 3) BAR0();
      SB();
      int k = 0;
#pragma unroll
      for (int hf = 0; hf < 2; ++hf)
#pragma unroll
        for (int P = 0; P < 8; ++P)
#pragma unroll
          for (int n = 0; n < 4; ++n) {
            const int tl = ord[4 * hf + n];
            if (P > tl) continue;
            const v2u a = hh[q & 1][2 * P], b = hh[q & 1][2 * P + 1];
            if (q == 0 && P == 0) mma0(acc[tl], v4u{a.x, a.y, b.x, b.y}, kf[q & 1][tl - P]);
            else mma(acc[tl], v4u{a.x, a.y, b.x, b.y}, kf[q & 1][tl - P]);
            SB();
            if (q == 0) {
              if (k < 2) fin(k);
              else if (k < 9) { rd(ho, ko, 1, 2 * k - 4); SB(); rd(ho, ko, 1, 2 * k - 3); }
              else if (k < 17) fin(k - 7);
              else if (k < 22) { rd(ho, ko, 1, 2 * k - 20); SB(); rd(ho, ko, 1, 2 * k - 19); }
              else if (k < 26) fin(k - 12);
            } else if (k < 24) rd(ho, ko, q + 1, k);
            else if (q == 2 && (k == 27 || k == 30)) fin(k / 3 + 5);
            else if (q == 3 && k < 28) sts16(L, xw + (k - 24) * 1024, __builtin_bit_cast(v4u, acc[ord[k - 24]]));
            SB();
            ++k;
          }
    });
    yo += rho ? 8192 : -0x40000000;
    set = nset;
    WAITC(63, 0);
    BAR0();
    if (rho == 0) STAMP(3);
    if (rho == 1) STAMP(4);
    if (rho == 3) STAMP(5);
    if (rho == 5) STAMP(6);
  }
  SETTLE();
#pragma unroll
  for (int k = 0; k < 16; ++k) fin(k);
  STAMP(7);
  DRAIN();
  setflag(p.ws, F_Y + v * 4 + w, tag);
  STAMP_OUT(0);
}

static __device__ __forceinline__ void conv_stage(const Params &p, LAS unsigned char *L, int v, int m) {
  STAMP_DECL;
  STAMP(0);
  const int l = threadIdx.x & 63;
  const srd_t sh = mks(p.h, D_ * S_ * 2), sws = mks(p.ws, WS_BYTES), swout = mks(p.wout, 896 * D_ * 2);
  const rsrc_t rws = mk(p.ws, WS_BYTES);
  const int vo = m * 1024 + l * 16;
  auto stage_h = [&](int rho, int set) {
    const int so = (14 * v + 2 * rho) * 4096, lb = set + m * 1024;
    unr<8>([&](auto qc) { constexpr int q = qc; dman<(q >> 2) * C_SLOT + (q & 3) * C_CP + 32, NT_H>(sh, vo, so + (q >> 2) * 4096 + (q & 3) * 2 + 2, lb); });
  };
  auto stage_kv = [&](int rho, int set) {
    const int so = WS_KVR + (14 * v + 2 * rho) * 4096, lb = set + m * 1024;
    dmac<C_KV>(sws, vo, so, lb);
    dmac<C_SLOT + C_KV>(sws, vo, so + 4096, lb);
  };
  u64 head;
  asm volatile("global_load_dwordx2 %0, %1, %2" : "=&v"(head) : "v"((14 * v + (l < 56 ? l >> 2 : 0)) * 4096), "s"(p.h) : "memory");
  stage_h(0, 0);
  u64 tag = epoch_load(p.ws);
  stage_h(1, 2 * C_SLOT);
  asm volatile("s_waitcnt vmcnt(16)" : "+v"(head)::"memory");
  head <<= 48 - 16 * (l & 3);
  auto put_head = [&](int rho, int set) {
    if (m == 0 && (l >> 3) == rho) *(LAS u64 *)(L + set + ((l >> 2) & 1) * C_SLOT + (l & 3) * C_CP + 24) = head;
  };
  {
    const v4u z = {0, 0, 0, 0};
    const int t = m * 64 + l;
    if (t < 192) sts16(L, (t >> 5) * C_SLOT + C_KV + 4096 + (t & 31) * 16, z);
    if (t < 24) { sts16(L, (t >> 2) * C_SLOT + (t & 3) * C_CP, z); *(LAS u64 *)(L + (t >> 2) * C_SLOT + (t & 3) * C_CP + 16) = 0; }
  }
  put_head(0, 0);
  put_head(1, 2 * C_SLOT);
  put_head(2, 4 * C_SLOT);
  EPOCH_WAIT(tag);
  tag += 1;
  if (m == 0) {
    __builtin_amdgcn_s_sleep(KSLEEP);
    wait_flags(p.ws, flk(F_KV + ((14 * v + (l < 32 ? 0 : 13)) >> 4) * 32 + (l & 31)), tag);
  }
  BAR0();
  STAMP(1);
  stage_kv(0, 0);
  stage_kv(1, 2 * C_SLOT);
  VM(2);
  BARW();
  stage_h(2, 4 * C_SLOT);
  stage_kv(2, 4 * C_SLOT);
  STAMP(2);
  setflagk(p.ws, F_HI + v * 4 + m, tag);
  int set = 0;
#pragma unroll 1
  for (int rho = 0; rho < 7; ++rho) {
    if (rho == 2) STAMP(3);
    if (rho == 3) STAMP(8);
    if (rho == 0) VM(11); else if (rho >= 5) DRAIN(); else if (rho > WOR) VM(10 + 2 * WOK); else VM(10);
    if (rho == 2) STAMP(4);
    if (rho == 3) STAMP(9);
    BARW();
    if (rho == 2) STAMP(5);
    if (rho == 3) STAMP(10);
    BAR0();
    if (rho == 2) STAMP(6);
    if (rho == 3) STAMP(11);
    if (rho < 4) {
      stage_h(rho + 3, set);
      if (rho == 2) STAMP(7);
      if (rho == 3) STAMP(12);
      stage_kv(rho + 3, set);
      put_head(rho + 3, set);
    }
    if (rho >= WOR && rho < WOR + 4 / WOK) {
#pragma unroll
      for (int q = 0; q < WOK; ++q) {
        const int n = ((v & 3) * 4 + (rho - WOR) * WOK + q) * 392 + m * 64 + l;
        touch(swout, n * 128, 0);
        touch(swout, min(n + 256, 6271) * 128, 0);
      }
    }
    set = set == 4 * C_SLOT ? 0 : set + 2 * C_SLOT;
    if (rho == 4 && v == 0 && m == 0) {
#pragma unroll 1
      for (u32 s = 0; s < SPIN_CAP; ++s) {
        u64 f[16], d = 0;
#pragma unroll
        for (int q = 0; q < 16; ++q) f[q] = ldflag(p.ws, flk(q * 64 + l));
#pragma unroll
        for (int q = 0; q < 16; ++q) d |= f[q] ^ tag;
        if (__all(d == 0)) break;
        asm volatile("" ::: "memory");
      }
      if (l == 0) stflag(p.ws, WS_EPOCH, tag);
    }
  }
  STAMP_OUT(1);
}

#define CODE_LINES 414
#define CPFW 4
__global__ void __launch_bounds__(512, 2) fused_kernel(Params p) {
  extern __shared__ __attribute__((aligned(16))) unsigned char smem[];
  LAS unsigned char *L = (LAS unsigned char *)smem;
  const int b = blockIdx.x, x = b & 7, r = b >> 3, wv = __builtin_amdgcn_readfirstlane(threadIdx.x >> 6);
  {
    u64 pc;
    const u32 line = min((u32)(r * 16 + (threadIdx.x & 15)), (u32)CODE_LINES);
    asm volatile("s_cmp_lg_u32 %2, %4\n\ts_cbranch_scc1 1f\n\ts_getpc_b64 %0\n\ts_mov_b32 m0, %3\n\ts_nop 0\n\tglobal_load_lds_dword %1, %0\n1:\n\t"
                 "s_setprio 3\n\ts_cmp_lt_u32 %2, 4\n\ts_cbranch_scc1 2f\n\ts_setprio 1\n2:"
                 : "=&s"(pc) : "v"(line * 128), "s"(wv), "n"(LDS_BYTES - 256), "n"(CPFW) : "m0", "scc", "memory");
  }
  if (r >= 28) {
    const int v = x * 4 + (r - 28);
    if (wv < 4) { if (wv & 1) conv_compute<1>(p, L, v, wv); else conv_compute<0>(p, L, v, wv); }
    else conv_stage(p, L, v, wv - 4);
  } else {
    Tile t;
    t.i = 4 * x + r / 7;
    t.j = r % 7;
    t.rot1 = mod7(2 * (t.i & 3) + t.j);
    t.rot2 = (4 * x + 4 + (32 * t.j) / 7 + DELTA * (t.i & 3)) & 31;
    v2u shk = {0, 0};
    u32 dvr = 0;
    if (wv < 4) kv_fetch(p, t, wv, shk, dvr);
    SB();
    asm volatile("" ::: "memory");
    if (wv < 4) gemm_compute(p, L, t, wv, shk, dvr);
    else gemm_move(p, L, t, wv - 4);
  }
}

extern "C" void kernel_launch(void *const *d_in, const int *in_sizes, int n_in, void *d_out, int out_size, void *d_ws, size_t ws_size,
                              hipStream_t stream) {
  Params p;
  p.x = (const u16 *)d_in[0];
  p.k = (const u16 *)d_in[1];
  p.v = (const u16 *)d_in[2];
  p.shk = (const u16 *)d_in[3];
  p.h = (const u16 *)d_in[4];
  p.dv = (const u16 *)d_in[5];
  p.win = (const u16 *)d_in[7];
  p.wsp = (const u16 *)d_in[8];
  p.bsp = (const u16 *)d_in[9];
  p.wout = (const u16 *)d_in[10];
  p.out = (u16 *)d_out;
  p.ws = (unsigned char *)d_ws;
  (void)hipFuncSetAttribute((const void *)fused_kernel, hipFuncAttributeMaxDynamicSharedMemorySize, LDS_BYTES);
  fused_kernel<<<256, 512, LDS_BYTES, stream>>>(p);
}
```

```cpp
#define PAIRX
#include <hip/hip_runtime.h>
#include <stdint.h>
#include <type_traits>

typedef unsigned short u16;
typedef unsigned int u32;
typedef unsigned long long u64;
typedef u32 v4u __attribute__((ext_vector_type(4)));
typedef u32 v2u __attribute__((ext_vector_type(2)));
typedef float f4 __attribute__((ext_vector_type(4)));
typedef float f2 __attribute__((ext_vector_type(2)));
typedef short s4 __attribute__((ext_vector_type(4)));
typedef __bf16 bf8 __attribute__((ext_vector_type(8)));
typedef __bf16 bf2 __attribute__((ext_vector_type(2)));
#define LAS __attribute__((address_space(3)))
#define GAS __attribute__((address_space(1)))
typedef __amdgpu_buffer_rsrc_t rsrc_t;
typedef int srd_t __attribute__((ext_vector_type(4)));

constexpr int S_ = 2048, D_ = 448, F_ = 1792;
constexpr int WS_EPOCH = 0;
#define FS 128
#define FULL 1
constexpr int WS_FL = 4096;
constexpr int F_KV = 0;
constexpr int F_HI = 896;
constexpr int F_Z = 1024;
constexpr int F_U = 1280;
constexpr int F_ZD = 256;
constexpr int F_UD = 512;
constexpr int F_Y = 1536;
constexpr int WS_ST = 262144;
constexpr int WS_ZB = 1 << 20;
constexpr int WS_UB = 5 << 20;
constexpr int WS_KVR = 9 << 20;
constexpr int WS_YB = 11 << 20;
constexpr int WS_STAMP = 14 << 20;
constexpr int WS_BYTES = 16 << 20;
constexpr int LDS_BYTES = 163840;
constexpr u32 SPIN_CAP = 1u << 16;

struct Params {
  const u16 *x, *k, *v, *shk, *h, *dv, *win, *wsp, *bsp, *wout;
  u16 *out;
  unsigned char *ws;
};

#define SB() __builtin_amdgcn_sched_barrier(0)
#define BARW() do { SB(); asm volatile("s_waitcnt lgkmcnt(0)\n\ts_barrier" ::: "memory"); SB(); } while (0)
#define BAR0() do { SB(); asm volatile("s_barrier" ::: "memory"); SB(); } while (0)
#define FRESH() do { asm volatile("" : "+v"(l)); g = l >> 4; c = l & 15; } while (0)
#define DRAIN() asm volatile("s_waitcnt vmcnt(0)" ::: "memory")
#define VM(n) asm volatile("s_waitcnt vmcnt(%0)" ::"n"(n) : "memory")
#define PIN(x) asm volatile("" : "+v"(x))
#define STAMP(n) SB()
#define STAMPA(n) do { } while (0)
#define STAMP_DECL do { } while (0)
#define STAMP_OUT(slot) do { } while (0)

static __device__ __forceinline__ rsrc_t mk(const void *p, int bytes) { return __builtin_amdgcn_make_buffer_rsrc((void *)p, 0, bytes, 0x00020000); }
static __device__ __forceinline__ v4u ld16(rsrc_t r, int vo, int so) { return __builtin_amdgcn_raw_buffer_load_b128(r, vo, so, 0); }
static __device__ __forceinline__ v4u ld16c(rsrc_t r, int vo, int so) { return __builtin_amdgcn_raw_buffer_load_b128(r, vo, so, 16); }
static __device__ __forceinline__ u32 ld4(rsrc_t r, int vo, int so) { return __builtin_amdgcn_raw_buffer_load_b32(r, vo, so, 0); }
static __device__ __forceinline__ void st16c(v4u v, rsrc_t r, int vo, int so) { __builtin_amdgcn_raw_buffer_store_b128(v, r, vo + so, 0, 16); }
static __device__ __forceinline__ srd_t mks(const void *p, int bytes) { return srd_t{(int)(u64)p, (int)((u64)p >> 32) & 0xffff, bytes, 0x00020000}; }
template <int IMM> static __device__ __forceinline__ void dma(srd_t r, int vo, int so, int base) {
  asm volatile("s_add_u32 m0, %0, %4\n\ts_nop 0\n\tbuffer_load_dwordx4 %1, %2, %3 offen lds" ::"s"(base), "v"(vo), "s"(r), "s"(so), "n"(IMM) : "m0", "scc", "memory");
}
template <int IMM, int NT> static __device__ __forceinline__ void dman(srd_t r, int vo, int so, int base) {
  if constexpr (NT) asm volatile("s_add_u32 m0, %0, %4\n\ts_nop 0\n\tbuffer_load_dwordx4 %1, %2, %3 offen nt lds" ::"s"(base), "v"(vo), "s"(r), "s"(so), "n"(IMM) : "m0", "scc", "memory");
  else dma<IMM>(r, vo, so, base);
}
#define NT_KV 0
#define NT_H 0
#define NT_SP 0
#define NT_X 0
#define NT_RING 0
template <int IMM> static __device__ __forceinline__ void dmac(srd_t r, int vo, int so, int base) {
  asm volatile("s_add_u32 m0, %0, %4\n\ts_nop 0\n\tbuffer_load_dwordx4 %1, %2, %3 offen sc1 lds" ::"s"(base), "v"(vo), "s"(r), "s"(so), "n"(IMM) : "m0", "scc", "memory");
}
static __device__ __forceinline__ void touch(srd_t r, int vo, int so) {
  asm volatile("s_mov_b32 m0, %3\n\ts_nop 0\n\tbuffer_load_dword %0, %1, %2 offen lds" ::"v"(vo), "s"(r), "s"(so), "n"(LDS_BYTES - 256) : "m0", "memory");
}
static __device__ __forceinline__ void touchc(srd_t r, int vo, int so) {
  asm volatile("s_mov_b32 m0, %3\n\ts_nop 0\n\tbuffer_load_dword %0, %1, %2 offen sc1 lds" ::"v"(vo), "s"(r), "s"(so), "n"(LDS_BYTES - 256) : "m0", "memory");
}
static __device__ __forceinline__ float one(float r) { asm("" : "+v"(r)); return r; }
static __device__ __forceinline__ float fadd(float a, float b) { return one(a + b); }
static __device__ __forceinline__ float fmul(float a, float b) { return one(a * b); }
static __device__ __forceinline__ float ffma(float a, float b, float c) { return one(__builtin_fmaf(a, b, c)); }
template <int N, typename Fn> static __device__ __forceinline__ void unr(Fn &&f) {
  if constexpr (N > 0) { unr<N - 1>(f); f(std::integral_constant<int, N - 1>{}); }
}
static __device__ __forceinline__ f4 mfma(v4u a, v4u b, f4 c) {
  return __builtin_amdgcn_mfma_f32_16x16x32_bf16(__builtin_bit_cast(bf8, a), __builtin_bit_cast(bf8, b), c, 0, 0, 0);
}
static __device__ __forceinline__ void mma(f4 &c, v4u a, v4u b) { asm volatile("v_mfma_f32_16x16x32_bf16 %0, %1, %2, %0" : "+v"(c) : "v"(a), "v"(b)); }
static __device__ __forceinline__ void mma0(f4 &c, v4u a, v4u b) { asm volatile("v_mfma_f32_16x16x32_bf16 %0, %1, %2, 0" : "=v"(c) : "v"(a), "v"(b)); }
typedef float f16v __attribute__((ext_vector_type(16)));
static __device__ __forceinline__ void mmb(f16v &c, v4u a, v4u b) { asm volatile("v_mfma_f32_32x32x16_bf16 %0, %1, %2, %0" : "+v"(c) : "v"(a), "v"(b)); }
template <bool FIRST> static __device__ __forceinline__ void mmaf(f4 &c, v4u a, v4u b) { if constexpr (FIRST) mma0(c, a, b); else mma(c, a, b); }
#define SETTLE() do { SB(); asm volatile("s_nop 7" ::: "memory"); SB(); } while (0)
#define WAITC(vm, lg) __builtin_amdgcn_s_waitcnt(((vm) & 15) | 0x70 | ((lg) << 8) | (((vm) >> 4) << 14))
static __device__ __forceinline__ float dot2(u32 a, u32 b, float c) { return __builtin_amdgcn_fdot2_f32_bf16(__builtin_bit_cast(bf2, a), __builtin_bit_cast(bf2, b), c, false); }
static __device__ __forceinline__ float asf(u32 u) { return __builtin_bit_cast(float, u); }
static __device__ __forceinline__ u32 asu(float f) { return __builtin_bit_cast(u32, f); }
static __device__ __forceinline__ float lo16(u32 u) { return asf(u << 16); }
static __device__ __forceinline__ float hi16(u32 u) { return asf(u & 0xffff0000u); }
static __device__ __forceinline__ u32 pk(float a, float b) { f2 t = {a, b}; return __builtin_bit_cast(u32, __builtin_convertvector(t, bf2)); }
static __device__ __forceinline__ float rbf(float a) { return lo16(pk(a, a)); }
static __device__ __forceinline__ v4u lds16(LAS unsigned char *L, int off) { return *(LAS v4u *)(L + off); }
static __device__ __forceinline__ f4 ldsf4(LAS unsigned char *L, int off) { return *(LAS f4 *)(L + off); }
static __device__ __forceinline__ void sts16(LAS unsigned char *L, int off, v4u v) { *(LAS v4u *)(L + off) = v; }
static __device__ __forceinline__ v2u trd(LAS unsigned char *L, int off) {
  return __builtin_bit_cast(v2u, __builtin_amdgcn_ds_read_tr16_b64_v4i16((LAS s4 *)(L + off)));
}
static __device__ __forceinline__ v4u trd2(LAS unsigned char *L, int off, int step) {
  const v2u a = trd(L, off), b = trd(L, off + step);
  return v4u{a.x, a.y, b.x, b.y};
}
static __device__ __forceinline__ f2 fma2(f2 a, f2 b, f2 c) { return __builtin_elementwise_fma(a, b, c); }
static __device__ __forceinline__ u32 pk2(f2 t) { return __builtin_bit_cast(u32, __builtin_convertvector(t, bf2)); }
static __device__ __forceinline__ f2 lo2(f4 v) { return __builtin_shufflevector(v, v, 0, 1); }
static __device__ __forceinline__ f2 hi2(f4 v) { return __builtin_shufflevector(v, v, 2, 3); }
static __device__ __forceinline__ void rowsum8(float (&a)[8], float (&b)[8]) {
#define DA(d, s, ctl, bm) "v_add_f32_dpp " d ", " s ", " s " " ctl " row_mask:0xf bank_mask:" bm "\n\t"
  asm volatile("s_nop 1\n\t"
               DA("%0", "%0", "row_mirror", "0x3") DA("%1", "%1", "row_mirror", "0x3") DA("%2", "%2", "row_mirror", "0x3") DA("%3", "%3", "row_mirror", "0x3")
               DA("%4", "%4", "row_mirror", "0x3") DA("%5", "%5", "row_mirror", "0x3") DA("%6", "%6", "row_mirror", "0x3") DA("%7", "%7", "row_mirror", "0x3")
               DA("%0", "%8", "row_mirror", "0xc") DA("%1", "%9", "row_mirror", "0xc") DA("%2", "%10", "row_mirror", "0xc") DA("%3", "%11", "row_mirror", "0xc")
               DA("%4", "%12", "row_mirror", "0xc") DA("%5", "%13", "row_mirror", "0xc") DA("%6", "%14", "row_mirror", "0xc") DA("%7", "%15", "row_mirror", "0xc")
               DA("%0", "%0", "row_half_mirror", "0x5") DA("%1", "%1", "row_half_mirror", "0x5") DA("%4", "%4", "row_half_mirror", "0x5") DA("%5", "%5", "row_half_mirror", "0x5")
               DA("%0", "%2", "row_half_mirror", "0xa") DA("%1", "%3", "row_half_mirror", "0xa") DA("%4", "%6", "row_half_mirror", "0xa") DA("%5", "%7", "row_half_mirror", "0xa")
               DA("%0", "%0", "quad_perm:[1,0,3,2]", "0xf") DA("%1", "%1", "quad_perm:[1,0,3,2]", "0xf") DA("%4", "%4", "quad_perm:[1,0,3,2]", "0xf") DA("%5", "%5", "quad_perm:[1,0,3,2]", "0xf")
               DA("%0", "%0", "quad_perm:[2,3,0,1]", "0xf") DA("%1", "%1", "quad_perm:[2,3,0,1]", "0xf") DA("%4", "%4", "quad_perm:[2,3,0,1]", "0xf") DA("%5", "%5", "quad_perm:[2,3,0,1]", "0xf")
               : "+v"(a[0]), "+v"(a[1]), "+v"(a[2]), "+v"(a[3]), "+v"(b[0]), "+v"(b[1]), "+v"(b[2]), "+v"(b[3])
               : "v"(a[4]), "v"(a[5]), "v"(a[6]), "v"(a[7]), "v"(b[4]), "v"(b[5]), "v"(b[6]), "v"(b[7]));
#undef DA
}
static __device__ __forceinline__ void lds_signal(LAS unsigned char *L, int off) {
  asm volatile("s_waitcnt lgkmcnt(0)" ::: "memory");
  if ((threadIdx.x & 63) == 0) __hip_atomic_fetch_add((LAS u32 *)(L + off), 1u, __ATOMIC_RELAXED, __HIP_MEMORY_SCOPE_WORKGROUP);
}
static __device__ __forceinline__ bool lds_last(LAS unsigned char *L, int off) {
  u32 n = 0;
  if ((threadIdx.x & 63) == 0) n = __hip_atomic_fetch_add((LAS u32 *)(L + off), 1u, __ATOMIC_RELAXED, __HIP_MEMORY_SCOPE_WORKGROUP);
  return __builtin_amdgcn_readfirstlane(n) == 3;
}
static __device__ __forceinline__ void lds_wait(LAS unsigned char *L, int off, u32 n) {
#pragma unroll 1
  for (u32 s = 0; s < (SPIN_CAP << 4); ++s)
  {
    if (__hip_atomic_load((LAS u32 *)(L + off), __ATOMIC_RELAXED, __HIP_MEMORY_SCOPE_WORKGROUP) >= n) break;
    __builtin_amdgcn_s_sleep(1);
  }
  asm volatile("" ::: "memory");
}
static __device__ __forceinline__ u64 ldflag(unsigned char *ws, int off) { return __hip_atomic_load((GAS u64 *)(ws + off), __ATOMIC_RELAXED, __HIP_MEMORY_SCOPE_AGENT); }
static __device__ __forceinline__ void stflag(unsigned char *ws, int off, u64 v) { __hip_atomic_store((GAS u64 *)(ws + off), v, __ATOMIC_RELAXED, __HIP_MEMORY_SCOPE_AGENT); }
static __device__ __forceinline__ int fl(int n) { return WS_FL + n * FS; }
#define FSK 8
static __device__ __forceinline__ int flk(int n) { return WS_FL + n * FSK; }
static __device__ __forceinline__ void setflagk(unsigned char *ws, int n, u64 tag) {
  const int l = threadIdx.x & 63;
  if (FULL && FSK >= 16) { if (l < FSK / 16) st16c(v4u{(u32)tag, (u32)(tag >> 32), (u32)tag, (u32)(tag >> 32)}, mk(ws, WS_BYTES), flk(n) + l * 16, 0); }
  else if (l == 0) stflag(ws, flk(n), tag);
}
static __device__ __forceinline__ void setflag(unsigned char *ws, int n, u64 tag) {
  const int l = threadIdx.x & 63;
  if (FULL) { if (l < FS / 16) st16c(v4u{(u32)tag, (u32)(tag >> 32), (u32)tag, (u32)(tag >> 32)}, mk(ws, WS_BYTES), fl(n) + l * 16, 0); }
  else if (l == 0) stflag(ws, fl(n), tag);
}
static __device__ __forceinline__ void wait_flags(unsigned char *ws, int off, u64 tag) {
#pragma unroll 1
  for (u32 s = 0; s < SPIN_CAP; ++s) {
    const u64 v = ldflag(ws, off);
    if (__all(v == tag)) break;
    __builtin_amdgcn_s_sleep(1);
  }
  asm volatile("" ::: "memory");
}
static __device__ __forceinline__ void hint_flags(const unsigned char *at, u64 tag) {
  typedef u64 v8q __attribute__((ext_vector_type(8)));
#pragma unroll 1
  for (u32 s = 0; s < 4096; ++s) {
    v8q f;
    asm volatile("s_load_dwordx16 %0, %1, 0x0 glc\n\ts_waitcnt lgkmcnt(0)" : "=s"(f) : "s"(at) : "memory");
    u64 d = 0;
#pragma unroll
    for (int q = 0; q < 8; ++q) d |= f[q] ^ tag;
    if (d == 0) break;
    __builtin_amdgcn_s_sleep(1);
  }
}
static __device__ __forceinline__ u32 lds_peek(LAS unsigned char *L, int off) { return __hip_atomic_load((LAS u32 *)(L + off), __ATOMIC_RELAXED, __HIP_MEMORY_SCOPE_WORKGROUP); }
static __device__ __forceinline__ void lds_check(LAS unsigned char *L, int off, u32 n, u32 seen) {
  if (__builtin_expect(__builtin_amdgcn_readfirstlane(seen) < n, 0)) lds_wait(L, off, n);
  asm volatile("" ::: "memory");
}
static __device__ __forceinline__ float inv_sigma(float var) {
  const float sd = rbf(__builtin_amdgcn_sqrtf(rbf(rbf(fmaxf(var, 0.f)) + 1e-5f)));
  return __builtin_amdgcn_rcpf(sd);
}
static __device__ __forceinline__ int mod7(int a) { return a >= 7 ? a - 7 : a; }

constexpr int L_KR = 81920;
constexpr int L_RV = L_KR + 20480;
constexpr int L_XR = 114688;
constexpr int L_Z2 = 114688;
constexpr int L_Z1 = 131072;
constexpr int L_KD = 147456;
constexpr int L_CS = 155648;
constexpr int L_XST = 156672;
constexpr int L_ZST = 157184;
constexpr int L_CNT = 157696;
constexpr int L_BS = 157952;

#define PMS 1
#define PTW 1
#define TSP 0
#define TZ1 0
#define NISS {18, 5, 5, 5, 5, 4, 0, 0}
#define NIZ {0, 0, 0, 0, 0, 1, 5, 5}
#define NIW {0, 0, 0, 0, 0, 0, 0, 0}
#define MIXZ 30
#define LASTA 2
#define W1 7
#define W2 12
#define KV1 1
#define KSLEEP 16
#define ZP -100
#define WOW 12
#define WSLP 4
#define AD 2
#define RREG 1
#define OP 8
#define UW 12
#define WOR 2
#define WOK 1
#define YLATE 1
#define WSO(n) ((mod7(((n) >> 2) + j) * 16 + ((n) & 3) * 4 + m) * 7168)
#define SIGE 2
#define TLC 0
#define TLG 0
#define UPOS 0
#define NO0 NO1
#define XAT 4
#define PMT 0
#define K1 8
#define T5 0
#define T6 T5
#define TS 2
#define RE 0
#define WTN 1
#define CLA 5
#define TRK {0, 0, 3, 3, 3, 3, 4}
#define TRS 4
#define ROT1 0
#define TD 0
#define TC 1
#define CLZ 13
#define CLU 3
#define HZ 1
#define HU 1
#define OWNM 1
#define NR1 12
#define X0 30
#define X1 32
#define X2 38
#define ZSL 0
#define ZSL2 8
#define NO1 12
constexpr int VLIM = 63;
#define RD 8
#define KVF_AT 3
#define SH_AT 6
#define TSLEEP 32
#define NS 10
#define PRE 0
#define DELTA 0
#define WOT 2
#define CLR_EARLY 1
#define CLR_SLEEP 8
#define PW 63
#define WSL 0
#define PC0 0
#define PC1 2
#define PM2 3
#define PM3 1
#define PCV 2
#define NB 6
#define NB2 4
struct Order {
  int seq[102], bar[8];
  constexpr int last(int a, int b) const { int r = 0; for (int q = 0; q < 102; ++q) if (seq[q] >= a && seq[q] < b) r = q; return r; }
};
constexpr Order make_order() {
  Order o{};
  constexpr int a[8] = NISS, z[8] = NIZ, w[8] = NIW;
  int n = 0, ia = 0, iz = 42, iw = 70;
  for (int s = 0; s < 8; ++s) {
    int ca = a[s], cz = z[s], cw = w[s];
    while (ca + cz + cw > 0) {
      for (int q = 0; q < 2 && ca > 0; ++q) { o.seq[n++] = ia++; --ca; }
      if (cz > 0) { o.seq[n++] = iz++; --cz; } else if (cw > 0) { o.seq[n++] = iw++; --cw; }
    }
    o.bar[s] = n;
  }
  while (n < 70 + NS) {
    for (int q = 0; q < MIXZ && iz < 70; ++q) o.seq[n++] = iz++;
    if (iw < 70 + NS && (iz == 70 || iw < 80)) o.seq[n++] = iw++;
  }
  while (n < 102) o.seq[n++] = 1000;
  return o;
}
constexpr Order ORD = make_order();
constexpr bool order_ok() {
  for (int q = 0; q < ORD.bar[7]; ++q) {
    int s = 0;
    while (q >= ORD.bar[s]) ++s;
    const int n = ORD.seq[q], st = n / 6;
    if (n < 42 && s > st) return false;
    if (n < 42 && n % 6 < 2 && st >= 4 && s < st - 2) return false;
    if (n < 42 && n % 6 >= 2 && st >= 5 && s < 2) return false;
    if (n >= 42 && n < 70 && s < (n - 42) / 4 + 3) return false;
  }
  return ORD.last(0, 42) < ORD.bar[6];
}
static_assert(order_ok(), "");
struct Tile { int i, j, rot1, rot2, e, ne; };

static __device__ __forceinline__ u64 epoch_load(const unsigned char *ws) {
  u64 e;
  asm volatile("s_load_dwordx2 %0, %1, 0x0" : "=s"(e) : "s"(ws) : "memory");
  return e;
}
#define EPOCH_WAIT(e) asm volatile("s_waitcnt lgkmcnt(0)" : "+s"(e)::"memory")
static __device__ __forceinline__ v4u widen(v2u x, v2u y) {
  const v2u a = __builtin_amdgcn_permlane16_swap(x.x, y.x, false, false), b = __builtin_amdgcn_permlane16_swap(x.y, y.y, false, false);
  return v4u{a.x, b.x, a.y, b.y};
}

static __device__ __forceinline__ void ztouch(const Params &p, const Tile t, int ck, int q, int l) {
  const int e = t.e + q;
  if (q < t.ne) touchc(mks(p.ws + WS_ZB + t.j * 524288, 524288), l * 128, ((t.rot2 + 4 * ck + (e >> 1)) & 31) * 16384 + (e & 1) * 8192);
}
static __device__ __forceinline__ void kv_fetch(const Params &p, const Tile t, int w, v2u &shk, u32 &dvr) {
  const int i = t.i, j = t.j, l = threadIdx.x & 63;
  const srd_t sk = mks(p.k, S_ * D_ * 2), sv = mks(p.v, S_ * D_ * 2);
  const int vo = (l >> 3) * 896 + j * 128 + (((l & 7) ^ ((l >> 2) & 4)) << 4), so = (64 * i + 16 * w) * 896, lb = w * 5120;
  dman<L_KR + 1024, NT_KV>(sk, vo, so, lb);
  dman<L_KR + 2048, NT_KV>(sk, vo, so + 7168, lb);
  dman<L_KR, NT_KV>(sk, vo, so > 0 ? so - 7168 : 0x40000000, lb);
  dman<L_KR + 3072, NT_KV>(sv, vo, so, lb);
  dman<L_KR + 4096, NT_KV>(sv, vo, so + 7168, lb);
  asm volatile("global_load_dwordx2 %0, %2, %3\n\tglobal_load_ushort %1, %4, %5" : "=&v"(shk), "=&v"(dvr) : "v"((64 * j + l) * 8), "s"(p.shk), "v"((64 * j + l) * 2), "s"(p.dv) : "memory");
}

static __device__ __forceinline__ void gemm_compute(const Params &p, LAS unsigned char *L, const Tile t, int w, v2u shk, u32 dvr) {
  STAMP_DECL;
  STAMP(0);
  const int i = t.i, j = t.j, rh = w >> 1, ch = w & 1;
  int l = threadIdx.x & 63, g = l >> 4, c = l & 15;
  u64 tag = epoch_load(p.ws);
  const rsrc_t rws = mk(p.ws, WS_BYTES);
  v4u xf[2][14];
  f4 acc[2][4], cs[2];
  int rb[4];
  v2u xe[4];
  auto ldx = [&]() {
    const rsrc_t rx = mk(p.x, S_ * D_ * 2);
#pragma unroll
    for (int q = 0; q < 4; ++q) xe[q] = __builtin_amdgcn_raw_buffer_load_b64(rx, (64 * i + 16 * w + c) * 896 + j * 128 + g * 8 + q * 32, 0, 0);
    SB();
  };
  auto clz = [&]() {
    const int o = WS_ZB + (j * 64 + 2 * i) * 8192 + w * 4096 + l * 16;
#pragma unroll
    for (int q = 0; q < 4; ++q) st16c(v4u{~0u, ~0u, ~0u, ~0u}, rws, o, q * 1024);
    SB();
  };
  if (HZ && CLZ == -1) clz();
  v4u ones = {0x3f803f80u, 0x3f803f80u, 0x3f803f80u, 0x3f803f80u};
  auto start = [&]() {
    const int rdoff = (8 * g + (c >> 2)) * 256 + ((((c >> 2) | ((g & 1) << 2)) ^ (4 * ch + 2 * rh)) << 5) + (c & 3) * 8;
#pragma unroll
    for (int n = 0; n < 4; ++n) { rb[n] = rdoff ^ (n << 5); PIN(rb[n]); }
    PIN(ones);
  };
  int xo0 = L_XR + (32 * rh + c) * 128 + ((g ^ (c & 7)) << 4), xo1 = xo0 ^ 64;
  int lno = L_RV + (16 * w + (l >> 3)) * 144 + (l & 7) * 16, lso = WS_KVR + (64 * j + 16 * w + (l >> 3)) * 4096 + (31 - i) * 128 + (l & 7) * 16;
  {
    __builtin_amdgcn_s_setprio(PC0);
    SB();
    if (threadIdx.x < 16) *(LAS u32 *)(L + L_CNT + threadIdx.x * 4) = 0;
    int ro = L_KR + w * 5120 + (c >> 2) * 128 + ((g ^ ((c >> 2) & 2)) << 5) + (c & 3) * 8;
    int wo = L_RV + l * 144 + (3 - w) * 32, ko = L_KD + l * 128 + ((w ^ ((c >> 1) & 3)) << 5);
    start();
    PIN(xo0); PIN(xo1); PIN(lno); PIN(lso); PIN(ro); PIN(wo); PIN(ko);
    f2 o[8];
#pragma unroll
    for (int q = 0; q < 8; ++q) { o[q] = f2{0, 0}; PIN(o[q]); }
    EPOCH_WAIT(tag);
    tag += 1;
    asm volatile("s_waitcnt vmcnt(%2)" : "+v"(shk), "+v"(dvr) : "n"(HZ && CLZ == -1 ? 4 : 0) : "memory");
    STAMP(1);
    v2u kr[5], vr[4];
#pragma unroll
    for (int q = 0; q < 5; ++q) kr[q] = trd(L, ro + 512 + q * 512);
#pragma unroll
    for (int q = 0; q < 4; ++q) vr[q] = trd(L, ro + 3072 + q * 512);
    SB();
    const u32 ta = __builtin_amdgcn_alignbit(shk.y, shk.y, 16), tb = __builtin_amdgcn_alignbit(shk.x, shk.x, 16);
    const float dd = lo16(dvr);
    u32 ke[10], kq[9];
#pragma unroll
    for (int q = 0; q < 5; ++q) { ke[2 * q] = kr[q].x; ke[2 * q + 1] = kr[q].y; }
#pragma unroll
    for (int q = 0; q < 9; ++q) kq[q] = __builtin_amdgcn_alignbit(ke[q + 1], ke[q], 16);
#pragma unroll
    for (int q = 0; q < 8; ++q) o[q] = f2{dot2(kq[q], ta, o[q].x), dot2(ke[q + 1], ta, o[q].y)};
    SB();
#pragma unroll
    for (int q = 0; q < 8; ++q) o[q] = f2{dot2(kq[q + 1], tb, o[q].x), dot2(ke[q + 2], tb, o[q].y)};
    SB();
#pragma unroll
    for (int q = 0; q < 8; ++q) { const u32 vv = (q & 1) ? vr[q >> 1].y : vr[q >> 1].x; o[q] *= f2{lo16(vv), hi16(vv)}; }
    u32 rv[8];
#pragma unroll
    for (int q = 0; q < 8; ++q) asm("v_cvt_pk_bf16_f32 %0, %1, %2" : "=v"(rv[q]) : "v"(o[7 - q].y), "v"(o[7 - q].x));
    sts16(L, wo, v4u{rv[0], rv[1], rv[2], rv[3]});
    sts16(L, wo + 16, v4u{rv[4], rv[5], rv[6], rv[7]});
    SB();
#pragma unroll
    for (int q = 0; q < 8; ++q) rv[q] = pk2(o[q] * dd);
    sts16(L, ko, v4u{rv[0], rv[1], rv[2], rv[3]});
    sts16(L, ko + 16, v4u{rv[4], rv[5], rv[6], rv[7]});
  }
  STAMP(2);

  v2u wh[2][8];
  auto rd1 = [&](int ks, int q) {
    if (q < 2) xf[q][ks] = lds16(L, ((ks & 1) ? xo1 : xo0) + ((ks >> 1) & 3) * 8192 + q * 2048);
    else {
      const int n = (q - 2) >> 1;
      if (ks == 7 && !(q & 1)) { rb[n] += 57344; PIN(rb[n]); }
      wh[ks & 1][q - 2] = trd(L, rb[n] + (ks % 7) * 8192 + (q & 1) * 1024);
    }
  };
  auto wfr = [&](int ks, int n) { const v2u a = wh[ks & 1][2 * n], b = wh[ks & 1][2 * n + 1]; return v4u{a.x, a.y, b.x, b.y}; };
  auto mm1 = [&](auto ksc, int q) {
    constexpr int ks = ksc;
    if (q < 8) mmaf<ks == 0>(acc[q & 1][q >> 1], xf[q & 1][ks], wfr(ks, q >> 1));
    else mmaf<ks == 0>(cs[q - 8], ones, wfr(ks, q - 8));
  };
  auto step = [&](auto ksc, int q0) {
#pragma unroll
    for (int q = 0; q < 10; ++q) { mm1(ksc, q); SB(); if (q + q0 < 10) rd1(ksc + 1, q + q0); SB(); }
  };
  auto put_cs = [&](int off) {
    const int o = L_CS + off + ((4 * ch + 2 * rh) * 16 + c) * 4;
    *(LAS float *)(L + o) = cs[0].x;
    *(LAS float *)(L + o + 64) = cs[1].x;
  };
  auto lastk = [&](int off) {
    mm1(std::integral_constant<int, 13>{}, 8);
    mm1(std::integral_constant<int, 13>{}, 9);
#pragma unroll
    for (int q = 0; q < 4; ++q) mm1(std::integral_constant<int, 13>{}, q);
    SB();
    asm volatile("s_nop 3" ::: "memory");
    SB();
    put_cs(off);
    SB();
#pragma unroll
    for (int q = 4; q < 8; ++q) mm1(std::integral_constant<int, 13>{}, q);
    SB();
  };
  STAMPA(1);
  __builtin_amdgcn_s_setprio(PC1);
  BARW();
  STAMPA(2);
  {
    v4u ln[2];
    ln[0] = lds16(L, lno);
    ln[1] = lds16(L, lno + 1152);
#pragma unroll
    for (int q = 0; q < 10; ++q) rd1(0, q);
    SB();
    st16c(ln[0], rws, lso, 0);
    st16c(ln[1], rws, lso, 32768);
    SB();
  }
  unr<13>([&](auto ksc) {
    constexpr int ks = ksc;
    if constexpr (ks & 1) {
      STAMPA(ks + 2);
      SB(); asm volatile("s_waitcnt lgkmcnt(8)\n\ts_barrier" ::: "memory"); SB();
      STAMPA(ks + 3);
      if (ks == KVF_AT) {
        DRAIN();
        setflagk(p.ws, F_KV + (4 * j + w) * 32 + i, tag);
        SB();
      }
    }
    if constexpr (HZ && ks == CLZ) clz();
    step(ksc, 0);
  });
  if (HZ && CLZ == 13) clz();
  lastk(0);
  if (HZ && CLZ == 14) clz();
  BARW();
  SETTLE();
  STAMP(6);
  FRESH();
  const u32 tagA = (u32)tag, tagB = (u32)(tag >> 32) ^ 0x9e3779b9u;
  f4 mm[2], rr[2];
  float cv[4];
  auto act_in = [&]() {
#pragma unroll
    for (int m = 0; m < 2; ++m) {
      mm[m] = ldsf4(L, L_XST + (32 * rh + 8 * g + 4 * m) * 4);
      rr[m] = ldsf4(L, L_XST + 256 + (32 * rh + 8 * g + 4 * m) * 4);
    }
    cv[0] = -cs[0].x;
    cv[1] = -cs[1].x;
  };
  auto act_in2 = [&](int off) {
    const int o = L_CS + off + ((4 * ch + (2 - 2 * rh)) * 16 + c) * 4;
    cv[2] = -*(LAS float *)(L + o);
    cv[3] = -*(LAS float *)(L + o + 64);
  };
  auto act = [&](int n, f2 (&z)[4]) {
    f2 x[4], a[4];
    const f2 c2 = {cv[n], cv[n]};
    SB();
#pragma unroll
    for (int m = 0; m < 2; ++m) {
      x[2 * m] = fma2(lo2(mm[m]), c2, lo2(acc[m][n]));
      x[2 * m + 1] = fma2(hi2(mm[m]), c2, hi2(acc[m][n]));
    }
    SB();
#pragma unroll
    for (int m = 0; m < 2; ++m) { x[2 * m] *= lo2(rr[m]); x[2 * m + 1] *= hi2(rr[m]); }
    SB();
#pragma unroll
    for (int q = 0; q < 4; ++q) a[q] = x[q] * x[q];
    SB();
#pragma unroll
    for (int q = 0; q < 4; ++q) a[q] = fma2(a[q], f2{-0.1029432397f, -0.1029432397f}, f2{-2.302208198f, -2.302208198f});
    SB();
#pragma unroll
    for (int q = 0; q < 4; ++q) a[q] *= x[q];
    SB();
#pragma unroll
    for (int q = 0; q < 4; ++q) a[q] = f2{__builtin_amdgcn_exp2f(a[q].x), __builtin_amdgcn_exp2f(a[q].y)};
    SB();
#pragma unroll
    for (int q = 0; q < 4; ++q) a[q] += 1.0f;
    SB();
#pragma unroll
    for (int q = 0; q < 4; ++q) a[q] = f2{__builtin_amdgcn_rcpf(a[q].x), __builtin_amdgcn_rcpf(a[q].y)};
    SB();
#pragma unroll
    for (int q = 0; q < 4; ++q) z[q] = x[q] * a[q];
    SB();
  };
  u32 seen;
  {
    act_in();
    act_in2(0);
    f2 s[4], u[4];
    v4u zp[4];
#pragma unroll
    for (int n = 0; n < 4; ++n) {
      f2 z[4];
      act(n, z);
#pragma unroll
      for (int q = 0; q < 4; ++q) {
        s[q] = n ? s[q] + z[q] : z[q];
        u[q] = n ? fma2(z[q], z[q], u[q]) : z[q] * z[q];
      }
      zp[n] = v4u{pk2(z[0]), pk2(z[1]), pk2(z[2]), pk2(z[3])};
    }
    seen = lds_peek(L, L_CNT);
    float sa[8], sq[8];
#pragma unroll
    for (int q = 0; q < 4; ++q) { sa[2 * q] = s[q].x; sa[2 * q + 1] = s[q].y; sq[2 * q] = u[q].x; sq[2 * q + 1] = u[q].y; }
    rowsum8(sa, sq);
    const int row = 8 * g + (c & 1) + ((c >> 1) & 6);
    if ((c & 2) == 0)
      st16c(v4u{asu((c & 1) ? sa[1] : sa[0]), tagA, asu((c & 1) ? sq[1] : sq[0]), tagB}, rws, ((i * 2 + rh) * 16 + 2 * j + ch) * 512 + row * 16, WS_ST);
    SB();
    const int zo = L_Z2 + (rh * 8 + 4 * ch) * 1024 + l * 16;
#pragma unroll
    for (int n = 0; n < 4; ++n) sts16(L, zo + ((n & 1) << 10) + (((n >> 1) ^ rh) << 11), zp[n]);
    SB();
    if (HZ && CLZ < 15) VM(1);
    if (l == 0) __hip_atomic_fetch_add((LAS u32 *)(L + L_CNT + 4), 1u, __ATOMIC_RELAXED, __HIP_MEMORY_SCOPE_WORKGROUP);
    SB();
    if (HZ && CLZ == 15) clz();
    lds_check(L, L_CNT, 4, seen);
#pragma unroll
    for (int n = 0; n < 4; ++n) { rb[n] -= 57344; PIN(rb[n]); }
#pragma unroll
    for (int q = 2; q < 10; ++q) rd1(0, q);
    SB();
  }
  SB();
  unr<13>([&](auto ksc) {
    if constexpr (HZ && CLZ == 15 && ksc == CLA) {
      VM(0);
      if (l == 0) __hip_atomic_fetch_add((LAS u32 *)(L + L_CNT + 52), 1u, __ATOMIC_RELAXED, __HIP_MEMORY_SCOPE_WORKGROUP);
    }
    if constexpr (ksc == 6) seen = lds_peek(L, L_CNT + 28);
    if constexpr (ksc == 7) lds_check(L, L_CNT + 28, 4, seen);
    SB();
    step(ksc, 2);
  });
  lastk(512);
  lds_signal(L, L_CNT + 8);
  SETTLE();
  FRESH();
  u32 bias[4];
  {
    act_in();
    const int po = L_Z1 + (64 * ch + c) * 128 + (((4 * rh + g) ^ (c & 7)) << 4);
#pragma unroll
    for (int n = 0; n < 4; ++n) {
      f2 z[4];
      if (n == 1) seen = lds_peek(L, L_CNT + 8);
      if (n == 2) {
        lds_check(L, L_CNT + 8, 4, seen);
        act_in2(512);
#pragma unroll
        for (int q = 0; q < 4; ++q) bias[q] = *(LAS u16 *)(L + L_BS + (16 * q + c) * 2);
      }
      if (n == 3) seen = lds_peek(L, L_CNT + 16);
      act(n, z);
      sts16(L, po + ((n & 1) << 11) + (((n >> 1) ^ rh) << 12), v4u{pk2(z[0]), pk2(z[1]), pk2(z[2]), pk2(z[3])});
    }
  }
  STAMP(8);
  FRESH();
  f4 g2[4][2];
  float z1[32];
  bool last;
  int uo[4], ob[4], uw = L_Z1 + w * 4096 + l * 16;
#pragma unroll
  for (int n = 0; n < 4; ++n) { ob[n] = ((4 * g + (c >> 2)) * 128 + ((((c >> 3) & 1) | ((g & 1) << 1)) << 5) + (c & 3) * 8) ^ (n << 5); PIN(ob[n]); }
#pragma unroll
  for (int m = 0; m < 4; ++m) { uo[m] = WS_UB + (i * 112 + 4 * j + w) * 1024 + l * 16 + m * 28672; PIN(uo[m]); }
  {
    v4u bf[8][2], af[4][4];
    const rsrc_t rz = mk(p.ws + WS_ZB + j * 524288, 524288);
    int bvo[2], bso = t.rot2 * 16384;
    bvo[0] = w * 2048 + l * 16;
    bvo[1] = bvo[0] + 8192;
    PIN(bvo[0]); PIN(bvo[1]);
    auto ldb = [&](int ks, int b, int so) { bf[ks & 7][b] = ld16c(rz, bvo[ks & 1] + b * 1024, so); };
#define BMASK 0x7ffff
    auto next = [&](int &so) { asm volatile("s_add_u32 %0, %0, 0x4000\n\ts_and_b32 %0, %0, %1" : "+s"(so) : "n"(BMASK) : "scc"); };
#pragma unroll
    for (int m = 0; m < 4; ++m) { const float b = lo16(bias[m]); g2[m][0] = g2[m][1] = f4{b, b, b, b}; PIN(g2[m][0]); PIN(g2[m][1]); }
    int ab[2];
    ab[0] = c * 128 + ((g ^ (c >> 1)) << 4);
    ab[1] = ab[0] ^ 64;
    PIN(ab[0]); PIN(ab[1]);
    auto rda = [&](int ks, int m) { af[ks & 3][m] = *(LAS v4u *)((LAS unsigned char *)(u32)ab[ks & 1] + ((ks >> 1) & 3) * 8192 + m * 2048); };
    auto turn = [&](int q) { ab[q] = (ab[q] + 32768) & 0x1ffff; PIN(ab[q]); };
    const int f0 = 4 * g + (c >> 2);
    int zr[4];
#pragma unroll
    for (int e = 0; e < 4; ++e) { zr[e] = (L_Z1 + (32 * w + f0) * 128 + (c & 1) * 8 + ((((c >> 1) & 1) ^ (f0 & 7)) << 4)) ^ (e << 5); PIN(zr[e]); }
    v2u zq[8];
    PIN(uw);
    if (ZP < 0 && w == 0) {
      lds_wait(L, L_CNT + 24, 4);
      __builtin_amdgcn_s_sleep(ZSL);
      const int fo = fl(F_Z + j * 32 + (l & 31));
#pragma unroll 1
      for (u32 s = 0; s < SPIN_CAP; ++s) {
        if (__all(ldflag(p.ws, fo) == tag)) break;
        __builtin_amdgcn_s_sleep(ZSL2);
      }
      asm volatile("" ::: "memory");
      lds_signal(L, L_CNT + 16);
    } else lds_check(L, L_CNT + 16, 1, seen);
    STAMP(9);
    if (TD && TC == 0) { ztouch(p, t, 1 + (w & 1), w >> 1, l); if (TD > 3 && !(w & 1)) ztouch(p, t, 3, w >> 1, l); SB(); }
#pragma unroll
    for (int ks = 0; ks < 7; ++ks) { ldb(ks, 0, bso); SB(); ldb(ks, 1, bso); SB(); if (ks & 1) next(bso); SB(); }
    if (TD && TC == 1) { ztouch(p, t, 1 + (w & 1), w >> 1, l); if (TD > 3 && !(w & 1)) ztouch(p, t, 3, w >> 1, l); SB(); }
    lds_wait(L, L_CNT + 12, 4);
    __builtin_amdgcn_s_setprio(3);
    STAMP(10);
#pragma unroll
    for (int ks = 0; ks < AD; ++ks)
#pragma unroll
      for (int m = 0; m < 4; ++m) rda(ks, m);
    SB();
    STAMP(5);
    auto wide = [&](int e) {
      const u32 z = (e & 2) ? zq[e >> 2].y : zq[e >> 2].x;
      if (e & 1) asm volatile("v_and_b32 %0, 0xffff0000, %1" : "=v"(z1[e]) : "v"(z));
      else asm volatile("v_lshlrev_b32 %0, 16, %1" : "=v"(z1[e]) : "v"(z));
    };
    auto chunk = [&](auto lastc) {
      constexpr bool LAST = lastc;
      unr<(LAST ? 6 : 8)>([&](auto sc) {
        constexpr int s = sc;
        WAITC(LAST && s ? 14 - 2 * s : 12, LAST && s == 2 ? 9 : 4);
        SB();
#pragma unroll
        for (int q = 0; q < 8; ++q) {
          mma(g2[q >> 1][q & 1], bf[s][q & 1], af[s & 3][q >> 1]);
          SB();
#define NOA 0
#define NOB 0
          if (!(q & 1) && (!NOA || LAST)) rda(s + 2, q >> 1);
          if ((q == 1 || q == 3) && (!LAST || s == 0) && (!NOB || LAST)) ldb(s + 7, q >> 1, bso);
          if (q == 5 && !(s & 1) && !LAST) next(bso);
          if (!LAST && q == (s == 5 ? 5 : 7) && (s == 5 || s == 6)) turn(~s & 1);
          if (LAST && s == 1 && (q & 1)) { zq[q - 1] = trd((LAS unsigned char *)(u32)zr[q >> 1], 0); zq[q] = trd((LAS unsigned char *)(u32)zr[q >> 1], 2048); }
          if (LAST && s >= 2 && (q & 1)) { wide(8 * (s - 2) + q - 1); wide(8 * (s - 2) + q); }
          SB();
        }
      });
      if (!LAST) BAR0();
    };
#pragma unroll 1
    for (int ck = 0; ck < 7; ++ck) {
      chunk(std::false_type{});
    }
    chunk(std::true_type{});
    u32 o[4][4];
    auto scale = [&](int m, int e) { asm volatile("v_mul_f32 %0, %0, %2\n\tv_mul_f32 %1, %1, %3" : "+v"(z1[8 * m + 2 * e]), "+v"(z1[8 * m + 2 * e + 1]) : "v"(g2[m][e >> 1][(2 * e) & 3]), "v"(g2[m][e >> 1][(2 * e + 1) & 3])); };
    auto pack = [&](int m, int e) {
      asm volatile("v_cvt_pk_bf16_f32 %0, %2, %3\n\tv_cvt_pk_bf16_f32 %1, %4, %5" : "=&v"(o[m][e]), "=&v"(o[m][e + 1])
                   : "v"(z1[8 * m + 2 * e]), "v"(z1[8 * m + 2 * e + 1]), "v"(z1[8 * m + 2 * e + 2]), "v"(z1[8 * m + 2 * e + 3]));
    };
    auto send = [&](int m) { st16c(v4u{o[m][0], o[m][1], o[m][2], o[m][3]}, rws, uo[m], 0); };
    auto keep = [&](int m) { sts16((LAS unsigned char *)(u32)uw, m * 1024, v4u{o[m][0], o[m][1], o[m][2], o[m][3]}); };
    auto two = [&](int s, int m, auto f0, auto f1) {
      mma(g2[m][0], bf[s][0], af[s & 3][m]); SB(); f0(); SB();
      mma(g2[m][1], bf[s][1], af[s & 3][m]); SB(); f1(); SB();
    };
    auto none = []() {};
    WAITC(2, 4);
    SB();
    two(6, 0, none, none);
    two(6, 1, none, [&]() { seen = lds_peek(L, L_CNT + 44); });
    WAITC(0, 1);
    SB();
    two(7, 0, none, none);
    two(6, 2, none, none);
    two(7, 1, none, none);
    two(6, 3, none, [&]() { asm volatile("s_nop 0"); scale(0, 0); scale(0, 1); });
    two(7, 2, [&]() { scale(0, 2); scale(0, 3); }, [&]() { pack(0, 0); pack(0, 2); });
    two(7, 3, [&]() { send(0); scale(1, 0); scale(1, 1); }, [&]() { keep(0); scale(1, 2); scale(1, 3); });
    STAMP(11);
    pack(1, 0); pack(1, 2);
    SB();
    send(1);
    keep(1);
    SB();
#pragma unroll
    for (int e = 0; e < 4; ++e) o[2][e] = pk2(f2{z1[16 + 2 * e], z1[17 + 2 * e]} * ((e & 1) ? hi2(g2[2][e >> 1]) : lo2(g2[2][e >> 1])));
    send(2);
    keep(2);
    SB();
#pragma unroll
    for (int e = 0; e < 4; ++e) o[3][e] = pk2(f2{z1[24 + 2 * e], z1[25 + 2 * e]} * ((e & 1) ? hi2(g2[3][e >> 1]) : lo2(g2[3][e >> 1])));
    send(3);
    keep(3);
    SB();
    u32 n36 = 0;
    if (l == 0) n36 = __hip_atomic_fetch_add((LAS u32 *)(L + L_CNT + 36), 1u, __ATOMIC_RELAXED, __HIP_MEMORY_SCOPE_WORKGROUP);
    SB();
    const f4 t4 = g2[0][0] + g2[0][1];
    const f2 t2 = lo2(t4) + hi2(t4);
    const bool bad = __builtin_amdgcn_ballot_w64(__builtin_isunordered(t2.x, t2.y)) != 0;
    SB();
    last = __builtin_amdgcn_readfirstlane(n36) == 3;
    if (HU && last) setflag(p.ws, F_U + i * 8 + j, tag);
    SB();
    if (HZ && __builtin_expect(bad, 0)) {
      FRESH();
      wait_flags(p.ws, fl(F_ZD + j * 32 + (l & 31)), tag);
      __builtin_amdgcn_fence(__ATOMIC_ACQUIRE, "agent");
      const rsrc_t ra = mk(p.wsp, S_ * S_ * 2);
      f4 h[4][2];
#pragma unroll
      for (int m = 0; m < 4; ++m) { const float b = lo16(*(LAS u16 *)(L + L_BS + (16 * m + c) * 2)); h[m][0] = h[m][1] = f4{b, b, b, b}; }
#pragma unroll 1
      for (int ks = 0; ks < 64; ++ks) {
        const v4u b0 = ld16c(rz, w * 2048 + l * 16, ks * 8192), b1 = ld16c(rz, w * 2048 + l * 16 + 1024, ks * 8192);
#pragma unroll
        for (int m = 0; m < 4; ++m) {
          const v4u a = ld16(ra, (64 * i + 16 * m + c) * 4096 + g * 16, ks * 64);
          h[m][0] = mfma(b0, a, h[m][0]);
          h[m][1] = mfma(b1, a, h[m][1]);
        }
      }
#pragma unroll
      for (int m = 0; m < 4; ++m) {
#pragma unroll
        for (int e = 0; e < 4; ++e) {
          const u32 z = (e & 1) ? zq[2 * m + (e >> 1)].y : zq[2 * m + (e >> 1)].x;
          o[m][e] = pk2(f2{lo16(z), hi16(z)} * ((e & 1) ? hi2(h[m][e >> 1]) : lo2(h[m][e >> 1])));
        }
        send(m);
      }
    }
  }
  SB();
  STAMP(7);
  {
    v4u uf[28], of[OP][4];
    int uso[7];
#pragma unroll
    for (int q = 1; q < 7; ++q) uso[q] = WS_UB + (i * 4 + w) * 28672 + mod7(q + j) * 4096;
    int uvo, yo, xo, oo, xr;
    auto ldu = [&](int ks) { uf[ks] = ld16c(rws, uvo + (ks & 3) * 1024, uso[ks >> 2]); };
    f4 o[4];
    auto rd = [&](int ks, int n) {
      if (ks == 14) { ob[n] += 57344; PIN(ob[n]); }
      of[ks % OP][n] = trd2((LAS unsigned char *)(u32)ob[n], (ks % 14) * 4096, 2048);
    };
    float xv[16], xn[16], kn[16], mr, rs;
    v2u kd[4];
    auto prep = [&](int q) {
      const int e = q >> 1;
      const u32 a = (e & 2) ? xe[e >> 2].y : xe[e >> 2].x, b = (e & 2) ? kd[e >> 2].y : kd[e >> 2].x;
      if (q & 1) asm volatile("v_fma_f32 %0, %2, %3, %4\n\tv_mul_f32 %1, %1, %0" : "=&v"(xn[e]), "+v"(kn[e]) : "v"(xv[e]), "v"(rs), "v"(mr));
      else if (e & 1) asm volatile("v_and_b32 %0, 0xffff0000, %2\n\tv_and_b32 %1, 0xffff0000, %3" : "=&v"(xv[e]), "=&v"(kn[e]) : "v"(a), "v"(b));
      else asm volatile("v_lshlrev_b32 %0, 16, %2\n\tv_lshlrev_b32 %1, 16, %3" : "=&v"(xv[e]), "=&v"(kn[e]) : "v"(a), "v"(b));
    };
    auto step = [&](auto ksc) {
      constexpr int ks = ksc;
#pragma unroll
      for (int n = 0; n < 4; ++n) {
        mmaf<ks == 0>(o[n], of[ks % OP][n], uf[ks]);
        SB();
        if (ks + OP < 28) rd(ks + OP, n);
        if (XAT == 4 && ks == 16) xe[n] = *(LAS v2u *)(L + (xr ^ (n << 5)));
        if (ks == 17) kd[n] = trd(L, yo + (L_KD - L_Z2) + n * 2048);
        if (ks == 17 && n == 3) { mr = *(LAS float *)(L + xo); rs = *(LAS float *)(L + xo + 256); }
        if (ks == 19 && n == 2) { mr = -mr * rs; PIN(mr); }
        if (ks >= 19 && ks < 27 && n < 2) prep(4 * (ks - 19) + 2 * n);
        if (ks >= 20 && n >= 2) prep(4 * (ks - 20) + 2 * n - 3);
        SB();
      }
    };
    lds_check(L, L_CNT + 44, 4, seen);
    STAMP(3);
#pragma unroll
    for (int q = 0; q < 4 * K1; ++q) rd(q >> 2, q & 3);
    SB();
    STAMP(4);
    if (!HU) {
      VM(0);
      last = lds_last(L, L_CNT + 20);
      if (last) setflag(p.ws, F_U + i * 8 + j, tag);
    }
    SB();
    STAMP(1);
    lds_wait(L, L_CNT + 36, 4);
    {
      const int ur = uw - w * 3072;
#pragma unroll
      for (int ks = 0; ks < 4; ++ks) uf[ks] = lds16((LAS unsigned char *)(u32)ur, ks * 4096);
    }
    seen = lds_peek(L, L_CNT + 40);
    SB();
    FRESH();
    uvo = l * 16;
    yo = L_Z2 + (ob[0] ^ (w << 5));
    xo = L_XST + (16 * w + c) * 4;
    oo = (64 * i + 16 * w + c) * 896 + j * 128 + (g & 1) * 32 + (g >> 1) * 16;
    xr = L_Z2 + 8192 + (16 * w + c) * 128 + (((g >> 1) ^ (c >> 1)) << 4) + (g & 1) * 8;
    int ufo = fl(F_U + i * 8 + (l < 7 ? l : 0));
    PIN(uvo); PIN(yo); PIN(xo); PIN(oo); PIN(xr); PIN(ufo);
    lds_check(L, L_CNT + 40, 4, seen);
#pragma unroll
    for (int q = 4 * K1; q < 4 * OP; ++q) rd(q >> 2, q & 3);
    SB();
    unr<UPOS>(step);
    u64 fv = 0;
    if (last) fv = ldflag(p.ws, ufo);
    SB();
    unr<4 - UPOS>([&](auto q) { step(std::integral_constant<int, UPOS + q>{}); });
    STAMP(2);
    if (last) {
      if (!__all(fv == tag)) wait_flags(p.ws, ufo, tag);
      lds_signal(L, L_CNT + 32);
    } else lds_wait(L, L_CNT + 32, 1);
    VM(0);
    STAMP(12);
#pragma unroll
    for (int ks = 4; ks < 4 + UW; ++ks) { ldu(ks); SB(); }
    BAR0();
    unr<24>([&](auto qc) {
      constexpr int ks = qc + 4;
      WAITC(ks < 28 - UW ? UW - 1 : 27 - ks, ks == 19 ? 8 : ks == 26 ? 0 : 15);
      SB();
      step(std::integral_constant<int, ks>{});
      if (ks + UW < 28) ldu(ks + UW);
      SB();
    });
    STAMP(13);
    SETTLE();
    const bool bad = __builtin_amdgcn_ballot_w64(__builtin_isunordered(o[3].x, o[3].x)) != 0;
    if ((HZ || HU) && __builtin_expect(bad, 0)) {
      wait_flags(p.ws, ufo + (F_UD - F_U) * FS, tag);
      __builtin_amdgcn_fence(__ATOMIC_ACQUIRE, "agent");
#pragma unroll
      for (int n = 0; n < 4; ++n) o[n] = f4{0, 0, 0, 0};
#pragma unroll 1
      for (int ks = 0; ks < 28; ++ks) {
        const v4u u = ld16c(rws, uvo + (ks & 3) * 1024, WS_UB + (i * 4 + w) * 28672 + mod7((ks >> 2) + j) * 4096);
#pragma unroll
        for (int n = 0; n < 4; ++n) o[n] = mfma(trd2((LAS unsigned char *)(u32)(ob[n] - 57344 + ks * 4096), 0, 2048), u, o[n]);
      }
    }
    v2u y[4], res[4];
    f2 a[8], e[8];
#pragma unroll
    for (int q = 0; q < 8; ++q) { a[q] = (q & 1) ? hi2(o[q >> 1]) : lo2(o[q >> 1]); e[q] = a[q] * -1.442695041f; }
#pragma unroll
    for (int q = 0; q < 8; ++q) e[q] = f2{__builtin_amdgcn_exp2f(e[q].x), __builtin_amdgcn_exp2f(e[q].y)} + 1.0f;
    asm volatile("s_barrier" : "+v"(e[0]), "+v"(e[1]), "+v"(e[2]), "+v"(e[3]), "+v"(e[4]), "+v"(e[5]), "+v"(e[6]), "+v"(e[7])::"memory");
    SB();
#pragma unroll
    for (int n = 0; n < 4; ++n) y[n] = trd(L, yo + n * 2048);
    SB();
#pragma unroll
    for (int q = 0; q < 8; ++q) e[q] = a[q] * f2{__builtin_amdgcn_rcpf(e[q].x), __builtin_amdgcn_rcpf(e[q].y)};
    asm volatile("" : "+v"(e[0]), "+v"(e[1]), "+v"(e[2]), "+v"(e[3]), "+v"(e[4]), "+v"(e[5]), "+v"(e[6]), "+v"(e[7]));
    SB();
    asm volatile("" : "+v"(y[0]), "+v"(y[1]), "+v"(y[2]), "+v"(y[3]));
#pragma unroll
    for (int q = 0; q < 8; ++q) {
      const u32 yy = (q & 1) ? y[q >> 1].y : y[q >> 1].x;
      const f2 tt = fma2(f2{lo16(yy), hi16(yy)}, f2{xn[2 * q], xn[2 * q + 1]}, f2{kn[2 * q], kn[2 * q + 1]});
      const u32 r = pk2(fma2(tt, e[q], f2{xv[2 * q], xv[2 * q + 1]}));
      if (q & 1) res[q >> 1].y = r; else res[q >> 1].x = r;
    }
    const rsrc_t ro = mk(p.out, S_ * D_ * 2);
    st16c(widen(res[0], res[1]), ro, oo, 0);
    st16c(widen(res[2], res[3]), ro, oo, 64);
  }
  STAMP_OUT(0);
}

static __device__ __forceinline__ void gemm_move(const Params &p, LAS unsigned char *L, const Tile t, int m) {
  STAMP_DECL;
  STAMP(0);
  const int i = t.i, j = t.j;
  int l = threadIdx.x & 63, g = l >> 4, c = l & 15;
  const srd_t sx_ = mks(p.x, S_ * D_ * 2), swin = mks(p.win, D_ * F_ * 2), swsp = mks(p.wsp, S_ * S_ * 2);
  const int lb = m * 1024, lb2 = m * 2048;
  const int pvo = (l >> 3) * 896 + j * 128 + (((((l & 7) >> 1) ^ g) << 5) | ((l & 1) << 4));
  const int rsw = ((l & 7) ^ (l >> 3)) << 4;
  const int xvo = (64 * i + 32 * (m >> 1) + 4 * (m & 1) + 8 * (l >> 5) + ((l >> 3) & 3)) * 896 + rsw;
  const int wvo = (4 * m + g) * 3584 + j * 256 + ((((c >> 1) ^ (g | ((m & 2) << 1))) << 5) | ((c & 1) << 4));
  int sb[7];
#pragma unroll
  for (int s = 0; s < 7; ++s) sb[s] = mod7(s + t.rot1);
  auto item = [&](auto nc) {
    constexpr int n = nc, s = n / 6, q = n % 6;
    if constexpr (n >= 42) dma<(n - 42) * 4096>(swin, wvo, (sb[(n - 42) >> 2] * 4 + ((n - 42) & 3)) * 57344, lb);
    else if constexpr (q < 2) dman<L_XR + (s & 3) * 8192 + q * 1024, NT_X>(sx_, xvo, sb[s] * 128 + q * 14336, lb2);
    else dma<(4 * s + q - 2) * 4096>(swin, wvo, (sb[s] * 4 + q - 2) * 57344 + 1792, lb);
  };
#define IC(n) std::integral_constant<int, (n)>{}
  const rsrc_t rwsp = mk(p.wsp, S_ * S_ * 2), rws = mk(p.ws, WS_BYTES);
  v4u ar[32];
  const int avo0 = (64 * i + 16 * m + (l >> 3)) * 4096 + (((l & 7) ^ g) << 4), avo1 = avo0 ^ 64;
  auto aso = [&](int n) { return (((n >> 1) + t.rot2) & 31) * 128 + (n & 1) * 32768; };
  auto lda = [&](int n) { const int avo = (n & 1) ? avo1 : avo0; ar[n] = n < 10 ? __builtin_amdgcn_raw_buffer_load_b128(rwsp, avo, aso(32 + n), NT_SP * 2) : ld16(rwsp, avo, aso(32 + n)); SB(); };
  auto items = [&](auto n0, auto cnt) { unr<cnt>([&](auto q) { constexpr int n = ORD.seq[n0 + q]; if constexpr (n < 70) item(IC(n)); else lda(n - 70); }); };
  constexpr const int *NI = ORD.bar;
  items(IC(0), IC(NI[0]));
  u64 tag = epoch_load(p.ws);
  EPOCH_WAIT(tag);
  tag += 1;
  {
    const v4u ones = {0x3f803f80u, 0x3f803f80u, 0x3f803f80u, 0x3f803f80u};
    f4 sx = {0, 0, 0, 0}, gr = {0, 0, 0, 0};
    int xo0 = L_XR + (16 * m + c) * 128 + ((g ^ (c & 7)) << 4), xo1 = xo0 ^ 64;
    PIN(xo0); PIN(xo1);
    unr<7>([&](auto sc) {
      constexpr int s = sc;
      VM(NI[s] - 1 - ORD.last(6 * s, 6 * s + 6) + (TLG && ORD.last(6 * s, 6 * s + 6) < NI[0]));
      v4u xs[2];
      xs[0] = lds16(L, xo0 + (s & 3) * 8192);
      xs[1] = lds16(L, xo1 + (s & 3) * 8192);
      STAMPA(2 * s + 1);
      BAR0();
      STAMPA(2 * s + 2);
      if constexpr (s < 6) items(IC(NI[s]), IC(NI[s + 1] - NI[s]));
      SB();
      if (PMS) __builtin_amdgcn_s_setprio(3);
#pragma unroll
      for (int q = 0; q < 2; ++q) { sx = mfma(xs[q], ones, sx); gr = mfma(xs[q], xs[q], gr); }
      SB();
      if (PMS) __builtin_amdgcn_s_setprio(1);
      SB();
      if constexpr (s == 6) items(IC(NI[6]), IC(LASTA));
    });
    SB();
    const int e = c & 3;
    const float s1 = e & 2 ? (e & 1 ? sx.w : sx.z) : (e & 1 ? sx.y : sx.x);
    const float s2 = e & 2 ? (e & 1 ? gr.w : gr.z) : (e & 1 ? gr.y : gr.x);
    const float mean = fmul(s1, 1.0f / 448.0f);
    const float r = inv_sigma(ffma(-mean, mean, fmul(s2, 1.0f / 448.0f)));
    if ((c >> 2) == g) {
      const int row = 32 * (m >> 1) + 8 * g + 4 * (m & 1) + e;
      *(LAS float *)(L + L_XST + row * 4) = rbf(mean);
      *(LAS float *)(L + L_XST + 256 + row * 4) = r;
    }
    SB();
    items(IC(NI[6] + LASTA), IC(NI[7] - NI[6] - LASTA));
  }
  BARW();
  STAMP(3);
  asm volatile("s_mov_b32 m0, %3\n\ts_nop 0\n\tbuffer_load_dword %0, %1, %2 offen lds" ::"v"(l * 4), "s"(mks(p.bsp, S_ * 2)), "s"(i * 128), "n"(L_BS) : "m0", "memory");
  unr<70 + NS - NI[7]>([&](auto q) {
    constexpr int n = NI[7] + q;
    VM(W1);
    if constexpr (n == ORD.last(42, 58) + 1 + W1) lds_signal(L, L_CNT);
    if constexpr (n == ORD.last(42, 70) + 1 + W1) lds_signal(L, L_CNT + 28);
    items(IC(n), IC(1));
  });
  static_assert(ORD.last(42, 58) + 1 + W1 < 70 + NS && NI[7] <= ORD.last(42, 58) + 1 + W1, "");
  STAMP(4);
  {
    const u32 tagA = (u32)tag, tagB = (u32)(tag >> 32) ^ 0x9e3779b9u;
    const int so = WS_ST + (i * 2 + (m >> 1)) * 8192 + (m & 1) * 256;
    const int vo = g * 512 + c * 16, vo3 = (g & 1) * 512 + c * 16 + 2048;
    v4u sg[4], zt[4];
    auto ldst = [&]() {
      sg[0] = ld16c(rws, vo, so); sg[1] = ld16c(rws, vo + 2048, so); sg[2] = ld16c(rws, vo, so + 4096); sg[3] = ld16c(rws, vo3, so + 4096);
    };
    lds_wait(L, L_CNT + 4, 4);
    __builtin_amdgcn_s_setprio(PM2);
    const int zo = ((m >> 1) * 8 + (l >> 5)) * 1024 + ((m & 1) * 32 + (l & 31)) * 16;
#pragma unroll
    for (int q = 0; q < 4; ++q) zt[q] = lds16(L, L_Z2 + zo + q * 2048);
    SB();
    if (PW < 60) VM(PW);
    ldst();
    SB();
#pragma unroll
    for (int n = 0; n < NB; ++n) lda(NS + n);
    SB();
    if constexpr (ORD.last(42, 70) + 1 + W1 >= 70 + NS) { VM(69 + NS - ORD.last(42, 70) + 4 + NB); lds_signal(L, L_CNT + 28); }
    SB();
    float zu[32];
#pragma unroll
    for (int q = 0; q < 16; ++q) { const u32 z = zt[q >> 2][q & 3]; zu[2 * q] = lo16(z); zu[2 * q + 1] = hi16(z); PIN(zu[2 * q]); PIN(zu[2 * q + 1]); }
    int zso = WS_ZB + (j * 64 + 2 * i) * 8192 + zo;
    PIN(zso);
    STAMP(5);
    auto late = [&]() {
      u32 d = 0;
#pragma unroll
      for (int q = 0; q < 4; ++q) d |= (sg[q].y ^ tagA) | (sg[q].w ^ tagB);
      return __builtin_amdgcn_ballot_w64(d != 0) != 0;
    };
    if (__builtin_expect(late(), 0)) {
#pragma unroll 1
      for (u32 s = 0; s < SPIN_CAP; ++s) {
        asm volatile("" ::: "memory");
        ldst();
        if (!late()) break;
      }
    }
    STAMP(2);
    float s = fadd(fadd(asf(sg[0].x), asf(sg[1].x)), fadd(asf(sg[2].x), g < 2 ? asf(sg[3].x) : 0.f));
    float u = fadd(fadd(asf(sg[0].z), asf(sg[1].z)), fadd(asf(sg[2].z), g < 2 ? asf(sg[3].z) : 0.f));
    {
      v2u a = __builtin_amdgcn_permlane16_swap(asu(s), asu(u), false, false);
      const float h = fadd(asf(a.x), asf(a.y));
      a = __builtin_amdgcn_permlane32_swap(asu(h), asu(h), false, false);
      const float k = fadd(asf(a.x), asf(a.y));
      a = __builtin_amdgcn_permlane16_swap(asu(k), asu(k), false, false);
      s = asf(a.x);
      u = asf(a.y);
    }
    const float mean = fmul(s, 1.0f / 896.0f);
    const float r = inv_sigma(ffma(-mean, mean, fmul(u, 1.0f / 896.0f)));
    if (g < 2) *(LAS float *)(L + L_ZST + g * 256 + (16 * m + c) * 4) = g ? r : -rbf(mean) * r;
    SB();
    const u32 cl = HZ && CLZ == 15 ? lds_peek(L, L_CNT + 52) : 0;
    f4 zb[2], zr[2];
#pragma unroll
    for (int q = 0; q < 2; ++q) {
      zb[q] = ldsf4(L, L_ZST + (16 * m + 8 * (g & 1) + 4 * q) * 4);
      zr[q] = ldsf4(L, L_ZST + 256 + (16 * m + 8 * (g & 1) + 4 * q) * 4);
    }
    SB();
#pragma unroll
    for (int n = 0; n < NB2; ++n) lda(NS + NB + n);
    if (HZ && CLZ == 15) lds_check(L, L_CNT + 52, 4, cl);
#pragma unroll
    for (int q = 0; q < 4; ++q) {
      u32 o[4];
#pragma unroll
      for (int e = 0; e < 4; ++e) {
        o[e] = pk(ffma(zu[8 * q + 2 * e], zr[e >> 1][(2 * e) & 3], zb[e >> 1][(2 * e) & 3]), ffma(zu[8 * q + 2 * e + 1], zr[e >> 1][(2 * e + 1) & 3], zb[e >> 1][(2 * e + 1) & 3]));
      }
      st16c(v4u{o[0], o[1], o[2], o[3]}, rws, zso, q * 2048);
      SB();
    }
    if (HZ) if (lds_last(L, L_CNT + 24)) setflag(p.ws, F_Z + j * 32 + i, tag);
    STAMP(6);
  }
  __builtin_amdgcn_s_setprio(PM3);
  constexpr int NA = NS + NB + NB2, NQ = 64 - NA;
  auto zflag = [&]() {
    if (!HZ) if (lds_last(L, L_CNT + 24)) setflag(p.ws, F_Z + j * 32 + i, tag);
    if (HZ) if (lds_last(L, L_CNT + 48)) setflag(p.ws, F_ZD + j * 32 + i, tag);
    SB(); STAMP(7);
  };
  auto ring = [&](auto nc) { constexpr int n = nc; dman<(n >> 1) * 8192 + (n & 1) * 1024, NT_RING>(swsp, (n & 1) ? avo1 : avo0, aso(n), lb2); };
  auto piece = [&](auto qc) {
    constexpr int n = qc - NR1;
    if constexpr (n < 0) lda(NA + qc); else if constexpr (n < 32) ring(IC(n < 0 || n > 31 ? 0 : n)); else lda(NA + qc - 32);
  };
  constexpr int XE[4] = {NR1 + X0 < NQ ? NR1 + X0 : NQ, NR1 + X1 < NQ ? NR1 + X1 : NQ, NR1 + X2 < NQ ? NR1 + X2 : NQ, NQ};
  static_assert(X0 >= 14 + 2 * AD && X1 >= 22 + 2 * AD && X2 >= 32 && NA + NR1 >= 19 && NA + NR1 <= 32 && W2 < NR1 + 5 + 2 * AD + RD, "");
  unr<XE[0]>([&](auto qc) {
    constexpr int n = qc - NR1;
    if constexpr (qc == W2) { VM(W2); zflag(); }
    if constexpr (qc > W2 && n < 14 + 2 * AD + RD) VM(RD - 1);
    if constexpr (n == 0) { lds_wait(L, L_CNT + 8, 4); STAMP(8); }
    if constexpr (n == 5 + 2 * AD + RD) lds_signal(L, L_CNT + 12);
    if constexpr (n == 28) lds_wait(L, L_CNT + 24, 4);
    if constexpr (n == ZP) if (m == 0) { wait_flags(p.ws, fl(F_Z + j * 32 + (l & 31)), tag); lds_signal(L, L_CNT + 16); }
    piece(qc);
  });
  STAMP(11);
  __builtin_amdgcn_s_setprio(0);
  const srd_t swout = mks(p.wout, 896 * D_ * 2);
  auto ldt = [&](auto nc) { constexpr int n = nc; dma<n * 4096>(swout, pvo, WSO(n), lb); SB(); __builtin_amdgcn_s_sleep(TS); SB(); };
  static_assert(T5 <= 8 && T6 <= 16 && T5 <= T6 && T6 <= NO0, "");
  v4u wr[28];
  const rsrc_t rwo = mk(p.wout, 896 * D_ * 2);
  auto trk = [&](auto cc) {
    constexpr int tr[7] = TRK, ck = cc;
    int n = NO1;
    for (int q = 0; q < ck; ++q) n += tr[q];
    if (RE == 3)
#pragma unroll
      for (int q = 0; q < tr[ck]; ++q) { if (q) __builtin_amdgcn_s_sleep(TRS); SB(); wr[n + q] = ld16(rwo, pvo, WSO(n + q)); SB(); }
  };
  int awo = m * 2048 + l * 16, awh = awo + 65536;
  PIN(awo); PIN(awh);
  {
    constexpr int cum[6] = {0, 6, 12, 19, 25, 32};
    unr<5>([&](auto kc) {
      constexpr int k = kc;
      if constexpr (k < 2) VM(XE[k] - NR1 - 14 - 2 * AD - 8 * k);
      if constexpr (k == 2) VM(XE[2] - NR1 - 32);
      BARW();
      if constexpr (TD && k + TD < 8) { ztouch(p, t, k + TD, m, l); SB(); }
      if constexpr (HU && k == CLU) {
        const int o = WS_UB + (i * 112 + 4 * j) * 1024 + m * 28672 + l * 16;
#pragma unroll
        for (int q = 0; q < 4; ++q) st16c(v4u{~0u, ~0u, ~0u, ~0u}, rws, o, q * 1024);
        SB();
      }
      constexpr int nq = k < 3 ? XE[k + 1] - XE[k] : 0, nw = cum[k + 1] - cum[k];
      unr<(nq > nw ? nq : nw)>([&](auto qc) {
        constexpr int n = cum[k] + qc;
        if constexpr (qc < nq) piece(IC(XE[k < 3 ? k : 0] + qc));
        if constexpr (qc < nw) sts16(L, ((n & 16) ? awh : awo) + ((n >> 1) & 7) * 8192 + (n & 1) * 1024, ar[n]);
        SB();
        __builtin_amdgcn_s_sleep(WSL);
        SB();
      });
      trk(IC(k + 1));
    });
  }
  unr<T5>(ldt);
  BARW();
  unr<T6 - T5>([&](auto q) { ldt(IC(T5 + q)); });
  VM(T6);
  trk(IC(6));
  BAR0();
  STAMP(9);
  {
    auto ldo = [&](auto nc) { constexpr int n = nc; VM(WOW); dma<n * 4096>(swout, pvo, WSO(n), lb); };
    const srd_t sws = mks(p.ws, WS_BYTES);
    const int yfo = fl(F_Y + ((64 * j + 16 * m + min(g * 7, 15)) / 14) * 4 + (l & 3));
    const int yvo = (64 * j + 16 * m + (l >> 3)) * 4096 + i * 128 + (((((l & 7) >> 1) ^ g) << 5) | ((l & 1) << 4));
    auto ldy = [&]() { dmac<L_Z2>(sws, yvo, WS_YB, lb2); dmac<L_Z2 + 1024>(sws, yvo, WS_YB + 32768, lb2); };
    __builtin_amdgcn_s_sleep(WSLP);
    static_assert(NO1 >= 4 + OP && NO1 <= 24, "");
    unr<NO0 - T6>([&](auto q) { ldo(IC(T6 + q)); });
    VM(NO0 - K1);
    lds_signal(L, L_CNT + 44);
    STAMP(1);
    const int xto = (64 * i + 16 * m + (l >> 3)) * 896 + j * 128 + (((l & 7) ^ g) << 4);
    lds_wait(L, L_CNT + 36, 4);
    __builtin_amdgcn_s_setprio(PMT);
    if (XAT == 4) { dma<L_Z2 + 8192>(sx_, xto, 0, lb2); dma<L_Z2 + 9216>(sx_, xto ^ 64, 7168, lb2); }
    unr<NO1 - NO0>([&](auto q) { ldo(std::integral_constant<int, NO0 + q>{}); });
    const rsrc_t rwout = mk(p.wout, 896 * D_ * 2);
#pragma unroll
    for (int n = NO1; n < 28; ++n) {
      if (n == NO1 + SIGE) { VM(SIGE + (XAT == 4 && NO0 == NO1 ? 2 : 0)); lds_signal(L, L_CNT + 40); STAMP(10); }
      ar[n] = ld16(rwout, pvo, WSO(n));
      SB();
    }
    STAMP(13);
    int wwo = m * 1024 + l * 16 + 49152;
    PIN(wwo);
#pragma unroll
    for (int n = NO1; n < 28; ++n) { sts16(L, wwo + (n - 12) * 4096, ar[n]); SB(); }
    STAMP(12);
    const bool early = !YLATE && __all(ldflag(p.ws, yfo) == tag);
    if (early) ldy();
    if (YLATE && !RREG) DRAIN();
    BARW();
    if ((HZ || HU) && m == 0) setflag(p.ws, F_UD + i * 8 + j, tag);
    if (!early) { wait_flags(p.ws, yfo, tag); ldy(); }
  }
  DRAIN();
  BAR0();
  STAMP_OUT(1);
}

constexpr int C_CP = 4160, C_KV = 4 * C_CP, C_SLOT = C_KV + 4608;

constexpr int C_EX = 131072;

template <int H> static __device__ __forceinline__ void conv_compute(const Params &p, LAS unsigned char *L, int v, int w) {
  STAMP_DECL;
  STAMP(0);
  __builtin_amdgcn_s_setprio(PCV);
  u64 tag = epoch_load(p.ws);
  constexpr int ord[8] = {H ? 0 : 2, H ? 1 : 3, H ? 6 : 4, H ? 7 : 5, H ? 2 : 0, H ? 3 : 1, H ? 4 : 6, H ? 5 : 7};
  const int l = threadIdx.x & 63, g = l >> 4, c = l & 15, ch = w >> 1;
  const rsrc_t rws = mk(p.ws, WS_BYTES);
  {
    const int t = threadIdx.x;
    const srd_t sws = mks(p.ws, WS_BYTES);
    touch(sws, t < 128 ? WS_ST + v * 16384 + t * 128 : WS_FL + (v * 52 + min(t & 63, 51)) * 128, 0);
  }
  const int hro = ch * C_SLOT + (c & 3) * C_CP + g * 16 + (c >> 2) * 8 + H * 64;
  const int kro = ch * C_SLOT + C_KV + (254 - 2 * c + g) * 16 - 7 * 512 + H * 64;
  v2u hh[2][16];
  v4u kf[2][8];
  auto rd = [&](int ho, int ko, int q, int n) {
    const int d = n / 3;
    if (n % 3 == 2) kf[q & 1][7 - d] = lds16(L, ko + d * 512 + q * 128);
    else hh[q & 1][2 * d + n % 3] = *(LAS v2u *)(L + ho + d * 512 + q * 128 + (n % 3) * 8);
  };
  f4 acc[8], in[4];
#pragma unroll
  for (int n = 0; n < 8; ++n) acc[n] = f4{0, 0, 0, 0};
  int xw = C_EX + w * 4096 + l * 16, xr = xw ^ 4096;
  int yo = WS_YB + (14 * v + ch) * 4096 + c * 32 + (g & 1) * 512 + (g >> 1) * 16 + 0x40000000;
  PIN(xw); PIN(xr);
  v2u y[4];
  auto fin = [&](int k) {
    if (k < 2) { in[2 * k] = ldsf4(L, xr + k * 2048); in[2 * k + 1] = ldsf4(L, xr + k * 2048 + 1024); }
    else if (k < 10) { const int n = (k - 2) >> 1, e = (k & 1) * 2; in[n][e] = fadd(in[n][e], acc[ord[4 + n]][e]); in[n][e + 1] = fadd(in[n][e + 1], acc[ord[4 + n]][e + 1]); }
    else if (k < 14) { const int n = k - 10; y[n] = v2u{pk(in[n].x, in[n].y), pk(in[n].z, in[n].w)}; PIN(y[n]); }
    else if (k < 16) st16c(widen(y[2 * k - 28], y[2 * k - 27]), rws, yo, ord[2 * k - 24] * 512);
  };
  EPOCH_WAIT(tag);
  tag += 1;
  if (KV1) BAR0();
  BAR0();
  STAMP(2);
#pragma unroll
  for (int n = 0; n < 24; ++n) { rd(hro, kro, 0, n); SB(); }
  WAITC(63, 0);
  int set = 0;
#pragma unroll 1
  for (int rho = 0; rho < 7; ++rho) {
    const int nset = set == 4 * C_SLOT ? 0 : set + 2 * C_SLOT;
    int ho = hro + set, ko = kro + set;
    unr<4>([&](auto qc) {
      constexpr int q = qc;
      if (q == 3) { ho = hro + nset - 512; ko = kro + nset - 512; PIN(ho); PIN(ko); }
      if (q > 0) WAITC(63, 0);
      if (q == 3) BAR0();
      SB();
      int k = 0;
#pragma unroll
      for (int hf = 0; hf < 2; ++hf)
#pragma unroll
        for (int P = 0; P < 8; ++P)
#pragma unroll
          for (int n = 0; n < 4; ++n) {
            const int tl = ord[4 * hf + n];
            if (P > tl) continue;
            const v2u a = hh[q & 1][2 * P], b = hh[q & 1][2 * P + 1];
            if (q == 0 && P == 0) mma0(acc[tl], v4u{a.x, a.y, b.x, b.y}, kf[q & 1][tl - P]);
            else mma(acc[tl], v4u{a.x, a.y, b.x, b.y}, kf[q & 1][tl - P]);
            SB();
            if (q == 0) {
              if (k < 2) fin(k);
              else if (k < 9) { rd(ho, ko, 1, 2 * k - 4); SB(); rd(ho, ko, 1, 2 * k - 3); }
              else if (k < 17) fin(k - 7);
              else if (k < 22) { rd(ho, ko, 1, 2 * k - 20); SB(); rd(ho, ko, 1, 2 * k - 19); }
              else if (k < 26) fin(k - 12);
            } else if (k < 24) rd(ho, ko, q + 1, k);
            else if (q == 2 && (k == 27 || k == 30)) fin(k / 3 + 5);
            else if (q == 3 && k < 28) sts16(L, xw + (k - 24) * 1024, __builtin_bit_cast(v4u, acc[ord[k - 24]]));
            SB();
            ++k;
          }
    });
    yo += rho ? 8192 : -0x40000000;
    set = nset;
    WAITC(63, 0);
    BAR0();
    if (rho == 0) STAMP(3);
    if (rho == 1) STAMP(4);
    if (rho == 3) STAMP(5);
    if (rho == 5) STAMP(6);
  }
  SETTLE();
#pragma unroll
  for (int k = 0; k < 16; ++k) fin(k);
  STAMP(7);
  DRAIN();
  setflag(p.ws, F_Y + v * 4 + w, tag);
  STAMP_OUT(0);
}

static __device__ __forceinline__ void conv_stage(const Params &p, LAS unsigned char *L, int v, int m) {
  STAMP_DECL;
  STAMP(0);
  const int l = threadIdx.x & 63;
  const srd_t sh = mks(p.h, D_ * S_ * 2), sws = mks(p.ws, WS_BYTES), swout = mks(p.wout, 896 * D_ * 2);
  const rsrc_t rws = mk(p.ws, WS_BYTES);
  const int vo = m * 1024 + l * 16;
  auto stage_h = [&](int rho, int set) {
    const int so = (14 * v + 2 * rho) * 4096, lb = set + m * 1024;
    unr<8>([&](auto qc) { constexpr int q = qc; dman<(q >> 2) * C_SLOT + (q & 3) * C_CP + 32, NT_H>(sh, vo, so + (q >> 2) * 4096 + (q & 3) * 2 + 2, lb); });
  };
  auto stage_kv = [&](int rho, int set) {
    const int so = WS_KVR + (14 * v + 2 * rho) * 4096, lb = set + m * 1024;
    dmac<C_KV>(sws, vo, so, lb);
    dmac<C_SLOT + C_KV>(sws, vo, so + 4096, lb);
  };
  u64 head;
  asm volatile("global_load_dwordx2 %0, %1, %2" : "=&v"(head) : "v"((14 * v + (l < 56 ? l >> 2 : 0)) * 4096), "s"(p.h) : "memory");
  stage_h(0, 0);
  u64 tag = epoch_load(p.ws);
  stage_h(1, 2 * C_SLOT);
  asm volatile("s_waitcnt vmcnt(16)" : "+v"(head)::"memory");
  head <<= 48 - 16 * (l & 3);
  auto put_head = [&](int rho, int set) {
    if (m == 0 && (l >> 3) == rho) *(LAS u64 *)(L + set + ((l >> 2) & 1) * C_SLOT + (l & 3) * C_CP + 24) = head;
  };
  {
    const v4u z = {0, 0, 0, 0};
    const int t = m * 64 + l;
    if (t < 192) sts16(L, (t >> 5) * C_SLOT + C_KV + 4096 + (t & 31) * 16, z);
    if (t < 24) { sts16(L, (t >> 2) * C_SLOT + (t & 3) * C_CP, z); *(LAS u64 *)(L + (t >> 2) * C_SLOT + (t & 3) * C_CP + 16) = 0; }
  }
  put_head(0, 0);
  put_head(1, 2 * C_SLOT);
  put_head(2, 4 * C_SLOT);
  EPOCH_WAIT(tag);
  tag += 1;
  if (m == 0) {
    __builtin_amdgcn_s_sleep(KSLEEP);
    wait_flags(p.ws, flk(F_KV + ((14 * v + (l < 32 ? 0 : 13)) >> 4) * 32 + (l & 31)), tag);
  }
  BAR0();
  STAMP(1);
  stage_kv(0, 0);
  stage_kv(1, 2 * C_SLOT);
  VM(2);
  BARW();
  stage_h(2, 4 * C_SLOT);
  stage_kv(2, 4 * C_SLOT);
  STAMP(2);
  setflagk(p.ws, F_HI + v * 4 + m, tag);
  int set = 0;
#pragma unroll 1
  for (int rho = 0; rho < 7; ++rho) {
    if (rho == 2) STAMP(3);
    if (rho == 3) STAMP(8);
    if (rho == 0) VM(11); else if (rho >= 5) DRAIN(); else if (rho > WOR && rho <= WOR + 4 / WOK) VM(10 + WTN * WOK); else VM(10);
    if (rho == 2) STAMP(4);
    if (rho == 3) STAMP(9);
    BARW();
    if (rho == 2) STAMP(5);
    if (rho == 3) STAMP(10);
    BAR0();
    if (rho == 2) STAMP(6);
    if (rho == 3) STAMP(11);
    if (rho < 4) {
      stage_h(rho + 3, set);
      if (rho == 2) STAMP(7);
      if (rho == 3) STAMP(12);
      stage_kv(rho + 3, set);
      put_head(rho + 3, set);
    }
    if (rho >= WOR && rho < WOR + 4 / WOK) {
#pragma unroll
      for (int q = 0; q < WOK; ++q) {
        const int n = ((v & 3) * 4 + (rho - WOR) * WOK + q) * 224 + m * 56 + min(l, 55);
        touch(swout, (n >> 2) * 896 + ((n & 3) + 3 * ((v >> 2) & 1)) * 128, 0);
      }
    }
    set = set == 4 * C_SLOT ? 0 : set + 2 * C_SLOT;
    if (rho == 4 && v == 0 && m == 0) {
#pragma unroll 1
      for (u32 s = 0; s < SPIN_CAP; ++s) {
        u64 f[16], d = 0;
#pragma unroll
        for (int q = 0; q < 16; ++q) f[q] = ldflag(p.ws, flk(q * 64 + l));
#pragma unroll
        for (int q = 0; q < 16; ++q) d |= f[q] ^ tag;
        if (__all(d == 0)) break;
        asm volatile("" ::: "memory");
      }
      if (l == 0) stflag(p.ws, WS_EPOCH, tag);
    }
  }
  STAMP_OUT(1);
}

#define CODE_LINES 414
#define CPFW 4
__global__ void __launch_bounds__(512, 2) fused_kernel(Params p) {
  extern __shared__ __attribute__((aligned(16))) unsigned char smem[];
  LAS unsigned char *L = (LAS unsigned char *)smem;
  const int b = blockIdx.x, x = b & 7, r = b >> 3, wv = __builtin_amdgcn_readfirstlane(threadIdx.x >> 6);
  {
    u64 pc;
    const u32 line = min((u32)(r * 16 + (threadIdx.x & 15)), (u32)CODE_LINES);
    asm volatile("s_cmp_lg_u32 %2, %4\n\ts_cbranch_scc1 1f\n\ts_getpc_b64 %0\n\ts_mov_b32 m0, %3\n\ts_nop 0\n\tglobal_load_lds_dword %1, %0\n1:\n\t"
                 "s_setprio 3\n\ts_cmp_lt_u32 %2, 4\n\ts_cbranch_scc1 2f\n\ts_setprio 1\n2:"
                 : "=&s"(pc) : "v"(line * 128), "s"(wv), "n"(LDS_BYTES - 256), "n"(CPFW) : "m0", "scc", "memory");
  }
  if (r >= 28) {
    const int v = x * 4 + (r - 28);
    if (wv < 4) { if (wv & 1) conv_compute<1>(p, L, v, wv); else conv_compute<0>(p, L, v, wv); }
    else conv_stage(p, L, v, wv - 4);
  } else {
    Tile t;
    const int pg = x >> 1, o = x & 1;
    if (r < 24) { t.i = 8 * pg + r / 3; t.j = 4 * o + r % 3; }
    else { t.i = 8 * pg + 4 * o + (r - 24); t.j = 3; }
    t.rot1 = 2 * ((t.i + t.j) & 3);
    t.rot2 = (8 * pg + 8 + (32 * (t.j == 3 ? 6 : 2 * (t.j & 3) + o)) / 7 + DELTA * (t.i & 7)) & 31;
    t.e = r < 24 ? t.i & 7 : 2 * (t.i & 3);
    t.ne = r < 24 ? 1 : 2;
    v2u shk = {0, 0};
    u32 dvr = 0;
    if (wv < 4) kv_fetch(p, t, wv, shk, dvr);
    SB();
    asm volatile("" ::: "memory");
    if (wv < 4) gemm_compute(p, L, t, wv, shk, dvr);
    else gemm_move(p, L, t, wv - 4);
  }
}

extern "C" void kernel_launch(void *const *d_in, const int *in_sizes, int n_in, void *d_out, int out_size, void *d_ws, size_t ws_size,
                              hipStream_t stream) {
  Params p;
  p.x = (const u16 *)d_in[0];
  p.k = (const u16 *)d_in[1];
  p.v = (const u16 *)d_in[2];
  p.shk = (const u16 *)d_in[3];
  p.h = (const u16 *)d_in[4];
  p.dv = (const u16 *)d_in[5];
  p.win = (const u16 *)d_in[7];
  p.wsp = (const u16 *)d_in[8];
  p.bsp = (const u16 *)d_in[9];
  p.wout = (const u16 *)d_in[10];
  p.out = (u16 *)d_out;
  p.ws = (unsigned char *)d_ws;
  (void)hipFuncSetAttribute((const void *)fused_kernel, hipFuncAttributeMaxDynamicSharedMemorySize, LDS_BYTES);
  fused_kernel<<<256, 512, LDS_BYTES, stream>>>(p);
}
```

```cpp
#define HS 0
#define HK 0
#define PAIRX
#include <hip/hip_runtime.h>
#include <stdint.h>
#include <type_traits>

typedef unsigned short u16;
typedef unsigned int u32;
typedef unsigned long long u64;
typedef u32 v4u __attribute__((ext_vector_type(4)));
typedef u32 v2u __attribute__((ext_vector_type(2)));
typedef float f4 __attribute__((ext_vector_type(4)));
typedef float f2 __attribute__((ext_vector_type(2)));
typedef short s4 __attribute__((ext_vector_type(4)));
typedef __bf16 bf8 __attribute__((ext_vector_type(8)));
typedef __bf16 bf2 __attribute__((ext_vector_type(2)));
#define LAS __attribute__((address_space(3)))
#define GAS __attribute__((address_space(1)))
typedef __amdgpu_buffer_rsrc_t rsrc_t;
typedef int srd_t __attribute__((ext_vector_type(4)));

constexpr int S_ = 2048, D_ = 448, F_ = 1792;
constexpr int WS_EPOCH = 0;
#define FS 128
#define FULL 1
constexpr int WS_FL = 4096;
constexpr int F_KV = 0;
constexpr int F_HI = 896;
constexpr int F_Z = 1024;
constexpr int F_U = 1280;
constexpr int F_ZD = 256;
constexpr int F_UD = 512;
constexpr int F_Y = 1536;
constexpr int WS_ST = 262144;
constexpr int WS_ZB = 1 << 20;
constexpr int WS_UB = 5 << 20;
constexpr int WS_KVR = 9 << 20;
constexpr int WS_YB = 11 << 20;
constexpr int WS_STAMP = 14 << 20;
constexpr int WS_BYTES = 16 << 20;
constexpr int LDS_BYTES = 163840;
constexpr u32 SPIN_CAP = 1u << 16;

struct Params {
  const u16 *x, *k, *v, *shk, *h, *dv, *win, *wsp, *bsp, *wout;
  u16 *out;
  unsigned char *ws;
};

#define SB() __builtin_amdgcn_sched_barrier(0)
#define BARW() do { SB(); asm volatile("s_waitcnt lgkmcnt(0)\n\ts_barrier" ::: "memory"); SB(); } while (0)
#define BAR0() do { SB(); asm volatile("s_barrier" ::: "memory"); SB(); } while (0)
#define FRESH() do { asm volatile("" : "+v"(l)); g = l >> 4; c = l & 15; } while (0)
#define DRAIN() asm volatile("s_waitcnt vmcnt(0)" ::: "memory")
#define VM(n) asm volatile("s_waitcnt vmcnt(%0)" ::"n"(n) : "memory")
#define PIN(x) asm volatile("" : "+v"(x))
#define STAMP(n) do { if constexpr ((n) == HS) { SB(); hs_ = __builtin_amdgcn_s_memrealtime(); SB(); } } while (0)
#define STAMPA(n) do { } while (0)
#define STAMP_DECL u64 hs_ = 0
#define STAMP_OUT(slot) do { DRAIN(); STAMP(14); if ((slot) + 2 * ((blockIdx.x >> 3) >= 28) == HK) for (int q_ = 0; q_ < 100000; ++q_) { if (__builtin_amdgcn_s_memrealtime() - hs_ >= 4000) break; __builtin_amdgcn_s_sleep(1); } } while (0)

static __device__ __forceinline__ rsrc_t mk(const void *p, int bytes) { return __builtin_amdgcn_make_buffer_rsrc((void *)p, 0, bytes, 0x00020000); }
static __device__ __forceinline__ v4u ld16(rsrc_t r, int vo, int so) { return __builtin_amdgcn_raw_buffer_load_b128(r, vo, so, 0); }
static __device__ __forceinline__ v4u ld16c(rsrc_t r, int vo, int so) { return __builtin_amdgcn_raw_buffer_load_b128(r, vo, so, 16); }
static __device__ __forceinline__ u32 ld4(rsrc_t r, int vo, int so) { return __builtin_amdgcn_raw_buffer_load_b32(r, vo, so, 0); }
static __device__ __forceinline__ void st16c(v4u v, rsrc_t r, int vo, int so) { __builtin_amdgcn_raw_buffer_store_b128(v, r, vo + so, 0, 16); }
static __device__ __forceinline__ srd_t mks(const void *p, int bytes) { return srd_t{(int)(u64)p, (int)((u64)p >> 32) & 0xffff, bytes, 0x00020000}; }
template <int IMM> static __device__ __forceinline__ void dma(srd_t r, int vo, int so, int base) {
  asm volatile("s_add_u32 m0, %0, %4\n\ts_nop 0\n\tbuffer_load_dwordx4 %1, %2, %3 offen lds" ::"s"(base), "v"(vo), "s"(r), "s"(so), "n"(IMM) : "m0", "scc", "memory");
}
template <int IMM, int NT> static __device__ __forceinline__ void dman(srd_t r, int vo, int so, int base) {
  if constexpr (NT) asm volatile("s_add_u32 m0, %0, %4\n\ts_nop 0\n\tbuffer_load_dwordx4 %1, %2, %3 offen nt lds" ::"s"(base), "v"(vo), "s"(r), "s"(so), "n"(IMM) : "m0", "scc", "memory");
  else dma<IMM>(r, vo, so, base);
}
#define NT_KV 0
#define NT_H 0
#define NT_SP 0
#define NT_X 0
#define NT_RING 0
template <int IMM> static __device__ __forceinline__ void dmac(srd_t r, int vo, int so, int base) {
  asm volatile("s_add_u32 m0, %0, %4\n\ts_nop 0\n\tbuffer_load_dwordx4 %1, %2, %3 offen sc1 lds" ::"s"(base), "v"(vo), "s"(r), "s"(so), "n"(IMM) : "m0", "scc", "memory");
}
static __device__ __forceinline__ void touch(srd_t r, int vo, int so) {
  asm volatile("s_mov_b32 m0, %3\n\ts_nop 0\n\tbuffer_load_dword %0, %1, %2 offen lds" ::"v"(vo), "s"(r), "s"(so), "n"(LDS_BYTES - 256) : "m0", "memory");
}
static __device__ __forceinline__ void touchc(srd_t r, int vo, int so) {
  asm volatile("s_mov_b32 m0, %3\n\ts_nop 0\n\tbuffer_load_dword %0, %1, %2 offen sc1 lds" ::"v"(vo), "s"(r), "s"(so), "n"(LDS_BYTES - 256) : "m0", "memory");
}
static __device__ __forceinline__ float one(float r) { asm("" : "+v"(r)); return r; }
static __device__ __forceinline__ float fadd(float a, float b) { return one(a + b); }
static __device__ __forceinline__ float fmul(float a, float b) { return one(a * b); }
static __device__ __forceinline__ float ffma(float a, float b, float c) { return one(__builtin_fmaf(a, b, c)); }
template <int N, typename Fn> static __device__ __forceinline__ void unr(Fn &&f) {
  if constexpr (N > 0) { unr<N - 1>(f); f(std::integral_constant<int, N - 1>{}); }
}
static __device__ __forceinline__ f4 mfma(v4u a, v4u b, f4 c) {
  return __builtin_amdgcn_mfma_f32_16x16x32_bf16(__builtin_bit_cast(bf8, a), __builtin_bit_cast(bf8, b), c, 0, 0, 0);
}
static __device__ __forceinline__ void mma(f4 &c, v4u a, v4u b) { asm volatile("v_mfma_f32_16x16x32_bf16 %0, %1, %2, %0" : "+v"(c) : "v"(a), "v"(b)); }
static __device__ __forceinline__ void mma0(f4 &c, v4u a, v4u b) { asm volatile("v_mfma_f32_16x16x32_bf16 %0, %1, %2, 0" : "=v"(c) : "v"(a), "v"(b)); }
typedef float f16v __attribute__((ext_vector_type(16)));
static __device__ __forceinline__ void mmb(f16v &c, v4u a, v4u b) { asm volatile("v_mfma_f32_32x32x16_bf16 %0, %1, %2, %0" : "+v"(c) : "v"(a), "v"(b)); }
template <bool FIRST> static __device__ __forceinline__ void mmaf(f4 &c, v4u a, v4u b) { if constexpr (FIRST) mma0(c, a, b); else mma(c, a, b); }
#define SETTLE() do { SB(); asm volatile("s_nop 7" ::: "memory"); SB(); } while (0)
#define WAITC(vm, lg) __builtin_amdgcn_s_waitcnt(((vm) & 15) | 0x70 | ((lg) << 8) | (((vm) >> 4) << 14))
static __device__ __forceinline__ float dot2(u32 a, u32 b, float c) { return __builtin_amdgcn_fdot2_f32_bf16(__builtin_bit_cast(bf2, a), __builtin_bit_cast(bf2, b), c, false); }
static __device__ __forceinline__ float asf(u32 u) { return __builtin_bit_cast(float, u); }
static __device__ __forceinline__ u32 asu(float f) { return __builtin_bit_cast(u32, f); }
static __device__ __forceinline__ float lo16(u32 u) { return asf(u << 16); }
static __device__ __forceinline__ float hi16(u32 u) { return asf(u & 0xffff0000u); }
static __device__ __forceinline__ u32 pk(float a, float b) { f2 t = {a, b}; return __builtin_bit_cast(u32, __builtin_convertvector(t, bf2)); }
static __device__ __forceinline__ float rbf(float a) { return lo16(pk(a, a)); }
static __device__ __forceinline__ v4u lds16(LAS unsigned char *L, int off) { return *(LAS v4u *)(L + off); }
static __device__ __forceinline__ f4 ldsf4(LAS unsigned char *L, int off) { return *(LAS f4 *)(L + off); }
static __device__ __forceinline__ void sts16(LAS unsigned char *L, int off, v4u v) { *(LAS v4u *)(L + off) = v; }
static __device__ __forceinline__ v2u trd(LAS unsigned char *L, int off) {
  return __builtin_bit_cast(v2u, __builtin_amdgcn_ds_read_tr16_b64_v4i16((LAS s4 *)(L + off)));
}
static __device__ __forceinline__ v4u trd2(LAS unsigned char *L, int off, int step) {
  const v2u a = trd(L, off), b = trd(L, off + step);
  return v4u{a.x, a.y, b.x, b.y};
}
static __device__ __forceinline__ f2 fma2(f2 a, f2 b, f2 c) { return __builtin_elementwise_fma(a, b, c); }
static __device__ __forceinline__ u32 pk2(f2 t) { return __builtin_bit_cast(u32, __builtin_convertvector(t, bf2)); }
static __device__ __forceinline__ f2 lo2(f4 v) { return __builtin_shufflevector(v, v, 0, 1); }
static __device__ __forceinline__ f2 hi2(f4 v) { return __builtin_shufflevector(v, v, 2, 3); }
static __device__ __forceinline__ void rowsum8(float (&a)[8], float (&b)[8]) {
#define DA(d, s, ctl, bm) "v_add_f32_dpp " d ", " s ", " s " " ctl " row_mask:0xf bank_mask:" bm "\n\t"
  asm volatile("s_nop 1\n\t"
               DA("%0", "%0", "row_mirror", "0x3") DA("%1", "%1", "row_mirror", "0x3") DA("%2", "%2", "row_mirror", "0x3") DA("%3", "%3", "row_mirror", "0x3")
               DA("%4", "%4", "row_mirror", "0x3") DA("%5", "%5", "row_mirror", "0x3") DA("%6", "%6", "row_mirror", "0x3") DA("%7", "%7", "row_mirror", "0x3")
               DA("%0", "%8", "row_mirror", "0xc") DA("%1", "%9", "row_mirror", "0xc") DA("%2", "%10", "row_mirror", "0xc") DA("%3", "%11", "row_mirror", "0xc")
               DA("%4", "%12", "row_mirror", "0xc") DA("%5", "%13", "row_mirror", "0xc") DA("%6", "%14", "row_mirror", "0xc") DA("%7", "%15", "row_mirror", "0xc")
               DA("%0", "%0", "row_half_mirror", "0x5") DA("%1", "%1", "row_half_mirror", "0x5") DA("%4", "%4", "row_half_mirror", "0x5") DA("%5", "%5", "row_half_mirror", "0x5")
               DA("%0", "%2", "row_half_mirror", "0xa") DA("%1", "%3", "row_half_mirror", "0xa") DA("%4", "%6", "row_half_mirror", "0xa") DA("%5", "%7", "row_half_mirror", "0xa")
               DA("%0", "%0", "quad_perm:[1,0,3,2]", "0xf") DA("%1", "%1", "quad_perm:[1,0,3,2]", "0xf") DA("%4", "%4", "quad_perm:[1,0,3,2]", "0xf") DA("%5", "%5", "quad_perm:[1,0,3,2]", "0xf")
               DA("%0", "%0", "quad_perm:[2,3,0,1]", "0xf") DA("%1", "%1", "quad_perm:[2,3,0,1]", "0xf") DA("%4", "%4", "quad_perm:[2,3,0,1]", "0xf") DA("%5", "%5", "quad_perm:[2,3,0,1]", "0xf")
               : "+v"(a[0]), "+v"(a[1]), "+v"(a[2]), "+v"(a[3]), "+v"(b[0]), "+v"(b[1]), "+v"(b[2]), "+v"(b[3])
               : "v"(a[4]), "v"(a[5]), "v"(a[6]), "v"(a[7]), "v"(b[4]), "v"(b[5]), "v"(b[6]), "v"(b[7]));
#undef DA
}
static __device__ __forceinline__ void lds_signal(LAS unsigned char *L, int off) {
  asm volatile("s_waitcnt lgkmcnt(0)" ::: "memory");
  if ((threadIdx.x & 63) == 0) __hip_atomic_fetch_add((LAS u32 *)(L + off), 1u, __ATOMIC_RELAXED, __HIP_MEMORY_SCOPE_WORKGROUP);
}
static __device__ __forceinline__ bool lds_last(LAS unsigned char *L, int off) {
  u32 n = 0;
  if ((threadIdx.x & 63) == 0) n = __hip_atomic_fetch_add((LAS u32 *)(L + off), 1u, __ATOMIC_RELAXED, __HIP_MEMORY_SCOPE_WORKGROUP);
  return __builtin_amdgcn_readfirstlane(n) == 3;
}
static __device__ __forceinline__ void lds_wait(LAS unsigned char *L, int off, u32 n) {
#pragma unroll 1
  for (u32 s = 0; s < (SPIN_CAP << 4); ++s)
  {
    if (__hip_atomic_load((LAS u32 *)(L + off), __ATOMIC_RELAXED, __HIP_MEMORY_SCOPE_WORKGROUP) >= n) break;
    __builtin_amdgcn_s_sleep(1);
  }
  asm volatile("" ::: "memory");
}
static __device__ __forceinline__ u64 ldflag(unsigned char *ws, int off) { return __hip_atomic_load((GAS u64 *)(ws + off), __ATOMIC_RELAXED, __HIP_MEMORY_SCOPE_AGENT); }
static __device__ __forceinline__ void stflag(unsigned char *ws, int off, u64 v) { __hip_atomic_store((GAS u64 *)(ws + off), v, __ATOMIC_RELAXED, __HIP_MEMORY_SCOPE_AGENT); }
static __device__ __forceinline__ int fl(int n) { return WS_FL + n * FS; }
#define FSK 8
static __device__ __forceinline__ int flk(int n) { return WS_FL + n * FSK; }
static __device__ __forceinline__ void setflagk(unsigned char *ws, int n, u64 tag) {
  const int l = threadIdx.x & 63;
  if (FULL && FSK >= 16) { if (l < FSK / 16) st16c(v4u{(u32)tag, (u32)(tag >> 32), (u32)tag, (u32)(tag >> 32)}, mk(ws, WS_BYTES), flk(n) + l * 16, 0); }
  else if (l == 0) stflag(ws, flk(n), tag);
}
static __device__ __forceinline__ void setflag(unsigned char *ws, int n, u64 tag) {
  const int l = threadIdx.x & 63;
  if (FULL) { if (l < FS / 16) st16c(v4u{(u32)tag, (u32)(tag >> 32), (u32)tag, (u32)(tag >> 32)}, mk(ws, WS_BYTES), fl(n) + l * 16, 0); }
  else if (l == 0) stflag(ws, fl(n), tag);
}
static __device__ __forceinline__ void wait_flags(unsigned char *ws, int off, u64 tag) {
#pragma unroll 1
  for (u32 s = 0; s < SPIN_CAP; ++s) {
    const u64 v = ldflag(ws, off);
    if (__all(v == tag)) break;
    __builtin_amdgcn_s_sleep(1);
  }
  asm volatile("" ::: "memory");
}
static __device__ __forceinline__ void hint_flags(const unsigned char *at, u64 tag) {
  typedef u64 v8q __attribute__((ext_vector_type(8)));
#pragma unroll 1
  for (u32 s = 0; s < 4096; ++s) {
    v8q f;
    asm volatile("s_load_dwordx16 %0, %1, 0x0 glc\n\ts_waitcnt lgkmcnt(0)" : "=s"(f) : "s"(at) : "memory");
    u64 d = 0;
#pragma unroll
    for (int q = 0; q < 8; ++q) d |= f[q] ^ tag;
    if (d == 0) break;
    __builtin_amdgcn_s_sleep(1);
  }
}
static __device__ __forceinline__ u32 lds_peek(LAS unsigned char *L, int off) { return __hip_atomic_load((LAS u32 *)(L + off), __ATOMIC_RELAXED, __HIP_MEMORY_SCOPE_WORKGROUP); }
static __device__ __forceinline__ void lds_check(LAS unsigned char *L, int off, u32 n, u32 seen) {
  if (__builtin_expect(__builtin_amdgcn_readfirstlane(seen) < n, 0)) lds_wait(L, off, n);
  asm volatile("" ::: "memory");
}
static __device__ __forceinline__ float inv_sigma(float var) {
  const float sd = rbf(__builtin_amdgcn_sqrtf(rbf(rbf(fmaxf(var, 0.f)) + 1e-5f)));
  return __builtin_amdgcn_rcpf(sd);
}
static __device__ __forceinline__ int mod7(int a) { return a >= 7 ? a - 7 : a; }

constexpr int L_KR = 81920;
constexpr int L_RV = L_KR + 20480;
constexpr int L_XR = 114688;
constexpr int L_Z2 = 114688;
constexpr int L_Z1 = 131072;
constexpr int L_KD = 147456;
constexpr int L_CS = 155648;
constexpr int L_XST = 156672;
constexpr int L_ZST = 157184;
constexpr int L_CNT = 157696;
constexpr int L_BS = 157952;

#define PMS 1
#define PTW 1
#define TSP 0
#define TZ1 0
#define NISS {18, 5, 5, 5, 5, 4, 0, 0}
#define NIZ {0, 0, 0, 0, 0, 1, 5, 5}
#define NIW {0, 0, 0, 0, 0, 0, 0, 0}
#define MIXZ 30
#define LASTA 2
#define W1 7
#define W2 12
#define KV1 1
#define KSLEEP 16
#define ZP -100
#define WOW 12
#define WSLP 4
#define AD 2
#define RREG 1
#define OP 8
#define UW 12
#define WOR 2
#define WOK 1
#define YLATE 1
#define WSO(n) ((mod7(((n) >> 2) + j) * 16 + ((n) & 3) * 4 + m) * 7168)
#define SIGE 2
#define TLC 0
#define TLG 0
#define UPOS 0
#define NO0 NO1
#define XAT 4
#define PMT 0
#define K1 8
#define T5 0
#define T6 T5
#define TS 2
#define RE 0
#define WTN 1
#define CLA 5
#define TRK {0, 0, 3, 3, 3, 3, 4}
#define TRS 4
#define ROT1 0
#define TD 0
#define TC 1
#define CLZ 13
#define CLU 3
#define HZ 1
#define HU 1
#define OWNM 1
#define NR1 12
#define X0 30
#define X1 32
#define X2 38
#define ZSL 0
#define ZSL2 8
#define NO1 12
constexpr int VLIM = 63;
#define RD 8
#define KVF_AT 3
#define SH_AT 6
#define TSLEEP 32
#define NS 10
#define PRE 0
#define DELTA 0
#define WOT 2
#define CLR_EARLY 1
#define CLR_SLEEP 8
#define PW 63
#define WSL 0
#define PC0 0
#define PC1 2
#define PM2 3
#define PM3 1
#define PCV 2
#define NB 6
#define NB2 4
struct Order {
  int seq[102], bar[8];
  constexpr int last(int a, int b) const { int r = 0; for (int q = 0; q < 102; ++q) if (seq[q] >= a && seq[q] < b) r = q; return r; }
};
constexpr Order make_order() {
  Order o{};
  constexpr int a[8] = NISS, z[8] = NIZ, w[8] = NIW;
  int n = 0, ia = 0, iz = 42, iw = 70;
  for (int s = 0; s < 8; ++s) {
    int ca = a[s], cz = z[s], cw = w[s];
    while (ca + cz + cw > 0) {
      for (int q = 0; q < 2 && ca > 0; ++q) { o.seq[n++] = ia++; --ca; }
      if (cz > 0) { o.seq[n++] = iz++; --cz; } else if (cw > 0) { o.seq[n++] = iw++; --cw; }
    }
    o.bar[s] = n;
  }
  while (n < 70 + NS) {
    for (int q = 0; q < MIXZ && iz < 70; ++q) o.seq[n++] = iz++;
    if (iw < 70 + NS && (iz == 70 || iw < 80)) o.seq[n++] = iw++;
  }
  while (n < 102) o.seq[n++] = 1000;
  return o;
}
constexpr Order ORD = make_order();
constexpr bool order_ok() {
  for (int q = 0; q < ORD.bar[7]; ++q) {
    int s = 0;
    while (q >= ORD.bar[s]) ++s;
    const int n = ORD.seq[q], st = n / 6;
    if (n < 42 && s > st) return false;
    if (n < 42 && n % 6 < 2 && st >= 4 && s < st - 2) return false;
    if (n < 42 && n % 6 >= 2 && st >= 5 && s < 2) return false;
    if (n >= 42 && n < 70 && s < (n - 42) / 4 + 3) return false;
  }
  return ORD.last(0, 42) < ORD.bar[6];
}
static_assert(order_ok(), "");
struct Tile { int i, j, rot1, rot2, e, ne; };

static __device__ __forceinline__ u64 epoch_load(const unsigned char *ws) {
  u64 e;
  asm volatile("s_load_dwordx2 %0, %1, 0x0" : "=s"(e) : "s"(ws) : "memory");
  return e;
}
#define EPOCH_WAIT(e) asm volatile("s_waitcnt lgkmcnt(0)" : "+s"(e)::"memory")
static __device__ __forceinline__ v4u widen(v2u x, v2u y) {
  const v2u a = __builtin_amdgcn_permlane16_swap(x.x, y.x, false, false), b = __builtin_amdgcn_permlane16_swap(x.y, y.y, false, false);
  return v4u{a.x, b.x, a.y, b.y};
}

static __device__ __forceinline__ void ztouch(const Params &p, const Tile t, int ck, int q, int l) {
  const int e = t.e + q;
  if (q < t.ne) touchc(mks(p.ws + WS_ZB + t.j * 524288, 524288), l * 128, ((t.rot2 + 4 * ck + (e >> 1)) & 31) * 16384 + (e & 1) * 8192);
}
static __device__ __forceinline__ void kv_fetch(const Params &p, const Tile t, int w, v2u &shk, u32 &dvr) {
  const int i = t.i, j = t.j, l = threadIdx.x & 63;
  const srd_t sk = mks(p.k, S_ * D_ * 2), sv = mks(p.v, S_ * D_ * 2);
  const int vo = (l >> 3) * 896 + j * 128 + (((l & 7) ^ ((l >> 2) & 4)) << 4), so = (64 * i + 16 * w) * 896, lb = w * 5120;
  dman<L_KR + 1024, NT_KV>(sk, vo, so, lb);
  dman<L_KR + 2048, NT_KV>(sk, vo, so + 7168, lb);
  dman<L_KR, NT_KV>(sk, vo, so > 0 ? so - 7168 : 0x40000000, lb);
  dman<L_KR + 3072, NT_KV>(sv, vo, so, lb);
  dman<L_KR + 4096, NT_KV>(sv, vo, so + 7168, lb);
  asm volatile("global_load_dwordx2 %0, %2, %3\n\tglobal_load_ushort %1, %4, %5" : "=&v"(shk), "=&v"(dvr) : "v"((64 * j + l) * 8), "s"(p.shk), "v"((64 * j + l) * 2), "s"(p.dv) : "memory");
}

static __device__ __forceinline__ void gemm_compute(const Params &p, LAS unsigned char *L, const Tile t, int w, v2u shk, u32 dvr) {
  STAMP_DECL;
  STAMP(0);
  const int i = t.i, j = t.j, rh = w >> 1, ch = w & 1;
  int l = threadIdx.x & 63, g = l >> 4, c = l & 15;
  u64 tag = epoch_load(p.ws);
  const rsrc_t rws = mk(p.ws, WS_BYTES);
  v4u xf[2][14];
  f4 acc[2][4], cs[2];
  int rb[4];
  v2u xe[4];
  auto ldx = [&]() {
    const rsrc_t rx = mk(p.x, S_ * D_ * 2);
#pragma unroll
    for (int q = 0; q < 4; ++q) xe[q] = __builtin_amdgcn_raw_buffer_load_b64(rx, (64 * i + 16 * w + c) * 896 + j * 128 + g * 8 + q * 32, 0, 0);
    SB();
  };
  auto clz = [&]() {
    const int o = WS_ZB + (j * 64 + 2 * i) * 8192 + w * 4096 + l * 16;
#pragma unroll
    for (int q = 0; q < 4; ++q) st16c(v4u{~0u, ~0u, ~0u, ~0u}, rws, o, q * 1024);
    SB();
  };
  if (HZ && CLZ == -1) clz();
  v4u ones = {0x3f803f80u, 0x3f803f80u, 0x3f803f80u, 0x3f803f80u};
  auto start = [&]() {
    const int rdoff = (8 * g + (c >> 2)) * 256 + ((((c >> 2) | ((g & 1) << 2)) ^ (4 * ch + 2 * rh)) << 5) + (c & 3) * 8;
#pragma unroll
    for (int n = 0; n < 4; ++n) { rb[n] = rdoff ^ (n << 5); PIN(rb[n]); }
    PIN(ones);
  };
  int xo0 = L_XR + (32 * rh + c) * 128 + ((g ^ (c & 7)) << 4), xo1 = xo0 ^ 64;
  int lno = L_RV + (16 * w + (l >> 3)) * 144 + (l & 7) * 16, lso = WS_KVR + (64 * j + 16 * w + (l >> 3)) * 4096 + (31 - i) * 128 + (l & 7) * 16;
  {
    __builtin_amdgcn_s_setprio(PC0);
    SB();
    if (threadIdx.x < 16) *(LAS u32 *)(L + L_CNT + threadIdx.x * 4) = 0;
    int ro = L_KR + w * 5120 + (c >> 2) * 128 + ((g ^ ((c >> 2) & 2)) << 5) + (c & 3) * 8;
    int wo = L_RV + l * 144 + (3 - w) * 32, ko = L_KD + l * 128 + ((w ^ ((c >> 1) & 3)) << 5);
    start();
    PIN(xo0); PIN(xo1); PIN(lno); PIN(lso); PIN(ro); PIN(wo); PIN(ko);
    f2 o[8];
#pragma unroll
    for (int q = 0; q < 8; ++q) { o[q] = f2{0, 0}; PIN(o[q]); }
    EPOCH_WAIT(tag);
    tag += 1;
    asm volatile("s_waitcnt vmcnt(%2)" : "+v"(shk), "+v"(dvr) : "n"(HZ && CLZ == -1 ? 4 : 0) : "memory");
    STAMP(1);
    v2u kr[5], vr[4];
#pragma unroll
    for (int q = 0; q < 5; ++q) kr[q] = trd(L, ro + 512 + q * 512);
#pragma unroll
    for (int q = 0; q < 4; ++q) vr[q] = trd(L, ro + 3072 + q * 512);
    SB();
    const u32 ta = __builtin_amdgcn_alignbit(shk.y, shk.y, 16), tb = __builtin_amdgcn_alignbit(shk.x, shk.x, 16);
    const float dd = lo16(dvr);
    u32 ke[10], kq[9];
#pragma unroll
    for (int q = 0; q < 5; ++q) { ke[2 * q] = kr[q].x; ke[2 * q + 1] = kr[q].y; }
#pragma unroll
    for (int q = 0; q < 9; ++q) kq[q] = __builtin_amdgcn_alignbit(ke[q + 1], ke[q], 16);
#pragma unroll
    for (int q = 0; q < 8; ++q) o[q] = f2{dot2(kq[q], ta, o[q].x), dot2(ke[q + 1], ta, o[q].y)};
    SB();
#pragma unroll
    for (int q = 0; q < 8; ++q) o[q] = f2{dot2(kq[q + 1], tb, o[q].x), dot2(ke[q + 2], tb, o[q].y)};
    SB();
#pragma unroll
    for (int q = 0; q < 8; ++q) { const u32 vv = (q & 1) ? vr[q >> 1].y : vr[q >> 1].x; o[q] *= f2{lo16(vv), hi16(vv)}; }
    u32 rv[8];
#pragma unroll
    for (int q = 0; q < 8; ++q) asm("v_cvt_pk_bf16_f32 %0, %1, %2" : "=v"(rv[q]) : "v"(o[7 - q].y), "v"(o[7 - q].x));
    sts16(L, wo, v4u{rv[0], rv[1], rv[2], rv[3]});
    sts16(L, wo + 16, v4u{rv[4], rv[5], rv[6], rv[7]});
    SB();
#pragma unroll
    for (int q = 0; q < 8; ++q) rv[q] = pk2(o[q] * dd);
    sts16(L, ko, v4u{rv[0], rv[1], rv[2], rv[3]});
    sts16(L, ko + 16, v4u{rv[4], rv[5], rv[6], rv[7]});
  }
  STAMP(2);

  v2u wh[2][8];
  auto rd1 = [&](int ks, int q) {
    if (q < 2) xf[q][ks] = lds16(L, ((ks & 1) ? xo1 : xo0) + ((ks >> 1) & 3) * 8192 + q * 2048);
    else {
      const int n = (q - 2) >> 1;
      if (ks == 7 && !(q & 1)) { rb[n] += 57344; PIN(rb[n]); }
      wh[ks & 1][q - 2] = trd(L, rb[n] + (ks % 7) * 8192 + (q & 1) * 1024);
    }
  };
  auto wfr = [&](int ks, int n) { const v2u a = wh[ks & 1][2 * n], b = wh[ks & 1][2 * n + 1]; return v4u{a.x, a.y, b.x, b.y}; };
  auto mm1 = [&](auto ksc, int q) {
    constexpr int ks = ksc;
    if (q < 8) mmaf<ks == 0>(acc[q & 1][q >> 1], xf[q & 1][ks], wfr(ks, q >> 1));
    else mmaf<ks == 0>(cs[q - 8], ones, wfr(ks, q - 8));
  };
  auto step = [&](auto ksc, int q0) {
#pragma unroll
    for (int q = 0; q < 10; ++q) { mm1(ksc, q); SB(); if (q + q0 < 10) rd1(ksc + 1, q + q0); SB(); }
  };
  auto put_cs = [&](int off) {
    const int o = L_CS + off + ((4 * ch + 2 * rh) * 16 + c) * 4;
    *(LAS float *)(L + o) = cs[0].x;
    *(LAS float *)(L + o + 64) = cs[1].x;
  };
  auto lastk = [&](int off) {
    mm1(std::integral_constant<int, 13>{}, 8);
    mm1(std::integral_constant<int, 13>{}, 9);
#pragma unroll
    for (int q = 0; q < 4; ++q) mm1(std::integral_constant<int, 13>{}, q);
    SB();
    asm volatile("s_nop 3" ::: "memory");
    SB();
    put_cs(off);
    SB();
#pragma unroll
    for (int q = 4; q < 8; ++q) mm1(std::integral_constant<int, 13>{}, q);
    SB();
  };
  STAMPA(1);
  __builtin_amdgcn_s_setprio(PC1);
  BARW();
  STAMPA(2);
  {
    v4u ln[2];
    ln[0] = lds16(L, lno);
    ln[1] = lds16(L, lno + 1152);
#pragma unroll
    for (int q = 0; q < 10; ++q) rd1(0, q);
    SB();
    st16c(ln[0], rws, lso, 0);
    st16c(ln[1], rws, lso, 32768);
    SB();
  }
  unr<13>([&](auto ksc) {
    constexpr int ks = ksc;
    if constexpr (ks & 1) {
      STAMPA(ks + 2);
      SB(); asm volatile("s_waitcnt lgkmcnt(8)\n\ts_barrier" ::: "memory"); SB();
      STAMPA(ks + 3);
      if (ks == KVF_AT) {
        DRAIN();
        setflagk(p.ws, F_KV + (4 * j + w) * 32 + i, tag);
        SB();
      }
    }
    if constexpr (HZ && ks == CLZ) clz();
    step(ksc, 0);
  });
  if (HZ && CLZ == 13) clz();
  lastk(0);
  if (HZ && CLZ == 14) clz();
  BARW();
  SETTLE();
  STAMP(6);
  FRESH();
  const u32 tagA = (u32)tag, tagB = (u32)(tag >> 32) ^ 0x9e3779b9u;
  f4 mm[2], rr[2];
  float cv[4];
  auto act_in = [&]() {
#pragma unroll
    for (int m = 0; m < 2; ++m) {
      mm[m] = ldsf4(L, L_XST + (32 * rh + 8 * g + 4 * m) * 4);
      rr[m] = ldsf4(L, L_XST + 256 + (32 * rh + 8 * g + 4 * m) * 4);
    }
    cv[0] = -cs[0].x;
    cv[1] = -cs[1].x;
  };
  auto act_in2 = [&](int off) {
    const int o = L_CS + off + ((4 * ch + (2 - 2 * rh)) * 16 + c) * 4;
    cv[2] = -*(LAS float *)(L + o);
    cv[3] = -*(LAS float *)(L + o + 64);
  };
  auto act = [&](int n, f2 (&z)[4]) {
    f2 x[4], a[4];
    const f2 c2 = {cv[n], cv[n]};
    SB();
#pragma unroll
    for (int m = 0; m < 2; ++m) {
      x[2 * m] = fma2(lo2(mm[m]), c2, lo2(acc[m][n]));
      x[2 * m + 1] = fma2(hi2(mm[m]), c2, hi2(acc[m][n]));
    }
    SB();
#pragma unroll
    for (int m = 0; m < 2; ++m) { x[2 * m] *= lo2(rr[m]); x[2 * m + 1] *= hi2(rr[m]); }
    SB();
#pragma unroll
    for (int q = 0; q < 4; ++q) a[q] = x[q] * x[q];
    SB();
#pragma unroll
    for (int q = 0; q < 4; ++q) a[q] = fma2(a[q], f2{-0.1029432397f, -0.1029432397f}, f2{-2.302208198f, -2.302208198f});
    SB();
#pragma unroll
    for (int q = 0; q < 4; ++q) a[q] *= x[q];
    SB();
#pragma unroll
    for (int q = 0; q < 4; ++q) a[q] = f2{__builtin_amdgcn_exp2f(a[q].x), __builtin_amdgcn_exp2f(a[q].y)};
    SB();
#pragma unroll
    for (int q = 0; q < 4; ++q) a[q] += 1.0f;
    SB();
#pragma unroll
    for (int q = 0; q < 4; ++q) a[q] = f2{__builtin_amdgcn_rcpf(a[q].x), __builtin_amdgcn_rcpf(a[q].y)};
    SB();
#pragma unroll
    for (int q = 0; q < 4; ++q) z[q] = x[q] * a[q];
    SB();
  };
  u32 seen;
  {
    act_in();
    act_in2(0);
    f2 s[4], u[4];
    v4u zp[4];
#pragma unroll
    for (int n = 0; n < 4; ++n) {
      f2 z[4];
      act(n, z);
#pragma unroll
      for (int q = 0; q < 4; ++q) {
        s[q] = n ? s[q] + z[q] : z[q];
        u[q] = n ? fma2(z[q], z[q], u[q]) : z[q] * z[q];
      }
      zp[n] = v4u{pk2(z[0]), pk2(z[1]), pk2(z[2]), pk2(z[3])};
    }
    seen = lds_peek(L, L_CNT);
    float sa[8], sq[8];
#pragma unroll
    for (int q = 0; q < 4; ++q) { sa[2 * q] = s[q].x; sa[2 * q + 1] = s[q].y; sq[2 * q] = u[q].x; sq[2 * q + 1] = u[q].y; }
    rowsum8(sa, sq);
    const int row = 8 * g + (c & 1) + ((c >> 1) & 6);
    if ((c & 2) == 0)
      st16c(v4u{asu((c & 1) ? sa[1] : sa[0]), tagA, asu((c & 1) ? sq[1] : sq[0]), tagB}, rws, ((i * 2 + rh) * 16 + 2 * j + ch) * 512 + row * 16, WS_ST);
    SB();
    const int zo = L_Z2 + (rh * 8 + 4 * ch) * 1024 + l * 16;
#pragma unroll
    for (int n = 0; n < 4; ++n) sts16(L, zo + ((n & 1) << 10) + (((n >> 1) ^ rh) << 11), zp[n]);
    SB();
    if (HZ && CLZ < 15) VM(1);
    if (l == 0) __hip_atomic_fetch_add((LAS u32 *)(L + L_CNT + 4), 1u, __ATOMIC_RELAXED, __HIP_MEMORY_SCOPE_WORKGROUP);
    SB();
    if (HZ && CLZ == 15) clz();
    lds_check(L, L_CNT, 4, seen);
#pragma unroll
    for (int n = 0; n < 4; ++n) { rb[n] -= 57344; PIN(rb[n]); }
#pragma unroll
    for (int q = 2; q < 10; ++q) rd1(0, q);
    SB();
  }
  SB();
  unr<13>([&](auto ksc) {
    if constexpr (HZ && CLZ == 15 && ksc == CLA) {
      VM(0);
      if (l == 0) __hip_atomic_fetch_add((LAS u32 *)(L + L_CNT + 52), 1u, __ATOMIC_RELAXED, __HIP_MEMORY_SCOPE_WORKGROUP);
    }
    if constexpr (ksc == 6) seen = lds_peek(L, L_CNT + 28);
    if constexpr (ksc == 7) lds_check(L, L_CNT + 28, 4, seen);
    SB();
    step(ksc, 2);
  });
  lastk(512);
  lds_signal(L, L_CNT + 8);
  SETTLE();
  FRESH();
  u32 bias[4];
  {
    act_in();
    const int po = L_Z1 + (64 * ch + c) * 128 + (((4 * rh + g) ^ (c & 7)) << 4);
#pragma unroll
    for (int n = 0; n < 4; ++n) {
      f2 z[4];
      if (n == 1) seen = lds_peek(L, L_CNT + 8);
      if (n == 2) {
        lds_check(L, L_CNT + 8, 4, seen);
        act_in2(512);
#pragma unroll
        for (int q = 0; q < 4; ++q) bias[q] = *(LAS u16 *)(L + L_BS + (16 * q + c) * 2);
      }
      if (n == 3) seen = lds_peek(L, L_CNT + 16);
      act(n, z);
      sts16(L, po + ((n & 1) << 11) + (((n >> 1) ^ rh) << 12), v4u{pk2(z[0]), pk2(z[1]), pk2(z[2]), pk2(z[3])});
    }
  }
  STAMP(8);
  FRESH();
  f4 g2[4][2];
  float z1[32];
  bool last;
  int uo[4], ob[4], uw = L_Z1 + w * 4096 + l * 16;
#pragma unroll
  for (int n = 0; n < 4; ++n) { ob[n] = ((4 * g + (c >> 2)) * 128 + ((((c >> 3) & 1) | ((g & 1) << 1)) << 5) + (c & 3) * 8) ^ (n << 5); PIN(ob[n]); }
#pragma unroll
  for (int m = 0; m < 4; ++m) { uo[m] = WS_UB + (i * 112 + 4 * j + w) * 1024 + l * 16 + m * 28672; PIN(uo[m]); }
  {
    v4u bf[8][2], af[4][4];
    const rsrc_t rz = mk(p.ws + WS_ZB + j * 524288, 524288);
    int bvo[2], bso = t.rot2 * 16384;
    bvo[0] = w * 2048 + l * 16;
    bvo[1] = bvo[0] + 8192;
    PIN(bvo[0]); PIN(bvo[1]);
    auto ldb = [&](int ks, int b, int so) { bf[ks & 7][b] = ld16c(rz, bvo[ks & 1] + b * 1024, so); };
#define BMASK 0x7ffff
    auto next = [&](int &so) { asm volatile("s_add_u32 %0, %0, 0x4000\n\ts_and_b32 %0, %0, %1" : "+s"(so) : "n"(BMASK) : "scc"); };
#pragma unroll
    for (int m = 0; m < 4; ++m) { const float b = lo16(bias[m]); g2[m][0] = g2[m][1] = f4{b, b, b, b}; PIN(g2[m][0]); PIN(g2[m][1]); }
    int ab[2];
    ab[0] = c * 128 + ((g ^ (c >> 1)) << 4);
    ab[1] = ab[0] ^ 64;
    PIN(ab[0]); PIN(ab[1]);
    auto rda = [&](int ks, int m) { af[ks & 3][m] = *(LAS v4u *)((LAS unsigned char *)(u32)ab[ks & 1] + ((ks >> 1) & 3) * 8192 + m * 2048); };
    auto turn = [&](int q) { ab[q] = (ab[q] + 32768) & 0x1ffff; PIN(ab[q]); };
    const int f0 = 4 * g + (c >> 2);
    int zr[4];
#pragma unroll
    for (int e = 0; e < 4; ++e) { zr[e] = (L_Z1 + (32 * w + f0) * 128 + (c & 1) * 8 + ((((c >> 1) & 1) ^ (f0 & 7)) << 4)) ^ (e << 5); PIN(zr[e]); }
    v2u zq[8];
    PIN(uw);
    if (ZP < 0 && w == 0) {
      lds_wait(L, L_CNT + 24, 4);
      __builtin_amdgcn_s_sleep(ZSL);
      const int fo = fl(F_Z + j * 32 + (l & 31));
#pragma unroll 1
      for (u32 s = 0; s < SPIN_CAP; ++s) {
        if (__all(ldflag(p.ws, fo) == tag)) break;
        __builtin_amdgcn_s_sleep(ZSL2);
      }
      asm volatile("" ::: "memory");
      lds_signal(L, L_CNT + 16);
    } else lds_check(L, L_CNT + 16, 1, seen);
    STAMP(9);
    if (TD && TC == 0) { ztouch(p, t, 1 + (w & 1), w >> 1, l); if (TD > 3 && !(w & 1)) ztouch(p, t, 3, w >> 1, l); SB(); }
#pragma unroll
    for (int ks = 0; ks < 7; ++ks) { ldb(ks, 0, bso); SB(); ldb(ks, 1, bso); SB(); if (ks & 1) next(bso); SB(); }
    if (TD && TC == 1) { ztouch(p, t, 1 + (w & 1), w >> 1, l); if (TD > 3 && !(w & 1)) ztouch(p, t, 3, w >> 1, l); SB(); }
    lds_wait(L, L_CNT + 12, 4);
    __builtin_amdgcn_s_setprio(3);
    STAMP(10);
#pragma unroll
    for (int ks = 0; ks < AD; ++ks)
#pragma unroll
      for (int m = 0; m < 4; ++m) rda(ks, m);
    SB();
    STAMP(5);
    auto wide = [&](int e) {
      const u32 z = (e & 2) ? zq[e >> 2].y : zq[e >> 2].x;
      if (e & 1) asm volatile("v_and_b32 %0, 0xffff0000, %1" : "=v"(z1[e]) : "v"(z));
      else asm volatile("v_lshlrev_b32 %0, 16, %1" : "=v"(z1[e]) : "v"(z));
    };
    auto chunk = [&](auto lastc) {
      constexpr bool LAST = lastc;
      unr<(LAST ? 6 : 8)>([&](auto sc) {
        constexpr int s = sc;
        WAITC(LAST && s ? 14 - 2 * s : 12, LAST && s == 2 ? 9 : 4);
        SB();
#pragma unroll
        for (int q = 0; q < 8; ++q) {
          mma(g2[q >> 1][q & 1], bf[s][q & 1], af[s & 3][q >> 1]);
          SB();
#define NOA 0
#define NOB 0
          if (!(q & 1) && (!NOA || LAST)) rda(s + 2, q >> 1);
          if ((q == 1 || q == 3) && (!LAST || s == 0) && (!NOB || LAST)) ldb(s + 7, q >> 1, bso);
          if (q == 5 && !(s & 1) && !LAST) next(bso);
          if (!LAST && q == (s == 5 ? 5 : 7) && (s == 5 || s == 6)) turn(~s & 1);
          if (LAST && s == 1 && (q & 1)) { zq[q - 1] = trd((LAS unsigned char *)(u32)zr[q >> 1], 0); zq[q] = trd((LAS unsigned char *)(u32)zr[q >> 1], 2048); }
          if (LAST && s >= 2 && (q & 1)) { wide(8 * (s - 2) + q - 1); wide(8 * (s - 2) + q); }
          SB();
        }
      });
      if (!LAST) BAR0();
    };
#pragma unroll 1
    for (int ck = 0; ck < 7; ++ck) {
      chunk(std::false_type{});
    }
    chunk(std::true_type{});
    u32 o[4][4];
    auto scale = [&](int m, int e) { asm volatile("v_mul_f32 %0, %0, %2\n\tv_mul_f32 %1, %1, %3" : "+v"(z1[8 * m + 2 * e]), "+v"(z1[8 * m + 2 * e + 1]) : "v"(g2[m][e >> 1][(2 * e) & 3]), "v"(g2[m][e >> 1][(2 * e + 1) & 3])); };
    auto pack = [&](int m, int e) {
      asm volatile("v_cvt_pk_bf16_f32 %0, %2, %3\n\tv_cvt_pk_bf16_f32 %1, %4, %5" : "=&v"(o[m][e]), "=&v"(o[m][e + 1])
                   : "v"(z1[8 * m + 2 * e]), "v"(z1[8 * m + 2 * e + 1]), "v"(z1[8 * m + 2 * e + 2]), "v"(z1[8 * m + 2 * e + 3]));
    };
    auto send = [&](int m) { st16c(v4u{o[m][0], o[m][1], o[m][2], o[m][3]}, rws, uo[m], 0); };
    auto keep = [&](int m) { sts16((LAS unsigned char *)(u32)uw, m * 1024, v4u{o[m][0], o[m][1], o[m][2], o[m][3]}); };
    auto two = [&](int s, int m, auto f0, auto f1) {
      mma(g2[m][0], bf[s][0], af[s & 3][m]); SB(); f0(); SB();
      mma(g2[m][1], bf[s][1], af[s & 3][m]); SB(); f1(); SB();
    };
    auto none = []() {};
    WAITC(2, 4);
    SB();
    two(6, 0, none, none);
    two(6, 1, none, [&]() { seen = lds_peek(L, L_CNT + 44); });
    WAITC(0, 1);
    SB();
    two(7, 0, none, none);
    two(6, 2, none, none);
    two(7, 1, none, none);
    two(6, 3, none, [&]() { asm volatile("s_nop 0"); scale(0, 0); scale(0, 1); });
    two(7, 2, [&]() { scale(0, 2); scale(0, 3); }, [&]() { pack(0, 0); pack(0, 2); });
    two(7, 3, [&]() { send(0); scale(1, 0); scale(1, 1); }, [&]() { keep(0); scale(1, 2); scale(1, 3); });
    STAMP(11);
    pack(1, 0); pack(1, 2);
    SB();
    send(1);
    keep(1);
    SB();
#pragma unroll
    for (int e = 0; e < 4; ++e) o[2][e] = pk2(f2{z1[16 + 2 * e], z1[17 + 2 * e]} * ((e & 1) ? hi2(g2[2][e >> 1]) : lo2(g2[2][e >> 1])));
    send(2);
    keep(2);
    SB();
#pragma unroll
    for (int e = 0; e < 4; ++e) o[3][e] = pk2(f2{z1[24 + 2 * e], z1[25 + 2 * e]} * ((e & 1) ? hi2(g2[3][e >> 1]) : lo2(g2[3][e >> 1])));
    send(3);
    keep(3);
    SB();
    u32 n36 = 0;
    if (l == 0) n36 = __hip_atomic_fetch_add((LAS u32 *)(L + L_CNT + 36), 1u, __ATOMIC_RELAXED, __HIP_MEMORY_SCOPE_WORKGROUP);
    SB();
    const f4 t4 = g2[0][0] + g2[0][1];
    const f2 t2 = lo2(t4) + hi2(t4);
    const bool bad = __builtin_amdgcn_ballot_w64(__builtin_isunordered(t2.x, t2.y)) != 0;
    SB();
    last = __builtin_amdgcn_readfirstlane(n36) == 3;
    if (HU && last) setflag(p.ws, F_U + i * 8 + j, tag);
    SB();
    if (HZ && __builtin_expect(bad, 0)) {
      FRESH();
      wait_flags(p.ws, fl(F_ZD + j * 32 + (l & 31)), tag);
      __builtin_amdgcn_fence(__ATOMIC_ACQUIRE, "agent");
      const rsrc_t ra = mk(p.wsp, S_ * S_ * 2);
      f4 h[4][2];
#pragma unroll
      for (int m = 0; m < 4; ++m) { const float b = lo16(*(LAS u16 *)(L + L_BS + (16 * m + c) * 2)); h[m][0] = h[m][1] = f4{b, b, b, b}; }
#pragma unroll 1
      for (int ks = 0; ks < 64; ++ks) {
        const v4u b0 = ld16c(rz, w * 2048 + l * 16, ks * 8192), b1 = ld16c(rz, w * 2048 + l * 16 + 1024, ks * 8192);
#pragma unroll
        for (int m = 0; m < 4; ++m) {
          const v4u a = ld16(ra, (64 * i + 16 * m + c) * 4096 + g * 16, ks * 64);
          h[m][0] = mfma(b0, a, h[m][0]);
          h[m][1] = mfma(b1, a, h[m][1]);
        }
      }
#pragma unroll
      for (int m = 0; m < 4; ++m) {
#pragma unroll
        for (int e = 0; e < 4; ++e) {
          const u32 z = (e & 1) ? zq[2 * m + (e >> 1)].y : zq[2 * m + (e >> 1)].x;
          o[m][e] = pk2(f2{lo16(z), hi16(z)} * ((e & 1) ? hi2(h[m][e >> 1]) : lo2(h[m][e >> 1])));
        }
        send(m);
      }
    }
  }
  SB();
  STAMP(7);
  {
    v4u uf[28], of[OP][4];
    int uso[7];
#pragma unroll
    for (int q = 1; q < 7; ++q) uso[q] = WS_UB + (i * 4 + w) * 28672 + mod7(q + j) * 4096;
    int uvo, yo, xo, oo, xr;
    auto ldu = [&](int ks) { uf[ks] = ld16c(rws, uvo + (ks & 3) * 1024, uso[ks >> 2]); };
    f4 o[4];
    auto rd = [&](int ks, int n) {
      if (ks == 14) { ob[n] += 57344; PIN(ob[n]); }
      of[ks % OP][n] = trd2((LAS unsigned char *)(u32)ob[n], (ks % 14) * 4096, 2048);
    };
    float xv[16], xn[16], kn[16], mr, rs;
    v2u kd[4];
    auto prep = [&](int q) {
      const int e = q >> 1;
      const u32 a = (e & 2) ? xe[e >> 2].y : xe[e >> 2].x, b = (e & 2) ? kd[e >> 2].y : kd[e >> 2].x;
      if (q & 1) asm volatile("v_fma_f32 %0, %2, %3, %4\n\tv_mul_f32 %1, %1, %0" : "=&v"(xn[e]), "+v"(kn[e]) : "v"(xv[e]), "v"(rs), "v"(mr));
      else if (e & 1) asm volatile("v_and_b32 %0, 0xffff0000, %2\n\tv_and_b32 %1, 0xffff0000, %3" : "=&v"(xv[e]), "=&v"(kn[e]) : "v"(a), "v"(b));
      else asm volatile("v_lshlrev_b32 %0, 16, %2\n\tv_lshlrev_b32 %1, 16, %3" : "=&v"(xv[e]), "=&v"(kn[e]) : "v"(a), "v"(b));
    };
    auto step = [&](auto ksc) {
      constexpr int ks = ksc;
#pragma unroll
      for (int n = 0; n < 4; ++n) {
        mmaf<ks == 0>(o[n], of[ks % OP][n], uf[ks]);
        SB();
        if (ks + OP < 28) rd(ks + OP, n);
        if (XAT == 4 && ks == 16) xe[n] = *(LAS v2u *)(L + (xr ^ (n << 5)));
        if (ks == 17) kd[n] = trd(L, yo + (L_KD - L_Z2) + n * 2048);
        if (ks == 17 && n == 3) { mr = *(LAS float *)(L + xo); rs = *(LAS float *)(L + xo + 256); }
        if (ks == 19 && n == 2) { mr = -mr * rs; PIN(mr); }
        if (ks >= 19 && ks < 27 && n < 2) prep(4 * (ks - 19) + 2 * n);
        if (ks >= 20 && n >= 2) prep(4 * (ks - 20) + 2 * n - 3);
        SB();
      }
    };
    lds_check(L, L_CNT + 44, 4, seen);
    STAMP(3);
#pragma unroll
    for (int q = 0; q < 4 * K1; ++q) rd(q >> 2, q & 3);
    SB();
    STAMP(4);
    if (!HU) {
      VM(0);
      last = lds_last(L, L_CNT + 20);
      if (last) setflag(p.ws, F_U + i * 8 + j, tag);
    }
    SB();
    STAMP(1);
    lds_wait(L, L_CNT + 36, 4);
    {
      const int ur = uw - w * 3072;
#pragma unroll
      for (int ks = 0; ks < 4; ++ks) uf[ks] = lds16((LAS unsigned char *)(u32)ur, ks * 4096);
    }
    seen = lds_peek(L, L_CNT + 40);
    SB();
    FRESH();
    uvo = l * 16;
    yo = L_Z2 + (ob[0] ^ (w << 5));
    xo = L_XST + (16 * w + c) * 4;
    oo = (64 * i + 16 * w + c) * 896 + j * 128 + (g & 1) * 32 + (g >> 1) * 16;
    xr = L_Z2 + 8192 + (16 * w + c) * 128 + (((g >> 1) ^ (c >> 1)) << 4) + (g & 1) * 8;
    int ufo = fl(F_U + i * 8 + (l < 7 ? l : 0));
    PIN(uvo); PIN(yo); PIN(xo); PIN(oo); PIN(xr); PIN(ufo);
    lds_check(L, L_CNT + 40, 4, seen);
#pragma unroll
    for (int q = 4 * K1; q < 4 * OP; ++q) rd(q >> 2, q & 3);
    SB();
    unr<UPOS>(step);
    u64 fv = 0;
    if (last) fv = ldflag(p.ws, ufo);
    SB();
    unr<4 - UPOS>([&](auto q) { step(std::integral_constant<int, UPOS + q>{}); });
    STAMP(2);
    if (last) {
      if (!__all(fv == tag)) wait_flags(p.ws, ufo, tag);
      lds_signal(L, L_CNT + 32);
    } else lds_wait(L, L_CNT + 32, 1);
    VM(0);
    STAMP(12);
#pragma unroll
    for (int ks = 4; ks < 4 + UW; ++ks) { ldu(ks); SB(); }
    BAR0();
    unr<24>([&](auto qc) {
      constexpr int ks = qc + 4;
      WAITC(ks < 28 - UW ? UW - 1 : 27 - ks, ks == 19 ? 8 : ks == 26 ? 0 : 15);
      SB();
      step(std::integral_constant<int, ks>{});
      if (ks + UW < 28) ldu(ks + UW);
      SB();
    });
    STAMP(13);
    SETTLE();
    const bool bad = __builtin_amdgcn_ballot_w64(__builtin_isunordered(o[3].x, o[3].x)) != 0;
    if ((HZ || HU) && __builtin_expect(bad, 0)) {
      wait_flags(p.ws, ufo + (F_UD - F_U) * FS, tag);
      __builtin_amdgcn_fence(__ATOMIC_ACQUIRE, "agent");
#pragma unroll
      for (int n = 0; n < 4; ++n) o[n] = f4{0, 0, 0, 0};
#pragma unroll 1
      for (int ks = 0; ks < 28; ++ks) {
        const v4u u = ld16c(rws, uvo + (ks & 3) * 1024, WS_UB + (i * 4 + w) * 28672 + mod7((ks >> 2) + j) * 4096);
#pragma unroll
        for (int n = 0; n < 4; ++n) o[n] = mfma(trd2((LAS unsigned char *)(u32)(ob[n] - 57344 + ks * 4096), 0, 2048), u, o[n]);
      }
    }
    v2u y[4], res[4];
    f2 a[8], e[8];
#pragma unroll
    for (int q = 0; q < 8; ++q) { a[q] = (q & 1) ? hi2(o[q >> 1]) : lo2(o[q >> 1]); e[q] = a[q] * -1.442695041f; }
#pragma unroll
    for (int q = 0; q < 8; ++q) e[q] = f2{__builtin_amdgcn_exp2f(e[q].x), __builtin_amdgcn_exp2f(e[q].y)} + 1.0f;
    asm volatile("s_barrier" : "+v"(e[0]), "+v"(e[1]), "+v"(e[2]), "+v"(e[3]), "+v"(e[4]), "+v"(e[5]), "+v"(e[6]), "+v"(e[7])::"memory");
    SB();
#pragma unroll
    for (int n = 0; n < 4; ++n) y[n] = trd(L, yo + n * 2048);
    SB();
#pragma unroll
    for (int q = 0; q < 8; ++q) e[q] = a[q] * f2{__builtin_amdgcn_rcpf(e[q].x), __builtin_amdgcn_rcpf(e[q].y)};
    asm volatile("" : "+v"(e[0]), "+v"(e[1]), "+v"(e[2]), "+v"(e[3]), "+v"(e[4]), "+v"(e[5]), "+v"(e[6]), "+v"(e[7]));
    SB();
    asm volatile("" : "+v"(y[0]), "+v"(y[1]), "+v"(y[2]), "+v"(y[3]));
#pragma unroll
    for (int q = 0; q < 8; ++q) {
      const u32 yy = (q & 1) ? y[q >> 1].y : y[q >> 1].x;
      const f2 tt = fma2(f2{lo16(yy), hi16(yy)}, f2{xn[2 * q], xn[2 * q + 1]}, f2{kn[2 * q], kn[2 * q + 1]});
      const u32 r = pk2(fma2(tt, e[q], f2{xv[2 * q], xv[2 * q + 1]}));
      if (q & 1) res[q >> 1].y = r; else res[q >> 1].x = r;
    }
    const rsrc_t ro = mk(p.out, S_ * D_ * 2);
    st16c(widen(res[0], res[1]), ro, oo, 0);
    st16c(widen(res[2], res[3]), ro, oo, 64);
  }
  STAMP_OUT(0);
}

static __device__ __forceinline__ void gemm_move(const Params &p, LAS unsigned char *L, const Tile t, int m) {
  STAMP_DECL;
  STAMP(0);
  const int i = t.i, j = t.j;
  int l = threadIdx.x & 63, g = l >> 4, c = l & 15;
  const srd_t sx_ = mks(p.x, S_ * D_ * 2), swin = mks(p.win, D_ * F_ * 2), swsp = mks(p.wsp, S_ * S_ * 2);
  const int lb = m * 1024, lb2 = m * 2048;
  const int pvo = (l >> 3) * 896 + j * 128 + (((((l & 7) >> 1) ^ g) << 5) | ((l & 1) << 4));
  const int rsw = ((l & 7) ^ (l >> 3)) << 4;
  const int xvo = (64 * i + 32 * (m >> 1) + 4 * (m & 1) + 8 * (l >> 5) + ((l >> 3) & 3)) * 896 + rsw;
  const int wvo = (4 * m + g) * 3584 + j * 256 + ((((c >> 1) ^ (g | ((m & 2) << 1))) << 5) | ((c & 1) << 4));
  int sb[7];
#pragma unroll
  for (int s = 0; s < 7; ++s) sb[s] = mod7(s + t.rot1);
  auto item = [&](auto nc) {
    constexpr int n = nc, s = n / 6, q = n % 6;
    if constexpr (n >= 42) dma<(n - 42) * 4096>(swin, wvo, (sb[(n - 42) >> 2] * 4 + ((n - 42) & 3)) * 57344, lb);
    else if constexpr (q < 2) dman<L_XR + (s & 3) * 8192 + q * 1024, NT_X>(sx_, xvo, sb[s] * 128 + q * 14336, lb2);
    else dma<(4 * s + q - 2) * 4096>(swin, wvo, (sb[s] * 4 + q - 2) * 57344 + 1792, lb);
  };
#define IC(n) std::integral_constant<int, (n)>{}
  const rsrc_t rwsp = mk(p.wsp, S_ * S_ * 2), rws = mk(p.ws, WS_BYTES);
  v4u ar[32];
  const int avo0 = (64 * i + 16 * m + (l >> 3)) * 4096 + (((l & 7) ^ g) << 4), avo1 = avo0 ^ 64;
  auto aso = [&](int n) { return (((n >> 1) + t.rot2) & 31) * 128 + (n & 1) * 32768; };
  auto lda = [&](int n) { const int avo = (n & 1) ? avo1 : avo0; ar[n] = n < 10 ? __builtin_amdgcn_raw_buffer_load_b128(rwsp, avo, aso(32 + n), NT_SP * 2) : ld16(rwsp, avo, aso(32 + n)); SB(); };
  auto items = [&](auto n0, auto cnt) { unr<cnt>([&](auto q) { constexpr int n = ORD.seq[n0 + q]; if constexpr (n < 70) item(IC(n)); else lda(n - 70); }); };
  constexpr const int *NI = ORD.bar;
  items(IC(0), IC(NI[0]));
  u64 tag = epoch_load(p.ws);
  EPOCH_WAIT(tag);
  tag += 1;
  {
    const v4u ones = {0x3f803f80u, 0x3f803f80u, 0x3f803f80u, 0x3f803f80u};
    f4 sx = {0, 0, 0, 0}, gr = {0, 0, 0, 0};
    int xo0 = L_XR + (16 * m + c) * 128 + ((g ^ (c & 7)) << 4), xo1 = xo0 ^ 64;
    PIN(xo0); PIN(xo1);
    unr<7>([&](auto sc) {
      constexpr int s = sc;
      VM(NI[s] - 1 - ORD.last(6 * s, 6 * s + 6) + (TLG && ORD.last(6 * s, 6 * s + 6) < NI[0]));
      v4u xs[2];
      xs[0] = lds16(L, xo0 + (s & 3) * 8192);
      xs[1] = lds16(L, xo1 + (s & 3) * 8192);
      STAMPA(2 * s + 1);
      BAR0();
      STAMPA(2 * s + 2);
      if constexpr (s < 6) items(IC(NI[s]), IC(NI[s + 1] - NI[s]));
      SB();
      if (PMS) __builtin_amdgcn_s_setprio(3);
#pragma unroll
      for (int q = 0; q < 2; ++q) { sx = mfma(xs[q], ones, sx); gr = mfma(xs[q], xs[q], gr); }
      SB();
      if (PMS) __builtin_amdgcn_s_setprio(1);
      SB();
      if constexpr (s == 6) items(IC(NI[6]), IC(LASTA));
    });
    SB();
    const int e = c & 3;
    const float s1 = e & 2 ? (e & 1 ? sx.w : sx.z) : (e & 1 ? sx.y : sx.x);
    const float s2 = e & 2 ? (e & 1 ? gr.w : gr.z) : (e & 1 ? gr.y : gr.x);
    const float mean = fmul(s1, 1.0f / 448.0f);
    const float r = inv_sigma(ffma(-mean, mean, fmul(s2, 1.0f / 448.0f)));
    if ((c >> 2) == g) {
      const int row = 32 * (m >> 1) + 8 * g + 4 * (m & 1) + e;
      *(LAS float *)(L + L_XST + row * 4) = rbf(mean);
      *(LAS float *)(L + L_XST + 256 + row * 4) = r;
    }
    SB();
    items(IC(NI[6] + LASTA), IC(NI[7] - NI[6] - LASTA));
  }
  BARW();
  STAMP(3);
  asm volatile("s_mov_b32 m0, %3\n\ts_nop 0\n\tbuffer_load_dword %0, %1, %2 offen lds" ::"v"(l * 4), "s"(mks(p.bsp, S_ * 2)), "s"(i * 128), "n"(L_BS) : "m0", "memory");
  unr<70 + NS - NI[7]>([&](auto q) {
    constexpr int n = NI[7] + q;
    VM(W1);
    if constexpr (n == ORD.last(42, 58) + 1 + W1) lds_signal(L, L_CNT);
    if constexpr (n == ORD.last(42, 70) + 1 + W1) lds_signal(L, L_CNT + 28);
    items(IC(n), IC(1));
  });
  static_assert(ORD.last(42, 58) + 1 + W1 < 70 + NS && NI[7] <= ORD.last(42, 58) + 1 + W1, "");
  STAMP(4);
  {
    const u32 tagA = (u32)tag, tagB = (u32)(tag >> 32) ^ 0x9e3779b9u;
    const int so = WS_ST + (i * 2 + (m >> 1)) * 8192 + (m & 1) * 256;
    const int vo = g * 512 + c * 16, vo3 = (g & 1) * 512 + c * 16 + 2048;
    v4u sg[4], zt[4];
    auto ldst = [&]() {
      sg[0] = ld16c(rws, vo, so); sg[1] = ld16c(rws, vo + 2048, so); sg[2] = ld16c(rws, vo, so + 4096); sg[3] = ld16c(rws, vo3, so + 4096);
    };
    lds_wait(L, L_CNT + 4, 4);
    __builtin_amdgcn_s_setprio(PM2);
    const int zo = ((m >> 1) * 8 + (l >> 5)) * 1024 + ((m & 1) * 32 + (l & 31)) * 16;
#pragma unroll
    for (int q = 0; q < 4; ++q) zt[q] = lds16(L, L_Z2 + zo + q * 2048);
    SB();
    if (PW < 60) VM(PW);
    ldst();
    SB();
#pragma unroll
    for (int n = 0; n < NB; ++n) lda(NS + n);
    SB();
    if constexpr (ORD.last(42, 70) + 1 + W1 >= 70 + NS) { VM(69 + NS - ORD.last(42, 70) + 4 + NB); lds_signal(L, L_CNT + 28); }
    SB();
    float zu[32];
#pragma unroll
    for (int q = 0; q < 16; ++q) { const u32 z = zt[q >> 2][q & 3]; zu[2 * q] = lo16(z); zu[2 * q + 1] = hi16(z); PIN(zu[2 * q]); PIN(zu[2 * q + 1]); }
    int zso = WS_ZB + (j * 64 + 2 * i) * 8192 + zo;
    PIN(zso);
    STAMP(5);
    auto late = [&]() {
      u32 d = 0;
#pragma unroll
      for (int q = 0; q < 4; ++q) d |= (sg[q].y ^ tagA) | (sg[q].w ^ tagB);
      return __builtin_amdgcn_ballot_w64(d != 0) != 0;
    };
    if (__builtin_expect(late(), 0)) {
#pragma unroll 1
      for (u32 s = 0; s < SPIN_CAP; ++s) {
        asm volatile("" ::: "memory");
        ldst();
        if (!late()) break;
      }
    }
    STAMP(2);
    float s = fadd(fadd(asf(sg[0].x), asf(sg[1].x)), fadd(asf(sg[2].x), g < 2 ? asf(sg[3].x) : 0.f));
    float u = fadd(fadd(asf(sg[0].z), asf(sg[1].z)), fadd(asf(sg[2].z), g < 2 ? asf(sg[3].z) : 0.f));
    {
      v2u a = __builtin_amdgcn_permlane16_swap(asu(s), asu(u), false, false);
      const float h = fadd(asf(a.x), asf(a.y));
      a = __builtin_amdgcn_permlane32_swap(asu(h), asu(h), false, false);
      const float k = fadd(asf(a.x), asf(a.y));
      a = __builtin_amdgcn_permlane16_swap(asu(k), asu(k), false, false);
      s = asf(a.x);
      u = asf(a.y);
    }
    const float mean = fmul(s, 1.0f / 896.0f);
    const float r = inv_sigma(ffma(-mean, mean, fmul(u, 1.0f / 896.0f)));
    if (g < 2) *(LAS float *)(L + L_ZST + g * 256 + (16 * m + c) * 4) = g ? r : -rbf(mean) * r;
    SB();
    const u32 cl = HZ && CLZ == 15 ? lds_peek(L, L_CNT + 52) : 0;
    f4 zb[2], zr[2];
#pragma unroll
    for (int q = 0; q < 2; ++q) {
      zb[q] = ldsf4(L, L_ZST + (16 * m + 8 * (g & 1) + 4 * q) * 4);
      zr[q] = ldsf4(L, L_ZST + 256 + (16 * m + 8 * (g & 1) + 4 * q) * 4);
    }
    SB();
#pragma unroll
    for (int n = 0; n < NB2; ++n) lda(NS + NB + n);
    if (HZ && CLZ == 15) lds_check(L, L_CNT + 52, 4, cl);
#pragma unroll
    for (int q = 0; q < 4; ++q) {
      u32 o[4];
#pragma unroll
      for (int e = 0; e < 4; ++e) {
        o[e] = pk(ffma(zu[8 * q + 2 * e], zr[e >> 1][(2 * e) & 3], zb[e >> 1][(2 * e) & 3]), ffma(zu[8 * q + 2 * e + 1], zr[e >> 1][(2 * e + 1) & 3], zb[e >> 1][(2 * e + 1) & 3]));
      }
      st16c(v4u{o[0], o[1], o[2], o[3]}, rws, zso, q * 2048);
      SB();
    }
    if (HZ) if (lds_last(L, L_CNT + 24)) setflag(p.ws, F_Z + j * 32 + i, tag);
    STAMP(6);
  }
  __builtin_amdgcn_s_setprio(PM3);
  constexpr int NA = NS + NB + NB2, NQ = 64 - NA;
  auto zflag = [&]() {
    if (!HZ) if (lds_last(L, L_CNT + 24)) setflag(p.ws, F_Z + j * 32 + i, tag);
    if (HZ) if (lds_last(L, L_CNT + 48)) setflag(p.ws, F_ZD + j * 32 + i, tag);
    SB(); STAMP(7);
  };
  auto ring = [&](auto nc) { constexpr int n = nc; dman<(n >> 1) * 8192 + (n & 1) * 1024, NT_RING>(swsp, (n & 1) ? avo1 : avo0, aso(n), lb2); };
  auto piece = [&](auto qc) {
    constexpr int n = qc - NR1;
    if constexpr (n < 0) lda(NA + qc); else if constexpr (n < 32) ring(IC(n < 0 || n > 31 ? 0 : n)); else lda(NA + qc - 32);
  };
  constexpr int XE[4] = {NR1 + X0 < NQ ? NR1 + X0 : NQ, NR1 + X1 < NQ ? NR1 + X1 : NQ, NR1 + X2 < NQ ? NR1 + X2 : NQ, NQ};
  static_assert(X0 >= 14 + 2 * AD && X1 >= 22 + 2 * AD && X2 >= 32 && NA + NR1 >= 19 && NA + NR1 <= 32 && W2 < NR1 + 5 + 2 * AD + RD, "");
  unr<XE[0]>([&](auto qc) {
    constexpr int n = qc - NR1;
    if constexpr (qc == W2) { VM(W2); zflag(); }
    if constexpr (qc > W2 && n < 14 + 2 * AD + RD) VM(RD - 1);
    if constexpr (n == 0) { lds_wait(L, L_CNT + 8, 4); STAMP(8); }
    if constexpr (n == 5 + 2 * AD + RD) lds_signal(L, L_CNT + 12);
    if constexpr (n == 28) lds_wait(L, L_CNT + 24, 4);
    if constexpr (n == ZP) if (m == 0) { wait_flags(p.ws, fl(F_Z + j * 32 + (l & 31)), tag); lds_signal(L, L_CNT + 16); }
    piece(qc);
  });
  STAMP(11);
  __builtin_amdgcn_s_setprio(0);
  const srd_t swout = mks(p.wout, 896 * D_ * 2);
  auto ldt = [&](auto nc) { constexpr int n = nc; dma<n * 4096>(swout, pvo, WSO(n), lb); SB(); __builtin_amdgcn_s_sleep(TS); SB(); };
  static_assert(T5 <= 8 && T6 <= 16 && T5 <= T6 && T6 <= NO0, "");
  v4u wr[28];
  const rsrc_t rwo = mk(p.wout, 896 * D_ * 2);
  auto trk = [&](auto cc) {
    constexpr int tr[7] = TRK, ck = cc;
    int n = NO1;
    for (int q = 0; q < ck; ++q) n += tr[q];
    if (RE == 3)
#pragma unroll
      for (int q = 0; q < tr[ck]; ++q) { if (q) __builtin_amdgcn_s_sleep(TRS); SB(); wr[n + q] = ld16(rwo, pvo, WSO(n + q)); SB(); }
  };
  int awo = m * 2048 + l * 16, awh = awo + 65536;
  PIN(awo); PIN(awh);
  {
    constexpr int cum[6] = {0, 6, 12, 19, 25, 32};
    unr<5>([&](auto kc) {
      constexpr int k = kc;
      if constexpr (k < 2) VM(XE[k] - NR1 - 14 - 2 * AD - 8 * k);
      if constexpr (k == 2) VM(XE[2] - NR1 - 32);
      BARW();
      if constexpr (TD && k + TD < 8) { ztouch(p, t, k + TD, m, l); SB(); }
      if constexpr (HU && k == CLU) {
        const int o = WS_UB + (i * 112 + 4 * j) * 1024 + m * 28672 + l * 16;
#pragma unroll
        for (int q = 0; q < 4; ++q) st16c(v4u{~0u, ~0u, ~0u, ~0u}, rws, o, q * 1024);
        SB();
      }
      constexpr int nq = k < 3 ? XE[k + 1] - XE[k] : 0, nw = cum[k + 1] - cum[k];
      unr<(nq > nw ? nq : nw)>([&](auto qc) {
        constexpr int n = cum[k] + qc;
        if constexpr (qc < nq) piece(IC(XE[k < 3 ? k : 0] + qc));
        if constexpr (qc < nw) sts16(L, ((n & 16) ? awh : awo) + ((n >> 1) & 7) * 8192 + (n & 1) * 1024, ar[n]);
        SB();
        __builtin_amdgcn_s_sleep(WSL);
        SB();
      });
      trk(IC(k + 1));
    });
  }
  unr<T5>(ldt);
  BARW();
  unr<T6 - T5>([&](auto q) { ldt(IC(T5 + q)); });
  VM(T6);
  trk(IC(6));
  BAR0();
  STAMP(9);
  {
    auto ldo = [&](auto nc) { constexpr int n = nc; VM(WOW); dma<n * 4096>(swout, pvo, WSO(n), lb); };
    const srd_t sws = mks(p.ws, WS_BYTES);
    const int yfo = fl(F_Y + ((64 * j + 16 * m + min(g * 7, 15)) / 14) * 4 + (l & 3));
    const int yvo = (64 * j + 16 * m + (l >> 3)) * 4096 + i * 128 + (((((l & 7) >> 1) ^ g) << 5) | ((l & 1) << 4));
    auto ldy = [&]() { dmac<L_Z2>(sws, yvo, WS_YB, lb2); dmac<L_Z2 + 1024>(sws, yvo, WS_YB + 32768, lb2); };
    __builtin_amdgcn_s_sleep(WSLP);
    static_assert(NO1 >= 4 + OP && NO1 <= 24, "");
    unr<NO0 - T6>([&](auto q) { ldo(IC(T6 + q)); });
    VM(NO0 - K1);
    lds_signal(L, L_CNT + 44);
    STAMP(1);
    const int xto = (64 * i + 16 * m + (l >> 3)) * 896 + j * 128 + (((l & 7) ^ g) << 4);
    lds_wait(L, L_CNT + 36, 4);
    __builtin_amdgcn_s_setprio(PMT);
    if (XAT == 4) { dma<L_Z2 + 8192>(sx_, xto, 0, lb2); dma<L_Z2 + 9216>(sx_, xto ^ 64, 7168, lb2); }
    unr<NO1 - NO0>([&](auto q) { ldo(std::integral_constant<int, NO0 + q>{}); });
    const rsrc_t rwout = mk(p.wout, 896 * D_ * 2);
#pragma unroll
    for (int n = NO1; n < 28; ++n) {
      if (n == NO1 + SIGE) { VM(SIGE + (XAT == 4 && NO0 == NO1 ? 2 : 0)); lds_signal(L, L_CNT + 40); STAMP(10); }
      ar[n] = ld16(rwout, pvo, WSO(n));
      SB();
    }
    STAMP(13);
    int wwo = m * 1024 + l * 16 + 49152;
    PIN(wwo);
#pragma unroll
    for (int n = NO1; n < 28; ++n) { sts16(L, wwo + (n - 12) * 4096, ar[n]); SB(); }
    STAMP(12);
    const bool early = !YLATE && __all(ldflag(p.ws, yfo) == tag);
    if (early) ldy();
    if (YLATE && !RREG) DRAIN();
    BARW();
    if ((HZ || HU) && m == 0) setflag(p.ws, F_UD + i * 8 + j, tag);
    if (!early) { wait_flags(p.ws, yfo, tag); ldy(); }
  }
  DRAIN();
  BAR0();
  STAMP_OUT(1);
}

constexpr int C_CP = 4160, C_KV = 4 * C_CP, C_SLOT = C_KV + 4608;

constexpr int C_EX = 131072;

template <int H> static __device__ __forceinline__ void conv_compute(const Params &p, LAS unsigned char *L, int v, int w) {
  STAMP_DECL;
  STAMP(0);
  __builtin_amdgcn_s_setprio(PCV);
  u64 tag = epoch_load(p.ws);
  constexpr int ord[8] = {H ? 0 : 2, H ? 1 : 3, H ? 6 : 4, H ? 7 : 5, H ? 2 : 0, H ? 3 : 1, H ? 4 : 6, H ? 5 : 7};
  const int l = threadIdx.x & 63, g = l >> 4, c = l & 15, ch = w >> 1;
  const rsrc_t rws = mk(p.ws, WS_BYTES);
  {
    const int t = threadIdx.x;
    const srd_t sws = mks(p.ws, WS_BYTES);
    touch(sws, t < 128 ? WS_ST + v * 16384 + t * 128 : WS_FL + (v * 52 + min(t & 63, 51)) * 128, 0);
  }
  const int hro = ch * C_SLOT + (c & 3) * C_CP + g * 16 + (c >> 2) * 8 + H * 64;
  const int kro = ch * C_SLOT + C_KV + (254 - 2 * c + g) * 16 - 7 * 512 + H * 64;
  v2u hh[2][16];
  v4u kf[2][8];
  auto rd = [&](int ho, int ko, int q, int n) {
    const int d = n / 3;
    if (n % 3 == 2) kf[q & 1][7 - d] = lds16(L, ko + d * 512 + q * 128);
    else hh[q & 1][2 * d + n % 3] = *(LAS v2u *)(L + ho + d * 512 + q * 128 + (n % 3) * 8);
  };
  f4 acc[8], in[4];
#pragma unroll
  for (int n = 0; n < 8; ++n) acc[n] = f4{0, 0, 0, 0};
  int xw = C_EX + w * 4096 + l * 16, xr = xw ^ 4096;
  int yo = WS_YB + (14 * v + ch) * 4096 + c * 32 + (g & 1) * 512 + (g >> 1) * 16 + 0x40000000;
  PIN(xw); PIN(xr);
  v2u y[4];
  auto fin = [&](int k) {
    if (k < 2) { in[2 * k] = ldsf4(L, xr + k * 2048); in[2 * k + 1] = ldsf4(L, xr + k * 2048 + 1024); }
    else if (k < 10) { const int n = (k - 2) >> 1, e = (k & 1) * 2; in[n][e] = fadd(in[n][e], acc[ord[4 + n]][e]); in[n][e + 1] = fadd(in[n][e + 1], acc[ord[4 + n]][e + 1]); }
    else if (k < 14) { const int n = k - 10; y[n] = v2u{pk(in[n].x, in[n].y), pk(in[n].z, in[n].w)}; PIN(y[n]); }
    else if (k < 16) st16c(widen(y[2 * k - 28], y[2 * k - 27]), rws, yo, ord[2 * k - 24] * 512);
  };
  EPOCH_WAIT(tag);
  tag += 1;
  if (KV1) BAR0();
  BAR0();
  STAMP(2);
#pragma unroll
  for (int n = 0; n < 24; ++n) { rd(hro, kro, 0, n); SB(); }
  WAITC(63, 0);
  int set = 0;
#pragma unroll 1
  for (int rho = 0; rho < 7; ++rho) {
    const int nset = set == 4 * C_SLOT ? 0 : set + 2 * C_SLOT;
    int ho = hro + set, ko = kro + set;
    unr<4>([&](auto qc) {
      constexpr int q = qc;
      if (q == 3) { ho = hro + nset - 512; ko = kro + nset - 512; PIN(ho); PIN(ko); }
      if (q > 0) WAITC(63, 0);
      if (q == 3) BAR0();
      SB();
      int k = 0;
#pragma unroll
      for (int hf = 0; hf < 2; ++hf)
#pragma unroll
        for (int P = 0; P < 8; ++P)
#pragma unroll
          for (int n = 0; n < 4; ++n) {
            const int tl = ord[4 * hf + n];
            if (P > tl) continue;
            const v2u a = hh[q & 1][2 * P], b = hh[q & 1][2 * P + 1];
            if (q == 0 && P == 0) mma0(acc[tl], v4u{a.x, a.y, b.x, b.y}, kf[q & 1][tl - P]);
            else mma(acc[tl], v4u{a.x, a.y, b.x, b.y}, kf[q & 1][tl - P]);
            SB();
            if (q == 0) {
              if (k < 2) fin(k);
              else if (k < 9) { rd(ho, ko, 1, 2 * k - 4); SB(); rd(ho, ko, 1, 2 * k - 3); }
              else if (k < 17) fin(k - 7);
              else if (k < 22) { rd(ho, ko, 1, 2 * k - 20); SB(); rd(ho, ko, 1, 2 * k - 19); }
              else if (k < 26) fin(k - 12);
            } else if (k < 24) rd(ho, ko, q + 1, k);
            else if (q == 2 && (k == 27 || k == 30)) fin(k / 3 + 5);
            else if (q == 3 && k < 28) sts16(L, xw + (k - 24) * 1024, __builtin_bit_cast(v4u, acc[ord[k - 24]]));
            SB();
            ++k;
          }
    });
    yo += rho ? 8192 : -0x40000000;
    set = nset;
    WAITC(63, 0);
    BAR0();
    if (rho == 0) STAMP(3);
    if (rho == 1) STAMP(4);
    if (rho == 3) STAMP(5);
    if (rho == 5) STAMP(6);
  }
  SETTLE();
#pragma unroll
  for (int k = 0; k < 16; ++k) fin(k);
  STAMP(7);
  DRAIN();
  setflag(p.ws, F_Y + v * 4 + w, tag);
  STAMP_OUT(0);
}

static __device__ __forceinline__ void conv_stage(const Params &p, LAS unsigned char *L, int v, int m) {
  STAMP_DECL;
  STAMP(0);
  const int l = threadIdx.x & 63;
  const srd_t sh = mks(p.h, D_ * S_ * 2), sws = mks(p.ws, WS_BYTES), swout = mks(p.wout, 896 * D_ * 2);
  const rsrc_t rws = mk(p.ws, WS_BYTES);
  const int vo = m * 1024 + l * 16;
  auto stage_h = [&](int rho, int set) {
    const int so = (14 * v + 2 * rho) * 4096, lb = set + m * 1024;
    unr<8>([&](auto qc) { constexpr int q = qc; dman<(q >> 2) * C_SLOT + (q & 3) * C_CP + 32, NT_H>(sh, vo, so + (q >> 2) * 4096 + (q & 3) * 2 + 2, lb); });
  };
  auto stage_kv = [&](int rho, int set) {
    const int so = WS_KVR + (14 * v + 2 * rho) * 4096, lb = set + m * 1024;
    dmac<C_KV>(sws, vo, so, lb);
    dmac<C_SLOT + C_KV>(sws, vo, so + 4096, lb);
  };
  u64 head;
  asm volatile("global_load_dwordx2 %0, %1, %2" : "=&v"(head) : "v"((14 * v + (l < 56 ? l >> 2 : 0)) * 4096), "s"(p.h) : "memory");
  stage_h(0, 0);
  u64 tag = epoch_load(p.ws);
  stage_h(1, 2 * C_SLOT);
  asm volatile("s_waitcnt vmcnt(16)" : "+v"(head)::"memory");
  head <<= 48 - 16 * (l & 3);
  auto put_head = [&](int rho, int set) {
    if (m == 0 && (l >> 3) == rho) *(LAS u64 *)(L + set + ((l >> 2) & 1) * C_SLOT + (l & 3) * C_CP + 24) = head;
  };
  {
    const v4u z = {0, 0, 0, 0};
    const int t = m * 64 + l;
    if (t < 192) sts16(L, (t >> 5) * C_SLOT + C_KV + 4096 + (t & 31) * 16, z);
    if (t < 24) { sts16(L, (t >> 2) * C_SLOT + (t & 3) * C_CP, z); *(LAS u64 *)(L + (t >> 2) * C_SLOT + (t & 3) * C_CP + 16) = 0; }
  }
  put_head(0, 0);
  put_head(1, 2 * C_SLOT);
  put_head(2, 4 * C_SLOT);
  EPOCH_WAIT(tag);
  tag += 1;
  if (m == 0) {
    __builtin_amdgcn_s_sleep(KSLEEP);
    wait_flags(p.ws, flk(F_KV + ((14 * v + (l < 32 ? 0 : 13)) >> 4) * 32 + (l & 31)), tag);
  }
  BAR0();
  STAMP(1);
  stage_kv(0, 0);
  stage_kv(1, 2 * C_SLOT);
  VM(2);
  BARW();
  stage_h(2, 4 * C_SLOT);
  stage_kv(2, 4 * C_SLOT);
  STAMP(2);
  setflagk(p.ws, F_HI + v * 4 + m, tag);
  int set = 0;
#pragma unroll 1
  for (int rho = 0; rho < 7; ++rho) {
    if (rho == 2) STAMP(3);
    if (rho == 3) STAMP(8);
    if (rho == 0) VM(11); else if (rho >= 5) DRAIN(); else if (rho > WOR && rho <= WOR + 4 / WOK) VM(10 + WTN * WOK); else VM(10);
    if (rho == 2) STAMP(4);
    if (rho == 3) STAMP(9);
    BARW();
    if (rho == 2) STAMP(5);
    if (rho == 3) STAMP(10);
    BAR0();
    if (rho == 2) STAMP(6);
    if (rho == 3) STAMP(11);
    if (rho < 4) {
      stage_h(rho + 3, set);
      if (rho == 2) STAMP(7);
      if (rho == 3) STAMP(12);
      stage_kv(rho + 3, set);
      put_head(rho + 3, set);
    }
    if (rho >= WOR && rho < WOR + 4 / WOK) {
#pragma unroll
      for (int q = 0; q < WOK; ++q) {
        const int n = ((v & 3) * 4 + (rho - WOR) * WOK + q) * 224 + m * 56 + min(l, 55);
        touch(swout, (n >> 2) * 896 + ((n & 3) + 3 * ((v >> 2) & 1)) * 128, 0);
      }
    }
    set = set == 4 * C_SLOT ? 0 : set + 2 * C_SLOT;
    if (rho == 4 && v == 0 && m == 0) {
#pragma unroll 1
      for (u32 s = 0; s < SPIN_CAP; ++s) {
        u64 f[16], d = 0;
#pragma unroll
        for (int q = 0; q < 16; ++q) f[q] = ldflag(p.ws, flk(q * 64 + l));
#pragma unroll
        for (int q = 0; q < 16; ++q) d |= f[q] ^ tag;
        if (__all(d == 0)) break;
        asm volatile("" ::: "memory");
      }
      if (l == 0) stflag(p.ws, WS_EPOCH, tag);
    }
  }
  STAMP_OUT(1);
}

#define CODE_LINES 414
#define CPFW 4
__global__ void __launch_bounds__(512, 2) fused_kernel(Params p) {
  extern __shared__ __attribute__((aligned(16))) unsigned char smem[];
  LAS unsigned char *L = (LAS unsigned char *)smem;
  const int b = blockIdx.x, x = b & 7, r = b >> 3, wv = __builtin_amdgcn_readfirstlane(threadIdx.x >> 6);
  {
    u64 pc;
    const u32 line = min((u32)(r * 16 + (threadIdx.x & 15)), (u32)CODE_LINES);
    asm volatile("s_cmp_lg_u32 %2, %4\n\ts_cbranch_scc1 1f\n\ts_getpc_b64 %0\n\ts_mov_b32 m0, %3\n\ts_nop 0\n\tglobal_load_lds_dword %1, %0\n1:\n\t"
                 "s_setprio 3\n\ts_cmp_lt_u32 %2, 4\n\ts_cbranch_scc1 2f\n\ts_setprio 1\n2:"
                 : "=&s"(pc) : "v"(line * 128), "s"(wv), "n"(LDS_BYTES - 256), "n"(CPFW) : "m0", "scc", "memory");
  }
  if (r >= 28) {
    const int v = x * 4 + (r - 28);
    if (wv < 4) { if (wv & 1) conv_compute<1>(p, L, v, wv); else conv_compute<0>(p, L, v, wv); }
    else conv_stage(p, L, v, wv - 4);
  } else {
    Tile t;
    const int pg = x >> 1, o = x & 1;
    if (r < 24) { t.i = 8 * pg + r / 3; t.j = 4 * o + r % 3; }
    else { t.i = 8 * pg + 4 * o + (r - 24); t.j = 3; }
    t.rot1 = 2 * ((t.i + t.j) & 3);
    t.rot2 = (8 * pg + 8 + (32 * (t.j == 3 ? 6 : 2 * (t.j & 3) + o)) / 7 + DELTA * (t.i & 7)) & 31;
    t.e = r < 24 ? t.i & 7 : 2 * (t.i & 3);
    t.ne = r < 24 ? 1 : 2;
    v2u shk = {0, 0};
    u32 dvr = 0;
    if (wv < 4) kv_fetch(p, t, wv, shk, dvr);
    SB();
    asm volatile("" ::: "memory");
    if (wv < 4) gemm_compute(p, L, t, wv, shk, dvr);
    else gemm_move(p, L, t, wv - 4);
  }
}

extern "C" void kernel_launch(void *const *d_in, const int *in_sizes, int n_in, void *d_out, int out_size, void *d_ws, size_t ws_size,
                              hipStream_t stream) {
  Params p;
  p.x = (const u16 *)d_in[0];
  p.k = (const u16 *)d_in[1];
  p.v = (const u16 *)d_in[2];
  p.shk = (const u16 *)d_in[3];
  p.h = (const u16 *)d_in[4];
  p.dv = (const u16 *)d_in[5];
  p.win = (const u16 *)d_in[7];
  p.wsp = (const u16 *)d_in[8];
  p.bsp = (const u16 *)d_in[9];
  p.wout = (const u16 *)d_in[10];
  p.out = (u16 *)d_out;
  p.ws = (unsigned char *)d_ws;
  (void)hipFuncSetAttribute((const void *)fused_kernel, hipFuncAttributeMaxDynamicSharedMemorySize, LDS_BYTES);
  fused_kernel<<<256, 512, LDS_BYTES, stream>>>(p);
}
```

```cpp
#define HCOND (b < 8)
#define PAIRX
#include <hip/hip_runtime.h>
#include <stdint.h>
#include <type_traits>

typedef unsigned short u16;
typedef unsigned int u32;
typedef unsigned long long u64;
typedef u32 v4u __attribute__((ext_vector_type(4)));
typedef u32 v2u __attribute__((ext_vector_type(2)));
typedef float f4 __attribute__((ext_vector_type(4)));
typedef float f2 __attribute__((ext_vector_type(2)));
typedef short s4 __attribute__((ext_vector_type(4)));
typedef __bf16 bf8 __attribute__((ext_vector_type(8)));
typedef __bf16 bf2 __attribute__((ext_vector_type(2)));
#define LAS __attribute__((address_space(3)))
#define GAS __attribute__((address_space(1)))
typedef __amdgpu_buffer_rsrc_t rsrc_t;
typedef int srd_t __attribute__((ext_vector_type(4)));

constexpr int S_ = 2048, D_ = 448, F_ = 1792;
constexpr int WS_EPOCH = 0;
#define FS 128
#define FULL 1
constexpr int WS_FL = 4096;
constexpr int F_KV = 0;
constexpr int F_HI = 896;
constexpr int F_Z = 1024;
constexpr int F_U = 1280;
constexpr int F_ZD = 256;
constexpr int F_UD = 512;
constexpr int F_Y = 1536;
constexpr int WS_ST = 262144;
constexpr int WS_ZB = 1 << 20;
constexpr int WS_UB = 5 << 20;
constexpr int WS_KVR = 9 << 20;
constexpr int WS_YB = 11 << 20;
constexpr int WS_STAMP = 14 << 20;
constexpr int WS_BYTES = 16 << 20;
constexpr int LDS_BYTES = 163840;
constexpr u32 SPIN_CAP = 1u << 16;

struct Params {
  const u16 *x, *k, *v, *shk, *h, *dv, *win, *wsp, *bsp, *wout;
  u16 *out;
  unsigned char *ws;
};

#define SB() __builtin_amdgcn_sched_barrier(0)
#define BARW() do { SB(); asm volatile("s_waitcnt lgkmcnt(0)\n\ts_barrier" ::: "memory"); SB(); } while (0)
#define BAR0() do { SB(); asm volatile("s_barrier" ::: "memory"); SB(); } while (0)
#define FRESH() do { asm volatile("" : "+v"(l)); g = l >> 4; c = l & 15; } while (0)
#define DRAIN() asm volatile("s_waitcnt vmcnt(0)" ::: "memory")
#define VM(n) asm volatile("s_waitcnt vmcnt(%0)" ::"n"(n) : "memory")
#define PIN(x) asm volatile("" : "+v"(x))
#define STAMP(n) SB()
#define STAMPA(n) do { } while (0)
#define STAMP_DECL do { } while (0)
#define STAMP_OUT(slot) do { } while (0)

static __device__ __forceinline__ rsrc_t mk(const void *p, int bytes) { return __builtin_amdgcn_make_buffer_rsrc((void *)p, 0, bytes, 0x00020000); }
static __device__ __forceinline__ v4u ld16(rsrc_t r, int vo, int so) { return __builtin_amdgcn_raw_buffer_load_b128(r, vo, so, 0); }
static __device__ __forceinline__ v4u ld16c(rsrc_t r, int vo, int so) { return __builtin_amdgcn_raw_buffer_load_b128(r, vo, so, 16); }
static __device__ __forceinline__ u32 ld4(rsrc_t r, int vo, int so) { return __builtin_amdgcn_raw_buffer_load_b32(r, vo, so, 0); }
static __device__ __forceinline__ void st16c(v4u v, rsrc_t r, int vo, int so) { __builtin_amdgcn_raw_buffer_store_b128(v, r, vo + so, 0, 16); }
static __device__ __forceinline__ srd_t mks(const void *p, int bytes) { return srd_t{(int)(u64)p, (int)((u64)p >> 32) & 0xffff, bytes, 0x00020000}; }
template <int IMM> static __device__ __forceinline__ void dma(srd_t r, int vo, int so, int base) {
  asm volatile("s_add_u32 m0, %0, %4\n\ts_nop 0\n\tbuffer_load_dwordx4 %1, %2, %3 offen lds" ::"s"(base), "v"(vo), "s"(r), "s"(so), "n"(IMM) : "m0", "scc", "memory");
}
template <int IMM, int NT> static __device__ __forceinline__ void dman(srd_t r, int vo, int so, int base) {
  if constexpr (NT) asm volatile("s_add_u32 m0, %0, %4\n\ts_nop 0\n\tbuffer_load_dwordx4 %1, %2, %3 offen nt lds" ::"s"(base), "v"(vo), "s"(r), "s"(so), "n"(IMM) : "m0", "scc", "memory");
  else dma<IMM>(r, vo, so, base);
}
#define NT_KV 0
#define NT_H 0
#define NT_SP 0
#define NT_X 0
#define NT_RING 0
template <int IMM> static __device__ __forceinline__ void dmac(srd_t r, int vo, int so, int base) {
  asm volatile("s_add_u32 m0, %0, %4\n\ts_nop 0\n\tbuffer_load_dwordx4 %1, %2, %3 offen sc1 lds" ::"s"(base), "v"(vo), "s"(r), "s"(so), "n"(IMM) : "m0", "scc", "memory");
}
static __device__ __forceinline__ void touch(srd_t r, int vo, int so) {
  asm volatile("s_mov_b32 m0, %3\n\ts_nop 0\n\tbuffer_load_dword %0, %1, %2 offen lds" ::"v"(vo), "s"(r), "s"(so), "n"(LDS_BYTES - 256) : "m0", "memory");
}
static __device__ __forceinline__ void touchc(srd_t r, int vo, int so) {
  asm volatile("s_mov_b32 m0, %3\n\ts_nop 0\n\tbuffer_load_dword %0, %1, %2 offen sc1 lds" ::"v"(vo), "s"(r), "s"(so), "n"(LDS_BYTES - 256) : "m0", "memory");
}
static __device__ __forceinline__ float one(float r) { asm("" : "+v"(r)); return r; }
static __device__ __forceinline__ float fadd(float a, float b) { return one(a + b); }
static __device__ __forceinline__ float fmul(float a, float b) { return one(a * b); }
static __device__ __forceinline__ float ffma(float a, float b, float c) { return one(__builtin_fmaf(a, b, c)); }
template <int N, typename Fn> static __device__ __forceinline__ void unr(Fn &&f) {
  if constexpr (N > 0) { unr<N - 1>(f); f(std::integral_constant<int, N - 1>{}); }
}
static __device__ __forceinline__ f4 mfma(v4u a, v4u b, f4 c) {
  return __builtin_amdgcn_mfma_f32_16x16x32_bf16(__builtin_bit_cast(bf8, a), __builtin_bit_cast(bf8, b), c, 0, 0, 0);
}
static __device__ __forceinline__ void mma(f4 &c, v4u a, v4u b) { asm volatile("v_mfma_f32_16x16x32_bf16 %0, %1, %2, %0" : "+v"(c) : "v"(a), "v"(b)); }
static __device__ __forceinline__ void mma0(f4 &c, v4u a, v4u b) { asm volatile("v_mfma_f32_16x16x32_bf16 %0, %1, %2, 0" : "=v"(c) : "v"(a), "v"(b)); }
typedef float f16v __attribute__((ext_vector_type(16)));
static __device__ __forceinline__ void mmb(f16v &c, v4u a, v4u b) { asm volatile("v_mfma_f32_32x32x16_bf16 %0, %1, %2, %0" : "+v"(c) : "v"(a), "v"(b)); }
template <bool FIRST> static __device__ __forceinline__ void mmaf(f4 &c, v4u a, v4u b) { if constexpr (FIRST) mma0(c, a, b); else mma(c, a, b); }
#define SETTLE() do { SB(); asm volatile("s_nop 7" ::: "memory"); SB(); } while (0)
#define WAITC(vm, lg) __builtin_amdgcn_s_waitcnt(((vm) & 15) | 0x70 | ((lg) << 8) | (((vm) >> 4) << 14))
static __device__ __forceinline__ float dot2(u32 a, u32 b, float c) { return __builtin_amdgcn_fdot2_f32_bf16(__builtin_bit_cast(bf2, a), __builtin_bit_cast(bf2, b), c, false); }
static __device__ __forceinline__ float asf(u32 u) { return __builtin_bit_cast(float, u); }
static __device__ __forceinline__ u32 asu(float f) { return __builtin_bit_cast(u32, f); }
static __device__ __forceinline__ float lo16(u32 u) { return asf(u << 16); }
static __device__ __forceinline__ float hi16(u32 u) { return asf(u & 0xffff0000u); }
static __device__ __forceinline__ u32 pk(float a, float b) { f2 t = {a, b}; return __builtin_bit_cast(u32, __builtin_convertvector(t, bf2)); }
static __device__ __forceinline__ float rbf(float a) { return lo16(pk(a, a)); }
static __device__ __forceinline__ v4u lds16(LAS unsigned char *L, int off) { return *(LAS v4u *)(L + off); }
static __device__ __forceinline__ f4 ldsf4(LAS unsigned char *L, int off) { return *(LAS f4 *)(L + off); }
static __device__ __forceinline__ void sts16(LAS unsigned char *L, int off, v4u v) { *(LAS v4u *)(L + off) = v; }
static __device__ __forceinline__ v2u trd(LAS unsigned char *L, int off) {
  return __builtin_bit_cast(v2u, __builtin_amdgcn_ds_read_tr16_b64_v4i16((LAS s4 *)(L + off)));
}
static __device__ __forceinline__ v4u trd2(LAS unsigned char *L, int off, int step) {
  const v2u a = trd(L, off), b = trd(L, off + step);
  return v4u{a.x, a.y, b.x, b.y};
}
static __device__ __forceinline__ f2 fma2(f2 a, f2 b, f2 c) { return __builtin_elementwise_fma(a, b, c); }
static __device__ __forceinline__ u32 pk2(f2 t) { return __builtin_bit_cast(u32, __builtin_convertvector(t, bf2)); }
static __device__ __forceinline__ f2 lo2(f4 v) { return __builtin_shufflevector(v, v, 0, 1); }
static __device__ __forceinline__ f2 hi2(f4 v) { return __builtin_shufflevector(v, v, 2, 3); }
static __device__ __forceinline__ void rowsum8(float (&a)[8], float (&b)[8]) {
#define DA(d, s, ctl, bm) "v_add_f32_dpp " d ", " s ", " s " " ctl " row_mask:0xf bank_mask:" bm "\n\t"
  asm volatile("s_nop 1\n\t"
               DA("%0", "%0", "row_mirror", "0x3") DA("%1", "%1", "row_mirror", "0x3") DA("%2", "%2", "row_mirror", "0x3") DA("%3", "%3", "row_mirror", "0x3")
               DA("%4", "%4", "row_mirror", "0x3") DA("%5", "%5", "row_mirror", "0x3") DA("%6", "%6", "row_mirror", "0x3") DA("%7", "%7", "row_mirror", "0x3")
               DA("%0", "%8", "row_mirror", "0xc") DA("%1", "%9", "row_mirror", "0xc") DA("%2", "%10", "row_mirror", "0xc") DA("%3", "%11", "row_mirror", "0xc")
               DA("%4", "%12", "row_mirror", "0xc") DA("%5", "%13", "row_mirror", "0xc") DA("%6", "%14", "row_mirror", "0xc") DA("%7", "%15", "row_mirror", "0xc")
               DA("%0", "%0", "row_half_mirror", "0x5") DA("%1", "%1", "row_half_mirror", "0x5") DA("%4", "%4", "row_half_mirror", "0x5") DA("%5", "%5", "row_half_mirror", "0x5")
               DA("%0", "%2", "row_half_mirror", "0xa") DA("%1", "%3", "row_half_mirror", "0xa") DA("%4", "%6", "row_half_mirror", "0xa") DA("%5", "%7", "row_half_mirror", "0xa")
               DA("%0", "%0", "quad_perm:[1,0,3,2]", "0xf") DA("%1", "%1", "quad_perm:[1,0,3,2]", "0xf") DA("%4", "%4", "quad_perm:[1,0,3,2]", "0xf") DA("%5", "%5", "quad_perm:[1,0,3,2]", "0xf")
               DA("%0", "%0", "quad_perm:[2,3,0,1]", "0xf") DA("%1", "%1", "quad_perm:[2,3,0,1]", "0xf") DA("%4", "%4", "quad_perm:[2,3,0,1]", "0xf") DA("%5", "%5", "quad_perm:[2,3,0,1]", "0xf")
               : "+v"(a[0]), "+v"(a[1]), "+v"(a[2]), "+v"(a[3]), "+v"(b[0]), "+v"(b[1]), "+v"(b[2]), "+v"(b[3])
               : "v"(a[4]), "v"(a[5]), "v"(a[6]), "v"(a[7]), "v"(b[4]), "v"(b[5]), "v"(b[6]), "v"(b[7]));
#undef DA
}
static __device__ __forceinline__ void lds_signal(LAS unsigned char *L, int off) {
  asm volatile("s_waitcnt lgkmcnt(0)" ::: "memory");
  if ((threadIdx.x & 63) == 0) __hip_atomic_fetch_add((LAS u32 *)(L + off), 1u, __ATOMIC_RELAXED, __HIP_MEMORY_SCOPE_WORKGROUP);
}
static __device__ __forceinline__ bool lds_last(LAS unsigned char *L, int off) {
  u32 n = 0;
  if ((threadIdx.x & 63) == 0) n = __hip_atomic_fetch_add((LAS u32 *)(L + off), 1u, __ATOMIC_RELAXED, __HIP_MEMORY_SCOPE_WORKGROUP);
  return __builtin_amdgcn_readfirstlane(n) == 3;
}
static __device__ __forceinline__ void lds_wait(LAS unsigned char *L, int off, u32 n) {
#pragma unroll 1
  for (u32 s = 0; s < (SPIN_CAP << 4); ++s)
  {
    if (__hip_atomic_load((LAS u32 *)(L + off), __ATOMIC_RELAXED, __HIP_MEMORY_SCOPE_WORKGROUP) >= n) break;
    __builtin_amdgcn_s_sleep(1);
  }
  asm volatile("" ::: "memory");
}
static __device__ __forceinline__ u64 ldflag(unsigned char *ws, int off) { return __hip_atomic_load((GAS u64 *)(ws + off), __ATOMIC_RELAXED, __HIP_MEMORY_SCOPE_AGENT); }
static __device__ __forceinline__ void stflag(unsigned char *ws, int off, u64 v) { __hip_atomic_store((GAS u64 *)(ws + off), v, __ATOMIC_RELAXED, __HIP_MEMORY_SCOPE_AGENT); }
static __device__ __forceinline__ int fl(int n) { return WS_FL + n * FS; }
#define FSK 8
static __device__ __forceinline__ int flk(int n) { return WS_FL + n * FSK; }
static __device__ __forceinline__ void setflagk(unsigned char *ws, int n, u64 tag) {
  const int l = threadIdx.x & 63;
  if (FULL && FSK >= 16) { if (l < FSK / 16) st16c(v4u{(u32)tag, (u32)(tag >> 32), (u32)tag, (u32)(tag >> 32)}, mk(ws, WS_BYTES), flk(n) + l * 16, 0); }
  else if (l == 0) stflag(ws, flk(n), tag);
}
static __device__ __forceinline__ void setflag(unsigned char *ws, int n, u64 tag) {
  const int l = threadIdx.x & 63;
  if (FULL) { if (l < FS / 16) st16c(v4u{(u32)tag, (u32)(tag >> 32), (u32)tag, (u32)(tag >> 32)}, mk(ws, WS_BYTES), fl(n) + l * 16, 0); }
  else if (l == 0) stflag(ws, fl(n), tag);
}
static __device__ __forceinline__ void wait_flags(unsigned char *ws, int off, u64 tag) {
#pragma unroll 1
  for (u32 s = 0; s < SPIN_CAP; ++s) {
    const u64 v = ldflag(ws, off);
    if (__all(v == tag)) break;
    __builtin_amdgcn_s_sleep(1);
  }
  asm volatile("" ::: "memory");
}
static __device__ __forceinline__ void hint_flags(const unsigned char *at, u64 tag) {
  typedef u64 v8q __attribute__((ext_vector_type(8)));
#pragma unroll 1
  for (u32 s = 0; s < 4096; ++s) {
    v8q f;
    asm volatile("s_load_dwordx16 %0, %1, 0x0 glc\n\ts_waitcnt lgkmcnt(0)" : "=s"(f) : "s"(at) : "memory");
    u64 d = 0;
#pragma unroll
    for (int q = 0; q < 8; ++q) d |= f[q] ^ tag;
    if (d == 0) break;
    __builtin_amdgcn_s_sleep(1);
  }
}
static __device__ __forceinline__ u32 lds_peek(LAS unsigned char *L, int off) { return __hip_atomic_load((LAS u32 *)(L + off), __ATOMIC_RELAXED, __HIP_MEMORY_SCOPE_WORKGROUP); }
static __device__ __forceinline__ void lds_check(LAS unsigned char *L, int off, u32 n, u32 seen) {
  if (__builtin_expect(__builtin_amdgcn_readfirstlane(seen) < n, 0)) lds_wait(L, off, n);
  asm volatile("" ::: "memory");
}
static __device__ __forceinline__ float inv_sigma(float var) {
  const float sd = rbf(__builtin_amdgcn_sqrtf(rbf(rbf(fmaxf(var, 0.f)) + 1e-5f)));
  return __builtin_amdgcn_rcpf(sd);
}
static __device__ __forceinline__ int mod7(int a) { return a >= 7 ? a - 7 : a; }

constexpr int L_KR = 81920;
constexpr int L_RV = L_KR + 20480;
constexpr int L_XR = 114688;
constexpr int L_Z2 = 114688;
constexpr int L_Z1 = 131072;
constexpr int L_KD = 147456;
constexpr int L_CS = 155648;
constexpr int L_XST = 156672;
constexpr int L_ZST = 157184;
constexpr int L_CNT = 157696;
constexpr int L_BS = 157952;

#define PMS 1
#define PTW 1
#define TSP 0
#define TZ1 0
#define NISS {18, 5, 5, 5, 5, 4, 0, 0}
#define NIZ {0, 0, 0, 0, 0, 1, 5, 5}
#define NIW {0, 0, 0, 0, 0, 0, 0, 0}
#define MIXZ 30
#define LASTA 2
#define W1 7
#define W2 12
#define KV1 1
#define KSLEEP 16
#define ZP -100
#define WOW 12
#define WSLP 4
#define AD 2
#define RREG 1
#define OP 8
#define UW 12
#define WOR 2
#define WOK 1
#define YLATE 1
#define WSO(n) ((mod7(((n) >> 2) + j) * 16 + ((n) & 3) * 4 + m) * 7168)
#define SIGE 2
#define TLC 0
#define TLG 0
#define UPOS 0
#define NO0 NO1
#define XAT 4
#define PMT 0
#define K1 8
#define T5 0
#define T6 T5
#define TS 2
#define RE 0
#define WTN 1
#define CLA 5
#define TRK {0, 0, 3, 3, 3, 3, 4}
#define TRS 4
#define ROT1 0
#define TD 0
#define TC 1
#define CLZ 13
#define CLU 3
#define HZ 1
#define HU 1
#define OWNM 1
#define NR1 12
#define X0 30
#define X1 32
#define X2 38
#define ZSL 0
#define ZSL2 8
#define NO1 12
constexpr int VLIM = 63;
#define RD 8
#define KVF_AT 3
#define SH_AT 6
#define TSLEEP 32
#define NS 10
#define PRE 0
#define DELTA 0
#define WOT 2
#define CLR_EARLY 1
#define CLR_SLEEP 8
#define PW 63
#define WSL 0
#define PC0 0
#define PC1 2
#define PM2 3
#define PM3 1
#define PCV 2
#define NB 6
#define NB2 4
struct Order {
  int seq[102], bar[8];
  constexpr int last(int a, int b) const { int r = 0; for (int q = 0; q < 102; ++q) if (seq[q] >= a && seq[q] < b) r = q; return r; }
};
constexpr Order make_order() {
  Order o{};
  constexpr int a[8] = NISS, z[8] = NIZ, w[8] = NIW;
  int n = 0, ia = 0, iz = 42, iw = 70;
  for (int s = 0; s < 8; ++s) {
    int ca = a[s], cz = z[s], cw = w[s];
    while (ca + cz + cw > 0) {
      for (int q = 0; q < 2 && ca > 0; ++q) { o.seq[n++] = ia++; --ca; }
      if (cz > 0) { o.seq[n++] = iz++; --cz; } else if (cw > 0) { o.seq[n++] = iw++; --cw; }
    }
    o.bar[s] = n;
  }
  while (n < 70 + NS) {
    for (int q = 0; q < MIXZ && iz < 70; ++q) o.seq[n++] = iz++;
    if (iw < 70 + NS && (iz == 70 || iw < 80)) o.seq[n++] = iw++;
  }
  while (n < 102) o.seq[n++] = 1000;
  return o;
}
constexpr Order ORD = make_order();
constexpr bool order_ok() {
  for (int q = 0; q < ORD.bar[7]; ++q) {
    int s = 0;
    while (q >= ORD.bar[s]) ++s;
    const int n = ORD.seq[q], st = n / 6;
    if (n < 42 && s > st) return false;
    if (n < 42 && n % 6 < 2 && st >= 4 && s < st - 2) return false;
    if (n < 42 && n % 6 >= 2 && st >= 5 && s < 2) return false;
    if (n >= 42 && n < 70 && s < (n - 42) / 4 + 3) return false;
  }
  return ORD.last(0, 42) < ORD.bar[6];
}
static_assert(order_ok(), "");
struct Tile { int i, j, rot1, rot2, e, ne; };

static __device__ __forceinline__ u64 epoch_load(const unsigned char *ws) {
  u64 e;
  asm volatile("s_load_dwordx2 %0, %1, 0x0" : "=s"(e) : "s"(ws) : "memory");
  return e;
}
#define EPOCH_WAIT(e) asm volatile("s_waitcnt lgkmcnt(0)" : "+s"(e)::"memory")
static __device__ __forceinline__ v4u widen(v2u x, v2u y) {
  const v2u a = __builtin_amdgcn_permlane16_swap(x.x, y.x, false, false), b = __builtin_amdgcn_permlane16_swap(x.y, y.y, false, false);
  return v4u{a.x, b.x, a.y, b.y};
}

static __device__ __forceinline__ void ztouch(const Params &p, const Tile t, int ck, int q, int l) {
  const int e = t.e + q;
  if (q < t.ne) touchc(mks(p.ws + WS_ZB + t.j * 524288, 524288), l * 128, ((t.rot2 + 4 * ck + (e >> 1)) & 31) * 16384 + (e & 1) * 8192);
}
static __device__ __forceinline__ void kv_fetch(const Params &p, const Tile t, int w, v2u &shk, u32 &dvr) {
  const int i = t.i, j = t.j, l = threadIdx.x & 63;
  const srd_t sk = mks(p.k, S_ * D_ * 2), sv = mks(p.v, S_ * D_ * 2);
  const int vo = (l >> 3) * 896 + j * 128 + (((l & 7) ^ ((l >> 2) & 4)) << 4), so = (64 * i + 16 * w) * 896, lb = w * 5120;
  dman<L_KR + 1024, NT_KV>(sk, vo, so, lb);
  dman<L_KR + 2048, NT_KV>(sk, vo, so + 7168, lb);
  dman<L_KR, NT_KV>(sk, vo, so > 0 ? so - 7168 : 0x40000000, lb);
  dman<L_KR + 3072, NT_KV>(sv, vo, so, lb);
  dman<L_KR + 4096, NT_KV>(sv, vo, so + 7168, lb);
  asm volatile("global_load_dwordx2 %0, %2, %3\n\tglobal_load_ushort %1, %4, %5" : "=&v"(shk), "=&v"(dvr) : "v"((64 * j + l) * 8), "s"(p.shk), "v"((64 * j + l) * 2), "s"(p.dv) : "memory");
}

static __device__ __forceinline__ void gemm_compute(const Params &p, LAS unsigned char *L, const Tile t, int w, v2u shk, u32 dvr) {
  STAMP_DECL;
  STAMP(0);
  const int i = t.i, j = t.j, rh = w >> 1, ch = w & 1;
  int l = threadIdx.x & 63, g = l >> 4, c = l & 15;
  u64 tag = epoch_load(p.ws);
  const rsrc_t rws = mk(p.ws, WS_BYTES);
  v4u xf[2][14];
  f4 acc[2][4], cs[2];
  int rb[4];
  v2u xe[4];
  auto ldx = [&]() {
    const rsrc_t rx = mk(p.x, S_ * D_ * 2);
#pragma unroll
    for (int q = 0; q < 4; ++q) xe[q] = __builtin_amdgcn_raw_buffer_load_b64(rx, (64 * i + 16 * w + c) * 896 + j * 128 + g * 8 + q * 32, 0, 0);
    SB();
  };
  auto clz = [&]() {
    const int o = WS_ZB + (j * 64 + 2 * i) * 8192 + w * 4096 + l * 16;
#pragma unroll
    for (int q = 0; q < 4; ++q) st16c(v4u{~0u, ~0u, ~0u, ~0u}, rws, o, q * 1024);
    SB();
  };
  if (HZ && CLZ == -1) clz();
  v4u ones = {0x3f803f80u, 0x3f803f80u, 0x3f803f80u, 0x3f803f80u};
  auto start = [&]() {
    const int rdoff = (8 * g + (c >> 2)) * 256 + ((((c >> 2) | ((g & 1) << 2)) ^ (4 * ch + 2 * rh)) << 5) + (c & 3) * 8;
#pragma unroll
    for (int n = 0; n < 4; ++n) { rb[n] = rdoff ^ (n << 5); PIN(rb[n]); }
    PIN(ones);
  };
  int xo0 = L_XR + (32 * rh + c) * 128 + ((g ^ (c & 7)) << 4), xo1 = xo0 ^ 64;
  int lno = L_RV + (16 * w + (l >> 3)) * 144 + (l & 7) * 16, lso = WS_KVR + (64 * j + 16 * w + (l >> 3)) * 4096 + (31 - i) * 128 + (l & 7) * 16;
  {
    __builtin_amdgcn_s_setprio(PC0);
    SB();
    if (threadIdx.x < 16) *(LAS u32 *)(L + L_CNT + threadIdx.x * 4) = 0;
    int ro = L_KR + w * 5120 + (c >> 2) * 128 + ((g ^ ((c >> 2) & 2)) << 5) + (c & 3) * 8;
    int wo = L_RV + l * 144 + (3 - w) * 32, ko = L_KD + l * 128 + ((w ^ ((c >> 1) & 3)) << 5);
    start();
    PIN(xo0); PIN(xo1); PIN(lno); PIN(lso); PIN(ro); PIN(wo); PIN(ko);
    f2 o[8];
#pragma unroll
    for (int q = 0; q < 8; ++q) { o[q] = f2{0, 0}; PIN(o[q]); }
    EPOCH_WAIT(tag);
    tag += 1;
    asm volatile("s_waitcnt vmcnt(%2)" : "+v"(shk), "+v"(dvr) : "n"(HZ && CLZ == -1 ? 4 : 0) : "memory");
    STAMP(1);
    v2u kr[5], vr[4];
#pragma unroll
    for (int q = 0; q < 5; ++q) kr[q] = trd(L, ro + 512 + q * 512);
#pragma unroll
    for (int q = 0; q < 4; ++q) vr[q] = trd(L, ro + 3072 + q * 512);
    SB();
    const u32 ta = __builtin_amdgcn_alignbit(shk.y, shk.y, 16), tb = __builtin_amdgcn_alignbit(shk.x, shk.x, 16);
    const float dd = lo16(dvr);
    u32 ke[10], kq[9];
#pragma unroll
    for (int q = 0; q < 5; ++q) { ke[2 * q] = kr[q].x; ke[2 * q + 1] = kr[q].y; }
#pragma unroll
    for (int q = 0; q < 9; ++q) kq[q] = __builtin_amdgcn_alignbit(ke[q + 1], ke[q], 16);
#pragma unroll
    for (int q = 0; q < 8; ++q) o[q] = f2{dot2(kq[q], ta, o[q].x), dot2(ke[q + 1], ta, o[q].y)};
    SB();
#pragma unroll
    for (int q = 0; q < 8; ++q) o[q] = f2{dot2(kq[q + 1], tb, o[q].x), dot2(ke[q + 2], tb, o[q].y)};
    SB();
#pragma unroll
    for (int q = 0; q < 8; ++q) { const u32 vv = (q & 1) ? vr[q >> 1].y : vr[q >> 1].x; o[q] *= f2{lo16(vv), hi16(vv)}; }
    u32 rv[8];
#pragma unroll
    for (int q = 0; q < 8; ++q) asm("v_cvt_pk_bf16_f32 %0, %1, %2" : "=v"(rv[q]) : "v"(o[7 - q].y), "v"(o[7 - q].x));
    sts16(L, wo, v4u{rv[0], rv[1], rv[2], rv[3]});
    sts16(L, wo + 16, v4u{rv[4], rv[5], rv[6], rv[7]});
    SB();
#pragma unroll
    for (int q = 0; q < 8; ++q) rv[q] = pk2(o[q] * dd);
    sts16(L, ko, v4u{rv[0], rv[1], rv[2], rv[3]});
    sts16(L, ko + 16, v4u{rv[4], rv[5], rv[6], rv[7]});
  }
  STAMP(2);

  v2u wh[2][8];
  auto rd1 = [&](int ks, int q) {
    if (q < 2) xf[q][ks] = lds16(L, ((ks & 1) ? xo1 : xo0) + ((ks >> 1) & 3) * 8192 + q * 2048);
    else {
      const int n = (q - 2) >> 1;
      if (ks == 7 && !(q & 1)) { rb[n] += 57344; PIN(rb[n]); }
      wh[ks & 1][q - 2] = trd(L, rb[n] + (ks % 7) * 8192 + (q & 1) * 1024);
    }
  };
  auto wfr = [&](int ks, int n) { const v2u a = wh[ks & 1][2 * n], b = wh[ks & 1][2 * n + 1]; return v4u{a.x, a.y, b.x, b.y}; };
  auto mm1 = [&](auto ksc, int q) {
    constexpr int ks = ksc;
    if (q < 8) mmaf<ks == 0>(acc[q & 1][q >> 1], xf[q & 1][ks], wfr(ks, q >> 1));
    else mmaf<ks == 0>(cs[q - 8], ones, wfr(ks, q - 8));
  };
  auto step = [&](auto ksc, int q0) {
#pragma unroll
    for (int q = 0; q < 10; ++q) { mm1(ksc, q); SB(); if (q + q0 < 10) rd1(ksc + 1, q + q0); SB(); }
  };
  auto put_cs = [&](int off) {
    const int o = L_CS + off + ((4 * ch + 2 * rh) * 16 + c) * 4;
    *(LAS float *)(L + o) = cs[0].x;
    *(LAS float *)(L + o + 64) = cs[1].x;
  };
  auto lastk = [&](int off) {
    mm1(std::integral_constant<int, 13>{}, 8);
    mm1(std::integral_constant<int, 13>{}, 9);
#pragma unroll
    for (int q = 0; q < 4; ++q) mm1(std::integral_constant<int, 13>{}, q);
    SB();
    asm volatile("s_nop 3" ::: "memory");
    SB();
    put_cs(off);
    SB();
#pragma unroll
    for (int q = 4; q < 8; ++q) mm1(std::integral_constant<int, 13>{}, q);
    SB();
  };
  STAMPA(1);
  __builtin_amdgcn_s_setprio(PC1);
  BARW();
  STAMPA(2);
  {
    v4u ln[2];
    ln[0] = lds16(L, lno);
    ln[1] = lds16(L, lno + 1152);
#pragma unroll
    for (int q = 0; q < 10; ++q) rd1(0, q);
    SB();
    st16c(ln[0], rws, lso, 0);
    st16c(ln[1], rws, lso, 32768);
    SB();
  }
  unr<13>([&](auto ksc) {
    constexpr int ks = ksc;
    if constexpr (ks & 1) {
      STAMPA(ks + 2);
      SB(); asm volatile("s_waitcnt lgkmcnt(8)\n\ts_barrier" ::: "memory"); SB();
      STAMPA(ks + 3);
      if (ks == KVF_AT) {
        DRAIN();
        setflagk(p.ws, F_KV + (4 * j + w) * 32 + i, tag);
        SB();
      }
    }
    if constexpr (HZ && ks == CLZ) clz();
    step(ksc, 0);
  });
  if (HZ && CLZ == 13) clz();
  lastk(0);
  if (HZ && CLZ == 14) clz();
  BARW();
  SETTLE();
  STAMP(6);
  FRESH();
  const u32 tagA = (u32)tag, tagB = (u32)(tag >> 32) ^ 0x9e3779b9u;
  f4 mm[2], rr[2];
  float cv[4];
  auto act_in = [&]() {
#pragma unroll
    for (int m = 0; m < 2; ++m) {
      mm[m] = ldsf4(L, L_XST + (32 * rh + 8 * g + 4 * m) * 4);
      rr[m] = ldsf4(L, L_XST + 256 + (32 * rh + 8 * g + 4 * m) * 4);
    }
    cv[0] = -cs[0].x;
    cv[1] = -cs[1].x;
  };
  auto act_in2 = [&](int off) {
    const int o = L_CS + off + ((4 * ch + (2 - 2 * rh)) * 16 + c) * 4;
    cv[2] = -*(LAS float *)(L + o);
    cv[3] = -*(LAS float *)(L + o + 64);
  };
  auto act = [&](int n, f2 (&z)[4]) {
    f2 x[4], a[4];
    const f2 c2 = {cv[n], cv[n]};
    SB();
#pragma unroll
    for (int m = 0; m < 2; ++m) {
      x[2 * m] = fma2(lo2(mm[m]), c2, lo2(acc[m][n]));
      x[2 * m + 1] = fma2(hi2(mm[m]), c2, hi2(acc[m][n]));
    }
    SB();
#pragma unroll
    for (int m = 0; m < 2; ++m) { x[2 * m] *= lo2(rr[m]); x[2 * m + 1] *= hi2(rr[m]); }
    SB();
#pragma unroll
    for (int q = 0; q < 4; ++q) a[q] = x[q] * x[q];
    SB();
#pragma unroll
    for (int q = 0; q < 4; ++q) a[q] = fma2(a[q], f2{-0.1029432397f, -0.1029432397f}, f2{-2.302208198f, -2.302208198f});
    SB();
#pragma unroll
    for (int q = 0; q < 4; ++q) a[q] *= x[q];
    SB();
#pragma unroll
    for (int q = 0; q < 4; ++q) a[q] = f2{__builtin_amdgcn_exp2f(a[q].x), __builtin_amdgcn_exp2f(a[q].y)};
    SB();
#pragma unroll
    for (int q = 0; q < 4; ++q) a[q] += 1.0f;
    SB();
#pragma unroll
    for (int q = 0; q < 4; ++q) a[q] = f2{__builtin_amdgcn_rcpf(a[q].x), __builtin_amdgcn_rcpf(a[q].y)};
    SB();
#pragma unroll
    for (int q = 0; q < 4; ++q) z[q] = x[q] * a[q];
    SB();
  };
  u32 seen;
  {
    act_in();
    act_in2(0);
    f2 s[4], u[4];
    v4u zp[4];
#pragma unroll
    for (int n = 0; n < 4; ++n) {
      f2 z[4];
      act(n, z);
#pragma unroll
      for (int q = 0; q < 4; ++q) {
        s[q] = n ? s[q] + z[q] : z[q];
        u[q] = n ? fma2(z[q], z[q], u[q]) : z[q] * z[q];
      }
      zp[n] = v4u{pk2(z[0]), pk2(z[1]), pk2(z[2]), pk2(z[3])};
    }
    seen = lds_peek(L, L_CNT);
    float sa[8], sq[8];
#pragma unroll
    for (int q = 0; q < 4; ++q) { sa[2 * q] = s[q].x; sa[2 * q + 1] = s[q].y; sq[2 * q] = u[q].x; sq[2 * q + 1] = u[q].y; }
    rowsum8(sa, sq);
    const int row = 8 * g + (c & 1) + ((c >> 1) & 6);
    if ((c & 2) == 0)
      st16c(v4u{asu((c & 1) ? sa[1] : sa[0]), tagA, asu((c & 1) ? sq[1] : sq[0]), tagB}, rws, ((i * 2 + rh) * 16 + 2 * j + ch) * 512 + row * 16, WS_ST);
    SB();
    const int zo = L_Z2 + (rh * 8 + 4 * ch) * 1024 + l * 16;
#pragma unroll
    for (int n = 0; n < 4; ++n) sts16(L, zo + ((n & 1) << 10) + (((n >> 1) ^ rh) << 11), zp[n]);
    SB();
    if (HZ && CLZ < 15) VM(1);
    if (l == 0) __hip_atomic_fetch_add((LAS u32 *)(L + L_CNT + 4), 1u, __ATOMIC_RELAXED, __HIP_MEMORY_SCOPE_WORKGROUP);
    SB();
    if (HZ && CLZ == 15) clz();
    lds_check(L, L_CNT, 4, seen);
#pragma unroll
    for (int n = 0; n < 4; ++n) { rb[n] -= 57344; PIN(rb[n]); }
#pragma unroll
    for (int q = 2; q < 10; ++q) rd1(0, q);
    SB();
  }
  SB();
  unr<13>([&](auto ksc) {
    if constexpr (HZ && CLZ == 15 && ksc == CLA) {
      VM(0);
      if (l == 0) __hip_atomic_fetch_add((LAS u32 *)(L + L_CNT + 52), 1u, __ATOMIC_RELAXED, __HIP_MEMORY_SCOPE_WORKGROUP);
    }
    if constexpr (ksc == 6) seen = lds_peek(L, L_CNT + 28);
    if constexpr (ksc == 7) lds_check(L, L_CNT + 28, 4, seen);
    SB();
    step(ksc, 2);
  });
  lastk(512);
  lds_signal(L, L_CNT + 8);
  SETTLE();
  FRESH();
  u32 bias[4];
  {
    act_in();
    const int po = L_Z1 + (64 * ch + c) * 128 + (((4 * rh + g) ^ (c & 7)) << 4);
#pragma unroll
    for (int n = 0; n < 4; ++n) {
      f2 z[4];
      if (n == 1) seen = lds_peek(L, L_CNT + 8);
      if (n == 2) {
        lds_check(L, L_CNT + 8, 4, seen);
        act_in2(512);
#pragma unroll
        for (int q = 0; q < 4; ++q) bias[q] = *(LAS u16 *)(L + L_BS + (16 * q + c) * 2);
      }
      if (n == 3) seen = lds_peek(L, L_CNT + 16);
      act(n, z);
      sts16(L, po + ((n & 1) << 11) + (((n >> 1) ^ rh) << 12), v4u{pk2(z[0]), pk2(z[1]), pk2(z[2]), pk2(z[3])});
    }
  }
  STAMP(8);
  FRESH();
  f4 g2[4][2];
  float z1[32];
  bool last;
  int uo[4], ob[4], uw = L_Z1 + w * 4096 + l * 16;
#pragma unroll
  for (int n = 0; n < 4; ++n) { ob[n] = ((4 * g + (c >> 2)) * 128 + ((((c >> 3) & 1) | ((g & 1) << 1)) << 5) + (c & 3) * 8) ^ (n << 5); PIN(ob[n]); }
#pragma unroll
  for (int m = 0; m < 4; ++m) { uo[m] = WS_UB + (i * 112 + 4 * j + w) * 1024 + l * 16 + m * 28672; PIN(uo[m]); }
  {
    v4u bf[8][2], af[4][4];
    const rsrc_t rz = mk(p.ws + WS_ZB + j * 524288, 524288);
    int bvo[2], bso = t.rot2 * 16384;
    bvo[0] = w * 2048 + l * 16;
    bvo[1] = bvo[0] + 8192;
    PIN(bvo[0]); PIN(bvo[1]);
    auto ldb = [&](int ks, int b, int so) { bf[ks & 7][b] = ld16c(rz, bvo[ks & 1] + b * 1024, so); };
#define BMASK 0x7ffff
    auto next = [&](int &so) { asm volatile("s_add_u32 %0, %0, 0x4000\n\ts_and_b32 %0, %0, %1" : "+s"(so) : "n"(BMASK) : "scc"); };
#pragma unroll
    for (int m = 0; m < 4; ++m) { const float b = lo16(bias[m]); g2[m][0] = g2[m][1] = f4{b, b, b, b}; PIN(g2[m][0]); PIN(g2[m][1]); }
    int ab[2];
    ab[0] = c * 128 + ((g ^ (c >> 1)) << 4);
    ab[1] = ab[0] ^ 64;
    PIN(ab[0]); PIN(ab[1]);
    auto rda = [&](int ks, int m) { af[ks & 3][m] = *(LAS v4u *)((LAS unsigned char *)(u32)ab[ks & 1] + ((ks >> 1) & 3) * 8192 + m * 2048); };
    auto turn = [&](int q) { ab[q] = (ab[q] + 32768) & 0x1ffff; PIN(ab[q]); };
    const int f0 = 4 * g + (c >> 2);
    int zr[4];
#pragma unroll
    for (int e = 0; e < 4; ++e) { zr[e] = (L_Z1 + (32 * w + f0) * 128 + (c & 1) * 8 + ((((c >> 1) & 1) ^ (f0 & 7)) << 4)) ^ (e << 5); PIN(zr[e]); }
    v2u zq[8];
    PIN(uw);
    if (ZP < 0 && w == 0) {
      lds_wait(L, L_CNT + 24, 4);
      __builtin_amdgcn_s_sleep(ZSL);
      const int fo = fl(F_Z + j * 32 + (l & 31));
#pragma unroll 1
      for (u32 s = 0; s < SPIN_CAP; ++s) {
        if (__all(ldflag(p.ws, fo) == tag)) break;
        __builtin_amdgcn_s_sleep(ZSL2);
      }
      asm volatile("" ::: "memory");
      lds_signal(L, L_CNT + 16);
    } else lds_check(L, L_CNT + 16, 1, seen);
    STAMP(9);
    if (TD && TC == 0) { ztouch(p, t, 1 + (w & 1), w >> 1, l); if (TD > 3 && !(w & 1)) ztouch(p, t, 3, w >> 1, l); SB(); }
#pragma unroll
    for (int ks = 0; ks < 7; ++ks) { ldb(ks, 0, bso); SB(); ldb(ks, 1, bso); SB(); if (ks & 1) next(bso); SB(); }
    if (TD && TC == 1) { ztouch(p, t, 1 + (w & 1), w >> 1, l); if (TD > 3 && !(w & 1)) ztouch(p, t, 3, w >> 1, l); SB(); }
    lds_wait(L, L_CNT + 12, 4);
    __builtin_amdgcn_s_setprio(3);
    STAMP(10);
#pragma unroll
    for (int ks = 0; ks < AD; ++ks)
#pragma unroll
      for (int m = 0; m < 4; ++m) rda(ks, m);
    SB();
    STAMP(5);
    auto wide = [&](int e) {
      const u32 z = (e & 2) ? zq[e >> 2].y : zq[e >> 2].x;
      if (e & 1) asm volatile("v_and_b32 %0, 0xffff0000, %1" : "=v"(z1[e]) : "v"(z));
      else asm volatile("v_lshlrev_b32 %0, 16, %1" : "=v"(z1[e]) : "v"(z));
    };
    auto chunk = [&](auto lastc) {
      constexpr bool LAST = lastc;
      unr<(LAST ? 6 : 8)>([&](auto sc) {
        constexpr int s = sc;
        WAITC(LAST && s ? 14 - 2 * s : 12, LAST && s == 2 ? 9 : 4);
        SB();
#pragma unroll
        for (int q = 0; q < 8; ++q) {
          mma(g2[q >> 1][q & 1], bf[s][q & 1], af[s & 3][q >> 1]);
          SB();
#define NOA 0
#define NOB 0
          if (!(q & 1) && (!NOA || LAST)) rda(s + 2, q >> 1);
          if ((q == 1 || q == 3) && (!LAST || s == 0) && (!NOB || LAST)) ldb(s + 7, q >> 1, bso);
          if (q == 5 && !(s & 1) && !LAST) next(bso);
          if (!LAST && q == (s == 5 ? 5 : 7) && (s == 5 || s == 6)) turn(~s & 1);
          if (LAST && s == 1 && (q & 1)) { zq[q - 1] = trd((LAS unsigned char *)(u32)zr[q >> 1], 0); zq[q] = trd((LAS unsigned char *)(u32)zr[q >> 1], 2048); }
          if (LAST && s >= 2 && (q & 1)) { wide(8 * (s - 2) + q - 1); wide(8 * (s - 2) + q); }
          SB();
        }
      });
      if (!LAST) BAR0();
    };
#pragma unroll 1
    for (int ck = 0; ck < 7; ++ck) {
      chunk(std::false_type{});
    }
    chunk(std::true_type{});
    u32 o[4][4];
    auto scale = [&](int m, int e) { asm volatile("v_mul_f32 %0, %0, %2\n\tv_mul_f32 %1, %1, %3" : "+v"(z1[8 * m + 2 * e]), "+v"(z1[8 * m + 2 * e + 1]) : "v"(g2[m][e >> 1][(2 * e) & 3]), "v"(g2[m][e >> 1][(2 * e + 1) & 3])); };
    auto pack = [&](int m, int e) {
      asm volatile("v_cvt_pk_bf16_f32 %0, %2, %3\n\tv_cvt_pk_bf16_f32 %1, %4, %5" : "=&v"(o[m][e]), "=&v"(o[m][e + 1])
                   : "v"(z1[8 * m + 2 * e]), "v"(z1[8 * m + 2 * e + 1]), "v"(z1[8 * m + 2 * e + 2]), "v"(z1[8 * m + 2 * e + 3]));
    };
    auto send = [&](int m) { st16c(v4u{o[m][0], o[m][1], o[m][2], o[m][3]}, rws, uo[m], 0); };
    auto keep = [&](int m) { sts16((LAS unsigned char *)(u32)uw, m * 1024, v4u{o[m][0], o[m][1], o[m][2], o[m][3]}); };
    auto two = [&](int s, int m, auto f0, auto f1) {
      mma(g2[m][0], bf[s][0], af[s & 3][m]); SB(); f0(); SB();
      mma(g2[m][1], bf[s][1], af[s & 3][m]); SB(); f1(); SB();
    };
    auto none = []() {};
    WAITC(2, 4);
    SB();
    two(6, 0, none, none);
    two(6, 1, none, [&]() { seen = lds_peek(L, L_CNT + 44); });
    WAITC(0, 1);
    SB();
    two(7, 0, none, none);
    two(6, 2, none, none);
    two(7, 1, none, none);
    two(6, 3, none, [&]() { asm volatile("s_nop 0"); scale(0, 0); scale(0, 1); });
    two(7, 2, [&]() { scale(0, 2); scale(0, 3); }, [&]() { pack(0, 0); pack(0, 2); });
    two(7, 3, [&]() { send(0); scale(1, 0); scale(1, 1); }, [&]() { keep(0); scale(1, 2); scale(1, 3); });
    STAMP(11);
    pack(1, 0); pack(1, 2);
    SB();
    send(1);
    keep(1);
    SB();
#pragma unroll
    for (int e = 0; e < 4; ++e) o[2][e] = pk2(f2{z1[16 + 2 * e], z1[17 + 2 * e]} * ((e & 1) ? hi2(g2[2][e >> 1]) : lo2(g2[2][e >> 1])));
    send(2);
    keep(2);
    SB();
#pragma unroll
    for (int e = 0; e < 4; ++e) o[3][e] = pk2(f2{z1[24 + 2 * e], z1[25 + 2 * e]} * ((e & 1) ? hi2(g2[3][e >> 1]) : lo2(g2[3][e >> 1])));
    send(3);
    keep(3);
    SB();
    u32 n36 = 0;
    if (l == 0) n36 = __hip_atomic_fetch_add((LAS u32 *)(L + L_CNT + 36), 1u, __ATOMIC_RELAXED, __HIP_MEMORY_SCOPE_WORKGROUP);
    SB();
    const f4 t4 = g2[0][0] + g2[0][1];
    const f2 t2 = lo2(t4) + hi2(t4);
    const bool bad = __builtin_amdgcn_ballot_w64(__builtin_isunordered(t2.x, t2.y)) != 0;
    SB();
    last = __builtin_amdgcn_readfirstlane(n36) == 3;
    if (HU && last) setflag(p.ws, F_U + i * 8 + j, tag);
    SB();
    if (HZ && __builtin_expect(bad, 0)) {
      FRESH();
      wait_flags(p.ws, fl(F_ZD + j * 32 + (l & 31)), tag);
      __builtin_amdgcn_fence(__ATOMIC_ACQUIRE, "agent");
      const rsrc_t ra = mk(p.wsp, S_ * S_ * 2);
      f4 h[4][2];
#pragma unroll
      for (int m = 0; m < 4; ++m) { const float b = lo16(*(LAS u16 *)(L + L_BS + (16 * m + c) * 2)); h[m][0] = h[m][1] = f4{b, b, b, b}; }
#pragma unroll 1
      for (int ks = 0; ks < 64; ++ks) {
        const v4u b0 = ld16c(rz, w * 2048 + l * 16, ks * 8192), b1 = ld16c(rz, w * 2048 + l * 16 + 1024, ks * 8192);
#pragma unroll
        for (int m = 0; m < 4; ++m) {
          const v4u a = ld16(ra, (64 * i + 16 * m + c) * 4096 + g * 16, ks * 64);
          h[m][0] = mfma(b0, a, h[m][0]);
          h[m][1] = mfma(b1, a, h[m][1]);
        }
      }
#pragma unroll
      for (int m = 0; m < 4; ++m) {
#pragma unroll
        for (int e = 0; e < 4; ++e) {
          const u32 z = (e & 1) ? zq[2 * m + (e >> 1)].y : zq[2 * m + (e >> 1)].x;
          o[m][e] = pk2(f2{lo16(z), hi16(z)} * ((e & 1) ? hi2(h[m][e >> 1]) : lo2(h[m][e >> 1])));
        }
        send(m);
      }
    }
  }
  SB();
  STAMP(7);
  {
    v4u uf[28], of[OP][4];
    int uso[7];
#pragma unroll
    for (int q = 1; q < 7; ++q) uso[q] = WS_UB + (i * 4 + w) * 28672 + mod7(q + j) * 4096;
    int uvo, yo, xo, oo, xr;
    auto ldu = [&](int ks) { uf[ks] = ld16c(rws, uvo + (ks & 3) * 1024, uso[ks >> 2]); };
    f4 o[4];
    auto rd = [&](int ks, int n) {
      if (ks == 14) { ob[n] += 57344; PIN(ob[n]); }
      of[ks % OP][n] = trd2((LAS unsigned char *)(u32)ob[n], (ks % 14) * 4096, 2048);
    };
    float xv[16], xn[16], kn[16], mr, rs;
    v2u kd[4];
    auto prep = [&](int q) {
      const int e = q >> 1;
      const u32 a = (e & 2) ? xe[e >> 2].y : xe[e >> 2].x, b = (e & 2) ? kd[e >> 2].y : kd[e >> 2].x;
      if (q & 1) asm volatile("v_fma_f32 %0, %2, %3, %4\n\tv_mul_f32 %1, %1, %0" : "=&v"(xn[e]), "+v"(kn[e]) : "v"(xv[e]), "v"(rs), "v"(mr));
      else if (e & 1) asm volatile("v_and_b32 %0, 0xffff0000, %2\n\tv_and_b32 %1, 0xffff0000, %3" : "=&v"(xv[e]), "=&v"(kn[e]) : "v"(a), "v"(b));
      else asm volatile("v_lshlrev_b32 %0, 16, %2\n\tv_lshlrev_b32 %1, 16, %3" : "=&v"(xv[e]), "=&v"(kn[e]) : "v"(a), "v"(b));
    };
    auto step = [&](auto ksc) {
      constexpr int ks = ksc;
#pragma unroll
      for (int n = 0; n < 4; ++n) {
        mmaf<ks == 0>(o[n], of[ks % OP][n], uf[ks]);
        SB();
        if (ks + OP < 28) rd(ks + OP, n);
        if (XAT == 4 && ks == 16) xe[n] = *(LAS v2u *)(L + (xr ^ (n << 5)));
        if (ks == 17) kd[n] = trd(L, yo + (L_KD - L_Z2) + n * 2048);
        if (ks == 17 && n == 3) { mr = *(LAS float *)(L + xo); rs = *(LAS float *)(L + xo + 256); }
        if (ks == 19 && n == 2) { mr = -mr * rs; PIN(mr); }
        if (ks >= 19 && ks < 27 && n < 2) prep(4 * (ks - 19) + 2 * n);
        if (ks >= 20 && n >= 2) prep(4 * (ks - 20) + 2 * n - 3);
        SB();
      }
    };
    lds_check(L, L_CNT + 44, 4, seen);
    STAMP(3);
#pragma unroll
    for (int q = 0; q < 4 * K1; ++q) rd(q >> 2, q & 3);
    SB();
    STAMP(4);
    if (!HU) {
      VM(0);
      last = lds_last(L, L_CNT + 20);
      if (last) setflag(p.ws, F_U + i * 8 + j, tag);
    }
    SB();
    STAMP(1);
    lds_wait(L, L_CNT + 36, 4);
    {
      const int ur = uw - w * 3072;
#pragma unroll
      for (int ks = 0; ks < 4; ++ks) uf[ks] = lds16((LAS unsigned char *)(u32)ur, ks * 4096);
    }
    seen = lds_peek(L, L_CNT + 40);
    SB();
    FRESH();
    uvo = l * 16;
    yo = L_Z2 + (ob[0] ^ (w << 5));
    xo = L_XST + (16 * w + c) * 4;
    oo = (64 * i + 16 * w + c) * 896 + j * 128 + (g & 1) * 32 + (g >> 1) * 16;
    xr = L_Z2 + 8192 + (16 * w + c) * 128 + (((g >> 1) ^ (c >> 1)) << 4) + (g & 1) * 8;
    int ufo = fl(F_U + i * 8 + (l < 7 ? l : 0));
    PIN(uvo); PIN(yo); PIN(xo); PIN(oo); PIN(xr); PIN(ufo);
    lds_check(L, L_CNT + 40, 4, seen);
#pragma unroll
    for (int q = 4 * K1; q < 4 * OP; ++q) rd(q >> 2, q & 3);
    SB();
    unr<UPOS>(step);
    u64 fv = 0;
    if (last) fv = ldflag(p.ws, ufo);
    SB();
    unr<4 - UPOS>([&](auto q) { step(std::integral_constant<int, UPOS + q>{}); });
    STAMP(2);
    if (last) {
      if (!__all(fv == tag)) wait_flags(p.ws, ufo, tag);
      lds_signal(L, L_CNT + 32);
    } else lds_wait(L, L_CNT + 32, 1);
    VM(0);
    STAMP(12);
#pragma unroll
    for (int ks = 4; ks < 4 + UW; ++ks) { ldu(ks); SB(); }
    BAR0();
    unr<24>([&](auto qc) {
      constexpr int ks = qc + 4;
      WAITC(ks < 28 - UW ? UW - 1 : 27 - ks, ks == 19 ? 8 : ks == 26 ? 0 : 15);
      SB();
      step(std::integral_constant<int, ks>{});
      if (ks + UW < 28) ldu(ks + UW);
      SB();
    });
    STAMP(13);
    SETTLE();
    const bool bad = __builtin_amdgcn_ballot_w64(__builtin_isunordered(o[3].x, o[3].x)) != 0;
    if ((HZ || HU) && __builtin_expect(bad, 0)) {
      wait_flags(p.ws, ufo + (F_UD - F_U) * FS, tag);
      __builtin_amdgcn_fence(__ATOMIC_ACQUIRE, "agent");
#pragma unroll
      for (int n = 0; n < 4; ++n) o[n] = f4{0, 0, 0, 0};
#pragma unroll 1
      for (int ks = 0; ks < 28; ++ks) {
        const v4u u = ld16c(rws, uvo + (ks & 3) * 1024, WS_UB + (i * 4 + w) * 28672 + mod7((ks >> 2) + j) * 4096);
#pragma unroll
        for (int n = 0; n < 4; ++n) o[n] = mfma(trd2((LAS unsigned char *)(u32)(ob[n] - 57344 + ks * 4096), 0, 2048), u, o[n]);
      }
    }
    v2u y[4], res[4];
    f2 a[8], e[8];
#pragma unroll
    for (int q = 0; q < 8; ++q) { a[q] = (q & 1) ? hi2(o[q >> 1]) : lo2(o[q >> 1]); e[q] = a[q] * -1.442695041f; }
#pragma unroll
    for (int q = 0; q < 8; ++q) e[q] = f2{__builtin_amdgcn_exp2f(e[q].x), __builtin_amdgcn_exp2f(e[q].y)} + 1.0f;
    asm volatile("s_barrier" : "+v"(e[0]), "+v"(e[1]), "+v"(e[2]), "+v"(e[3]), "+v"(e[4]), "+v"(e[5]), "+v"(e[6]), "+v"(e[7])::"memory");
    SB();
#pragma unroll
    for (int n = 0; n < 4; ++n) y[n] = trd(L, yo + n * 2048);
    SB();
#pragma unroll
    for (int q = 0; q < 8; ++q) e[q] = a[q] * f2{__builtin_amdgcn_rcpf(e[q].x), __builtin_amdgcn_rcpf(e[q].y)};
    asm volatile("" : "+v"(e[0]), "+v"(e[1]), "+v"(e[2]), "+v"(e[3]), "+v"(e[4]), "+v"(e[5]), "+v"(e[6]), "+v"(e[7]));
    SB();
    asm volatile("" : "+v"(y[0]), "+v"(y[1]), "+v"(y[2]), "+v"(y[3]));
#pragma unroll
    for (int q = 0; q < 8; ++q) {
      const u32 yy = (q & 1) ? y[q >> 1].y : y[q >> 1].x;
      const f2 tt = fma2(f2{lo16(yy), hi16(yy)}, f2{xn[2 * q], xn[2 * q + 1]}, f2{kn[2 * q], kn[2 * q + 1]});
      const u32 r = pk2(fma2(tt, e[q], f2{xv[2 * q], xv[2 * q + 1]}));
      if (q & 1) res[q >> 1].y = r; else res[q >> 1].x = r;
    }
    const rsrc_t ro = mk(p.out, S_ * D_ * 2);
    st16c(widen(res[0], res[1]), ro, oo, 0);
    st16c(widen(res[2], res[3]), ro, oo, 64);
  }
  STAMP_OUT(0);
}

static __device__ __forceinline__ void gemm_move(const Params &p, LAS unsigned char *L, const Tile t, int m) {
  STAMP_DECL;
  STAMP(0);
  const int i = t.i, j = t.j;
  int l = threadIdx.x & 63, g = l >> 4, c = l & 15;
  const srd_t sx_ = mks(p.x, S_ * D_ * 2), swin = mks(p.win, D_ * F_ * 2), swsp = mks(p.wsp, S_ * S_ * 2);
  const int lb = m * 1024, lb2 = m * 2048;
  const int pvo = (l >> 3) * 896 + j * 128 + (((((l & 7) >> 1) ^ g) << 5) | ((l & 1) << 4));
  const int rsw = ((l & 7) ^ (l >> 3)) << 4;
  const int xvo = (64 * i + 32 * (m >> 1) + 4 * (m & 1) + 8 * (l >> 5) + ((l >> 3) & 3)) * 896 + rsw;
  const int wvo = (4 * m + g) * 3584 + j * 256 + ((((c >> 1) ^ (g | ((m & 2) << 1))) << 5) | ((c & 1) << 4));
  int sb[7];
#pragma unroll
  for (int s = 0; s < 7; ++s) sb[s] = mod7(s + t.rot1);
  auto item = [&](auto nc) {
    constexpr int n = nc, s = n / 6, q = n % 6;
    if constexpr (n >= 42) dma<(n - 42) * 4096>(swin, wvo, (sb[(n - 42) >> 2] * 4 + ((n - 42) & 3)) * 57344, lb);
    else if constexpr (q < 2) dman<L_XR + (s & 3) * 8192 + q * 1024, NT_X>(sx_, xvo, sb[s] * 128 + q * 14336, lb2);
    else dma<(4 * s + q - 2) * 4096>(swin, wvo, (sb[s] * 4 + q - 2) * 57344 + 1792, lb);
  };
#define IC(n) std::integral_constant<int, (n)>{}
  const rsrc_t rwsp = mk(p.wsp, S_ * S_ * 2), rws = mk(p.ws, WS_BYTES);
  v4u ar[32];
  const int avo0 = (64 * i + 16 * m + (l >> 3)) * 4096 + (((l & 7) ^ g) << 4), avo1 = avo0 ^ 64;
  auto aso = [&](int n) { return (((n >> 1) + t.rot2) & 31) * 128 + (n & 1) * 32768; };
  auto lda = [&](int n) { const int avo = (n & 1) ? avo1 : avo0; ar[n] = n < 10 ? __builtin_amdgcn_raw_buffer_load_b128(rwsp, avo, aso(32 + n), NT_SP * 2) : ld16(rwsp, avo, aso(32 + n)); SB(); };
  auto items = [&](auto n0, auto cnt) { unr<cnt>([&](auto q) { constexpr int n = ORD.seq[n0 + q]; if constexpr (n < 70) item(IC(n)); else lda(n - 70); }); };
  constexpr const int *NI = ORD.bar;
  items(IC(0), IC(NI[0]));
  u64 tag = epoch_load(p.ws);
  EPOCH_WAIT(tag);
  tag += 1;
  {
    const v4u ones = {0x3f803f80u, 0x3f803f80u, 0x3f803f80u, 0x3f803f80u};
    f4 sx = {0, 0, 0, 0}, gr = {0, 0, 0, 0};
    int xo0 = L_XR + (16 * m + c) * 128 + ((g ^ (c & 7)) << 4), xo1 = xo0 ^ 64;
    PIN(xo0); PIN(xo1);
    unr<7>([&](auto sc) {
      constexpr int s = sc;
      VM(NI[s] - 1 - ORD.last(6 * s, 6 * s + 6) + (TLG && ORD.last(6 * s, 6 * s + 6) < NI[0]));
      v4u xs[2];
      xs[0] = lds16(L, xo0 + (s & 3) * 8192);
      xs[1] = lds16(L, xo1 + (s & 3) * 8192);
      STAMPA(2 * s + 1);
      BAR0();
      STAMPA(2 * s + 2);
      if constexpr (s < 6) items(IC(NI[s]), IC(NI[s + 1] - NI[s]));
      SB();
      if (PMS) __builtin_amdgcn_s_setprio(3);
#pragma unroll
      for (int q = 0; q < 2; ++q) { sx = mfma(xs[q], ones, sx); gr = mfma(xs[q], xs[q], gr); }
      SB();
      if (PMS) __builtin_amdgcn_s_setprio(1);
      SB();
      if constexpr (s == 6) items(IC(NI[6]), IC(LASTA));
    });
    SB();
    const int e = c & 3;
    const float s1 = e & 2 ? (e & 1 ? sx.w : sx.z) : (e & 1 ? sx.y : sx.x);
    const float s2 = e & 2 ? (e & 1 ? gr.w : gr.z) : (e & 1 ? gr.y : gr.x);
    const float mean = fmul(s1, 1.0f / 448.0f);
    const float r = inv_sigma(ffma(-mean, mean, fmul(s2, 1.0f / 448.0f)));
    if ((c >> 2) == g) {
      const int row = 32 * (m >> 1) + 8 * g + 4 * (m & 1) + e;
      *(LAS float *)(L + L_XST + row * 4) = rbf(mean);
      *(LAS float *)(L + L_XST + 256 + row * 4) = r;
    }
    SB();
    items(IC(NI[6] + LASTA), IC(NI[7] - NI[6] - LASTA));
  }
  BARW();
  STAMP(3);
  asm volatile("s_mov_b32 m0, %3\n\ts_nop 0\n\tbuffer_load_dword %0, %1, %2 offen lds" ::"v"(l * 4), "s"(mks(p.bsp, S_ * 2)), "s"(i * 128), "n"(L_BS) : "m0", "memory");
  unr<70 + NS - NI[7]>([&](auto q) {
    constexpr int n = NI[7] + q;
    VM(W1);
    if constexpr (n == ORD.last(42, 58) + 1 + W1) lds_signal(L, L_CNT);
    if constexpr (n == ORD.last(42, 70) + 1 + W1) lds_signal(L, L_CNT + 28);
    items(IC(n), IC(1));
  });
  static_assert(ORD.last(42, 58) + 1 + W1 < 70 + NS && NI[7] <= ORD.last(42, 58) + 1 + W1, "");
  STAMP(4);
  {
    const u32 tagA = (u32)tag, tagB = (u32)(tag >> 32) ^ 0x9e3779b9u;
    const int so = WS_ST + (i * 2 + (m >> 1)) * 8192 + (m & 1) * 256;
    const int vo = g * 512 + c * 16, vo3 = (g & 1) * 512 + c * 16 + 2048;
    v4u sg[4], zt[4];
    auto ldst = [&]() {
      sg[0] = ld16c(rws, vo, so); sg[1] = ld16c(rws, vo + 2048, so); sg[2] = ld16c(rws, vo, so + 4096); sg[3] = ld16c(rws, vo3, so + 4096);
    };
    lds_wait(L, L_CNT + 4, 4);
    __builtin_amdgcn_s_setprio(PM2);
    const int zo = ((m >> 1) * 8 + (l >> 5)) * 1024 + ((m & 1) * 32 + (l & 31)) * 16;
#pragma unroll
    for (int q = 0; q < 4; ++q) zt[q] = lds16(L, L_Z2 + zo + q * 2048);
    SB();
    if (PW < 60) VM(PW);
    ldst();
    SB();
#pragma unroll
    for (int n = 0; n < NB; ++n) lda(NS + n);
    SB();
    if constexpr (ORD.last(42, 70) + 1 + W1 >= 70 + NS) { VM(69 + NS - ORD.last(42, 70) + 4 + NB); lds_signal(L, L_CNT + 28); }
    SB();
    float zu[32];
#pragma unroll
    for (int q = 0; q < 16; ++q) { const u32 z = zt[q >> 2][q & 3]; zu[2 * q] = lo16(z); zu[2 * q + 1] = hi16(z); PIN(zu[2 * q]); PIN(zu[2 * q + 1]); }
    int zso = WS_ZB + (j * 64 + 2 * i) * 8192 + zo;
    PIN(zso);
    STAMP(5);
    auto late = [&]() {
      u32 d = 0;
#pragma unroll
      for (int q = 0; q < 4; ++q) d |= (sg[q].y ^ tagA) | (sg[q].w ^ tagB);
      return __builtin_amdgcn_ballot_w64(d != 0) != 0;
    };
    if (__builtin_expect(late(), 0)) {
#pragma unroll 1
      for (u32 s = 0; s < SPIN_CAP; ++s) {
        asm volatile("" ::: "memory");
        ldst();
        if (!late()) break;
      }
    }
    STAMP(2);
    float s = fadd(fadd(asf(sg[0].x), asf(sg[1].x)), fadd(asf(sg[2].x), g < 2 ? asf(sg[3].x) : 0.f));
    float u = fadd(fadd(asf(sg[0].z), asf(sg[1].z)), fadd(asf(sg[2].z), g < 2 ? asf(sg[3].z) : 0.f));
    {
      v2u a = __builtin_amdgcn_permlane16_swap(asu(s), asu(u), false, false);
      const float h = fadd(asf(a.x), asf(a.y));
      a = __builtin_amdgcn_permlane32_swap(asu(h), asu(h), false, false);
      const float k = fadd(asf(a.x), asf(a.y));
      a = __builtin_amdgcn_permlane16_swap(asu(k), asu(k), false, false);
      s = asf(a.x);
      u = asf(a.y);
    }
    const float mean = fmul(s, 1.0f / 896.0f);
    const float r = inv_sigma(ffma(-mean, mean, fmul(u, 1.0f / 896.0f)));
    if (g < 2) *(LAS float *)(L + L_ZST + g * 256 + (16 * m + c) * 4) = g ? r : -rbf(mean) * r;
    SB();
    const u32 cl = HZ && CLZ == 15 ? lds_peek(L, L_CNT + 52) : 0;
    f4 zb[2], zr[2];
#pragma unroll
    for (int q = 0; q < 2; ++q) {
      zb[q] = ldsf4(L, L_ZST + (16 * m + 8 * (g & 1) + 4 * q) * 4);
      zr[q] = ldsf4(L, L_ZST + 256 + (16 * m + 8 * (g & 1) + 4 * q) * 4);
    }
    SB();
#pragma unroll
    for (int n = 0; n < NB2; ++n) lda(NS + NB + n);
    if (HZ && CLZ == 15) lds_check(L, L_CNT + 52, 4, cl);
#pragma unroll
    for (int q = 0; q < 4; ++q) {
      u32 o[4];
#pragma unroll
      for (int e = 0; e < 4; ++e) {
        o[e] = pk(ffma(zu[8 * q + 2 * e], zr[e >> 1][(2 * e) & 3], zb[e >> 1][(2 * e) & 3]), ffma(zu[8 * q + 2 * e + 1], zr[e >> 1][(2 * e + 1) & 3], zb[e >> 1][(2 * e + 1) & 3]));
      }
      st16c(v4u{o[0], o[1], o[2], o[3]}, rws, zso, q * 2048);
      SB();
    }
    if (HZ) if (lds_last(L, L_CNT + 24)) setflag(p.ws, F_Z + j * 32 + i, tag);
    STAMP(6);
  }
  __builtin_amdgcn_s_setprio(PM3);
  constexpr int NA = NS + NB + NB2, NQ = 64 - NA;
  auto zflag = [&]() {
    if (!HZ) if (lds_last(L, L_CNT + 24)) setflag(p.ws, F_Z + j * 32 + i, tag);
    if (HZ) if (lds_last(L, L_CNT + 48)) setflag(p.ws, F_ZD + j * 32 + i, tag);
    SB(); STAMP(7);
  };
  auto ring = [&](auto nc) { constexpr int n = nc; dman<(n >> 1) * 8192 + (n & 1) * 1024, NT_RING>(swsp, (n & 1) ? avo1 : avo0, aso(n), lb2); };
  auto piece = [&](auto qc) {
    constexpr int n = qc - NR1;
    if constexpr (n < 0) lda(NA + qc); else if constexpr (n < 32) ring(IC(n < 0 || n > 31 ? 0 : n)); else lda(NA + qc - 32);
  };
  constexpr int XE[4] = {NR1 + X0 < NQ ? NR1 + X0 : NQ, NR1 + X1 < NQ ? NR1 + X1 : NQ, NR1 + X2 < NQ ? NR1 + X2 : NQ, NQ};
  static_assert(X0 >= 14 + 2 * AD && X1 >= 22 + 2 * AD && X2 >= 32 && NA + NR1 >= 19 && NA + NR1 <= 32 && W2 < NR1 + 5 + 2 * AD + RD, "");
  unr<XE[0]>([&](auto qc) {
    constexpr int n = qc - NR1;
    if constexpr (qc == W2) { VM(W2); zflag(); }
    if constexpr (qc > W2 && n < 14 + 2 * AD + RD) VM(RD - 1);
    if constexpr (n == 0) { lds_wait(L, L_CNT + 8, 4); STAMP(8); }
    if constexpr (n == 5 + 2 * AD + RD) lds_signal(L, L_CNT + 12);
    if constexpr (n == 28) lds_wait(L, L_CNT + 24, 4);
    if constexpr (n == ZP) if (m == 0) { wait_flags(p.ws, fl(F_Z + j * 32 + (l & 31)), tag); lds_signal(L, L_CNT + 16); }
    piece(qc);
  });
  STAMP(11);
  __builtin_amdgcn_s_setprio(0);
  const srd_t swout = mks(p.wout, 896 * D_ * 2);
  auto ldt = [&](auto nc) { constexpr int n = nc; dma<n * 4096>(swout, pvo, WSO(n), lb); SB(); __builtin_amdgcn_s_sleep(TS); SB(); };
  static_assert(T5 <= 8 && T6 <= 16 && T5 <= T6 && T6 <= NO0, "");
  v4u wr[28];
  const rsrc_t rwo = mk(p.wout, 896 * D_ * 2);
  auto trk = [&](auto cc) {
    constexpr int tr[7] = TRK, ck = cc;
    int n = NO1;
    for (int q = 0; q < ck; ++q) n += tr[q];
    if (RE == 3)
#pragma unroll
      for (int q = 0; q < tr[ck]; ++q) { if (q) __builtin_amdgcn_s_sleep(TRS); SB(); wr[n + q] = ld16(rwo, pvo, WSO(n + q)); SB(); }
  };
  int awo = m * 2048 + l * 16, awh = awo + 65536;
  PIN(awo); PIN(awh);
  {
    constexpr int cum[6] = {0, 6, 12, 19, 25, 32};
    unr<5>([&](auto kc) {
      constexpr int k = kc;
      if constexpr (k < 2) VM(XE[k] - NR1 - 14 - 2 * AD - 8 * k);
      if constexpr (k == 2) VM(XE[2] - NR1 - 32);
      BARW();
      if constexpr (TD && k + TD < 8) { ztouch(p, t, k + TD, m, l); SB(); }
      if constexpr (HU && k == CLU) {
        const int o = WS_UB + (i * 112 + 4 * j) * 1024 + m * 28672 + l * 16;
#pragma unroll
        for (int q = 0; q < 4; ++q) st16c(v4u{~0u, ~0u, ~0u, ~0u}, rws, o, q * 1024);
        SB();
      }
      constexpr int nq = k < 3 ? XE[k + 1] - XE[k] : 0, nw = cum[k + 1] - cum[k];
      unr<(nq > nw ? nq : nw)>([&](auto qc) {
        constexpr int n = cum[k] + qc;
        if constexpr (qc < nq) piece(IC(XE[k < 3 ? k : 0] + qc));
        if constexpr (qc < nw) sts16(L, ((n & 16) ? awh : awo) + ((n >> 1) & 7) * 8192 + (n & 1) * 1024, ar[n]);
        SB();
        __builtin_amdgcn_s_sleep(WSL);
        SB();
      });
      trk(IC(k + 1));
    });
  }
  unr<T5>(ldt);
  BARW();
  unr<T6 - T5>([&](auto q) { ldt(IC(T5 + q)); });
  VM(T6);
  trk(IC(6));
  BAR0();
  STAMP(9);
  {
    auto ldo = [&](auto nc) { constexpr int n = nc; VM(WOW); dma<n * 4096>(swout, pvo, WSO(n), lb); };
    const srd_t sws = mks(p.ws, WS_BYTES);
    const int yfo = fl(F_Y + ((64 * j + 16 * m + min(g * 7, 15)) / 14) * 4 + (l & 3));
    const int yvo = (64 * j + 16 * m + (l >> 3)) * 4096 + i * 128 + (((((l & 7) >> 1) ^ g) << 5) | ((l & 1) << 4));
    auto ldy = [&]() { dmac<L_Z2>(sws, yvo, WS_YB, lb2); dmac<L_Z2 + 1024>(sws, yvo, WS_YB + 32768, lb2); };
    __builtin_amdgcn_s_sleep(WSLP);
    static_assert(NO1 >= 4 + OP && NO1 <= 24, "");
    unr<NO0 - T6>([&](auto q) { ldo(IC(T6 + q)); });
    VM(NO0 - K1);
    lds_signal(L, L_CNT + 44);
    STAMP(1);
    const int xto = (64 * i + 16 * m + (l >> 3)) * 896 + j * 128 + (((l & 7) ^ g) << 4);
    lds_wait(L, L_CNT + 36, 4);
    __builtin_amdgcn_s_setprio(PMT);
    if (XAT == 4) { dma<L_Z2 + 8192>(sx_, xto, 0, lb2); dma<L_Z2 + 9216>(sx_, xto ^ 64, 7168, lb2); }
    unr<NO1 - NO0>([&](auto q) { ldo(std::integral_constant<int, NO0 + q>{}); });
    const rsrc_t rwout = mk(p.wout, 896 * D_ * 2);
#pragma unroll
    for (int n = NO1; n < 28; ++n) {
      if (n == NO1 + SIGE) { VM(SIGE + (XAT == 4 && NO0 == NO1 ? 2 : 0)); lds_signal(L, L_CNT + 40); STAMP(10); }
      ar[n] = ld16(rwout, pvo, WSO(n));
      SB();
    }
    STAMP(13);
    int wwo = m * 1024 + l * 16 + 49152;
    PIN(wwo);
#pragma unroll
    for (int n = NO1; n < 28; ++n) { sts16(L, wwo + (n - 12) * 4096, ar[n]); SB(); }
    STAMP(12);
    const bool early = !YLATE && __all(ldflag(p.ws, yfo) == tag);
    if (early) ldy();
    if (YLATE && !RREG) DRAIN();
    BARW();
    if ((HZ || HU) && m == 0) setflag(p.ws, F_UD + i * 8 + j, tag);
    if (!early) { wait_flags(p.ws, yfo, tag); ldy(); }
  }
  DRAIN();
  BAR0();
  STAMP_OUT(1);
}

constexpr int C_CP = 4160, C_KV = 4 * C_CP, C_SLOT = C_KV + 4608;

constexpr int C_EX = 131072;

template <int H> static __device__ __forceinline__ void conv_compute(const Params &p, LAS unsigned char *L, int v, int w) {
  STAMP_DECL;
  STAMP(0);
  __builtin_amdgcn_s_setprio(PCV);
  u64 tag = epoch_load(p.ws);
  constexpr int ord[8] = {H ? 0 : 2, H ? 1 : 3, H ? 6 : 4, H ? 7 : 5, H ? 2 : 0, H ? 3 : 1, H ? 4 : 6, H ? 5 : 7};
  const int l = threadIdx.x & 63, g = l >> 4, c = l & 15, ch = w >> 1;
  const rsrc_t rws = mk(p.ws, WS_BYTES);
  {
    const int t = threadIdx.x;
    const srd_t sws = mks(p.ws, WS_BYTES);
    touch(sws, t < 128 ? WS_ST + v * 16384 + t * 128 : WS_FL + (v * 52 + min(t & 63, 51)) * 128, 0);
  }
  const int hro = ch * C_SLOT + (c & 3) * C_CP + g * 16 + (c >> 2) * 8 + H * 64;
  const int kro = ch * C_SLOT + C_KV + (254 - 2 * c + g) * 16 - 7 * 512 + H * 64;
  v2u hh[2][16];
  v4u kf[2][8];
  auto rd = [&](int ho, int ko, int q, int n) {
    const int d = n / 3;
    if (n % 3 == 2) kf[q & 1][7 - d] = lds16(L, ko + d * 512 + q * 128);
    else hh[q & 1][2 * d + n % 3] = *(LAS v2u *)(L + ho + d * 512 + q * 128 + (n % 3) * 8);
  };
  f4 acc[8], in[4];
#pragma unroll
  for (int n = 0; n < 8; ++n) acc[n] = f4{0, 0, 0, 0};
  int xw = C_EX + w * 4096 + l * 16, xr = xw ^ 4096;
  int yo = WS_YB + (14 * v + ch) * 4096 + c * 32 + (g & 1) * 512 + (g >> 1) * 16 + 0x40000000;
  PIN(xw); PIN(xr);
  v2u y[4];
  auto fin = [&](int k) {
    if (k < 2) { in[2 * k] = ldsf4(L, xr + k * 2048); in[2 * k + 1] = ldsf4(L, xr + k * 2048 + 1024); }
    else if (k < 10) { const int n = (k - 2) >> 1, e = (k & 1) * 2; in[n][e] = fadd(in[n][e], acc[ord[4 + n]][e]); in[n][e + 1] = fadd(in[n][e + 1], acc[ord[4 + n]][e + 1]); }
    else if (k < 14) { const int n = k - 10; y[n] = v2u{pk(in[n].x, in[n].y), pk(in[n].z, in[n].w)}; PIN(y[n]); }
    else if (k < 16) st16c(widen(y[2 * k - 28], y[2 * k - 27]), rws, yo, ord[2 * k - 24] * 512);
  };
  EPOCH_WAIT(tag);
  tag += 1;
  if (KV1) BAR0();
  BAR0();
  STAMP(2);
#pragma unroll
  for (int n = 0; n < 24; ++n) { rd(hro, kro, 0, n); SB(); }
  WAITC(63, 0);
  int set = 0;
#pragma unroll 1
  for (int rho = 0; rho < 7; ++rho) {
    const int nset = set == 4 * C_SLOT ? 0 : set + 2 * C_SLOT;
    int ho = hro + set, ko = kro + set;
    unr<4>([&](auto qc) {
      constexpr int q = qc;
      if (q == 3) { ho = hro + nset - 512; ko = kro + nset - 512; PIN(ho); PIN(ko); }
      if (q > 0) WAITC(63, 0);
      if (q == 3) BAR0();
      SB();
      int k = 0;
#pragma unroll
      for (int hf = 0; hf < 2; ++hf)
#pragma unroll
        for (int P = 0; P < 8; ++P)
#pragma unroll
          for (int n = 0; n < 4; ++n) {
            const int tl = ord[4 * hf + n];
            if (P > tl) continue;
            const v2u a = hh[q & 1][2 * P], b = hh[q & 1][2 * P + 1];
            if (q == 0 && P == 0) mma0(acc[tl], v4u{a.x, a.y, b.x, b.y}, kf[q & 1][tl - P]);
            else mma(acc[tl], v4u{a.x, a.y, b.x, b.y}, kf[q & 1][tl - P]);
            SB();
            if (q == 0) {
              if (k < 2) fin(k);
              else if (k < 9) { rd(ho, ko, 1, 2 * k - 4); SB(); rd(ho, ko, 1, 2 * k - 3); }
              else if (k < 17) fin(k - 7);
              else if (k < 22) { rd(ho, ko, 1, 2 * k - 20); SB(); rd(ho, ko, 1, 2 * k - 19); }
              else if (k < 26) fin(k - 12);
            } else if (k < 24) rd(ho, ko, q + 1, k);
            else if (q == 2 && (k == 27 || k == 30)) fin(k / 3 + 5);
            else if (q == 3 && k < 28) sts16(L, xw + (k - 24) * 1024, __builtin_bit_cast(v4u, acc[ord[k - 24]]));
            SB();
            ++k;
          }
    });
    yo += rho ? 8192 : -0x40000000;
    set = nset;
    WAITC(63, 0);
    BAR0();
    if (rho == 0) STAMP(3);
    if (rho == 1) STAMP(4);
    if (rho == 3) STAMP(5);
    if (rho == 5) STAMP(6);
  }
  SETTLE();
#pragma unroll
  for (int k = 0; k < 16; ++k) fin(k);
  STAMP(7);
  DRAIN();
  setflag(p.ws, F_Y + v * 4 + w, tag);
  STAMP_OUT(0);
}

static __device__ __forceinline__ void conv_stage(const Params &p, LAS unsigned char *L, int v, int m) {
  STAMP_DECL;
  STAMP(0);
  const int l = threadIdx.x & 63;
  const srd_t sh = mks(p.h, D_ * S_ * 2), sws = mks(p.ws, WS_BYTES), swout = mks(p.wout, 896 * D_ * 2);
  const rsrc_t rws = mk(p.ws, WS_BYTES);
  const int vo = m * 1024 + l * 16;
  auto stage_h = [&](int rho, int set) {
    const int so = (14 * v + 2 * rho) * 4096, lb = set + m * 1024;
    unr<8>([&](auto qc) { constexpr int q = qc; dman<(q >> 2) * C_SLOT + (q & 3) * C_CP + 32, NT_H>(sh, vo, so + (q >> 2) * 4096 + (q & 3) * 2 + 2, lb); });
  };
  auto stage_kv = [&](int rho, int set) {
    const int so = WS_KVR + (14 * v + 2 * rho) * 4096, lb = set + m * 1024;
    dmac<C_KV>(sws, vo, so, lb);
    dmac<C_SLOT + C_KV>(sws, vo, so + 4096, lb);
  };
  u64 head;
  asm volatile("global_load_dwordx2 %0, %1, %2" : "=&v"(head) : "v"((14 * v + (l < 56 ? l >> 2 : 0)) * 4096), "s"(p.h) : "memory");
  stage_h(0, 0);
  u64 tag = epoch_load(p.ws);
  stage_h(1, 2 * C_SLOT);
  asm volatile("s_waitcnt vmcnt(16)" : "+v"(head)::"memory");
  head <<= 48 - 16 * (l & 3);
  auto put_head = [&](int rho, int set) {
    if (m == 0 && (l >> 3) == rho) *(LAS u64 *)(L + set + ((l >> 2) & 1) * C_SLOT + (l & 3) * C_CP + 24) = head;
  };
  {
    const v4u z = {0, 0, 0, 0};
    const int t = m * 64 + l;
    if (t < 192) sts16(L, (t >> 5) * C_SLOT + C_KV + 4096 + (t & 31) * 16, z);
    if (t < 24) { sts16(L, (t >> 2) * C_SLOT + (t & 3) * C_CP, z); *(LAS u64 *)(L + (t >> 2) * C_SLOT + (t & 3) * C_CP + 16) = 0; }
  }
  put_head(0, 0);
  put_head(1, 2 * C_SLOT);
  put_head(2, 4 * C_SLOT);
  EPOCH_WAIT(tag);
  tag += 1;
  if (m == 0) {
    __builtin_amdgcn_s_sleep(KSLEEP);
    wait_flags(p.ws, flk(F_KV + ((14 * v + (l < 32 ? 0 : 13)) >> 4) * 32 + (l & 31)), tag);
  }
  BAR0();
  STAMP(1);
  stage_kv(0, 0);
  stage_kv(1, 2 * C_SLOT);
  VM(2);
  BARW();
  stage_h(2, 4 * C_SLOT);
  stage_kv(2, 4 * C_SLOT);
  STAMP(2);
  setflagk(p.ws, F_HI + v * 4 + m, tag);
  int set = 0;
#pragma unroll 1
  for (int rho = 0; rho < 7; ++rho) {
    if (rho == 2) STAMP(3);
    if (rho == 3) STAMP(8);
    if (rho == 0) VM(11); else if (rho >= 5) DRAIN(); else if (rho > WOR && rho <= WOR + 4 / WOK) VM(10 + WTN * WOK); else VM(10);
    if (rho == 2) STAMP(4);
    if (rho == 3) STAMP(9);
    BARW();
    if (rho == 2) STAMP(5);
    if (rho == 3) STAMP(10);
    BAR0();
    if (rho == 2) STAMP(6);
    if (rho == 3) STAMP(11);
    if (rho < 4) {
      stage_h(rho + 3, set);
      if (rho == 2) STAMP(7);
      if (rho == 3) STAMP(12);
      stage_kv(rho + 3, set);
      put_head(rho + 3, set);
    }
    if (rho >= WOR && rho < WOR + 4 / WOK) {
#pragma unroll
      for (int q = 0; q < WOK; ++q) {
        const int n = ((v & 3) * 4 + (rho - WOR) * WOK + q) * 224 + m * 56 + min(l, 55);
        touch(swout, (n >> 2) * 896 + ((n & 3) + 3 * ((v >> 2) & 1)) * 128, 0);
      }
    }
    set = set == 4 * C_SLOT ? 0 : set + 2 * C_SLOT;
    if (rho == 4 && v == 0 && m == 0) {
#pragma unroll 1
      for (u32 s = 0; s < SPIN_CAP; ++s) {
        u64 f[16], d = 0;
#pragma unroll
        for (int q = 0; q < 16; ++q) f[q] = ldflag(p.ws, flk(q * 64 + l));
#pragma unroll
        for (int q = 0; q < 16; ++q) d |= f[q] ^ tag;
        if (__all(d == 0)) break;
        asm volatile("" ::: "memory");
      }
      if (l == 0) stflag(p.ws, WS_EPOCH, tag);
    }
  }
  STAMP_OUT(1);
}

#define CODE_LINES 414
#define CPFW 4
__global__ void __launch_bounds__(512, 2) fused_kernel(Params p) {
  extern __shared__ __attribute__((aligned(16))) unsigned char smem[];
  u64 he;
  asm volatile("s_memrealtime %0" : "=s"(he));
  LAS unsigned char *L = (LAS unsigned char *)smem;
  const int b = blockIdx.x, x = b & 7, r = b >> 3, wv = __builtin_amdgcn_readfirstlane(threadIdx.x >> 6);
  {
    u64 pc;
    const u32 line = min((u32)(r * 16 + (threadIdx.x & 15)), (u32)CODE_LINES);
    asm volatile("s_cmp_lg_u32 %2, %4\n\ts_cbranch_scc1 1f\n\ts_getpc_b64 %0\n\ts_mov_b32 m0, %3\n\ts_nop 0\n\tglobal_load_lds_dword %1, %0\n1:\n\t"
                 "s_setprio 3\n\ts_cmp_lt_u32 %2, 4\n\ts_cbranch_scc1 2f\n\ts_setprio 1\n2:"
                 : "=&s"(pc) : "v"(line * 128), "s"(wv), "n"(LDS_BYTES - 256), "n"(CPFW) : "m0", "scc", "memory");
  }
  if (r >= 28) {
    const int v = x * 4 + (r - 28);
    if (wv < 4) { if (wv & 1) conv_compute<1>(p, L, v, wv); else conv_compute<0>(p, L, v, wv); }
    else conv_stage(p, L, v, wv - 4);
  } else {
    Tile t;
    const int pg = x >> 1, o = x & 1;
    if (r < 24) { t.i = 8 * pg + r / 3; t.j = 4 * o + r % 3; }
    else { t.i = 8 * pg + 4 * o + (r - 24); t.j = 3; }
    t.rot1 = 2 * ((t.i + t.j) & 3);
    t.rot2 = (8 * pg + 8 + (32 * (t.j == 3 ? 6 : 2 * (t.j & 3) + o)) / 7 + DELTA * (t.i & 7)) & 31;
    t.e = r < 24 ? t.i & 7 : 2 * (t.i & 3);
    t.ne = r < 24 ? 1 : 2;
    v2u shk = {0, 0};
    u32 dvr = 0;
    if (wv < 4) kv_fetch(p, t, wv, shk, dvr);
    SB();
    asm volatile("" ::: "memory");
    if (wv < 4) gemm_compute(p, L, t, wv, shk, dvr);
    else gemm_move(p, L, t, wv - 4);
  }
  if (HCOND) for (int q_ = 0; q_ < 100000; ++q_) { if (__builtin_amdgcn_s_memrealtime() - he >= 4000) break; __builtin_amdgcn_s_sleep(1); }
}

extern "C" void kernel_launch(void *const *d_in, const int *in_sizes, int n_in, void *d_out, int out_size, void *d_ws, size_t ws_size,
                              hipStream_t stream) {
  Params p;
  p.x = (const u16 *)d_in[0];
  p.k = (const u16 *)d_in[1];
  p.v = (const u16 *)d_in[2];
  p.shk = (const u16 *)d_in[3];
  p.h = (const u16 *)d_in[4];
  p.dv = (const u16 *)d_in[5];
  p.win = (const u16 *)d_in[7];
  p.wsp = (const u16 *)d_in[8];
  p.bsp = (const u16 *)d_in[9];
  p.wout = (const u16 *)d_in[10];
  p.out = (u16 *)d_out;
  p.ws = (unsigned char *)d_ws;
  (void)hipFuncSetAttribute((const void *)fused_kernel, hipFuncAttributeMaxDynamicSharedMemorySize, LDS_BYTES);
  fused_kernel<<<256, 512, LDS_BYTES, stream>>>(p);
}
```

```cpp
#define PAIRX
#include <hip/hip_runtime.h>
#include <stdint.h>
#include <type_traits>

typedef unsigned short u16;
typedef unsigned int u32;
typedef unsigned long long u64;
typedef u32 v4u __attribute__((ext_vector_type(4)));
typedef u32 v2u __attribute__((ext_vector_type(2)));
typedef float f4 __attribute__((ext_vector_type(4)));
typedef float f2 __attribute__((ext_vector_type(2)));
typedef short s4 __attribute__((ext_vector_type(4)));
typedef __bf16 bf8 __attribute__((ext_vector_type(8)));
typedef __bf16 bf2 __attribute__((ext_vector_type(2)));
#define LAS __attribute__((address_space(3)))
#define GAS __attribute__((address_space(1)))
typedef __amdgpu_buffer_rsrc_t rsrc_t;
typedef int srd_t __attribute__((ext_vector_type(4)));

constexpr int S_ = 2048, D_ = 448, F_ = 1792;
constexpr int WS_EPOCH = 0;
#define FS 128
#define FULL 1
constexpr int WS_FL = 4096;
constexpr int F_KV = 0;
constexpr int F_HI = 896;
constexpr int F_Z = 1024;
constexpr int F_U = 1280;
constexpr int F_ZD = 256;
constexpr int F_UD = 512;
constexpr int F_Y = 1536;
constexpr int WS_ST = 262144;
constexpr int WS_ZB = 1 << 20;
constexpr int WS_UB = 5 << 20;
constexpr int WS_KVR = 9 << 20;
constexpr int WS_YB = 11 << 20;
constexpr int WS_STAMP = 14 << 20;
constexpr int WS_BYTES = 16 << 20;
constexpr int LDS_BYTES = 163840;
constexpr u32 SPIN_CAP = 1u << 16;

struct Params {
  const u16 *x, *k, *v, *shk, *h, *dv, *win, *wsp, *bsp, *wout;
  u16 *out;
  unsigned char *ws;
};

#define SB() __builtin_amdgcn_sched_barrier(0)
#define BARW() do { SB(); asm volatile("s_waitcnt lgkmcnt(0)\n\ts_barrier" ::: "memory"); SB(); } while (0)
#define BAR0() do { SB(); asm volatile("s_barrier" ::: "memory"); SB(); } while (0)
#define FRESH() do { asm volatile("" : "+v"(l)); g = l >> 4; c = l & 15; } while (0)
#define DRAIN() asm volatile("s_waitcnt vmcnt(0)" ::: "memory")
#define VM(n) asm volatile("s_waitcnt vmcnt(%0)" ::"n"(n) : "memory")
#define PIN(x) asm volatile("" : "+v"(x))
#define STAMP(n) SB()
#define STAMPA(n) do { } while (0)
#define STAMP_DECL do { } while (0)
#define STAMP_OUT(slot) do { } while (0)

static __device__ __forceinline__ rsrc_t mk(const void *p, int bytes) { return __builtin_amdgcn_make_buffer_rsrc((void *)p, 0, bytes, 0x00020000); }
static __device__ __forceinline__ v4u ld16(rsrc_t r, int vo, int so) { return __builtin_amdgcn_raw_buffer_load_b128(r, vo, so, 0); }
static __device__ __forceinline__ v4u ld16c(rsrc_t r, int vo, int so) { return __builtin_amdgcn_raw_buffer_load_b128(r, vo, so, 16); }
static __device__ __forceinline__ u32 ld4(rsrc_t r, int vo, int so) { return __builtin_amdgcn_raw_buffer_load_b32(r, vo, so, 0); }
static __device__ __forceinline__ void st16c(v4u v, rsrc_t r, int vo, int so) { __builtin_amdgcn_raw_buffer_store_b128(v, r, vo + so, 0, 16); }
static __device__ __forceinline__ srd_t mks(const void *p, int bytes) { return srd_t{(int)(u64)p, (int)((u64)p >> 32) & 0xffff, bytes, 0x00020000}; }
template <int IMM> static __device__ __forceinline__ void dma(srd_t r, int vo, int so, int base) {
  asm volatile("s_add_u32 m0, %0, %4\n\ts_nop 0\n\tbuffer_load_dwordx4 %1, %2, %3 offen lds" ::"s"(base), "v"(vo), "s"(r), "s"(so), "n"(IMM) : "m0", "scc", "memory");
}
template <int IMM, int NT> static __device__ __forceinline__ void dman(srd_t r, int vo, int so, int base) {
  if constexpr (NT) asm volatile("s_add_u32 m0, %0, %4\n\ts_nop 0\n\tbuffer_load_dwordx4 %1, %2, %3 offen nt lds" ::"s"(base), "v"(vo), "s"(r), "s"(so), "n"(IMM) : "m0", "scc", "memory");
  else dma<IMM>(r, vo, so, base);
}
#define NT_KV 0
#define NT_H 0
#define NT_SP 0
#define NT_X 0
#define NT_RING 0
template <int IMM> static __device__ __forceinline__ void dmac(srd_t r, int vo, int so, int base) {
  asm volatile("s_add_u32 m0, %0, %4\n\ts_nop 0\n\tbuffer_load_dwordx4 %1, %2, %3 offen sc1 lds" ::"s"(base), "v"(vo), "s"(r), "s"(so), "n"(IMM) : "m0", "scc", "memory");
}
static __device__ __forceinline__ void touch(srd_t r, int vo, int so) {
  asm volatile("s_mov_b32 m0, %3\n\ts_nop 0\n\tbuffer_load_dword %0, %1, %2 offen lds" ::"v"(vo), "s"(r), "s"(so), "n"(LDS_BYTES - 256) : "m0", "memory");
}
static __device__ __forceinline__ void touchc(srd_t r, int vo, int so) {
  asm volatile("s_mov_b32 m0, %3\n\ts_nop 0\n\tbuffer_load_dword %0, %1, %2 offen sc1 lds" ::"v"(vo), "s"(r), "s"(so), "n"(LDS_BYTES - 256) : "m0", "memory");
}
static __device__ __forceinline__ float one(float r) { asm("" : "+v"(r)); return r; }
static __device__ __forceinline__ float fadd(float a, float b) { return one(a + b); }
static __device__ __forceinline__ float fmul(float a, float b) { return one(a * b); }
static __device__ __forceinline__ float ffma(float a, float b, float c) { return one(__builtin_fmaf(a, b, c)); }
template <int N, typename Fn> static __device__ __forceinline__ void unr(Fn &&f) {
  if constexpr (N > 0) { unr<N - 1>(f); f(std::integral_constant<int, N - 1>{}); }
}
static __device__ __forceinline__ f4 mfma(v4u a, v4u b, f4 c) {
  return __builtin_amdgcn_mfma_f32_16x16x32_bf16(__builtin_bit_cast(bf8, a), __builtin_bit_cast(bf8, b), c, 0, 0, 0);
}
static __device__ __forceinline__ void mma(f4 &c, v4u a, v4u b) { asm volatile("v_mfma_f32_16x16x32_bf16 %0, %1, %2, %0" : "+v"(c) : "v"(a), "v"(b)); }
static __device__ __forceinline__ void mma0(f4 &c, v4u a, v4u b) { asm volatile("v_mfma_f32_16x16x32_bf16 %0, %1, %2, 0" : "=v"(c) : "v"(a), "v"(b)); }
typedef float f16v __attribute__((ext_vector_type(16)));
static __device__ __forceinline__ void mmb(f16v &c, v4u a, v4u b) { asm volatile("v_mfma_f32_32x32x16_bf16 %0, %1, %2, %0" : "+v"(c) : "v"(a), "v"(b)); }
template <bool FIRST> static __device__ __forceinline__ void mmaf(f4 &c, v4u a, v4u b) { if constexpr (FIRST) mma0(c, a, b); else mma(c, a, b); }
#define SETTLE() do { SB(); asm volatile("s_nop 7" ::: "memory"); SB(); } while (0)
#define WAITC(vm, lg) __builtin_amdgcn_s_waitcnt(((vm) & 15) | 0x70 | ((lg) << 8) | (((vm) >> 4) << 14))
static __device__ __forceinline__ float dot2(u32 a, u32 b, float c) { return __builtin_amdgcn_fdot2_f32_bf16(__builtin_bit_cast(bf2, a), __builtin_bit_cast(bf2, b), c, false); }
static __device__ __forceinline__ float asf(u32 u) { return __builtin_bit_cast(float, u); }
static __device__ __forceinline__ u32 asu(float f) { return __builtin_bit_cast(u32, f); }
static __device__ __forceinline__ float lo16(u32 u) { return asf(u << 16); }
static __device__ __forceinline__ float hi16(u32 u) { return asf(u & 0xffff0000u); }
static __device__ __forceinline__ u32 pk(float a, float b) { f2 t = {a, b}; return __builtin_bit_cast(u32, __builtin_convertvector(t, bf2)); }
static __device__ __forceinline__ float rbf(float a) { return lo16(pk(a, a)); }
static __device__ __forceinline__ v4u lds16(LAS unsigned char *L, int off) { return *(LAS v4u *)(L + off); }
static __device__ __forceinline__ f4 ldsf4(LAS unsigned char *L, int off) { return *(LAS f4 *)(L + off); }
static __device__ __forceinline__ void sts16(LAS unsigned char *L, int off, v4u v) { *(LAS v4u *)(L + off) = v; }
static __device__ __forceinline__ v2u trd(LAS unsigned char *L, int off) {
  return __builtin_bit_cast(v2u, __builtin_amdgcn_ds_read_tr16_b64_v4i16((LAS s4 *)(L + off)));
}
static __device__ __forceinline__ v4u trd2(LAS unsigned char *L, int off, int step) {
  const v2u a = trd(L, off), b = trd(L, off + step);
  return v4u{a.x, a.y, b.x, b.y};
}
static __device__ __forceinline__ f2 fma2(f2 a, f2 b, f2 c) { return __builtin_elementwise_fma(a, b, c); }
static __device__ __forceinline__ u32 pk2(f2 t) { return __builtin_bit_cast(u32, __builtin_convertvector(t, bf2)); }
static __device__ __forceinline__ f2 lo2(f4 v) { return __builtin_shufflevector(v, v, 0, 1); }
static __device__ __forceinline__ f2 hi2(f4 v) { return __builtin_shufflevector(v, v, 2, 3); }
static __device__ __forceinline__ void rowsum8(float (&a)[8], float (&b)[8]) {
#define DA(d, s, ctl, bm) "v_add_f32_dpp " d ", " s ", " s " " ctl " row_mask:0xf bank_mask:" bm "\n\t"
  asm volatile("s_nop 1\n\t"
               DA("%0", "%0", "row_mirror", "0x3") DA("%1", "%1", "row_mirror", "0x3") DA("%2", "%2", "row_mirror", "0x3") DA("%3", "%3", "row_mirror", "0x3")
               DA("%4", "%4", "row_mirror", "0x3") DA("%5", "%5", "row_mirror", "0x3") DA("%6", "%6", "row_mirror", "0x3") DA("%7", "%7", "row_mirror", "0x3")
               DA("%0", "%8", "row_mirror", "0xc") DA("%1", "%9", "row_mirror", "0xc") DA("%2", "%10", "row_mirror", "0xc") DA("%3", "%11", "row_mirror", "0xc")
               DA("%4", "%12", "row_mirror", "0xc") DA("%5", "%13", "row_mirror", "0xc") DA("%6", "%14", "row_mirror", "0xc") DA("%7", "%15", "row_mirror", "0xc")
               DA("%0", "%0", "row_half_mirror", "0x5") DA("%1", "%1", "row_half_mirror", "0x5") DA("%4", "%4", "row_half_mirror", "0x5") DA("%5", "%5", "row_half_mirror", "0x5")
               DA("%0", "%2", "row_half_mirror", "0xa") DA("%1", "%3", "row_half_mirror", "0xa") DA("%4", "%6", "row_half_mirror", "0xa") DA("%5", "%7", "row_half_mirror", "0xa")
               DA("%0", "%0", "quad_perm:[1,0,3,2]", "0xf") DA("%1", "%1", "quad_perm:[1,0,3,2]", "0xf") DA("%4", "%4", "quad_perm:[1,0,3,2]", "0xf") DA("%5", "%5", "quad_perm:[1,0,3,2]", "0xf")
               DA("%0", "%0", "quad_perm:[2,3,0,1]", "0xf") DA("%1", "%1", "quad_perm:[2,3,0,1]", "0xf") DA("%4", "%4", "quad_perm:[2,3,0,1]", "0xf") DA("%5", "%5", "quad_perm:[2,3,0,1]", "0xf")
               : "+v"(a[0]), "+v"(a[1]), "+v"(a[2]), "+v"(a[3]), "+v"(b[0]), "+v"(b[1]), "+v"(b[2]), "+v"(b[3])
               : "v"(a[4]), "v"(a[5]), "v"(a[6]), "v"(a[7]), "v"(b[4]), "v"(b[5]), "v"(b[6]), "v"(b[7]));
#undef DA
}
static __device__ __forceinline__ void lds_signal(LAS unsigned char *L, int off) {
  asm volatile("s_waitcnt lgkmcnt(0)" ::: "memory");
  if ((threadIdx.x & 63) == 0) __hip_atomic_fetch_add((LAS u32 *)(L + off), 1u, __ATOMIC_RELAXED, __HIP_MEMORY_SCOPE_WORKGROUP);
}
static __device__ __forceinline__ bool lds_last(LAS unsigned char *L, int off) {
  u32 n = 0;
  if ((threadIdx.x & 63) == 0) n = __hip_atomic_fetch_add((LAS u32 *)(L + off), 1u, __ATOMIC_RELAXED, __HIP_MEMORY_SCOPE_WORKGROUP);
  return __builtin_amdgcn_readfirstlane(n) == 3;
}
static __device__ __forceinline__ void lds_wait(LAS unsigned char *L, int off, u32 n) {
#pragma unroll 1
  for (u32 s = 0; s < (SPIN_CAP << 4); ++s)
  {
    if (__hip_atomic_load((LAS u32 *)(L + off), __ATOMIC_RELAXED, __HIP_MEMORY_SCOPE_WORKGROUP) >= n) break;
    __builtin_amdgcn_s_sleep(1);
  }
  asm volatile("" ::: "memory");
}
static __device__ __forceinline__ u64 ldflag(unsigned char *ws, int off) { return __hip_atomic_load((GAS u64 *)(ws + off), __ATOMIC_RELAXED, __HIP_MEMORY_SCOPE_AGENT); }
static __device__ __forceinline__ void stflag(unsigned char *ws, int off, u64 v) { __hip_atomic_store((GAS u64 *)(ws + off), v, __ATOMIC_RELAXED, __HIP_MEMORY_SCOPE_AGENT); }
static __device__ __forceinline__ int fl(int n) { return WS_FL + n * FS; }
#define FSK 8
static __device__ __forceinline__ int flk(int n) { return WS_FL + n * FSK; }
static __device__ __forceinline__ void setflagk(unsigned char *ws, int n, u64 tag) {
  const int l = threadIdx.x & 63;
  if (FULL && FSK >= 16) { if (l < FSK / 16) st16c(v4u{(u32)tag, (u32)(tag >> 32), (u32)tag, (u32)(tag >> 32)}, mk(ws, WS_BYTES), flk(n) + l * 16, 0); }
  else if (l == 0) stflag(ws, flk(n), tag);
}
static __device__ __forceinline__ void setflag(unsigned char *ws, int n, u64 tag) {
  const int l = threadIdx.x & 63;
  if (FULL) { if (l < FS / 16) st16c(v4u{(u32)tag, (u32)(tag >> 32), (u32)tag, (u32)(tag >> 32)}, mk(ws, WS_BYTES), fl(n) + l * 16, 0); }
  else if (l == 0) stflag(ws, fl(n), tag);
}
static __device__ __forceinline__ void wait_flags(unsigned char *ws, int off, u64 tag) {
#pragma unroll 1
  for (u32 s = 0; s < SPIN_CAP; ++s) {
    const u64 v = ldflag(ws, off);
    if (__all(v == tag)) break;
    __builtin_amdgcn_s_sleep(1);
  }
  asm volatile("" ::: "memory");
}
static __device__ __forceinline__ void hint_flags(const unsigned char *at, u64 tag) {
  typedef u64 v8q __attribute__((ext_vector_type(8)));
#pragma unroll 1
  for (u32 s = 0; s < 4096; ++s) {
    v8q f;
    asm volatile("s_load_dwordx16 %0, %1, 0x0 glc\n\ts_waitcnt lgkmcnt(0)" : "=s"(f) : "s"(at) : "memory");
    u64 d = 0;
#pragma unroll
    for (int q = 0; q < 8; ++q) d |= f[q] ^ tag;
    if (d == 0) break;
    __builtin_amdgcn_s_sleep(1);
  }
}
static __device__ __forceinline__ u32 lds_peek(LAS unsigned char *L, int off) { return __hip_atomic_load((LAS u32 *)(L + off), __ATOMIC_RELAXED, __HIP_MEMORY_SCOPE_WORKGROUP); }
static __device__ __forceinline__ void lds_check(LAS unsigned char *L, int off, u32 n, u32 seen) {
  if (__builtin_expect(__builtin_amdgcn_readfirstlane(seen) < n, 0)) lds_wait(L, off, n);
  asm volatile("" ::: "memory");
}
static __device__ __forceinline__ float inv_sigma(float var) {
  const float sd = rbf(__builtin_amdgcn_sqrtf(rbf(rbf(fmaxf(var, 0.f)) + 1e-5f)));
  return __builtin_amdgcn_rcpf(sd);
}
static __device__ __forceinline__ int mod7(int a) { return a >= 7 ? a - 7 : a; }

constexpr int L_KR = 81920;
constexpr int L_RV = L_KR + 20480;
constexpr int L_XR = 114688;
constexpr int L_Z2 = 114688;
constexpr int L_Z1 = 131072;
constexpr int L_KD = 147456;
constexpr int L_CS = 155648;
constexpr int L_XST = 156672;
constexpr int L_ZST = 157184;
constexpr int L_CNT = 157696;
constexpr int L_BS = 157952;

#define PMS 1
#define PTW 1
#define TSP 0
#define TZ1 0
#define NISS {18, 5, 5, 5, 5, 4, 0, 0}
#define NIZ {0, 0, 0, 0, 0, 1, 5, 5}
#define NIW {0, 0, 0, 0, 0, 0, 0, 0}
#define MIXZ 30
#define LASTA 2
#define W1 7
#define W2 12
#define KV1 1
#define KSLEEP 16
#define ZP -100
#define WOW 12
#define WSLP 4
#define AD 2
#define RREG 1
#define OP 8
#define UW 12
#define WOR 2
#define WOK 1
#define YLATE 1
#define WSO(n) ((mod7(((n) >> 2) + j) * 16 + ((n) & 3) * 4 + m) * 7168)
#define SIGE 2
#define TLC 0
#define TLG 0
#define UPOS 0
#define NO0 NO1
#define XAT 4
#define PMT 0
#define K1 8
#define T5 0
#define T6 T5
#define TS 2
#define RE 0
#define WTN 1
#define CLA 5
#define TRK {0, 0, 3, 3, 3, 3, 4}
#define TRS 4
#define ROT1 0
#define TD 0
#define TC 1
#define CLZ 13
#define CLU 3
#define HZ 1
#define HU 1
#define OWNM 1
#define NR1 12
#define X0 30
#define X1 32
#define X2 38
#define ZSL 0
#define ZSL2 8
#define NO1 12
constexpr int VLIM = 63;
#define RD 8
#define KVF_AT 3
#define SH_AT 6
#define TSLEEP 32
#define NS 10
#define PRE 0
#define DELTA 0
#define WOT 2
#define CLR_EARLY 1
#define CLR_SLEEP 8
#define PW 63
#define WSL 0
#define PC0 0
#define PC1 2
#define PM2 3
#define PM3 1
#define PCV 2
#define NB 6
#define NB2 4
struct Order {
  int seq[102], bar[8];
  constexpr int last(int a, int b) const { int r = 0; for (int q = 0; q < 102; ++q) if (seq[q] >= a && seq[q] < b) r = q; return r; }
};
constexpr Order make_order() {
  Order o{};
  constexpr int a[8] = NISS, z[8] = NIZ, w[8] = NIW;
  int n = 0, ia = 0, iz = 42, iw = 70;
  for (int s = 0; s < 8; ++s) {
    int ca = a[s], cz = z[s], cw = w[s];
    while (ca + cz + cw > 0) {
      for (int q = 0; q < 2 && ca > 0; ++q) { o.seq[n++] = ia++; --ca; }
      if (cz > 0) { o.seq[n++] = iz++; --cz; } else if (cw > 0) { o.seq[n++] = iw++; --cw; }
    }
    o.bar[s] = n;
  }
  while (n < 70 + NS) {
    for (int q = 0; q < MIXZ && iz < 70; ++q) o.seq[n++] = iz++;
    if (iw < 70 + NS && (iz == 70 || iw < 80)) o.seq[n++] = iw++;
  }
  while (n < 102) o.seq[n++] = 1000;
  return o;
}
constexpr Order ORD = make_order();
constexpr bool order_ok() {
  for (int q = 0; q < ORD.bar[7]; ++q) {
    int s = 0;
    while (q >= ORD.bar[s]) ++s;
    const int n = ORD.seq[q], st = n / 6;
    if (n < 42 && s > st) return false;
    if (n < 42 && n % 6 < 2 && st >= 4 && s < st - 2) return false;
    if (n < 42 && n % 6 >= 2 && st >= 5 && s < 2) return false;
    if (n >= 42 && n < 70 && s < (n - 42) / 4 + 3) return false;
  }
  return ORD.last(0, 42) < ORD.bar[6];
}
static_assert(order_ok(), "");
struct Tile { int i, j, rot1, rot2, e, ne; };

static __device__ __forceinline__ u64 epoch_load(const unsigned char *ws) {
  u64 e;
  asm volatile("s_load_dwordx2 %0, %1, 0x0" : "=s"(e) : "s"(ws) : "memory");
  return e;
}
#define EPOCH_WAIT(e) asm volatile("s_waitcnt lgkmcnt(0)" : "+s"(e)::"memory")
static __device__ __forceinline__ v4u widen(v2u x, v2u y) {
  const v2u a = __builtin_amdgcn_permlane16_swap(x.x, y.x, false, false), b = __builtin_amdgcn_permlane16_swap(x.y, y.y, false, false);
  return v4u{a.x, b.x, a.y, b.y};
}

static __device__ __forceinline__ void ztouch(const Params &p, const Tile t, int ck, int q, int l) {
  const int e = t.e + q;
  if (q < t.ne) touchc(mks(p.ws + WS_ZB + t.j * 524288, 524288), l * 128, ((t.rot2 + 4 * ck + (e >> 1)) & 31) * 16384 + (e & 1) * 8192);
}
static __device__ __forceinline__ void kv_fetch(const Params &p, const Tile t, int w, v2u &shk, u32 &dvr) {
  const int i = t.i, j = t.j, l = threadIdx.x & 63;
  const srd_t sk = mks(p.k, S_ * D_ * 2), sv = mks(p.v, S_ * D_ * 2);
  const int vo = (l >> 3) * 896 + j * 128 + (((l & 7) ^ ((l >> 2) & 4)) << 4), so = (64 * i + 16 * w) * 896, lb = w * 5120;
  dman<L_KR + 1024, NT_KV>(sk, vo, so, lb);
  dman<L_KR + 2048, NT_KV>(sk, vo, so + 7168, lb);
  dman<L_KR, NT_KV>(sk, vo, so > 0 ? so - 7168 : 0x40000000, lb);
  dman<L_KR + 3072, NT_KV>(sv, vo, so, lb);
  dman<L_KR + 4096, NT_KV>(sv, vo, so + 7168, lb);
  asm volatile("global_load_dwordx2 %0, %2, %3\n\tglobal_load_ushort %1, %4, %5" : "=&v"(shk), "=&v"(dvr) : "v"((64 * j + l) * 8), "s"(p.shk), "v"((64 * j + l) * 2), "s"(p.dv) : "memory");
}

static __device__ __forceinline__ void gemm_compute(const Params &p, LAS unsigned char *L, const Tile t, int w, v2u shk, u32 dvr) {
  STAMP_DECL;
  STAMP(0);
  const int i = t.i, j = t.j, rh = w >> 1, ch = w & 1;
  int l = threadIdx.x & 63, g = l >> 4, c = l & 15;
  u64 tag = epoch_load(p.ws);
  const rsrc_t rws = mk(p.ws, WS_BYTES);
  v4u xf[2][14];
  f4 acc[2][4], cs[2];
  int rb[4];
  v2u xe[4];
  auto ldx = [&]() {
    const rsrc_t rx = mk(p.x, S_ * D_ * 2);
#pragma unroll
    for (int q = 0; q < 4; ++q) xe[q] = __builtin_amdgcn_raw_buffer_load_b64(rx, (64 * i + 16 * w + c) * 896 + j * 128 + g * 8 + q * 32, 0, 0);
    SB();
  };
  auto clz = [&]() {
    const int o = WS_ZB + (j * 64 + 2 * i) * 8192 + w * 4096 + l * 16;
#pragma unroll
    for (int q = 0; q < 4; ++q) st16c(v4u{~0u, ~0u, ~0u, ~0u}, rws, o, q * 1024);
    SB();
  };
  if (HZ && CLZ == -1) clz();
  v4u ones = {0x3f803f80u, 0x3f803f80u, 0x3f803f80u, 0x3f803f80u};
  auto start = [&]() {
    const int rdoff = (8 * g + (c >> 2)) * 256 + ((((c >> 2) | ((g & 1) << 2)) ^ (4 * ch + 2 * rh)) << 5) + (c & 3) * 8;
#pragma unroll
    for (int n = 0; n < 4; ++n) { rb[n] = rdoff ^ (n << 5); PIN(rb[n]); }
    PIN(ones);
  };
  int xo0 = L_XR + (32 * rh + c) * 128 + ((g ^ (c & 7)) << 4), xo1 = xo0 ^ 64;
  int lno = L_RV + (16 * w + (l >> 3)) * 144 + (l & 7) * 16, lso = WS_KVR + (64 * j + 16 * w + (l >> 3)) * 4096 + (31 - i) * 128 + (l & 7) * 16;
  {
    __builtin_amdgcn_s_setprio(PC0);
    SB();
    if (threadIdx.x < 16) *(LAS u32 *)(L + L_CNT + threadIdx.x * 4) = 0;
    int ro = L_KR + w * 5120 + (c >> 2) * 128 + ((g ^ ((c >> 2) & 2)) << 5) + (c & 3) * 8;
    int wo = L_RV + l * 144 + (3 - w) * 32, ko = L_KD + l * 128 + ((w ^ ((c >> 1) & 3)) << 5);
    start();
    PIN(xo0); PIN(xo1); PIN(lno); PIN(lso); PIN(ro); PIN(wo); PIN(ko);
    f2 o[8];
#pragma unroll
    for (int q = 0; q < 8; ++q) { o[q] = f2{0, 0}; PIN(o[q]); }
    EPOCH_WAIT(tag);
    tag += 1;
    asm volatile("s_waitcnt vmcnt(%2)" : "+v"(shk), "+v"(dvr) : "n"(HZ && CLZ == -1 ? 4 : 0) : "memory");
    STAMP(1);
    v2u kr[5], vr[4];
#pragma unroll
    for (int q = 0; q < 5; ++q) kr[q] = trd(L, ro + 512 + q * 512);
#pragma unroll
    for (int q = 0; q < 4; ++q) vr[q] = trd(L, ro + 3072 + q * 512);
    SB();
    const u32 ta = __builtin_amdgcn_alignbit(shk.y, shk.y, 16), tb = __builtin_amdgcn_alignbit(shk.x, shk.x, 16);
    const float dd = lo16(dvr);
    u32 ke[10], kq[9];
#pragma unroll
    for (int q = 0; q < 5; ++q) { ke[2 * q] = kr[q].x; ke[2 * q + 1] = kr[q].y; }
#pragma unroll
    for (int q = 0; q < 9; ++q) kq[q] = __builtin_amdgcn_alignbit(ke[q + 1], ke[q], 16);
#pragma unroll
    for (int q = 0; q < 8; ++q) o[q] = f2{dot2(kq[q], ta, o[q].x), dot2(ke[q + 1], ta, o[q].y)};
    SB();
#pragma unroll
    for (int q = 0; q < 8; ++q) o[q] = f2{dot2(kq[q + 1], tb, o[q].x), dot2(ke[q + 2], tb, o[q].y)};
    SB();
#pragma unroll
    for (int q = 0; q < 8; ++q) { const u32 vv = (q & 1) ? vr[q >> 1].y : vr[q >> 1].x; o[q] *= f2{lo16(vv), hi16(vv)}; }
    u32 rv[8];
#pragma unroll
    for (int q = 0; q < 8; ++q) asm("v_cvt_pk_bf16_f32 %0, %1, %2" : "=v"(rv[q]) : "v"(o[7 - q].y), "v"(o[7 - q].x));
    sts16(L, wo, v4u{rv[0], rv[1], rv[2], rv[3]});
    sts16(L, wo + 16, v4u{rv[4], rv[5], rv[6], rv[7]});
    SB();
#pragma unroll
    for (int q = 0; q < 8; ++q) rv[q] = pk2(o[q] * dd);
    sts16(L, ko, v4u{rv[0], rv[1], rv[2], rv[3]});
    sts16(L, ko + 16, v4u{rv[4], rv[5], rv[6], rv[7]});
  }
  STAMP(2);

  v2u wh[2][8];
  auto rd1 = [&](int ks, int q) {
    if (q < 2) xf[q][ks] = lds16(L, ((ks & 1) ? xo1 : xo0) + ((ks >> 1) & 3) * 8192 + q * 2048);
    else {
      const int n = (q - 2) >> 1;
      if (ks == 7 && !(q & 1)) { rb[n] += 57344; PIN(rb[n]); }
      wh[ks & 1][q - 2] = trd(L, rb[n] + (ks % 7) * 8192 + (q & 1) * 1024);
    }
  };
  auto wfr = [&](int ks, int n) { const v2u a = wh[ks & 1][2 * n], b = wh[ks & 1][2 * n + 1]; return v4u{a.x, a.y, b.x, b.y}; };
  auto mm1 = [&](auto ksc, int q) {
    constexpr int ks = ksc;
    if (q < 8) mmaf<ks == 0>(acc[q & 1][q >> 1], xf[q & 1][ks], wfr(ks, q >> 1));
    else mmaf<ks == 0>(cs[q - 8], ones, wfr(ks, q - 8));
  };
  auto step = [&](auto ksc, int q0) {
#pragma unroll
    for (int q = 0; q < 10; ++q) { mm1(ksc, q); SB(); if (q + q0 < 10) rd1(ksc + 1, q + q0); SB(); }
  };
  auto put_cs = [&](int off) {
    const int o = L_CS + off + ((4 * ch + 2 * rh) * 16 + c) * 4;
    *(LAS float *)(L + o) = cs[0].x;
    *(LAS float *)(L + o + 64) = cs[1].x;
  };
  auto lastk = [&](int off) {
    mm1(std::integral_constant<int, 13>{}, 8);
    mm1(std::integral_constant<int, 13>{}, 9);
#pragma unroll
    for (int q = 0; q < 4; ++q) mm1(std::integral_constant<int, 13>{}, q);
    SB();
    asm volatile("s_nop 3" ::: "memory");
    SB();
    put_cs(off);
    SB();
#pragma unroll
    for (int q = 4; q < 8; ++q) mm1(std::integral_constant<int, 13>{}, q);
    SB();
  };
  STAMPA(1);
  __builtin_amdgcn_s_setprio(PC1);
  BARW();
  STAMPA(2);
  {
    v4u ln[2];
    ln[0] = lds16(L, lno);
    ln[1] = lds16(L, lno + 1152);
#pragma unroll
    for (int q = 0; q < 10; ++q) rd1(0, q);
    SB();
    st16c(ln[0], rws, lso, 0);
    st16c(ln[1], rws, lso, 32768);
    SB();
  }
  unr<13>([&](auto ksc) {
    constexpr int ks = ksc;
    if constexpr (ks & 1) {
      STAMPA(ks + 2);
      SB(); asm volatile("s_waitcnt lgkmcnt(8)\n\ts_barrier" ::: "memory"); SB();
      STAMPA(ks + 3);
      if (ks == KVF_AT) {
        DRAIN();
        setflagk(p.ws, F_KV + (4 * j + w) * 32 + i, tag);
        SB();
      }
    }
    if constexpr (HZ && ks == CLZ) clz();
    step(ksc, 0);
  });
  if (HZ && CLZ == 13) clz();
  lastk(0);
  if (HZ && CLZ == 14) clz();
  BARW();
  SETTLE();
  STAMP(6);
  FRESH();
  const u32 tagA = (u32)tag, tagB = (u32)(tag >> 32) ^ 0x9e3779b9u;
  f4 mm[2], rr[2];
  float cv[4];
  auto act_in = [&]() {
#pragma unroll
    for (int m = 0; m < 2; ++m) {
      mm[m] = ldsf4(L, L_XST + (32 * rh + 8 * g + 4 * m) * 4);
      rr[m] = ldsf4(L, L_XST + 256 + (32 * rh + 8 * g + 4 * m) * 4);
    }
    cv[0] = -cs[0].x;
    cv[1] = -cs[1].x;
  };
  auto act_in2 = [&](int off) {
    const int o = L_CS + off + ((4 * ch + (2 - 2 * rh)) * 16 + c) * 4;
    cv[2] = -*(LAS float *)(L + o);
    cv[3] = -*(LAS float *)(L + o + 64);
  };
  auto act = [&](int n, f2 (&z)[4]) {
    f2 x[4], a[4];
    const f2 c2 = {cv[n], cv[n]};
    SB();
#pragma unroll
    for (int m = 0; m < 2; ++m) {
      x[2 * m] = fma2(lo2(mm[m]), c2, lo2(acc[m][n]));
      x[2 * m + 1] = fma2(hi2(mm[m]), c2, hi2(acc[m][n]));
    }
    SB();
#pragma unroll
    for (int m = 0; m < 2; ++m) { x[2 * m] *= lo2(rr[m]); x[2 * m + 1] *= hi2(rr[m]); }
    SB();
#pragma unroll
    for (int q = 0; q < 4; ++q) a[q] = x[q] * x[q];
    SB();
#pragma unroll
    for (int q = 0; q < 4; ++q) a[q] = fma2(a[q], f2{-0.1029432397f, -0.1029432397f}, f2{-2.302208198f, -2.302208198f});
    SB();
#pragma unroll
    for (int q = 0; q < 4; ++q) a[q] *= x[q];
    SB();
#pragma unroll
    for (int q = 0; q < 4; ++q) a[q] = f2{__builtin_amdgcn_exp2f(a[q].x), __builtin_amdgcn_exp2f(a[q].y)};
    SB();
#pragma unroll
    for (int q = 0; q < 4; ++q) a[q] += 1.0f;
    SB();
#pragma unroll
    for (int q = 0; q < 4; ++q) a[q] = f2{__builtin_amdgcn_rcpf(a[q].x), __builtin_amdgcn_rcpf(a[q].y)};
    SB();
#pragma unroll
    for (int q = 0; q < 4; ++q) z[q] = x[q] * a[q];
    SB();
  };
  u32 seen;
  {
    act_in();
    act_in2(0);
    f2 s[4], u[4];
    v4u zp[4];
#pragma unroll
    for (int n = 0; n < 4; ++n) {
      f2 z[4];
      act(n, z);
#pragma unroll
      for (int q = 0; q < 4; ++q) {
        s[q] = n ? s[q] + z[q] : z[q];
        u[q] = n ? fma2(z[q], z[q], u[q]) : z[q] * z[q];
      }
      zp[n] = v4u{pk2(z[0]), pk2(z[1]), pk2(z[2]), pk2(z[3])};
    }
    seen = lds_peek(L, L_CNT);
    float sa[8], sq[8];
#pragma unroll
    for (int q = 0; q < 4; ++q) { sa[2 * q] = s[q].x; sa[2 * q + 1] = s[q].y; sq[2 * q] = u[q].x; sq[2 * q + 1] = u[q].y; }
    rowsum8(sa, sq);
    const int row = 8 * g + (c & 1) + ((c >> 1) & 6);
    if ((c & 2) == 0)
      st16c(v4u{asu((c & 1) ? sa[1] : sa[0]), tagA, asu((c & 1) ? sq[1] : sq[0]), tagB}, rws, ((i * 2 + rh) * 16 + 2 * j + ch) * 512 + row * 16, WS_ST);
    SB();
    const int zo = L_Z2 + (rh * 8 + 4 * ch) * 1024 + l * 16;
#pragma unroll
    for (int n = 0; n < 4; ++n) sts16(L, zo + ((n & 1) << 10) + (((n >> 1) ^ rh) << 11), zp[n]);
    SB();
    if (HZ && CLZ < 15) VM(1);
    if (l == 0) __hip_atomic_fetch_add((LAS u32 *)(L + L_CNT + 4), 1u, __ATOMIC_RELAXED, __HIP_MEMORY_SCOPE_WORKGROUP);
    SB();
    if (HZ && CLZ == 15) clz();
    lds_check(L, L_CNT, 4, seen);
#pragma unroll
    for (int n = 0; n < 4; ++n) { rb[n] -= 57344; PIN(rb[n]); }
#pragma unroll
    for (int q = 2; q < 10; ++q) rd1(0, q);
    SB();
  }
  SB();
  unr<13>([&](auto ksc) {
    if constexpr (HZ && CLZ == 15 && ksc == CLA) {
      VM(0);
      if (l == 0) __hip_atomic_fetch_add((LAS u32 *)(L + L_CNT + 52), 1u, __ATOMIC_RELAXED, __HIP_MEMORY_SCOPE_WORKGROUP);
    }
    if constexpr (ksc == 6) seen = lds_peek(L, L_CNT + 28);
    if constexpr (ksc == 7) lds_check(L, L_CNT + 28, 4, seen);
    SB();
    step(ksc, 2);
  });
  lastk(512);
  lds_signal(L, L_CNT + 8);
  SETTLE();
  FRESH();
  u32 bias[4];
  {
    act_in();
    const int po = L_Z1 + (64 * ch + c) * 128 + (((4 * rh + g) ^ (c & 7)) << 4);
#pragma unroll
    for (int n = 0; n < 4; ++n) {
      f2 z[4];
      if (n == 1) seen = lds_peek(L, L_CNT + 8);
      if (n == 2) {
        lds_check(L, L_CNT + 8, 4, seen);
        act_in2(512);
#pragma unroll
        for (int q = 0; q < 4; ++q) bias[q] = *(LAS u16 *)(L + L_BS + (16 * q + c) * 2);
      }
      if (n == 3) seen = lds_peek(L, L_CNT + 16);
      act(n, z);
      sts16(L, po + ((n & 1) << 11) + (((n >> 1) ^ rh) << 12), v4u{pk2(z[0]), pk2(z[1]), pk2(z[2]), pk2(z[3])});
    }
  }
  STAMP(8);
  FRESH();
  f4 g2[4][2];
  float z1[32];
  bool last;
  int uo[4], ob[4], uw = L_Z1 + w * 4096 + l * 16;
#pragma unroll
  for (int n = 0; n < 4; ++n) { ob[n] = ((4 * g + (c >> 2)) * 128 + ((((c >> 3) & 1) | ((g & 1) << 1)) << 5) + (c & 3) * 8) ^ (n << 5); PIN(ob[n]); }
#pragma unroll
  for (int m = 0; m < 4; ++m) { uo[m] = WS_UB + (i * 112 + 4 * j + w) * 1024 + l * 16 + m * 28672; PIN(uo[m]); }
  {
    v4u bf[8][2], af[4][4];
    const rsrc_t rz = mk(p.ws + WS_ZB + j * 524288, 524288);
    int bvo[2], bso = t.rot2 * 16384;
    bvo[0] = w * 2048 + l * 16;
    bvo[1] = bvo[0] + 8192;
    PIN(bvo[0]); PIN(bvo[1]);
    auto ldb = [&](int ks, int b, int so) { bf[ks & 7][b] = ld16c(rz, bvo[ks & 1] + b * 1024, so); };
#define BMASK 0x7ffff
    auto next = [&](int &so) { asm volatile("s_add_u32 %0, %0, 0x4000\n\ts_and_b32 %0, %0, %1" : "+s"(so) : "n"(BMASK) : "scc"); };
#pragma unroll
    for (int m = 0; m < 4; ++m) { const float b = lo16(bias[m]); g2[m][0] = g2[m][1] = f4{b, b, b, b}; PIN(g2[m][0]); PIN(g2[m][1]); }
    int ab[2];
    ab[0] = c * 128 + ((g ^ (c >> 1)) << 4);
    ab[1] = ab[0] ^ 64;
    PIN(ab[0]); PIN(ab[1]);
    auto rda = [&](int ks, int m) { af[ks & 3][m] = *(LAS v4u *)((LAS unsigned char *)(u32)ab[ks & 1] + ((ks >> 1) & 3) * 8192 + m * 2048); };
    auto turn = [&](int q) { ab[q] = (ab[q] + 32768) & 0x1ffff; PIN(ab[q]); };
    const int f0 = 4 * g + (c >> 2);
    int zr[4];
#pragma unroll
    for (int e = 0; e < 4; ++e) { zr[e] = (L_Z1 + (32 * w + f0) * 128 + (c & 1) * 8 + ((((c >> 1) & 1) ^ (f0 & 7)) << 4)) ^ (e << 5); PIN(zr[e]); }
    v2u zq[8];
    PIN(uw);
    if (ZP < 0 && w == 0) {
      lds_wait(L, L_CNT + 24, 4);
      __builtin_amdgcn_s_sleep(ZSL);
      const int fo = fl(F_Z + j * 32 + (l & 31));
#pragma unroll 1
      for (u32 s = 0; s < SPIN_CAP; ++s) {
        if (__all(ldflag(p.ws, fo) == tag)) break;
        __builtin_amdgcn_s_sleep(ZSL2);
      }
      asm volatile("" ::: "memory");
      lds_signal(L, L_CNT + 16);
    } else lds_check(L, L_CNT + 16, 1, seen);
    STAMP(9);
    if (TD && TC == 0) { ztouch(p, t, 1 + (w & 1), w >> 1, l); if (TD > 3 && !(w & 1)) ztouch(p, t, 3, w >> 1, l); SB(); }
#pragma unroll
    for (int ks = 0; ks < 7; ++ks) { ldb(ks, 0, bso); SB(); ldb(ks, 1, bso); SB(); if (ks & 1) next(bso); SB(); }
    if (TD && TC == 1) { ztouch(p, t, 1 + (w & 1), w >> 1, l); if (TD > 3 && !(w & 1)) ztouch(p, t, 3, w >> 1, l); SB(); }
    lds_wait(L, L_CNT + 12, 4);
    __builtin_amdgcn_s_setprio(3);
    STAMP(10);
#pragma unroll
    for (int ks = 0; ks < AD; ++ks)
#pragma unroll
      for (int m = 0; m < 4; ++m) rda(ks, m);
    SB();
    STAMP(5);
    auto wide = [&](int e) {
      const u32 z = (e & 2) ? zq[e >> 2].y : zq[e >> 2].x;
      if (e & 1) asm volatile("v_and_b32 %0, 0xffff0000, %1" : "=v"(z1[e]) : "v"(z));
      else asm volatile("v_lshlrev_b32 %0, 16, %1" : "=v"(z1[e]) : "v"(z));
    };
    auto chunk = [&](auto lastc) {
      constexpr bool LAST = lastc;
      unr<(LAST ? 6 : 8)>([&](auto sc) {
        constexpr int s = sc;
        WAITC(LAST && s ? 14 - 2 * s : 12, LAST && s == 2 ? 9 : 4);
        SB();
#pragma unroll
        for (int q = 0; q < 8; ++q) {
          mma(g2[q >> 1][q & 1], bf[s][q & 1], af[s & 3][q >> 1]);
          SB();
#define NOA 0
#define NOB 0
          if (!(q & 1) && (!NOA || LAST)) rda(s + 2, q >> 1);
          if ((q == 1 || q == 3) && (!LAST || s == 0) && (!NOB || LAST)) ldb(s + 7, q >> 1, bso);
          if (q == 5 && !(s & 1) && !LAST) next(bso);
          if (!LAST && q == (s == 5 ? 5 : 7) && (s == 5 || s == 6)) turn(~s & 1);
          if (LAST && s == 1 && (q & 1)) { zq[q - 1] = trd((LAS unsigned char *)(u32)zr[q >> 1], 0); zq[q] = trd((LAS unsigned char *)(u32)zr[q >> 1], 2048); }
          if (LAST && s >= 2 && (q & 1)) { wide(8 * (s - 2) + q - 1); wide(8 * (s - 2) + q); }
          SB();
        }
      });
      if (!LAST) BAR0();
    };
#pragma unroll 1
    for (int ck = 0; ck < 7; ++ck) {
      chunk(std::false_type{});
    }
    chunk(std::true_type{});
    u32 o[4][4];
    auto scale = [&](int m, int e) { asm volatile("v_mul_f32 %0, %0, %2\n\tv_mul_f32 %1, %1, %3" : "+v"(z1[8 * m + 2 * e]), "+v"(z1[8 * m + 2 * e + 1]) : "v"(g2[m][e >> 1][(2 * e) & 3]), "v"(g2[m][e >> 1][(2 * e + 1) & 3])); };
    auto pack = [&](int m, int e) {
      asm volatile("v_cvt_pk_bf16_f32 %0, %2, %3\n\tv_cvt_pk_bf16_f32 %1, %4, %5" : "=&v"(o[m][e]), "=&v"(o[m][e + 1])
                   : "v"(z1[8 * m + 2 * e]), "v"(z1[8 * m + 2 * e + 1]), "v"(z1[8 * m + 2 * e + 2]), "v"(z1[8 * m + 2 * e + 3]));
    };
    auto send = [&](int m) { st16c(v4u{o[m][0], o[m][1], o[m][2], o[m][3]}, rws, uo[m], 0); };
    auto keep = [&](int m) { sts16((LAS unsigned char *)(u32)uw, m * 1024, v4u{o[m][0], o[m][1], o[m][2], o[m][3]}); };
    auto two = [&](int s, int m, auto f0, auto f1) {
      mma(g2[m][0], bf[s][0], af[s & 3][m]); SB(); f0(); SB();
      mma(g2[m][1], bf[s][1], af[s & 3][m]); SB(); f1(); SB();
    };
    auto none = []() {};
    WAITC(2, 4);
    SB();
    two(6, 0, none, none);
    two(6, 1, none, [&]() { seen = lds_peek(L, L_CNT + 44); });
    WAITC(0, 1);
    SB();
    two(7, 0, none, none);
    two(6, 2, none, none);
    two(7, 1, none, none);
    two(6, 3, none, [&]() { asm volatile("s_nop 0"); scale(0, 0); scale(0, 1); });
    two(7, 2, [&]() { scale(0, 2); scale(0, 3); }, [&]() { pack(0, 0); pack(0, 2); });
    two(7, 3, [&]() { send(0); scale(1, 0); scale(1, 1); }, [&]() { keep(0); scale(1, 2); scale(1, 3); });
    STAMP(11);
    pack(1, 0); pack(1, 2);
    SB();
    send(1);
    keep(1);
    SB();
#pragma unroll
    for (int e = 0; e < 4; ++e) o[2][e] = pk2(f2{z1[16 + 2 * e], z1[17 + 2 * e]} * ((e & 1) ? hi2(g2[2][e >> 1]) : lo2(g2[2][e >> 1])));
    send(2);
    keep(2);
    SB();
#pragma unroll
    for (int e = 0; e < 4; ++e) o[3][e] = pk2(f2{z1[24 + 2 * e], z1[25 + 2 * e]} * ((e & 1) ? hi2(g2[3][e >> 1]) : lo2(g2[3][e >> 1])));
    send(3);
    keep(3);
    SB();
    u32 n36 = 0;
    if (l == 0) n36 = __hip_atomic_fetch_add((LAS u32 *)(L + L_CNT + 36), 1u, __ATOMIC_RELAXED, __HIP_MEMORY_SCOPE_WORKGROUP);
    SB();
    const f4 t4 = g2[0][0] + g2[0][1];
    const f2 t2 = lo2(t4) + hi2(t4);
    const bool bad = __builtin_amdgcn_ballot_w64(__builtin_isunordered(t2.x, t2.y)) != 0;
    SB();
    last = __builtin_amdgcn_readfirstlane(n36) == 3;
    if (HU && last) setflag(p.ws, F_U + i * 8 + j, tag);
    SB();
    if (HZ && __builtin_expect(bad, 0)) {
      FRESH();
      wait_flags(p.ws, fl(F_ZD + j * 32 + (l & 31)), tag);
      __builtin_amdgcn_fence(__ATOMIC_ACQUIRE, "agent");
      const rsrc_t ra = mk(p.wsp, S_ * S_ * 2);
      f4 h[4][2];
#pragma unroll
      for (int m = 0; m < 4; ++m) { const float b = lo16(*(LAS u16 *)(L + L_BS + (16 * m + c) * 2)); h[m][0] = h[m][1] = f4{b, b, b, b}; }
#pragma unroll 1
      for (int ks = 0; ks < 64; ++ks) {
        const v4u b0 = ld16c(rz, w * 2048 + l * 16, ks * 8192), b1 = ld16c(rz, w * 2048 + l * 16 + 1024, ks * 8192);
#pragma unroll
        for (int m = 0; m < 4; ++m) {
          const v4u a = ld16(ra, (64 * i + 16 * m + c) * 4096 + g * 16, ks * 64);
          h[m][0] = mfma(b0, a, h[m][0]);
          h[m][1] = mfma(b1, a, h[m][1]);
        }
      }
#pragma unroll
      for (int m = 0; m < 4; ++m) {
#pragma unroll
        for (int e = 0; e < 4; ++e) {
          const u32 z = (e & 1) ? zq[2 * m + (e >> 1)].y : zq[2 * m + (e >> 1)].x;
          o[m][e] = pk2(f2{lo16(z), hi16(z)} * ((e & 1) ? hi2(h[m][e >> 1]) : lo2(h[m][e >> 1])));
        }
        send(m);
      }
    }
  }
  SB();
  STAMP(7);
  {
    v4u uf[28], of[OP][4];
    int uso[7];
#pragma unroll
    for (int q = 1; q < 7; ++q) uso[q] = WS_UB + (i * 4 + w) * 28672 + mod7(q + j) * 4096;
    int uvo, yo, xo, oo, xr;
    auto ldu = [&](int ks) { uf[ks] = ld16c(rws, uvo + (ks & 3) * 1024, uso[ks >> 2]); };
    f4 o[4];
    auto rd = [&](int ks, int n) {
      if (ks == 14) { ob[n] += 57344; PIN(ob[n]); }
      of[ks % OP][n] = trd2((LAS unsigned char *)(u32)ob[n], (ks % 14) * 4096, 2048);
    };
    float xv[16], xn[16], kn[16], mr, rs;
    v2u kd[4];
    auto prep = [&](int q) {
      const int e = q >> 1;
      const u32 a = (e & 2) ? xe[e >> 2].y : xe[e >> 2].x, b = (e & 2) ? kd[e >> 2].y : kd[e >> 2].x;
      if (q & 1) asm volatile("v_fma_f32 %0, %2, %3, %4\n\tv_mul_f32 %1, %1, %0" : "=&v"(xn[e]), "+v"(kn[e]) : "v"(xv[e]), "v"(rs), "v"(mr));
      else if (e & 1) asm volatile("v_and_b32 %0, 0xffff0000, %2\n\tv_and_b32 %1, 0xffff0000, %3" : "=&v"(xv[e]), "=&v"(kn[e]) : "v"(a), "v"(b));
      else asm volatile("v_lshlrev_b32 %0, 16, %2\n\tv_lshlrev_b32 %1, 16, %3" : "=&v"(xv[e]), "=&v"(kn[e]) : "v"(a), "v"(b));
    };
    auto step = [&](auto ksc) {
      constexpr int ks = ksc;
#pragma unroll
      for (int n = 0; n < 4; ++n) {
        mmaf<ks == 0>(o[n], of[ks % OP][n], uf[ks]);
        SB();
        if (ks + OP < 28) rd(ks + OP, n);
        if (XAT == 4 && ks == 16) xe[n] = *(LAS v2u *)(L + (xr ^ (n << 5)));
        if (ks == 17) kd[n] = trd(L, yo + (L_KD - L_Z2) + n * 2048);
        if (ks == 17 && n == 3) { mr = *(LAS float *)(L + xo); rs = *(LAS float *)(L + xo + 256); }
        if (ks == 19 && n == 2) { mr = -mr * rs; PIN(mr); }
        if (ks >= 19 && ks < 27 && n < 2) prep(4 * (ks - 19) + 2 * n);
        if (ks >= 20 && n >= 2) prep(4 * (ks - 20) + 2 * n - 3);
        SB();
      }
    };
    lds_check(L, L_CNT + 44, 4, seen);
    STAMP(3);
#pragma unroll
    for (int q = 0; q < 4 * K1; ++q) rd(q >> 2, q & 3);
    SB();
    STAMP(4);
    if (!HU) {
      VM(0);
      last = lds_last(L, L_CNT + 20);
      if (last) setflag(p.ws, F_U + i * 8 + j, tag);
    }
    SB();
    STAMP(1);
    lds_wait(L, L_CNT + 36, 4);
    {
      const int ur = uw - w * 3072;
#pragma unroll
      for (int ks = 0; ks < 4; ++ks) uf[ks] = lds16((LAS unsigned char *)(u32)ur, ks * 4096);
    }
    seen = lds_peek(L, L_CNT + 40);
    SB();
    FRESH();
    uvo = l * 16;
    yo = L_Z2 + (ob[0] ^ (w << 5));
    xo = L_XST + (16 * w + c) * 4;
    oo = (64 * i + 16 * w + c) * 896 + j * 128 + (g & 1) * 32 + (g >> 1) * 16;
    xr = L_Z2 + 8192 + (16 * w + c) * 128 + (((g >> 1) ^ (c >> 1)) << 4) + (g & 1) * 8;
    int ufo = fl(F_U + i * 8 + (l < 7 ? l : 0));
    PIN(uvo); PIN(yo); PIN(xo); PIN(oo); PIN(xr); PIN(ufo);
    lds_check(L, L_CNT + 40, 4, seen);
#pragma unroll
    for (int q = 4 * K1; q < 4 * OP; ++q) rd(q >> 2, q & 3);
    SB();
    unr<UPOS>(step);
    u64 fv = 0;
    if (last) fv = ldflag(p.ws, ufo);
    SB();
    unr<4 - UPOS>([&](auto q) { step(std::integral_constant<int, UPOS + q>{}); });
    STAMP(2);
    if (last) {
      if (!__all(fv == tag)) wait_flags(p.ws, ufo, tag);
      lds_signal(L, L_CNT + 32);
    } else lds_wait(L, L_CNT + 32, 1);
    VM(0);
    STAMP(12);
#pragma unroll
    for (int ks = 4; ks < 4 + UW; ++ks) { ldu(ks); SB(); }
    BAR0();
    unr<24>([&](auto qc) {
      constexpr int ks = qc + 4;
      WAITC(ks < 28 - UW ? UW - 1 : 27 - ks, ks == 19 ? 8 : ks == 26 ? 0 : 15);
      SB();
      step(std::integral_constant<int, ks>{});
      if (ks + UW < 28) ldu(ks + UW);
      SB();
    });
    STAMP(13);
    SETTLE();
    const bool bad = __builtin_amdgcn_ballot_w64(__builtin_isunordered(o[3].x, o[3].x)) != 0;
    if ((HZ || HU) && __builtin_expect(bad, 0)) {
      wait_flags(p.ws, ufo + (F_UD - F_U) * FS, tag);
      __builtin_amdgcn_fence(__ATOMIC_ACQUIRE, "agent");
#pragma unroll
      for (int n = 0; n < 4; ++n) o[n] = f4{1000.f, 1000.f, 1000.f, 1000.f};
#pragma unroll 1
      for (int ks = 0; ks < 28; ++ks) {
        const v4u u = ld16c(rws, uvo + (ks & 3) * 1024, WS_UB + (i * 4 + w) * 28672 + mod7((ks >> 2) + j) * 4096);
#pragma unroll
        for (int n = 0; n < 4; ++n) o[n] = mfma(trd2((LAS unsigned char *)(u32)(ob[n] - 57344 + ks * 4096), 0, 2048), u, o[n]);
      }
    }
    v2u y[4], res[4];
    f2 a[8], e[8];
#pragma unroll
    for (int q = 0; q < 8; ++q) { a[q] = (q & 1) ? hi2(o[q >> 1]) : lo2(o[q >> 1]); e[q] = a[q] * -1.442695041f; }
#pragma unroll
    for (int q = 0; q < 8; ++q) e[q] = f2{__builtin_amdgcn_exp2f(e[q].x), __builtin_amdgcn_exp2f(e[q].y)} + 1.0f;
    asm volatile("s_barrier" : "+v"(e[0]), "+v"(e[1]), "+v"(e[2]), "+v"(e[3]), "+v"(e[4]), "+v"(e[5]), "+v"(e[6]), "+v"(e[7])::"memory");
    SB();
#pragma unroll
    for (int n = 0; n < 4; ++n) y[n] = trd(L, yo + n * 2048);
    SB();
#pragma unroll
    for (int q = 0; q < 8; ++q) e[q] = a[q] * f2{__builtin_amdgcn_rcpf(e[q].x), __builtin_amdgcn_rcpf(e[q].y)};
    asm volatile("" : "+v"(e[0]), "+v"(e[1]), "+v"(e[2]), "+v"(e[3]), "+v"(e[4]), "+v"(e[5]), "+v"(e[6]), "+v"(e[7]));
    SB();
    asm volatile("" : "+v"(y[0]), "+v"(y[1]), "+v"(y[2]), "+v"(y[3]));
#pragma unroll
    for (int q = 0; q < 8; ++q) {
      const u32 yy = (q & 1) ? y[q >> 1].y : y[q >> 1].x;
      const f2 tt = fma2(f2{lo16(yy), hi16(yy)}, f2{xn[2 * q], xn[2 * q + 1]}, f2{kn[2 * q], kn[2 * q + 1]});
      const u32 r = pk2(fma2(tt, e[q], f2{xv[2 * q], xv[2 * q + 1]}));
      if (q & 1) res[q >> 1].y = r; else res[q >> 1].x = r;
    }
    const rsrc_t ro = mk(p.out, S_ * D_ * 2);
    st16c(widen(res[0], res[1]), ro, oo, 0);
    st16c(widen(res[2], res[3]), ro, oo, 64);
  }
  STAMP_OUT(0);
}

static __device__ __forceinline__ void gemm_move(const Params &p, LAS unsigned char *L, const Tile t, int m) {
  STAMP_DECL;
  STAMP(0);
  const int i = t.i, j = t.j;
  int l = threadIdx.x & 63, g = l >> 4, c = l & 15;
  const srd_t sx_ = mks(p.x, S_ * D_ * 2), swin = mks(p.win, D_ * F_ * 2), swsp = mks(p.wsp, S_ * S_ * 2);
  const int lb = m * 1024, lb2 = m * 2048;
  const int pvo = (l >> 3) * 896 + j * 128 + (((((l & 7) >> 1) ^ g) << 5) | ((l & 1) << 4));
  const int rsw = ((l & 7) ^ (l >> 3)) << 4;
  const int xvo = (64 * i + 32 * (m >> 1) + 4 * (m & 1) + 8 * (l >> 5) + ((l >> 3) & 3)) * 896 + rsw;
  const int wvo = (4 * m + g) * 3584 + j * 256 + ((((c >> 1) ^ (g | ((m & 2) << 1))) << 5) | ((c & 1) << 4));
  int sb[7];
#pragma unroll
  for (int s = 0; s < 7; ++s) sb[s] = mod7(s + t.rot1);
  auto item = [&](auto nc) {
    constexpr int n = nc, s = n / 6, q = n % 6;
    if constexpr (n >= 42) dma<(n - 42) * 4096>(swin, wvo, (sb[(n - 42) >> 2] * 4 + ((n - 42) & 3)) * 57344, lb);
    else if constexpr (q < 2) dman<L_XR + (s & 3) * 8192 + q * 1024, NT_X>(sx_, xvo, sb[s] * 128 + q * 14336, lb2);
    else dma<(4 * s + q - 2) * 4096>(swin, wvo, (sb[s] * 4 + q - 2) * 57344 + 1792, lb);
  };
#define IC(n) std::integral_constant<int, (n)>{}
  const rsrc_t rwsp = mk(p.wsp, S_ * S_ * 2), rws = mk(p.ws, WS_BYTES);
  v4u ar[32];
  const int avo0 = (64 * i + 16 * m + (l >> 3)) * 4096 + (((l & 7) ^ g) << 4), avo1 = avo0 ^ 64;
  auto aso = [&](int n) { return (((n >> 1) + t.rot2) & 31) * 128 + (n & 1) * 32768; };
  auto lda = [&](int n) { const int avo = (n & 1) ? avo1 : avo0; ar[n] = n < 10 ? __builtin_amdgcn_raw_buffer_load_b128(rwsp, avo, aso(32 + n), NT_SP * 2) : ld16(rwsp, avo, aso(32 + n)); SB(); };
  auto items = [&](auto n0, auto cnt) { unr<cnt>([&](auto q) { constexpr int n = ORD.seq[n0 + q]; if constexpr (n < 70) item(IC(n)); else lda(n - 70); }); };
  constexpr const int *NI = ORD.bar;
  items(IC(0), IC(NI[0]));
  u64 tag = epoch_load(p.ws);
  EPOCH_WAIT(tag);
  tag += 1;
  {
    const v4u ones = {0x3f803f80u, 0x3f803f80u, 0x3f803f80u, 0x3f803f80u};
    f4 sx = {0, 0, 0, 0}, gr = {0, 0, 0, 0};
    int xo0 = L_XR + (16 * m + c) * 128 + ((g ^ (c & 7)) << 4), xo1 = xo0 ^ 64;
    PIN(xo0); PIN(xo1);
    unr<7>([&](auto sc) {
      constexpr int s = sc;
      VM(NI[s] - 1 - ORD.last(6 * s, 6 * s + 6) + (TLG && ORD.last(6 * s, 6 * s + 6) < NI[0]));
      v4u xs[2];
      xs[0] = lds16(L, xo0 + (s & 3) * 8192);
      xs[1] = lds16(L, xo1 + (s & 3) * 8192);
      STAMPA(2 * s + 1);
      BAR0();
      STAMPA(2 * s + 2);
      if constexpr (s < 6) items(IC(NI[s]), IC(NI[s + 1] - NI[s]));
      SB();
      if (PMS) __builtin_amdgcn_s_setprio(3);
#pragma unroll
      for (int q = 0; q < 2; ++q) { sx = mfma(xs[q], ones, sx); gr = mfma(xs[q], xs[q], gr); }
      SB();
      if (PMS) __builtin_amdgcn_s_setprio(1);
      SB();
      if constexpr (s == 6) items(IC(NI[6]), IC(LASTA));
    });
    SB();
    const int e = c & 3;
    const float s1 = e & 2 ? (e & 1 ? sx.w : sx.z) : (e & 1 ? sx.y : sx.x);
    const float s2 = e & 2 ? (e & 1 ? gr.w : gr.z) : (e & 1 ? gr.y : gr.x);
    const float mean = fmul(s1, 1.0f / 448.0f);
    const float r = inv_sigma(ffma(-mean, mean, fmul(s2, 1.0f / 448.0f)));
    if ((c >> 2) == g) {
      const int row = 32 * (m >> 1) + 8 * g + 4 * (m & 1) + e;
      *(LAS float *)(L + L_XST + row * 4) = rbf(mean);
      *(LAS float *)(L + L_XST + 256 + row * 4) = r;
    }
    SB();
    items(IC(NI[6] + LASTA), IC(NI[7] - NI[6] - LASTA));
  }
  BARW();
  STAMP(3);
  asm volatile("s_mov_b32 m0, %3\n\ts_nop 0\n\tbuffer_load_dword %0, %1, %2 offen lds" ::"v"(l * 4), "s"(mks(p.bsp, S_ * 2)), "s"(i * 128), "n"(L_BS) : "m0", "memory");
  unr<70 + NS - NI[7]>([&](auto q) {
    constexpr int n = NI[7] + q;
    VM(W1);
    if constexpr (n == ORD.last(42, 58) + 1 + W1) lds_signal(L, L_CNT);
    if constexpr (n == ORD.last(42, 70) + 1 + W1) lds_signal(L, L_CNT + 28);
    items(IC(n), IC(1));
  });
  static_assert(ORD.last(42, 58) + 1 + W1 < 70 + NS && NI[7] <= ORD.last(42, 58) + 1 + W1, "");
  STAMP(4);
  {
    const u32 tagA = (u32)tag, tagB = (u32)(tag >> 32) ^ 0x9e3779b9u;
    const int so = WS_ST + (i * 2 + (m >> 1)) * 8192 + (m & 1) * 256;
    const int vo = g * 512 + c * 16, vo3 = (g & 1) * 512 + c * 16 + 2048;
    v4u sg[4], zt[4];
    auto ldst = [&]() {
      sg[0] = ld16c(rws, vo, so); sg[1] = ld16c(rws, vo + 2048, so); sg[2] = ld16c(rws, vo, so + 4096); sg[3] = ld16c(rws, vo3, so + 4096);
    };
    lds_wait(L, L_CNT + 4, 4);
    __builtin_amdgcn_s_setprio(PM2);
    const int zo = ((m >> 1) * 8 + (l >> 5)) * 1024 + ((m & 1) * 32 + (l & 31)) * 16;
#pragma unroll
    for (int q = 0; q < 4; ++q) zt[q] = lds16(L, L_Z2 + zo + q * 2048);
    SB();
    if (PW < 60) VM(PW);
    ldst();
    SB();
#pragma unroll
    for (int n = 0; n < NB; ++n) lda(NS + n);
    SB();
    if constexpr (ORD.last(42, 70) + 1 + W1 >= 70 + NS) { VM(69 + NS - ORD.last(42, 70) + 4 + NB); lds_signal(L, L_CNT + 28); }
    SB();
    float zu[32];
#pragma unroll
    for (int q = 0; q < 16; ++q) { const u32 z = zt[q >> 2][q & 3]; zu[2 * q] = lo16(z); zu[2 * q + 1] = hi16(z); PIN(zu[2 * q]); PIN(zu[2 * q + 1]); }
    int zso = WS_ZB + (j * 64 + 2 * i) * 8192 + zo;
    PIN(zso);
    STAMP(5);
    auto late = [&]() {
      u32 d = 0;
#pragma unroll
      for (int q = 0; q < 4; ++q) d |= (sg[q].y ^ tagA) | (sg[q].w ^ tagB);
      return __builtin_amdgcn_ballot_w64(d != 0) != 0;
    };
    if (__builtin_expect(late(), 0)) {
#pragma unroll 1
      for (u32 s = 0; s < SPIN_CAP; ++s) {
        asm volatile("" ::: "memory");
        ldst();
        if (!late()) break;
      }
    }
    STAMP(2);
    float s = fadd(fadd(asf(sg[0].x), asf(sg[1].x)), fadd(asf(sg[2].x), g < 2 ? asf(sg[3].x) : 0.f));
    float u = fadd(fadd(asf(sg[0].z), asf(sg[1].z)), fadd(asf(sg[2].z), g < 2 ? asf(sg[3].z) : 0.f));
    {
      v2u a = __builtin_amdgcn_permlane16_swap(asu(s), asu(u), false, false);
      const float h = fadd(asf(a.x), asf(a.y));
      a = __builtin_amdgcn_permlane32_swap(asu(h), asu(h), false, false);
      const float k = fadd(asf(a.x), asf(a.y));
      a = __builtin_amdgcn_permlane16_swap(asu(k), asu(k), false, false);
      s = asf(a.x);
      u = asf(a.y);
    }
    const float mean = fmul(s, 1.0f / 896.0f);
    const float r = inv_sigma(ffma(-mean, mean, fmul(u, 1.0f / 896.0f)));
    if (g < 2) *(LAS float *)(L + L_ZST + g * 256 + (16 * m + c) * 4) = g ? r : -rbf(mean) * r;
    SB();
    const u32 cl = HZ && CLZ == 15 ? lds_peek(L, L_CNT + 52) : 0;
    f4 zb[2], zr[2];
#pragma unroll
    for (int q = 0; q < 2; ++q) {
      zb[q] = ldsf4(L, L_ZST + (16 * m + 8 * (g & 1) + 4 * q) * 4);
      zr[q] = ldsf4(L, L_ZST + 256 + (16 * m + 8 * (g & 1) + 4 * q) * 4);
    }
    SB();
#pragma unroll
    for (int n = 0; n < NB2; ++n) lda(NS + NB + n);
    if (HZ && CLZ == 15) lds_check(L, L_CNT + 52, 4, cl);
#pragma unroll
    for (int q = 0; q < 4; ++q) {
      u32 o[4];
#pragma unroll
      for (int e = 0; e < 4; ++e) {
        o[e] = pk(ffma(zu[8 * q + 2 * e], zr[e >> 1][(2 * e) & 3], zb[e >> 1][(2 * e) & 3]), ffma(zu[8 * q + 2 * e + 1], zr[e >> 1][(2 * e + 1) & 3], zb[e >> 1][(2 * e + 1) & 3]));
      }
      st16c(v4u{o[0], o[1], o[2], o[3]}, rws, zso, q * 2048);
      SB();
    }
    if (HZ) if (lds_last(L, L_CNT + 24)) setflag(p.ws, F_Z + j * 32 + i, tag);
    STAMP(6);
  }
  __builtin_amdgcn_s_setprio(PM3);
  constexpr int NA = NS + NB + NB2, NQ = 64 - NA;
  auto zflag = [&]() {
    if (!HZ) if (lds_last(L, L_CNT + 24)) setflag(p.ws, F_Z + j * 32 + i, tag);
    if (HZ) if (lds_last(L, L_CNT + 48)) setflag(p.ws, F_ZD + j * 32 + i, tag);
    SB(); STAMP(7);
  };
  auto ring = [&](auto nc) { constexpr int n = nc; dman<(n >> 1) * 8192 + (n & 1) * 1024, NT_RING>(swsp, (n & 1) ? avo1 : avo0, aso(n), lb2); };
  auto piece = [&](auto qc) {
    constexpr int n = qc - NR1;
    if constexpr (n < 0) lda(NA + qc); else if constexpr (n < 32) ring(IC(n < 0 || n > 31 ? 0 : n)); else lda(NA + qc - 32);
  };
  constexpr int XE[4] = {NR1 + X0 < NQ ? NR1 + X0 : NQ, NR1 + X1 < NQ ? NR1 + X1 : NQ, NR1 + X2 < NQ ? NR1 + X2 : NQ, NQ};
  static_assert(X0 >= 14 + 2 * AD && X1 >= 22 + 2 * AD && X2 >= 32 && NA + NR1 >= 19 && NA + NR1 <= 32 && W2 < NR1 + 5 + 2 * AD + RD, "");
  unr<XE[0]>([&](auto qc) {
    constexpr int n = qc - NR1;
    if constexpr (qc == W2) { VM(W2); zflag(); }
    if constexpr (qc > W2 && n < 14 + 2 * AD + RD) VM(RD - 1);
    if constexpr (n == 0) { lds_wait(L, L_CNT + 8, 4); STAMP(8); }
    if constexpr (n == 5 + 2 * AD + RD) lds_signal(L, L_CNT + 12);
    if constexpr (n == 28) lds_wait(L, L_CNT + 24, 4);
    if constexpr (n == ZP) if (m == 0) { wait_flags(p.ws, fl(F_Z + j * 32 + (l & 31)), tag); lds_signal(L, L_CNT + 16); }
    piece(qc);
  });
  STAMP(11);
  __builtin_amdgcn_s_setprio(0);
  const srd_t swout = mks(p.wout, 896 * D_ * 2);
  auto ldt = [&](auto nc) { constexpr int n = nc; dma<n * 4096>(swout, pvo, WSO(n), lb); SB(); __builtin_amdgcn_s_sleep(TS); SB(); };
  static_assert(T5 <= 8 && T6 <= 16 && T5 <= T6 && T6 <= NO0, "");
  v4u wr[28];
  const rsrc_t rwo = mk(p.wout, 896 * D_ * 2);
  auto trk = [&](auto cc) {
    constexpr int tr[7] = TRK, ck = cc;
    int n = NO1;
    for (int q = 0; q < ck; ++q) n += tr[q];
    if (RE == 3)
#pragma unroll
      for (int q = 0; q < tr[ck]; ++q) { if (q) __builtin_amdgcn_s_sleep(TRS); SB(); wr[n + q] = ld16(rwo, pvo, WSO(n + q)); SB(); }
  };
  int awo = m * 2048 + l * 16, awh = awo + 65536;
  PIN(awo); PIN(awh);
  {
    constexpr int cum[6] = {0, 6, 12, 19, 25, 32};
    unr<5>([&](auto kc) {
      constexpr int k = kc;
      if constexpr (k < 2) VM(XE[k] - NR1 - 14 - 2 * AD - 8 * k);
      if constexpr (k == 2) VM(XE[2] - NR1 - 32);
      BARW();
      if constexpr (TD && k + TD < 8) { ztouch(p, t, k + TD, m, l); SB(); }
      if constexpr (HU && k == CLU) {
        const int o = WS_UB + (i * 112 + 4 * j) * 1024 + m * 28672 + l * 16;
#pragma unroll
        for (int q = 0; q < 4; ++q) st16c(v4u{~0u, ~0u, ~0u, ~0u}, rws, o, q * 1024);
        SB();
      }
      constexpr int nq = k < 3 ? XE[k + 1] - XE[k] : 0, nw = cum[k + 1] - cum[k];
      unr<(nq > nw ? nq : nw)>([&](auto qc) {
        constexpr int n = cum[k] + qc;
        if constexpr (qc < nq) piece(IC(XE[k < 3 ? k : 0] + qc));
        if constexpr (qc < nw) sts16(L, ((n & 16) ? awh : awo) + ((n >> 1) & 7) * 8192 + (n & 1) * 1024, ar[n]);
        SB();
        __builtin_amdgcn_s_sleep(WSL);
        SB();
      });
      trk(IC(k + 1));
    });
  }
  unr<T5>(ldt);
  BARW();
  unr<T6 - T5>([&](auto q) { ldt(IC(T5 + q)); });
  VM(T6);
  trk(IC(6));
  BAR0();
  STAMP(9);
  {
    auto ldo = [&](auto nc) { constexpr int n = nc; VM(WOW); dma<n * 4096>(swout, pvo, WSO(n), lb); };
    const srd_t sws = mks(p.ws, WS_BYTES);
    const int yfo = fl(F_Y + ((64 * j + 16 * m + min(g * 7, 15)) / 14) * 4 + (l & 3));
    const int yvo = (64 * j + 16 * m + (l >> 3)) * 4096 + i * 128 + (((((l & 7) >> 1) ^ g) << 5) | ((l & 1) << 4));
    auto ldy = [&]() { dmac<L_Z2>(sws, yvo, WS_YB, lb2); dmac<L_Z2 + 1024>(sws, yvo, WS_YB + 32768, lb2); };
    __builtin_amdgcn_s_sleep(WSLP);
    static_assert(NO1 >= 4 + OP && NO1 <= 24, "");
    unr<NO0 - T6>([&](auto q) { ldo(IC(T6 + q)); });
    VM(NO0 - K1);
    lds_signal(L, L_CNT + 44);
    STAMP(1);
    const int xto = (64 * i + 16 * m + (l >> 3)) * 896 + j * 128 + (((l & 7) ^ g) << 4);
    lds_wait(L, L_CNT + 36, 4);
    __builtin_amdgcn_s_setprio(PMT);
    if (XAT == 4) { dma<L_Z2 + 8192>(sx_, xto, 0, lb2); dma<L_Z2 + 9216>(sx_, xto ^ 64, 7168, lb2); }
    unr<NO1 - NO0>([&](auto q) { ldo(std::integral_constant<int, NO0 + q>{}); });
    const rsrc_t rwout = mk(p.wout, 896 * D_ * 2);
#pragma unroll
    for (int n = NO1; n < 28; ++n) {
      if (n == NO1 + SIGE) { VM(SIGE + (XAT == 4 && NO0 == NO1 ? 2 : 0)); lds_signal(L, L_CNT + 40); STAMP(10); }
      ar[n] = ld16(rwout, pvo, WSO(n));
      SB();
    }
    STAMP(13);
    int wwo = m * 1024 + l * 16 + 49152;
    PIN(wwo);
#pragma unroll
    for (int n = NO1; n < 28; ++n) { sts16(L, wwo + (n - 12) * 4096, ar[n]); SB(); }
    STAMP(12);
    const bool early = !YLATE && __all(ldflag(p.ws, yfo) == tag);
    if (early) ldy();
    if (YLATE && !RREG) DRAIN();
    BARW();
    if ((HZ || HU) && m == 0) setflag(p.ws, F_UD + i * 8 + j, tag);
    if (!early) { wait_flags(p.ws, yfo, tag); ldy(); }
  }
  DRAIN();
  BAR0();
  STAMP_OUT(1);
}

constexpr int C_CP = 4160, C_KV = 4 * C_CP, C_SLOT = C_KV + 4608;

constexpr int C_EX = 131072;

template <int H> static __device__ __forceinline__ void conv_compute(const Params &p, LAS unsigned char *L, int v, int w) {
  STAMP_DECL;
  STAMP(0);
  __builtin_amdgcn_s_setprio(PCV);
  u64 tag = epoch_load(p.ws);
  constexpr int ord[8] = {H ? 0 : 2, H ? 1 : 3, H ? 6 : 4, H ? 7 : 5, H ? 2 : 0, H ? 3 : 1, H ? 4 : 6, H ? 5 : 7};
  const int l = threadIdx.x & 63, g = l >> 4, c = l & 15, ch = w >> 1;
  const rsrc_t rws = mk(p.ws, WS_BYTES);
  {
    const int t = threadIdx.x;
    const srd_t sws = mks(p.ws, WS_BYTES);
    touch(sws, t < 128 ? WS_ST + v * 16384 + t * 128 : WS_FL + (v * 52 + min(t & 63, 51)) * 128, 0);
  }
  const int hro = ch * C_SLOT + (c & 3) * C_CP + g * 16 + (c >> 2) * 8 + H * 64;
  const int kro = ch * C_SLOT + C_KV + (254 - 2 * c + g) * 16 - 7 * 512 + H * 64;
  v2u hh[2][16];
  v4u kf[2][8];
  auto rd = [&](int ho, int ko, int q, int n) {
    const int d = n / 3;
    if (n % 3 == 2) kf[q & 1][7 - d] = lds16(L, ko + d * 512 + q * 128);
    else hh[q & 1][2 * d + n % 3] = *(LAS v2u *)(L + ho + d * 512 + q * 128 + (n % 3) * 8);
  };
  f4 acc[8], in[4];
#pragma unroll
  for (int n = 0; n < 8; ++n) acc[n] = f4{0, 0, 0, 0};
  int xw = C_EX + w * 4096 + l * 16, xr = xw ^ 4096;
  int yo = WS_YB + (14 * v + ch) * 4096 + c * 32 + (g & 1) * 512 + (g >> 1) * 16 + 0x40000000;
  PIN(xw); PIN(xr);
  v2u y[4];
  auto fin = [&](int k) {
    if (k < 2) { in[2 * k] = ldsf4(L, xr + k * 2048); in[2 * k + 1] = ldsf4(L, xr + k * 2048 + 1024); }
    else if (k < 10) { const int n = (k - 2) >> 1, e = (k & 1) * 2; in[n][e] = fadd(in[n][e], acc[ord[4 + n]][e]); in[n][e + 1] = fadd(in[n][e + 1], acc[ord[4 + n]][e + 1]); }
    else if (k < 14) { const int n = k - 10; y[n] = v2u{pk(in[n].x, in[n].y), pk(in[n].z, in[n].w)}; PIN(y[n]); }
    else if (k < 16) st16c(widen(y[2 * k - 28], y[2 * k - 27]), rws, yo, ord[2 * k - 24] * 512);
  };
  EPOCH_WAIT(tag);
  tag += 1;
  if (KV1) BAR0();
  BAR0();
  STAMP(2);
#pragma unroll
  for (int n = 0; n < 24; ++n) { rd(hro, kro, 0, n); SB(); }
  WAITC(63, 0);
  int set = 0;
#pragma unroll 1
  for (int rho = 0; rho < 7; ++rho) {
    const int nset = set == 4 * C_SLOT ? 0 : set + 2 * C_SLOT;
    int ho = hro + set, ko = kro + set;
    unr<4>([&](auto qc) {
      constexpr int q = qc;
      if (q == 3) { ho = hro + nset - 512; ko = kro + nset - 512; PIN(ho); PIN(ko); }
      if (q > 0) WAITC(63, 0);
      if (q == 3) BAR0();
      SB();
      int k = 0;
#pragma unroll
      for (int hf = 0; hf < 2; ++hf)
#pragma unroll
        for (int P = 0; P < 8; ++P)
#pragma unroll
          for (int n = 0; n < 4; ++n) {
            const int tl = ord[4 * hf + n];
            if (P > tl) continue;
            const v2u a = hh[q & 1][2 * P], b = hh[q & 1][2 * P + 1];
            if (q == 0 && P == 0) mma0(acc[tl], v4u{a.x, a.y, b.x, b.y}, kf[q & 1][tl - P]);
            else mma(acc[tl], v4u{a.x, a.y, b.x, b.y}, kf[q & 1][tl - P]);
            SB();
            if (q == 0) {
              if (k < 2) fin(k);
              else if (k < 9) { rd(ho, ko, 1, 2 * k - 4); SB(); rd(ho, ko, 1, 2 * k - 3); }
              else if (k < 17) fin(k - 7);
              else if (k < 22) { rd(ho, ko, 1, 2 * k - 20); SB(); rd(ho, ko, 1, 2 * k - 19); }
              else if (k < 26) fin(k - 12);
            } else if (k < 24) rd(ho, ko, q + 1, k);
            else if (q == 2 && (k == 27 || k == 30)) fin(k / 3 + 5);
            else if (q == 3 && k < 28) sts16(L, xw + (k - 24) * 1024, __builtin_bit_cast(v4u, acc[ord[k - 24]]));
            SB();
            ++k;
          }
    });
    yo += rho ? 8192 : -0x40000000;
    set = nset;
    WAITC(63, 0);
    BAR0();
    if (rho == 0) STAMP(3);
    if (rho == 1) STAMP(4);
    if (rho == 3) STAMP(5);
    if (rho == 5) STAMP(6);
  }
  SETTLE();
#pragma unroll
  for (int k = 0; k < 16; ++k) fin(k);
  STAMP(7);
  DRAIN();
  setflag(p.ws, F_Y + v * 4 + w, tag);
  STAMP_OUT(0);
}

static __device__ __forceinline__ void conv_stage(const Params &p, LAS unsigned char *L, int v, int m) {
  STAMP_DECL;
  STAMP(0);
  const int l = threadIdx.x & 63;
  const srd_t sh = mks(p.h, D_ * S_ * 2), sws = mks(p.ws, WS_BYTES), swout = mks(p.wout, 896 * D_ * 2);
  const rsrc_t rws = mk(p.ws, WS_BYTES);
  const int vo = m * 1024 + l * 16;
  auto stage_h = [&](int rho, int set) {
    const int so = (14 * v + 2 * rho) * 4096, lb = set + m * 1024;
    unr<8>([&](auto qc) { constexpr int q = qc; dman<(q >> 2) * C_SLOT + (q & 3) * C_CP + 32, NT_H>(sh, vo, so + (q >> 2) * 4096 + (q & 3) * 2 + 2, lb); });
  };
  auto stage_kv = [&](int rho, int set) {
    const int so = WS_KVR + (14 * v + 2 * rho) * 4096, lb = set + m * 1024;
    dmac<C_KV>(sws, vo, so, lb);
    dmac<C_SLOT + C_KV>(sws, vo, so + 4096, lb);
  };
  u64 head;
  asm volatile("global_load_dwordx2 %0, %1, %2" : "=&v"(head) : "v"((14 * v + (l < 56 ? l >> 2 : 0)) * 4096), "s"(p.h) : "memory");
  stage_h(0, 0);
  u64 tag = epoch_load(p.ws);
  stage_h(1, 2 * C_SLOT);
  asm volatile("s_waitcnt vmcnt(16)" : "+v"(head)::"memory");
  head <<= 48 - 16 * (l & 3);
  auto put_head = [&](int rho, int set) {
    if (m == 0 && (l >> 3) == rho) *(LAS u64 *)(L + set + ((l >> 2) & 1) * C_SLOT + (l & 3) * C_CP + 24) = head;
  };
  {
    const v4u z = {0, 0, 0, 0};
    const int t = m * 64 + l;
    if (t < 192) sts16(L, (t >> 5) * C_SLOT + C_KV + 4096 + (t & 31) * 16, z);
    if (t < 24) { sts16(L, (t >> 2) * C_SLOT + (t & 3) * C_CP, z); *(LAS u64 *)(L + (t >> 2) * C_SLOT + (t & 3) * C_CP + 16) = 0; }
  }
  put_head(0, 0);
  put_head(1, 2 * C_SLOT);
  put_head(2, 4 * C_SLOT);
  EPOCH_WAIT(tag);
  tag += 1;
  if (m == 0) {
    __builtin_amdgcn_s_sleep(KSLEEP);
    wait_flags(p.ws, flk(F_KV + ((14 * v + (l < 32 ? 0 : 13)) >> 4) * 32 + (l & 31)), tag);
  }
  BAR0();
  STAMP(1);
  stage_kv(0, 0);
  stage_kv(1, 2 * C_SLOT);
  VM(2);
  BARW();
  stage_h(2, 4 * C_SLOT);
  stage_kv(2, 4 * C_SLOT);
  STAMP(2);
  setflagk(p.ws, F_HI + v * 4 + m, tag);
  int set = 0;
#pragma unroll 1
  for (int rho = 0; rho < 7; ++rho) {
    if (rho == 2) STAMP(3);
    if (rho == 3) STAMP(8);
    if (rho == 0) VM(11); else if (rho >= 5) DRAIN(); else if (rho > WOR && rho <= WOR + 4 / WOK) VM(10 + WTN * WOK); else VM(10);
    if (rho == 2) STAMP(4);
    if (rho == 3) STAMP(9);
    BARW();
    if (rho == 2) STAMP(5);
    if (rho == 3) STAMP(10);
    BAR0();
    if (rho == 2) STAMP(6);
    if (rho == 3) STAMP(11);
    if (rho < 4) {
      stage_h(rho + 3, set);
      if (rho == 2) STAMP(7);
      if (rho == 3) STAMP(12);
      stage_kv(rho + 3, set);
      put_head(rho + 3, set);
    }
    if (rho >= WOR && rho < WOR + 4 / WOK) {
#pragma unroll
      for (int q = 0; q < WOK; ++q) {
        const int n = ((v & 3) * 4 + (rho - WOR) * WOK + q) * 224 + m * 56 + min(l, 55);
        touch(swout, (n >> 2) * 896 + ((n & 3) + 3 * ((v >> 2) & 1)) * 128, 0);
      }
    }
    set = set == 4 * C_SLOT ? 0 : set + 2 * C_SLOT;
    if (rho == 4 && v == 0 && m == 0) {
#pragma unroll 1
      for (u32 s = 0; s < SPIN_CAP; ++s) {
        u64 f[16], d = 0;
#pragma unroll
        for (int q = 0; q < 16; ++q) f[q] = ldflag(p.ws, flk(q * 64 + l));
#pragma unroll
        for (int q = 0; q < 16; ++q) d |= f[q] ^ tag;
        if (__all(d == 0)) break;
        asm volatile("" ::: "memory");
      }
      if (l == 0) stflag(p.ws, WS_EPOCH, tag);
    }
  }
  STAMP_OUT(1);
}

#define CODE_LINES 414
#define CPFW 4
__global__ void __launch_bounds__(512, 2) fused_kernel(Params p) {
  extern __shared__ __attribute__((aligned(16))) unsigned char smem[];
  LAS unsigned char *L = (LAS unsigned char *)smem;
  const int b = blockIdx.x, x = b & 7, r = b >> 3, wv = __builtin_amdgcn_readfirstlane(threadIdx.x >> 6);
  {
    u64 pc;
    const u32 line = min((u32)(r * 16 + (threadIdx.x & 15)), (u32)CODE_LINES);
    asm volatile("s_cmp_lg_u32 %2, %4\n\ts_cbranch_scc1 1f\n\ts_getpc_b64 %0\n\ts_mov_b32 m0, %3\n\ts_nop 0\n\tglobal_load_lds_dword %1, %0\n1:\n\t"
                 "s_setprio 3\n\ts_cmp_lt_u32 %2, 4\n\ts_cbranch_scc1 2f\n\ts_setprio 1\n2:"
                 : "=&s"(pc) : "v"(line * 128), "s"(wv), "n"(LDS_BYTES - 256), "n"(CPFW) : "m0", "scc", "memory");
  }
  if (r >= 28) {
    const int v = x * 4 + (r - 28);
    if (wv < 4) { if (wv & 1) conv_compute<1>(p, L, v, wv); else conv_compute<0>(p, L, v, wv); }
    else conv_stage(p, L, v, wv - 4);
  } else {
    Tile t;
    const int pg = x >> 1, o = x & 1;
    if (r < 24) { t.i = 8 * pg + r / 3; t.j = 4 * o + r % 3; }
    else { t.i = 8 * pg + 4 * o + (r - 24); t.j = 3; }
    t.rot1 = 2 * ((t.i + t.j) & 3);
    t.rot2 = (8 * pg + 8 + (32 * (t.j == 3 ? 6 : 2 * (t.j & 3) + o)) / 7 + DELTA * (t.i & 7)) & 31;
    t.e = r < 24 ? t.i & 7 : 2 * (t.i & 3);
    t.ne = r < 24 ? 1 : 2;
    v2u shk = {0, 0};
    u32 dvr = 0;
    if (wv < 4) kv_fetch(p, t, wv, shk, dvr);
    SB();
    asm volatile("" ::: "memory");
    if (wv < 4) gemm_compute(p, L, t, wv, shk, dvr);
    else gemm_move(p, L, t, wv - 4);
  }
}

extern "C" void kernel_launch(void *const *d_in, const int *in_sizes, int n_in, void *d_out, int out_size, void *d_ws, size_t ws_size,
                              hipStream_t stream) {
  Params p;
  p.x = (const u16 *)d_in[0];
  p.k = (const u16 *)d_in[1];
  p.v = (const u16 *)d_in[2];
  p.shk = (const u16 *)d_in[3];
  p.h = (const u16 *)d_in[4];
  p.dv = (const u16 *)d_in[5];
  p.win = (const u16 *)d_in[7];
  p.wsp = (const u16 *)d_in[8];
  p.bsp = (const u16 *)d_in[9];
  p.wout = (const u16 *)d_in[10];
  p.out = (u16 *)d_out;
  p.ws = (unsigned char *)d_ws;
  (void)hipFuncSetAttribute((const void *)fused_kernel, hipFuncAttributeMaxDynamicSharedMemorySize, LDS_BYTES);
  fused_kernel<<<256, 512, LDS_BYTES, stream>>>(p);
}
```

```cpp
#define TSLEEP 56
#define TSP 2
#define RD 5
#define X0 26
#define ZSL 16
#define ZSL2 2
#define HZ 0
#define SPEC 0
#define SS 1
#define NO1 14
#define CODE_LINES 322
#define PAIRX
#include <hip/hip_runtime.h>
#include <stdint.h>
#include <type_traits>

typedef unsigned short u16;
typedef unsigned int u32;
typedef unsigned long long u64;
typedef u32 v4u __attribute__((ext_vector_type(4)));
typedef u32 v2u __attribute__((ext_vector_type(2)));
typedef float f4 __attribute__((ext_vector_type(4)));
typedef float f2 __attribute__((ext_vector_type(2)));
typedef short s4 __attribute__((ext_vector_type(4)));
typedef __bf16 bf8 __attribute__((ext_vector_type(8)));
typedef __bf16 bf2 __attribute__((ext_vector_type(2)));
#define LAS __attribute__((address_space(3)))
#define GAS __attribute__((address_space(1)))
typedef __amdgpu_buffer_rsrc_t rsrc_t;
typedef int srd_t __attribute__((ext_vector_type(4)));

constexpr int S_ = 2048, D_ = 448, F_ = 1792;
constexpr int WS_EPOCH = 0;
#define FS 128
#define FULL 1
constexpr int WS_FL = 4096;
constexpr int F_KV = 0;
constexpr int F_HI = 896;
constexpr int F_Z = 1024;
constexpr int F_U = 1280;
constexpr int F_ZD = 256;
constexpr int F_UD = 512;
constexpr int F_Y = 1536;
constexpr int WS_ST = 262144;
constexpr int WS_ZB = 1 << 20;
constexpr int WS_UB = 5 << 20;
constexpr int WS_KVR = 9 << 20;
constexpr int WS_YB = 11 << 20;
constexpr int WS_STAMP = 14 << 20;
constexpr int WS_BYTES = 16 << 20;
constexpr int LDS_BYTES = 163840;
constexpr u32 SPIN_CAP = 1u << 16;

struct Params {
  const u16 *x, *k, *v, *shk, *h, *dv, *win, *wsp, *bsp, *wout;
  u16 *out;
  unsigned char *ws;
};

#define SB() __builtin_amdgcn_sched_barrier(0)
#define BARW() do { SB(); asm volatile("s_waitcnt lgkmcnt(0)\n\ts_barrier" ::: "memory"); SB(); } while (0)
#define BAR0() do { SB(); asm volatile("s_barrier" ::: "memory"); SB(); } while (0)
#define FRESH() do { asm volatile("" : "+v"(l)); g = l >> 4; c = l & 15; } while (0)
#define DRAIN() asm volatile("s_waitcnt vmcnt(0)" ::: "memory")
#define VM(n) asm volatile("s_waitcnt vmcnt(%0)" ::"n"(n) : "memory")
#define PIN(x) asm volatile("" : "+v"(x))
#define STAMP(n) SB()
#define STAMPA(n) do { } while (0)
#define STAMP_DECL do { } while (0)
#define STAMP_OUT(slot) do { } while (0)

static __device__ __forceinline__ rsrc_t mk(const void *p, int bytes) { return __builtin_amdgcn_make_buffer_rsrc((void *)p, 0, bytes, 0x00020000); }
static __device__ __forceinline__ v4u ld16(rsrc_t r, int vo, int so) { return __builtin_amdgcn_raw_buffer_load_b128(r, vo, so, 0); }
static __device__ __forceinline__ v4u ld16c(rsrc_t r, int vo, int so) { return __builtin_amdgcn_raw_buffer_load_b128(r, vo, so, 16); }
static __device__ __forceinline__ u32 ld4(rsrc_t r, int vo, int so) { return __builtin_amdgcn_raw_buffer_load_b32(r, vo, so, 0); }
static __device__ __forceinline__ void st16c(v4u v, rsrc_t r, int vo, int so) { __builtin_amdgcn_raw_buffer_store_b128(v, r, vo + so, 0, 16); }
static __device__ __forceinline__ srd_t mks(const void *p, int bytes) { return srd_t{(int)(u64)p, (int)((u64)p >> 32) & 0xffff, bytes, 0x00020000}; }
template <int IMM> static __device__ __forceinline__ void dma(srd_t r, int vo, int so, int base) {
  asm volatile("s_add_u32 m0, %0, %4\n\ts_nop 0\n\tbuffer_load_dwordx4 %1, %2, %3 offen lds" ::"s"(base), "v"(vo), "s"(r), "s"(so), "n"(IMM) : "m0", "scc", "memory");
}
template <int IMM, int NT> static __device__ __forceinline__ void dman(srd_t r, int vo, int so, int base) {
  if constexpr (NT) asm volatile("s_add_u32 m0, %0, %4\n\ts_nop 0\n\tbuffer_load_dwordx4 %1, %2, %3 offen nt lds" ::"s"(base), "v"(vo), "s"(r), "s"(so), "n"(IMM) : "m0", "scc", "memory");
  else dma<IMM>(r, vo, so, base);
}
#define NT_KV 0
#define NT_H 0
#define NT_SP 0
#define NT_X 0
#define NT_RING 0
template <int IMM> static __device__ __forceinline__ void dmac(srd_t r, int vo, int so, int base) {
  asm volatile("s_add_u32 m0, %0, %4\n\ts_nop 0\n\tbuffer_load_dwordx4 %1, %2, %3 offen sc1 lds" ::"s"(base), "v"(vo), "s"(r), "s"(so), "n"(IMM) : "m0", "scc", "memory");
}
static __device__ __forceinline__ void touch(srd_t r, int vo, int so) {
  asm volatile("s_mov_b32 m0, %3\n\ts_nop 0\n\tbuffer_load_dword %0, %1, %2 offen lds" ::"v"(vo), "s"(r), "s"(so), "n"(LDS_BYTES - 256) : "m0", "memory");
}
static __device__ __forceinline__ void touchc(srd_t r, int vo, int so) {
  asm volatile("s_mov_b32 m0, %3\n\ts_nop 0\n\tbuffer_load_dword %0, %1, %2 offen sc1 lds" ::"v"(vo), "s"(r), "s"(so), "n"(LDS_BYTES - 256) : "m0", "memory");
}
static __device__ __forceinline__ float one(float r) { asm("" : "+v"(r)); return r; }
static __device__ __forceinline__ float fadd(float a, float b) { return one(a + b); }
static __device__ __forceinline__ float fmul(float a, float b) { return one(a * b); }
static __device__ __forceinline__ float ffma(float a, float b, float c) { return one(__builtin_fmaf(a, b, c)); }
template <int N, typename Fn> static __device__ __forceinline__ void unr(Fn &&f) {
  if constexpr (N > 0) { unr<N - 1>(f); f(std::integral_constant<int, N - 1>{}); }
}
static __device__ __forceinline__ f4 mfma(v4u a, v4u b, f4 c) {
  return __builtin_amdgcn_mfma_f32_16x16x32_bf16(__builtin_bit_cast(bf8, a), __builtin_bit_cast(bf8, b), c, 0, 0, 0);
}
static __device__ __forceinline__ void mma(f4 &c, v4u a, v4u b) { asm volatile("v_mfma_f32_16x16x32_bf16 %0, %1, %2, %0" : "+v"(c) : "v"(a), "v"(b)); }
static __device__ __forceinline__ void mma0(f4 &c, v4u a, v4u b) { asm volatile("v_mfma_f32_16x16x32_bf16 %0, %1, %2, 0" : "=v"(c) : "v"(a), "v"(b)); }
typedef float f16v __attribute__((ext_vector_type(16)));
static __device__ __forceinline__ void mmb(f16v &c, v4u a, v4u b) { asm volatile("v_mfma_f32_32x32x16_bf16 %0, %1, %2, %0" : "+v"(c) : "v"(a), "v"(b)); }
template <bool FIRST> static __device__ __forceinline__ void mmaf(f4 &c, v4u a, v4u b) { if constexpr (FIRST) mma0(c, a, b); else mma(c, a, b); }
#define SETTLE() do { SB(); asm volatile("s_nop 7" ::: "memory"); SB(); } while (0)
#define WAITC(vm, lg) __builtin_amdgcn_s_waitcnt(((vm) & 15) | 0x70 | ((lg) << 8) | (((vm) >> 4) << 14))
static __device__ __forceinline__ float dot2(u32 a, u32 b, float c) { return __builtin_amdgcn_fdot2_f32_bf16(__builtin_bit_cast(bf2, a), __builtin_bit_cast(bf2, b), c, false); }
static __device__ __forceinline__ float asf(u32 u) { return __builtin_bit_cast(float, u); }
static __device__ __forceinline__ u32 asu(float f) { return __builtin_bit_cast(u32, f); }
static __device__ __forceinline__ float lo16(u32 u) { return asf(u << 16); }
static __device__ __forceinline__ float hi16(u32 u) { return asf(u & 0xffff0000u); }
static __device__ __forceinline__ u32 pk(float a, float b) { f2 t = {a, b}; return __builtin_bit_cast(u32, __builtin_convertvector(t, bf2)); }
static __device__ __forceinline__ float rbf(float a) { return lo16(pk(a, a)); }
static __device__ __forceinline__ v4u lds16(LAS unsigned char *L, int off) { return *(LAS v4u *)(L + off); }
static __device__ __forceinline__ f4 ldsf4(LAS unsigned char *L, int off) { return *(LAS f4 *)(L + off); }
static __device__ __forceinline__ void sts16(LAS unsigned char *L, int off, v4u v) { *(LAS v4u *)(L + off) = v; }
static __device__ __forceinline__ v2u trd(LAS unsigned char *L, int off) {
  return __builtin_bit_cast(v2u, __builtin_amdgcn_ds_read_tr16_b64_v4i16((LAS s4 *)(L + off)));
}
static __device__ __forceinline__ v4u trd2(LAS unsigned char *L, int off, int step) {
  const v2u a = trd(L, off), b = trd(L, off + step);
  return v4u{a.x, a.y, b.x, b.y};
}
static __device__ __forceinline__ f2 fma2(f2 a, f2 b, f2 c) { return __builtin_elementwise_fma(a, b, c); }
static __device__ __forceinline__ u32 pk2(f2 t) { return __builtin_bit_cast(u32, __builtin_convertvector(t, bf2)); }
static __device__ __forceinline__ f2 lo2(f4 v) { return __builtin_shufflevector(v, v, 0, 1); }
static __device__ __forceinline__ f2 hi2(f4 v) { return __builtin_shufflevector(v, v, 2, 3); }
static __device__ __forceinline__ void rowsum8(float (&a)[8], float (&b)[8]) {
#define DA(d, s, ctl, bm) "v_add_f32_dpp " d ", " s ", " s " " ctl " row_mask:0xf bank_mask:" bm "\n\t"
  asm volatile("s_nop 1\n\t"
               DA("%0", "%0", "row_mirror", "0x3") DA("%1", "%1", "row_mirror", "0x3") DA("%2", "%2", "row_mirror", "0x3") DA("%3", "%3", "row_mirror", "0x3")
               DA("%4", "%4", "row_mirror", "0x3") DA("%5", "%5", "row_mirror", "0x3") DA("%6", "%6", "row_mirror", "0x3") DA("%7", "%7", "row_mirror", "0x3")
               DA("%0", "%8", "row_mirror", "0xc") DA("%1", "%9", "row_mirror", "0xc") DA("%2", "%10", "row_mirror", "0xc") DA("%3", "%11", "row_mirror", "0xc")
               DA("%4", "%12", "row_mirror", "0xc") DA("%5", "%13", "row_mirror", "0xc") DA("%6", "%14", "row_mirror", "0xc") DA("%7", "%15", "row_mirror", "0xc")
               DA("%0", "%0", "row_half_mirror", "0x5") DA("%1", "%1", "row_half_mirror", "0x5") DA("%4", "%4", "row_half_mirror", "0x5") DA("%5", "%5", "row_half_mirror", "0x5")
               DA("%0", "%2", "row_half_mirror", "0xa") DA("%1", "%3", "row_half_mirror", "0xa") DA("%4", "%6", "row_half_mirror", "0xa") DA("%5", "%7", "row_half_mirror", "0xa")
               DA("%0", "%0", "quad_perm:[1,0,3,2]", "0xf") DA("%1", "%1", "quad_perm:[1,0,3,2]", "0xf") DA("%4", "%4", "quad_perm:[1,0,3,2]", "0xf") DA("%5", "%5", "quad_perm:[1,0,3,2]", "0xf")
               DA("%0", "%0", "quad_perm:[2,3,0,1]", "0xf") DA("%1", "%1", "quad_perm:[2,3,0,1]", "0xf") DA("%4", "%4", "quad_perm:[2,3,0,1]", "0xf") DA("%5", "%5", "quad_perm:[2,3,0,1]", "0xf")
               : "+v"(a[0]), "+v"(a[1]), "+v"(a[2]), "+v"(a[3]), "+v"(b[0]), "+v"(b[1]), "+v"(b[2]), "+v"(b[3])
               : "v"(a[4]), "v"(a[5]), "v"(a[6]), "v"(a[7]), "v"(b[4]), "v"(b[5]), "v"(b[6]), "v"(b[7]));
#undef DA
}
static __device__ __forceinline__ void lds_signal(LAS unsigned char *L, int off) {
  asm volatile("s_waitcnt lgkmcnt(0)" ::: "memory");
  if ((threadIdx.x & 63) == 0) __hip_atomic_fetch_add((LAS u32 *)(L + off), 1u, __ATOMIC_RELAXED, __HIP_MEMORY_SCOPE_WORKGROUP);
}
static __device__ __forceinline__ bool lds_last(LAS unsigned char *L, int off) {
  u32 n = 0;
  if ((threadIdx.x & 63) == 0) n = __hip_atomic_fetch_add((LAS u32 *)(L + off), 1u, __ATOMIC_RELAXED, __HIP_MEMORY_SCOPE_WORKGROUP);
  return __builtin_amdgcn_readfirstlane(n) == 3;
}
static __device__ __forceinline__ void lds_wait(LAS unsigned char *L, int off, u32 n) {
#pragma unroll 1
  for (u32 s = 0; s < (SPIN_CAP << 4); ++s)
  {
    if (__hip_atomic_load((LAS u32 *)(L + off), __ATOMIC_RELAXED, __HIP_MEMORY_SCOPE_WORKGROUP) >= n) break;
    __builtin_amdgcn_s_sleep(1);
  }
  asm volatile("" ::: "memory");
}
static __device__ __forceinline__ u64 ldflag(unsigned char *ws, int off) { return __hip_atomic_load((GAS u64 *)(ws + off), __ATOMIC_RELAXED, __HIP_MEMORY_SCOPE_AGENT); }
static __device__ __forceinline__ void stflag(unsigned char *ws, int off, u64 v) { __hip_atomic_store((GAS u64 *)(ws + off), v, __ATOMIC_RELAXED, __HIP_MEMORY_SCOPE_AGENT); }
static __device__ __forceinline__ int fl(int n) { return WS_FL + n * FS; }
#define FSK 8
static __device__ __forceinline__ int flk(int n) { return WS_FL + n * FSK; }
static __device__ __forceinline__ void setflagk(unsigned char *ws, int n, u64 tag) {
  const int l = threadIdx.x & 63;
  if (FULL && FSK >= 16) { if (l < FSK / 16) st16c(v4u{(u32)tag, (u32)(tag >> 32), (u32)tag, (u32)(tag >> 32)}, mk(ws, WS_BYTES), flk(n) + l * 16, 0); }
  else if (l == 0) stflag(ws, flk(n), tag);
}
static __device__ __forceinline__ void setflag(unsigned char *ws, int n, u64 tag) {
  const int l = threadIdx.x & 63;
  if (FULL) { if (l < FS / 16) st16c(v4u{(u32)tag, (u32)(tag >> 32), (u32)tag, (u32)(tag >> 32)}, mk(ws, WS_BYTES), fl(n) + l * 16, 0); }
  else if (l == 0) stflag(ws, fl(n), tag);
}
static __device__ __forceinline__ void wait_flags(unsigned char *ws, int off, u64 tag) {
#pragma unroll 1
  for (u32 s = 0; s < SPIN_CAP; ++s) {
    const u64 v = ldflag(ws, off);
    if (__all(v == tag)) break;
    __builtin_amdgcn_s_sleep(1);
  }
  asm volatile("" ::: "memory");
}
static __device__ __forceinline__ void hint_flags(const unsigned char *at, u64 tag) {
  typedef u64 v8q __attribute__((ext_vector_type(8)));
#pragma unroll 1
  for (u32 s = 0; s < 4096; ++s) {
    v8q f;
    asm volatile("s_load_dwordx16 %0, %1, 0x0 glc\n\ts_waitcnt lgkmcnt(0)" : "=s"(f) : "s"(at) : "memory");
    u64 d = 0;
#pragma unroll
    for (int q = 0; q < 8; ++q) d |= f[q] ^ tag;
    if (d == 0) break;
    __builtin_amdgcn_s_sleep(1);
  }
}
static __device__ __forceinline__ u32 lds_peek(LAS unsigned char *L, int off) { return __hip_atomic_load((LAS u32 *)(L + off), __ATOMIC_RELAXED, __HIP_MEMORY_SCOPE_WORKGROUP); }
static __device__ __forceinline__ void lds_check(LAS unsigned char *L, int off, u32 n, u32 seen) {
  if (__builtin_expect(__builtin_amdgcn_readfirstlane(seen) < n, 0)) lds_wait(L, off, n);
  asm volatile("" ::: "memory");
}
static __device__ __forceinline__ float inv_sigma(float var) {
  const float sd = rbf(__builtin_amdgcn_sqrtf(rbf(rbf(fmaxf(var, 0.f)) + 1e-5f)));
  return __builtin_amdgcn_rcpf(sd);
}
static __device__ __forceinline__ int mod7(int a) { return a >= 7 ? a - 7 : a; }

constexpr int L_KR = 81920;
constexpr int L_RV = L_KR + 20480;
constexpr int L_XR = 114688;
constexpr int L_Z2 = 114688;
constexpr int L_Z1 = 131072;
constexpr int L_KD = 147456;
constexpr int L_CS = 155648;
constexpr int L_XST = 156672;
constexpr int L_ZST = 157184;
constexpr int L_CNT = 157696;
constexpr int L_BS = 157952;

#define PMS 1
#define PTW 1
#define TZ1 0
#define NISS {18, 5, 5, 5, 5, 4, 0, 0}
#define NIZ {0, 0, 0, 0, 0, 1, 5, 5}
#define NIW {0, 0, 0, 0, 0, 0, 0, 0}
#define MIXZ 30
#define LASTA 2
#define W1 7
#define W2 12
#define KV1 1
#define KSLEEP 16
#define ZP -100
#define WOW 12
#define WSLP 4
#define AD 2
#define RREG 1
#define OP 8
#define UW 12
#define WOR 2
#define WOK 1
#define YLATE 1
#define WSO(n) ((mod7((SS * ((n) >> 2)) % 7 + j) * 16 + ((n) & 3) * 4 + m) * 7168)
#define SIGE 2
#define TLC 0
#define TLG 0
#define UPOS 0
#define NO0 NO1
#define XAT 4
#define PMT 0
#define K1 8
#define T5 0
#define T6 T5
#define TS 2
#define RE 0
#define WTN 1
#define CLA 5
#define TRK {0, 0, 3, 3, 3, 3, 4}
#define TRS 4
#define XE_ 1
#define ROT1 0
#define TD 0
#define TC 1
#define CLZ 13
#define CLU 3
#define HU 1
#define OWNM 1
#define NR1 12
#define X1 32
#define X2 38
constexpr int VLIM = 63;
#define KVF_AT 3
#define SH_AT 6
#define NS 10
#define PRE 0
#define DELTA 0
#define WOT 2
#define CLR_EARLY 1
#define CLR_SLEEP 8
#define PW 63
#define WSL 0
#define PC0 0
#define PC1 2
#define PM2 3
#define PM3 1
#define PCV 2
#define NB 6
#define NB2 4
struct Order {
  int seq[102], bar[8];
  constexpr int last(int a, int b) const { int r = 0; for (int q = 0; q < 102; ++q) if (seq[q] >= a && seq[q] < b) r = q; return r; }
};
constexpr Order make_order() {
  Order o{};
  constexpr int a[8] = NISS, z[8] = NIZ, w[8] = NIW;
  int n = 0, ia = 0, iz = 42, iw = 70;
  for (int s = 0; s < 8; ++s) {
    int ca = a[s], cz = z[s], cw = w[s];
    while (ca + cz + cw > 0) {
      for (int q = 0; q < 2 && ca > 0; ++q) { o.seq[n++] = ia++; --ca; }
      if (cz > 0) { o.seq[n++] = iz++; --cz; } else if (cw > 0) { o.seq[n++] = iw++; --cw; }
    }
    o.bar[s] = n;
  }
  while (n < 70 + NS) {
    for (int q = 0; q < MIXZ && iz < 70; ++q) o.seq[n++] = iz++;
    if (iw < 70 + NS && (iz == 70 || iw < 80)) o.seq[n++] = iw++;
  }
  while (n < 102) o.seq[n++] = 1000;
  return o;
}
constexpr Order ORD = make_order();
constexpr bool order_ok() {
  for (int q = 0; q < ORD.bar[7]; ++q) {
    int s = 0;
    while (q >= ORD.bar[s]) ++s;
    const int n = ORD.seq[q], st = n / 6;
    if (n < 42 && s > st) return false;
    if (n < 42 && n % 6 < 2 && st >= 4 && s < st - 2) return false;
    if (n < 42 && n % 6 >= 2 && st >= 5 && s < 2) return false;
    if (n >= 42 && n < 70 && s < (n - 42) / 4 + 3) return false;
  }
  return ORD.last(0, 42) < ORD.bar[6];
}
static_assert(order_ok(), "");
struct Tile { int i, j, rot1, rot2, e, ne; };

static __device__ __forceinline__ u64 epoch_load(const unsigned char *ws) {
  u64 e;
  asm volatile("s_load_dwordx2 %0, %1, 0x0" : "=s"(e) : "s"(ws) : "memory");
  return e;
}
#define EPOCH_WAIT(e) asm volatile("s_waitcnt lgkmcnt(0)" : "+s"(e)::"memory")
static __device__ __forceinline__ v4u widen(v2u x, v2u y) {
  const v2u a = __builtin_amdgcn_permlane16_swap(x.x, y.x, false, false), b = __builtin_amdgcn_permlane16_swap(x.y, y.y, false, false);
  return v4u{a.x, b.x, a.y, b.y};
}

static __device__ __forceinline__ void ztouch(const Params &p, const Tile t, int ck, int q, int l) {
  const int e = t.e + q;
  if (q < t.ne) touchc(mks(p.ws + WS_ZB + t.j * 524288, 524288), l * 128, ((t.rot2 + 4 * ck + (e >> 1)) & 31) * 16384 + (e & 1) * 8192);
}
static __device__ __forceinline__ void kv_fetch(const Params &p, const Tile t, int w, v2u &shk, u32 &dvr) {
  const int i = t.i, j = t.j, l = threadIdx.x & 63;
  const srd_t sk = mks(p.k, S_ * D_ * 2), sv = mks(p.v, S_ * D_ * 2);
  const int vo = (l >> 3) * 896 + j * 128 + (((l & 7) ^ ((l >> 2) & 4)) << 4), so = (64 * i + 16 * w) * 896, lb = w * 5120;
  dman<L_KR + 1024, NT_KV>(sk, vo, so, lb);
  dman<L_KR + 2048, NT_KV>(sk, vo, so + 7168, lb);
  dman<L_KR, NT_KV>(sk, vo, so > 0 ? so - 7168 : 0x40000000, lb);
  dman<L_KR + 3072, NT_KV>(sv, vo, so, lb);
  dman<L_KR + 4096, NT_KV>(sv, vo, so + 7168, lb);
  asm volatile("global_load_dwordx2 %0, %2, %3\n\tglobal_load_ushort %1, %4, %5" : "=&v"(shk), "=&v"(dvr) : "v"((64 * j + l) * 8), "s"(p.shk), "v"((64 * j + l) * 2), "s"(p.dv) : "memory");
}

static __device__ __forceinline__ void gemm_compute(const Params &p, LAS unsigned char *L, const Tile t, int w, v2u shk, u32 dvr) {
  STAMP_DECL;
  STAMP(0);
  const int i = t.i, j = t.j, rh = w >> 1, ch = w & 1;
  int l = threadIdx.x & 63, g = l >> 4, c = l & 15;
  u64 tag = epoch_load(p.ws);
  const rsrc_t rws = mk(p.ws, WS_BYTES);
  v4u xf[2][14];
  f4 acc[2][4], cs[2];
  int rb[4];
  v2u xe[4];
  auto ldx = [&]() {
    const rsrc_t rx = mk(p.x, S_ * D_ * 2);
#pragma unroll
    for (int q = 0; q < 4; ++q) xe[q] = __builtin_amdgcn_raw_buffer_load_b64(rx, (64 * i + 16 * w + c) * 896 + j * 128 + g * 8 + q * 32, 0, 0);
    SB();
  };
  auto clz = [&]() {
    const int o = WS_ZB + (j * 64 + 2 * i) * 8192 + w * 4096 + l * 16;
#pragma unroll
    for (int q = 0; q < 4; ++q) st16c(v4u{~0u, ~0u, ~0u, ~0u}, rws, o, q * 1024);
    SB();
  };
  if (HZ && CLZ == -1) clz();
  v4u ones = {0x3f803f80u, 0x3f803f80u, 0x3f803f80u, 0x3f803f80u};
  auto start = [&]() {
    const int rdoff = (8 * g + (c >> 2)) * 256 + ((((c >> 2) | ((g & 1) << 2)) ^ (4 * ch + 2 * rh)) << 5) + (c & 3) * 8;
#pragma unroll
    for (int n = 0; n < 4; ++n) { rb[n] = rdoff ^ (n << 5); PIN(rb[n]); }
    PIN(ones);
  };
  int xo0 = L_XR + (32 * rh + c) * 128 + ((g ^ (c & 7)) << 4), xo1 = xo0 ^ 64;
  int lno = L_RV + (16 * w + (l >> 3)) * 144 + (l & 7) * 16, lso = WS_KVR + (64 * j + 16 * w + (l >> 3)) * 4096 + (31 - i) * 128 + (l & 7) * 16;
  {
    __builtin_amdgcn_s_setprio(PC0);
    SB();
    if (threadIdx.x < 16) *(LAS u32 *)(L + L_CNT + threadIdx.x * 4) = 0;
    int ro = L_KR + w * 5120 + (c >> 2) * 128 + ((g ^ ((c >> 2) & 2)) << 5) + (c & 3) * 8;
    int wo = L_RV + l * 144 + (3 - w) * 32, ko = L_KD + l * 128 + ((w ^ ((c >> 1) & 3)) << 5);
    start();
    PIN(xo0); PIN(xo1); PIN(lno); PIN(lso); PIN(ro); PIN(wo); PIN(ko);
    f2 o[8];
#pragma unroll
    for (int q = 0; q < 8; ++q) { o[q] = f2{0, 0}; PIN(o[q]); }
    EPOCH_WAIT(tag);
    tag += 1;
    asm volatile("s_waitcnt vmcnt(%2)" : "+v"(shk), "+v"(dvr) : "n"(HZ && CLZ == -1 ? 4 : 0) : "memory");
    STAMP(1);
    v2u kr[5], vr[4];
#pragma unroll
    for (int q = 0; q < 5; ++q) kr[q] = trd(L, ro + 512 + q * 512);
#pragma unroll
    for (int q = 0; q < 4; ++q) vr[q] = trd(L, ro + 3072 + q * 512);
    SB();
    const u32 ta = __builtin_amdgcn_alignbit(shk.y, shk.y, 16), tb = __builtin_amdgcn_alignbit(shk.x, shk.x, 16);
    const float dd = lo16(dvr);
    u32 ke[10], kq[9];
#pragma unroll
    for (int q = 0; q < 5; ++q) { ke[2 * q] = kr[q].x; ke[2 * q + 1] = kr[q].y; }
#pragma unroll
    for (int q = 0; q < 9; ++q) kq[q] = __builtin_amdgcn_alignbit(ke[q + 1], ke[q], 16);
#pragma unroll
    for (int q = 0; q < 8; ++q) o[q] = f2{dot2(kq[q], ta, o[q].x), dot2(ke[q + 1], ta, o[q].y)};
    SB();
#pragma unroll
    for (int q = 0; q < 8; ++q) o[q] = f2{dot2(kq[q + 1], tb, o[q].x), dot2(ke[q + 2], tb, o[q].y)};
    SB();
#pragma unroll
    for (int q = 0; q < 8; ++q) { const u32 vv = (q & 1) ? vr[q >> 1].y : vr[q >> 1].x; o[q] *= f2{lo16(vv), hi16(vv)}; }
    u32 rv[8];
#pragma unroll
    for (int q = 0; q < 8; ++q) asm("v_cvt_pk_bf16_f32 %0, %1, %2" : "=v"(rv[q]) : "v"(o[7 - q].y), "v"(o[7 - q].x));
    sts16(L, wo, v4u{rv[0], rv[1], rv[2], rv[3]});
    sts16(L, wo + 16, v4u{rv[4], rv[5], rv[6], rv[7]});
    SB();
#pragma unroll
    for (int q = 0; q < 8; ++q) rv[q] = pk2(o[q] * dd);
    sts16(L, ko, v4u{rv[0], rv[1], rv[2], rv[3]});
    sts16(L, ko + 16, v4u{rv[4], rv[5], rv[6], rv[7]});
  }
  STAMP(2);

  v2u wh[2][8];
  auto rd1 = [&](int ks, int q) {
    if (q < 2) xf[q][ks] = lds16(L, ((ks & 1) ? xo1 : xo0) + ((ks >> 1) & 3) * 8192 + q * 2048);
    else {
      const int n = (q - 2) >> 1;
      if (ks == 7 && !(q & 1)) { rb[n] += 57344; PIN(rb[n]); }
      wh[ks & 1][q - 2] = trd(L, rb[n] + (ks % 7) * 8192 + (q & 1) * 1024);
    }
  };
  auto wfr = [&](int ks, int n) { const v2u a = wh[ks & 1][2 * n], b = wh[ks & 1][2 * n + 1]; return v4u{a.x, a.y, b.x, b.y}; };
  auto mm1 = [&](auto ksc, int q) {
    constexpr int ks = ksc;
    if (q < 8) mmaf<ks == 0>(acc[q & 1][q >> 1], xf[q & 1][ks], wfr(ks, q >> 1));
    else mmaf<ks == 0>(cs[q - 8], ones, wfr(ks, q - 8));
  };
  auto step = [&](auto ksc, int q0) {
#pragma unroll
    for (int q = 0; q < 10; ++q) { mm1(ksc, q); SB(); if (q + q0 < 10) rd1(ksc + 1, q + q0); SB(); }
  };
  auto put_cs = [&](int off) {
    const int o = L_CS + off + ((4 * ch + 2 * rh) * 16 + c) * 4;
    *(LAS float *)(L + o) = cs[0].x;
    *(LAS float *)(L + o + 64) = cs[1].x;
  };
  auto lastk = [&](int off) {
    mm1(std::integral_constant<int, 13>{}, 8);
    mm1(std::integral_constant<int, 13>{}, 9);
#pragma unroll
    for (int q = 0; q < 4; ++q) mm1(std::integral_constant<int, 13>{}, q);
    SB();
    asm volatile("s_nop 3" ::: "memory");
    SB();
    put_cs(off);
    SB();
#pragma unroll
    for (int q = 4; q < 8; ++q) mm1(std::integral_constant<int, 13>{}, q);
    SB();
  };
  STAMPA(1);
  __builtin_amdgcn_s_setprio(PC1);
  BARW();
  STAMPA(2);
  {
    v4u ln[2];
    ln[0] = lds16(L, lno);
    ln[1] = lds16(L, lno + 1152);
#pragma unroll
    for (int q = 0; q < 10; ++q) rd1(0, q);
    SB();
    st16c(ln[0], rws, lso, 0);
    st16c(ln[1], rws, lso, 32768);
    SB();
  }
  unr<13>([&](auto ksc) {
    constexpr int ks = ksc;
    if constexpr (ks & 1) {
      STAMPA(ks + 2);
      SB(); asm volatile("s_waitcnt lgkmcnt(8)\n\ts_barrier" ::: "memory"); SB();
      STAMPA(ks + 3);
      if (ks == KVF_AT) {
        DRAIN();
        setflagk(p.ws, F_KV + (4 * j + w) * 32 + i, tag);
        SB();
      }
    }
    if constexpr (HZ && ks == CLZ) clz();
    step(ksc, 0);
  });
  if (HZ && CLZ == 13) clz();
  lastk(0);
  if (HZ && CLZ == 14) clz();
  BARW();
  SETTLE();
  STAMP(6);
  FRESH();
  const u32 tagA = (u32)tag, tagB = (u32)(tag >> 32) ^ 0x9e3779b9u;
  f4 mm[2], rr[2];
  float cv[4];
  auto act_in = [&]() {
#pragma unroll
    for (int m = 0; m < 2; ++m) {
      mm[m] = ldsf4(L, L_XST + (32 * rh + 8 * g + 4 * m) * 4);
      rr[m] = ldsf4(L, L_XST + 256 + (32 * rh + 8 * g + 4 * m) * 4);
    }
    cv[0] = -cs[0].x;
    cv[1] = -cs[1].x;
  };
  auto act_in2 = [&](int off) {
    const int o = L_CS + off + ((4 * ch + (2 - 2 * rh)) * 16 + c) * 4;
    cv[2] = -*(LAS float *)(L + o);
    cv[3] = -*(LAS float *)(L + o + 64);
  };
  auto act = [&](int n, f2 (&z)[4]) {
    f2 x[4], a[4];
    const f2 c2 = {cv[n], cv[n]};
    SB();
#pragma unroll
    for (int m = 0; m < 2; ++m) {
      x[2 * m] = fma2(lo2(mm[m]), c2, lo2(acc[m][n]));
      x[2 * m + 1] = fma2(hi2(mm[m]), c2, hi2(acc[m][n]));
    }
    SB();
#pragma unroll
    for (int m = 0; m < 2; ++m) { x[2 * m] *= lo2(rr[m]); x[2 * m + 1] *= hi2(rr[m]); }
    SB();
#pragma unroll
    for (int q = 0; q < 4; ++q) a[q] = x[q] * x[q];
    SB();
#pragma unroll
    for (int q = 0; q < 4; ++q) a[q] = fma2(a[q], f2{-0.1029432397f, -0.1029432397f}, f2{-2.302208198f, -2.302208198f});
    SB();
#pragma unroll
    for (int q = 0; q < 4; ++q) a[q] *= x[q];
    SB();
#pragma unroll
    for (int q = 0; q < 4; ++q) a[q] = f2{__builtin_amdgcn_exp2f(a[q].x), __builtin_amdgcn_exp2f(a[q].y)};
    SB();
#pragma unroll
    for (int q = 0; q < 4; ++q) a[q] += 1.0f;
    SB();
#pragma unroll
    for (int q = 0; q < 4; ++q) a[q] = f2{__builtin_amdgcn_rcpf(a[q].x), __builtin_amdgcn_rcpf(a[q].y)};
    SB();
#pragma unroll
    for (int q = 0; q < 4; ++q) z[q] = x[q] * a[q];
    SB();
  };
  u32 seen;
  {
    act_in();
    act_in2(0);
    f2 s[4], u[4];
    v4u zp[4];
#pragma unroll
    for (int n = 0; n < 4; ++n) {
      f2 z[4];
      act(n, z);
#pragma unroll
      for (int q = 0; q < 4; ++q) {
        s[q] = n ? s[q] + z[q] : z[q];
        u[q] = n ? fma2(z[q], z[q], u[q]) : z[q] * z[q];
      }
      zp[n] = v4u{pk2(z[0]), pk2(z[1]), pk2(z[2]), pk2(z[3])};
    }
    seen = lds_peek(L, L_CNT);
    float sa[8], sq[8];
#pragma unroll
    for (int q = 0; q < 4; ++q) { sa[2 * q] = s[q].x; sa[2 * q + 1] = s[q].y; sq[2 * q] = u[q].x; sq[2 * q + 1] = u[q].y; }
    rowsum8(sa, sq);
    const int row = 8 * g + (c & 1) + ((c >> 1) & 6);
    if ((c & 2) == 0)
      st16c(v4u{asu((c & 1) ? sa[1] : sa[0]), tagA, asu((c & 1) ? sq[1] : sq[0]), tagB}, rws, ((i * 2 + rh) * 16 + 2 * j + ch) * 512 + row * 16, WS_ST);
    SB();
    const int zo = L_Z2 + (rh * 8 + 4 * ch) * 1024 + l * 16;
#pragma unroll
    for (int n = 0; n < 4; ++n) sts16(L, zo + ((n & 1) << 10) + (((n >> 1) ^ rh) << 11), zp[n]);
    SB();
    if (HZ && CLZ < 15) VM(1);
    if (l == 0) __hip_atomic_fetch_add((LAS u32 *)(L + L_CNT + 4), 1u, __ATOMIC_RELAXED, __HIP_MEMORY_SCOPE_WORKGROUP);
    SB();
    if (HZ && CLZ == 15) clz();
    lds_check(L, L_CNT, 4, seen);
#pragma unroll
    for (int n = 0; n < 4; ++n) { rb[n] -= 57344; PIN(rb[n]); }
#pragma unroll
    for (int q = 2; q < 10; ++q) rd1(0, q);
    SB();
  }
  SB();
  unr<13>([&](auto ksc) {
    if constexpr (HZ && CLZ == 15 && ksc == CLA) {
      VM(0);
      if (l == 0) __hip_atomic_fetch_add((LAS u32 *)(L + L_CNT + 52), 1u, __ATOMIC_RELAXED, __HIP_MEMORY_SCOPE_WORKGROUP);
    }
    if constexpr (ksc == 6) seen = lds_peek(L, L_CNT + 28);
    if constexpr (ksc == 7) lds_check(L, L_CNT + 28, 4, seen);
    SB();
    step(ksc, 2);
  });
  lastk(512);
  lds_signal(L, L_CNT + 8);
  SETTLE();
  FRESH();
  u32 bias[4];
  {
    act_in();
    const int po = L_Z1 + (64 * ch + c) * 128 + (((4 * rh + g) ^ (c & 7)) << 4);
#pragma unroll
    for (int n = 0; n < 4; ++n) {
      f2 z[4];
      if (n == 1) seen = lds_peek(L, L_CNT + 8);
      if (n == 2) {
        lds_check(L, L_CNT + 8, 4, seen);
        act_in2(512);
#pragma unroll
        for (int q = 0; q < 4; ++q) bias[q] = *(LAS u16 *)(L + L_BS + (16 * q + c) * 2);
      }
      if (n == 3) seen = lds_peek(L, L_CNT + 16);
      act(n, z);
      sts16(L, po + ((n & 1) << 11) + (((n >> 1) ^ rh) << 12), v4u{pk2(z[0]), pk2(z[1]), pk2(z[2]), pk2(z[3])});
    }
  }
  STAMP(8);
  FRESH();
  f4 g2[4][2];
  float z1[32];
  bool last;
  int uo[4], ob[4], uw = L_Z1 + w * 4096 + l * 16;
#pragma unroll
  for (int n = 0; n < 4; ++n) { ob[n] = ((4 * g + (c >> 2)) * 128 + ((((c >> 3) & 1) | ((g & 1) << 1)) << 5) + (c & 3) * 8) ^ (n << 5); PIN(ob[n]); }
#pragma unroll
  for (int m = 0; m < 4; ++m) { uo[m] = WS_UB + (i * 112 + 4 * j + w) * 1024 + l * 16 + m * 28672; PIN(uo[m]); }
  {
    v4u bf[8][2], af[4][4];
    const rsrc_t rz = mk(p.ws + WS_ZB + j * 524288, 524288);
    int bvo[2], bso = t.rot2 * 16384;
    bvo[0] = w * 2048 + l * 16;
    bvo[1] = bvo[0] + 8192;
    PIN(bvo[0]); PIN(bvo[1]);
    auto ldb = [&](int ks, int b, int so) { bf[ks & 7][b] = ld16c(rz, bvo[ks & 1] + b * 1024, so); };
#define BMASK 0x7ffff
    auto next = [&](int &so) { asm volatile("s_add_u32 %0, %0, 0x4000\n\ts_and_b32 %0, %0, %1" : "+s"(so) : "n"(BMASK) : "scc"); };
#pragma unroll
    for (int m = 0; m < 4; ++m) { const float b = lo16(bias[m]); g2[m][0] = g2[m][1] = f4{b, b, b, b}; PIN(g2[m][0]); PIN(g2[m][1]); }
    int ab[2];
    ab[0] = c * 128 + ((g ^ (c >> 1)) << 4);
    ab[1] = ab[0] ^ 64;
    PIN(ab[0]); PIN(ab[1]);
    auto rda = [&](int ks, int m) { af[ks & 3][m] = *(LAS v4u *)((LAS unsigned char *)(u32)ab[ks & 1] + ((ks >> 1) & 3) * 8192 + m * 2048); };
    auto turn = [&](int q) { ab[q] = (ab[q] + 32768) & 0x1ffff; PIN(ab[q]); };
    const int f0 = 4 * g + (c >> 2);
    int zr[4];
#pragma unroll
    for (int e = 0; e < 4; ++e) { zr[e] = (L_Z1 + (32 * w + f0) * 128 + (c & 1) * 8 + ((((c >> 1) & 1) ^ (f0 & 7)) << 4)) ^ (e << 5); PIN(zr[e]); }
    v2u zq[8];
    PIN(uw);
    if (ZP < 0 && w == 0) {
      lds_wait(L, L_CNT + 24, 4);
      __builtin_amdgcn_s_sleep(ZSL);
      const int fo = fl(F_Z + j * 32 + (l & 31));
#pragma unroll 1
      for (u32 s = 0; s < SPIN_CAP; ++s) {
        if (__all(ldflag(p.ws, fo) == tag)) break;
        __builtin_amdgcn_s_sleep(ZSL2);
      }
      asm volatile("" ::: "memory");
      lds_signal(L, L_CNT + 16);
    } else lds_check(L, L_CNT + 16, 1, seen);
    STAMP(9);
    if (TD && TC == 0) { ztouch(p, t, 1 + (w & 1), w >> 1, l); if (TD > 3 && !(w & 1)) ztouch(p, t, 3, w >> 1, l); SB(); }
#pragma unroll
    for (int ks = 0; ks < 7; ++ks) { ldb(ks, 0, bso); SB(); ldb(ks, 1, bso); SB(); if (ks & 1) next(bso); SB(); }
    if (TD && TC == 1) { ztouch(p, t, 1 + (w & 1), w >> 1, l); if (TD > 3 && !(w & 1)) ztouch(p, t, 3, w >> 1, l); SB(); }
    lds_wait(L, L_CNT + 12, 4);
    __builtin_amdgcn_s_setprio(3);
    STAMP(10);
#pragma unroll
    for (int ks = 0; ks < AD; ++ks)
#pragma unroll
      for (int m = 0; m < 4; ++m) rda(ks, m);
    SB();
    STAMP(5);
    auto wide = [&](int e) {
      const u32 z = (e & 2) ? zq[e >> 2].y : zq[e >> 2].x;
      if (e & 1) asm volatile("v_and_b32 %0, 0xffff0000, %1" : "=v"(z1[e]) : "v"(z));
      else asm volatile("v_lshlrev_b32 %0, 16, %1" : "=v"(z1[e]) : "v"(z));
    };
    auto chunk = [&](auto lastc) {
      constexpr bool LAST = lastc;
      unr<(LAST ? 6 : 8)>([&](auto sc) {
        constexpr int s = sc;
        WAITC(LAST && s ? 14 - 2 * s : 12, LAST && s == 2 ? 9 : 4);
        SB();
#pragma unroll
        for (int q = 0; q < 8; ++q) {
          mma(g2[q >> 1][q & 1], bf[s][q & 1], af[s & 3][q >> 1]);
          SB();
#define NOA 0
#define NOB 0
          if (!(q & 1) && (!NOA || LAST)) rda(s + 2, q >> 1);
          if ((q == 1 || q == 3) && (!LAST || s == 0) && (!NOB || LAST)) ldb(s + 7, q >> 1, bso);
          if (q == 5 && !(s & 1) && !LAST) next(bso);
          if (!LAST && q == (s == 5 ? 5 : 7) && (s == 5 || s == 6)) turn(~s & 1);
          if (LAST && s == 1 && (q & 1)) { zq[q - 1] = trd((LAS unsigned char *)(u32)zr[q >> 1], 0); zq[q] = trd((LAS unsigned char *)(u32)zr[q >> 1], 2048); }
          if (LAST && s >= 2 && (q & 1)) { wide(8 * (s - 2) + q - 1); wide(8 * (s - 2) + q); }
          SB();
        }
      });
      if (!LAST) BAR0();
    };
#pragma unroll 1
    for (int ck = 0; ck < 7; ++ck) {
      chunk(std::false_type{});
    }
    chunk(std::true_type{});
    u32 o[4][4];
    auto scale = [&](int m, int e) { asm volatile("v_mul_f32 %0, %0, %2\n\tv_mul_f32 %1, %1, %3" : "+v"(z1[8 * m + 2 * e]), "+v"(z1[8 * m + 2 * e + 1]) : "v"(g2[m][e >> 1][(2 * e) & 3]), "v"(g2[m][e >> 1][(2 * e + 1) & 3])); };
    auto pack = [&](int m, int e) {
      asm volatile("v_cvt_pk_bf16_f32 %0, %2, %3\n\tv_cvt_pk_bf16_f32 %1, %4, %5" : "=&v"(o[m][e]), "=&v"(o[m][e + 1])
                   : "v"(z1[8 * m + 2 * e]), "v"(z1[8 * m + 2 * e + 1]), "v"(z1[8 * m + 2 * e + 2]), "v"(z1[8 * m + 2 * e + 3]));
    };
    auto send = [&](int m) { st16c(v4u{o[m][0], o[m][1], o[m][2], o[m][3]}, rws, uo[m], 0); };
    auto keep = [&](int m) { sts16((LAS unsigned char *)(u32)uw, m * 1024, v4u{o[m][0], o[m][1], o[m][2], o[m][3]}); };
    auto two = [&](int s, int m, auto f0, auto f1) {
      mma(g2[m][0], bf[s][0], af[s & 3][m]); SB(); f0(); SB();
      mma(g2[m][1], bf[s][1], af[s & 3][m]); SB(); f1(); SB();
    };
    auto none = []() {};
    WAITC(2, 4);
    SB();
    two(6, 0, none, none);
    two(6, 1, none, [&]() { seen = lds_peek(L, L_CNT + 44); });
    WAITC(0, 1);
    SB();
    two(7, 0, none, none);
    two(6, 2, none, none);
    two(7, 1, none, none);
    two(6, 3, none, [&]() { asm volatile("s_nop 0"); scale(0, 0); scale(0, 1); });
    two(7, 2, [&]() { scale(0, 2); scale(0, 3); }, [&]() { pack(0, 0); pack(0, 2); });
    two(7, 3, [&]() { send(0); scale(1, 0); scale(1, 1); }, [&]() { keep(0); scale(1, 2); scale(1, 3); });
    STAMP(11);
    pack(1, 0); pack(1, 2);
    SB();
    send(1);
    keep(1);
    SB();
#pragma unroll
    for (int e = 0; e < 4; ++e) o[2][e] = pk2(f2{z1[16 + 2 * e], z1[17 + 2 * e]} * ((e & 1) ? hi2(g2[2][e >> 1]) : lo2(g2[2][e >> 1])));
    send(2);
    keep(2);
    SB();
#pragma unroll
    for (int e = 0; e < 4; ++e) o[3][e] = pk2(f2{z1[24 + 2 * e], z1[25 + 2 * e]} * ((e & 1) ? hi2(g2[3][e >> 1]) : lo2(g2[3][e >> 1])));
    send(3);
    keep(3);
    SB();
    u32 n36 = 0;
    if (l == 0) n36 = __hip_atomic_fetch_add((LAS u32 *)(L + L_CNT + 36), 1u, __ATOMIC_RELAXED, __HIP_MEMORY_SCOPE_WORKGROUP);
    SB();
    const f4 t4 = g2[0][0] + g2[0][1];
    const f2 t2 = lo2(t4) + hi2(t4);
    const bool bad = __builtin_amdgcn_ballot_w64(__builtin_isunordered(t2.x, t2.y)) != 0;
    SB();
    last = __builtin_amdgcn_readfirstlane(n36) == 3;
    if (HU && last) setflag(p.ws, F_U + i * 8 + j, tag);
    SB();
    if (HZ && __builtin_expect(bad, 0)) {
      FRESH();
      wait_flags(p.ws, fl(F_ZD + j * 32 + (l & 31)), tag);
      __builtin_amdgcn_fence(__ATOMIC_ACQUIRE, "agent");
      const rsrc_t ra = mk(p.wsp, S_ * S_ * 2);
      f4 h[4][2];
#pragma unroll
      for (int m = 0; m < 4; ++m) { const float b = lo16(*(LAS u16 *)(L + L_BS + (16 * m + c) * 2)); h[m][0] = h[m][1] = f4{b, b, b, b}; }
#pragma unroll 1
      for (int ks = 0; ks < 64; ++ks) {
        const v4u b0 = ld16c(rz, w * 2048 + l * 16, ks * 8192), b1 = ld16c(rz, w * 2048 + l * 16 + 1024, ks * 8192);
#pragma unroll
        for (int m = 0; m < 4; ++m) {
          const v4u a = ld16(ra, (64 * i + 16 * m + c) * 4096 + g * 16, ks * 64);
          h[m][0] = mfma(b0, a, h[m][0]);
          h[m][1] = mfma(b1, a, h[m][1]);
        }
      }
#pragma unroll
      for (int m = 0; m < 4; ++m) {
#pragma unroll
        for (int e = 0; e < 4; ++e) {
          const u32 z = (e & 1) ? zq[2 * m + (e >> 1)].y : zq[2 * m + (e >> 1)].x;
          o[m][e] = pk2(f2{lo16(z), hi16(z)} * ((e & 1) ? hi2(h[m][e >> 1]) : lo2(h[m][e >> 1])));
        }
        send(m);
      }
    }
  }
  SB();
  STAMP(7);
  {
    v4u uf[28], of[OP][4];
    int uso[7];
#pragma unroll
    for (int q = 1; q < 7; ++q) uso[q] = WS_UB + (i * 4 + w) * 28672 + mod7((SS * q) % 7 + j) * 4096;
    int uvo, yo, xo, oo, xr;
    auto ldu = [&](int ks) { uf[ks] = ld16c(rws, uvo + (ks & 3) * 1024, uso[ks >> 2]); };
    f4 o[4];
    auto rd = [&](int ks, int n) {
      if (ks == 14) { ob[n] += 57344; PIN(ob[n]); }
      of[ks % OP][n] = trd2((LAS unsigned char *)(u32)ob[n], (ks % 14) * 4096, 2048);
    };
    float xv[16], xn[16], kn[16], mr, rs;
    v2u kd[4];
    auto prep = [&](int q) {
      const int e = q >> 1;
      const u32 a = (e & 2) ? xe[e >> 2].y : xe[e >> 2].x, b = (e & 2) ? kd[e >> 2].y : kd[e >> 2].x;
      if (q & 1) asm volatile("v_fma_f32 %0, %2, %3, %4\n\tv_mul_f32 %1, %1, %0" : "=&v"(xn[e]), "+v"(kn[e]) : "v"(xv[e]), "v"(rs), "v"(mr));
      else if (e & 1) asm volatile("v_and_b32 %0, 0xffff0000, %2\n\tv_and_b32 %1, 0xffff0000, %3" : "=&v"(xv[e]), "=&v"(kn[e]) : "v"(a), "v"(b));
      else asm volatile("v_lshlrev_b32 %0, 16, %2\n\tv_lshlrev_b32 %1, 16, %3" : "=&v"(xv[e]), "=&v"(kn[e]) : "v"(a), "v"(b));
    };
    auto step = [&](auto ksc) {
      constexpr int ks = ksc;
#pragma unroll
      for (int n = 0; n < 4; ++n) {
        mmaf<ks == 0>(o[n], of[ks % OP][n], uf[ks]);
        SB();
        if (ks + OP < 28) rd(ks + OP, n);
        if (XAT == 4 && ks == 16) xe[n] = *(LAS v2u *)(L + (xr ^ (n << 5)));
        if (ks == 17) kd[n] = trd(L, yo + (L_KD - L_Z2) + n * 2048);
        if (ks == 17 && n == 3) { mr = *(LAS float *)(L + xo); rs = *(LAS float *)(L + xo + 256); }
        if (ks == 19 && n == 2) { mr = -mr * rs; PIN(mr); }
        if (ks >= 19 && ks < 27 && n < 2) prep(4 * (ks - 19) + 2 * n);
        if (ks >= 20 && n >= 2) prep(4 * (ks - 20) + 2 * n - 3);
        SB();
      }
    };
    lds_check(L, L_CNT + 44, 4, seen);
    STAMP(3);
#pragma unroll
    for (int q = 0; q < 4 * K1; ++q) rd(q >> 2, q & 3);
    SB();
    STAMP(4);
    if (!HU) {
      VM(0);
      last = lds_last(L, L_CNT + 20);
      if (last) setflag(p.ws, F_U + i * 8 + j, tag);
    }
    SB();
    STAMP(1);
    lds_wait(L, L_CNT + 36, 4);
    {
      const int ur = uw - w * 3072;
#pragma unroll
      for (int ks = 0; ks < 4; ++ks) uf[ks] = lds16((LAS unsigned char *)(u32)ur, ks * 4096);
    }
    seen = lds_peek(L, L_CNT + 40);
    SB();
    FRESH();
    uvo = l * 16;
    yo = L_Z2 + (ob[0] ^ (w << 5));
    xo = L_XST + (16 * w + c) * 4;
    oo = (64 * i + 16 * w + c) * 896 + j * 128 + (g & 1) * 32 + (g >> 1) * 16;
    xr = L_Z2 + 8192 + (16 * w + c) * 128 + (((g >> 1) ^ (c >> 1)) << 4) + (g & 1) * 8;
    int ufo = fl(F_U + i * 8 + (l < 7 ? l : 0));
    PIN(uvo); PIN(yo); PIN(xo); PIN(oo); PIN(xr); PIN(ufo);
    lds_check(L, L_CNT + 40, 4, seen);
#pragma unroll
    for (int q = 4 * K1; q < 4 * OP; ++q) rd(q >> 2, q & 3);
    SB();
    unr<UPOS>(step);
    u64 fv = 0;
    if (last) fv = ldflag(p.ws, ufo);
    SB();
    unr<4 - UPOS>([&](auto q) { step(std::integral_constant<int, UPOS + q>{}); });
    STAMP(2);
    if (last) {
      if (!__all(fv == tag)) wait_flags(p.ws, ufo, tag);
      lds_signal(L, L_CNT + 32);
    } else lds_wait(L, L_CNT + 32, 1);
    VM(0);
    STAMP(12);
#pragma unroll
    for (int ks = 4; ks < 4 + UW; ++ks) { ldu(ks); SB(); }
    BAR0();
    unr<24>([&](auto qc) {
      constexpr int ks = qc + 4;
      WAITC(ks < 28 - UW ? UW - 1 : 27 - ks, ks == 19 ? (18 + OP < 28 ? 8 : 0) : ks == 26 ? 0 : 15);
      SB();
      step(std::integral_constant<int, ks>{});
      if (ks + UW < 28) ldu(ks + UW);
      SB();
    });
    STAMP(13);
    SETTLE();
    const bool bad = __builtin_amdgcn_ballot_w64(__builtin_isunordered(o[3].x, o[3].x)) != 0;
    if ((HZ || HU) && __builtin_expect(bad, 0)) {
      wait_flags(p.ws, ufo + (F_UD - F_U) * FS, tag);
      __builtin_amdgcn_fence(__ATOMIC_ACQUIRE, "agent");
#pragma unroll
      for (int n = 0; n < 4; ++n) o[n] = f4{0, 0, 0, 0};
#pragma unroll 1
      for (int ks = 0; ks < 28; ++ks) {
        const v4u u = ld16c(rws, uvo + (ks & 3) * 1024, WS_UB + (i * 4 + w) * 28672 + mod7((SS * (ks >> 2)) % 7 + j) * 4096);
#pragma unroll
        for (int n = 0; n < 4; ++n) o[n] = mfma(trd2((LAS unsigned char *)(u32)(ob[n] - 57344 + ks * 4096), 0, 2048), u, o[n]);
      }
    }
    v2u y[4], res[4];
    f2 a[8], e[8];
#pragma unroll
    for (int q = 0; q < 8; ++q) { a[q] = (q & 1) ? hi2(o[q >> 1]) : lo2(o[q >> 1]); e[q] = a[q] * -1.442695041f; }
#pragma unroll
    for (int q = 0; q < 8; ++q) e[q] = f2{__builtin_amdgcn_exp2f(e[q].x), __builtin_amdgcn_exp2f(e[q].y)} + 1.0f;
    asm volatile("s_barrier" : "+v"(e[0]), "+v"(e[1]), "+v"(e[2]), "+v"(e[3]), "+v"(e[4]), "+v"(e[5]), "+v"(e[6]), "+v"(e[7])::"memory");
    SB();
#pragma unroll
    for (int n = 0; n < 4; ++n) y[n] = trd(L, yo + n * 2048);
    SB();
#pragma unroll
    for (int q = 0; q < 8; ++q) e[q] = a[q] * f2{__builtin_amdgcn_rcpf(e[q].x), __builtin_amdgcn_rcpf(e[q].y)};
    asm volatile("" : "+v"(e[0]), "+v"(e[1]), "+v"(e[2]), "+v"(e[3]), "+v"(e[4]), "+v"(e[5]), "+v"(e[6]), "+v"(e[7]));
    SB();
    asm volatile("" : "+v"(y[0]), "+v"(y[1]), "+v"(y[2]), "+v"(y[3]));
#pragma unroll
    for (int q = 0; q < 8; ++q) {
      const u32 yy = (q & 1) ? y[q >> 1].y : y[q >> 1].x;
      const f2 tt = fma2(f2{lo16(yy), hi16(yy)}, f2{xn[2 * q], xn[2 * q + 1]}, f2{kn[2 * q], kn[2 * q + 1]});
      const u32 r = pk2(fma2(tt, e[q], f2{xv[2 * q], xv[2 * q + 1]}));
      if (q & 1) res[q >> 1].y = r; else res[q >> 1].x = r;
    }
    const rsrc_t ro = mk(p.out, S_ * D_ * 2);
    st16c(widen(res[0], res[1]), ro, oo, 0);
    st16c(widen(res[2], res[3]), ro, oo, 64);
  }
  STAMP_OUT(0);
}

static __device__ __forceinline__ void gemm_move(const Params &p, LAS unsigned char *L, const Tile t, int m) {
  STAMP_DECL;
  STAMP(0);
  const int i = t.i, j = t.j;
  int l = threadIdx.x & 63, g = l >> 4, c = l & 15;
  const srd_t sx_ = mks(p.x, S_ * D_ * 2), swin = mks(p.win, D_ * F_ * 2), swsp = mks(p.wsp, S_ * S_ * 2);
  const int lb = m * 1024, lb2 = m * 2048;
  const int pvo = (l >> 3) * 896 + j * 128 + (((((l & 7) >> 1) ^ g) << 5) | ((l & 1) << 4));
  const int rsw = ((l & 7) ^ (l >> 3)) << 4;
  const int xvo = (64 * i + 32 * (m >> 1) + 4 * (m & 1) + 8 * (l >> 5) + ((l >> 3) & 3)) * 896 + rsw;
  const int wvo = (4 * m + g) * 3584 + j * 256 + ((((c >> 1) ^ (g | ((m & 2) << 1))) << 5) | ((c & 1) << 4));
  int sb[7];
#pragma unroll
  for (int s = 0; s < 7; ++s) sb[s] = mod7(s + t.rot1);
  auto item = [&](auto nc) {
    constexpr int n = nc, s = n / 6, q = n % 6;
    if constexpr (n >= 42) dma<(n - 42) * 4096>(swin, wvo, (sb[(n - 42) >> 2] * 4 + ((n - 42) & 3)) * 57344, lb);
    else if constexpr (q < 2) dman<L_XR + (s & 3) * 8192 + q * 1024, NT_X>(sx_, xvo, sb[s] * 128 + q * 14336, lb2);
    else dma<(4 * s + q - 2) * 4096>(swin, wvo, (sb[s] * 4 + q - 2) * 57344 + 1792, lb);
  };
#define IC(n) std::integral_constant<int, (n)>{}
  const rsrc_t rwsp = mk(p.wsp, S_ * S_ * 2), rws = mk(p.ws, WS_BYTES);
  v4u ar[32];
  const int avo0 = (64 * i + 16 * m + (l >> 3)) * 4096 + (((l & 7) ^ g) << 4), avo1 = avo0 ^ 64;
  auto aso = [&](int n) { return (((n >> 1) + t.rot2) & 31) * 128 + (n & 1) * 32768; };
  auto lda = [&](int n) { const int avo = (n & 1) ? avo1 : avo0; ar[n] = n < 10 ? __builtin_amdgcn_raw_buffer_load_b128(rwsp, avo, aso(32 + n), NT_SP * 2) : ld16(rwsp, avo, aso(32 + n)); SB(); };
  auto items = [&](auto n0, auto cnt) { unr<cnt>([&](auto q) { constexpr int n = ORD.seq[n0 + q]; if constexpr (n < 70) item(IC(n)); else lda(n - 70); }); };
  constexpr const int *NI = ORD.bar;
  items(IC(0), IC(NI[0]));
  u64 tag = epoch_load(p.ws);
  EPOCH_WAIT(tag);
  tag += 1;
  {
    const v4u ones = {0x3f803f80u, 0x3f803f80u, 0x3f803f80u, 0x3f803f80u};
    f4 sx = {0, 0, 0, 0}, gr = {0, 0, 0, 0};
    int xo0 = L_XR + (16 * m + c) * 128 + ((g ^ (c & 7)) << 4), xo1 = xo0 ^ 64;
    PIN(xo0); PIN(xo1);
    unr<7>([&](auto sc) {
      constexpr int s = sc;
      VM(NI[s] - 1 - ORD.last(6 * s, 6 * s + 6) + (TLG && ORD.last(6 * s, 6 * s + 6) < NI[0]));
      v4u xs[2];
      xs[0] = lds16(L, xo0 + (s & 3) * 8192);
      xs[1] = lds16(L, xo1 + (s & 3) * 8192);
      STAMPA(2 * s + 1);
      BAR0();
      STAMPA(2 * s + 2);
      if constexpr (s < 6) items(IC(NI[s]), IC(NI[s + 1] - NI[s]));
      SB();
      if (PMS) __builtin_amdgcn_s_setprio(3);
#pragma unroll
      for (int q = 0; q < 2; ++q) { sx = mfma(xs[q], ones, sx); gr = mfma(xs[q], xs[q], gr); }
      SB();
      if (PMS) __builtin_amdgcn_s_setprio(1);
      SB();
      if constexpr (s == 6) items(IC(NI[6]), IC(LASTA));
    });
    SB();
    const int e = c & 3;
    const float s1 = e & 2 ? (e & 1 ? sx.w : sx.z) : (e & 1 ? sx.y : sx.x);
    const float s2 = e & 2 ? (e & 1 ? gr.w : gr.z) : (e & 1 ? gr.y : gr.x);
    const float mean = fmul(s1, 1.0f / 448.0f);
    const float r = inv_sigma(ffma(-mean, mean, fmul(s2, 1.0f / 448.0f)));
    if ((c >> 2) == g) {
      const int row = 32 * (m >> 1) + 8 * g + 4 * (m & 1) + e;
      *(LAS float *)(L + L_XST + row * 4) = rbf(mean);
      *(LAS float *)(L + L_XST + 256 + row * 4) = r;
    }
    SB();
    items(IC(NI[6] + LASTA), IC(NI[7] - NI[6] - LASTA));
  }
  BARW();
  STAMP(3);
  asm volatile("s_mov_b32 m0, %3\n\ts_nop 0\n\tbuffer_load_dword %0, %1, %2 offen lds" ::"v"(l * 4), "s"(mks(p.bsp, S_ * 2)), "s"(i * 128), "n"(L_BS) : "m0", "memory");
  unr<70 + NS - NI[7]>([&](auto q) {
    constexpr int n = NI[7] + q;
    VM(W1);
    if constexpr (n == ORD.last(42, 58) + 1 + W1) lds_signal(L, L_CNT);
    if constexpr (n == ORD.last(42, 70) + 1 + W1) lds_signal(L, L_CNT + 28);
    items(IC(n), IC(1));
  });
  static_assert(ORD.last(42, 58) + 1 + W1 < 70 + NS && NI[7] <= ORD.last(42, 58) + 1 + W1, "");
  STAMP(4);
  {
    const u32 tagA = (u32)tag, tagB = (u32)(tag >> 32) ^ 0x9e3779b9u;
    const int so = WS_ST + (i * 2 + (m >> 1)) * 8192 + (m & 1) * 256;
    const int vo = g * 512 + c * 16, vo3 = (g & 1) * 512 + c * 16 + 2048;
    v4u sg[4], zt[4];
    auto ldst = [&]() {
      sg[0] = ld16c(rws, vo, so); sg[1] = ld16c(rws, vo + 2048, so); sg[2] = ld16c(rws, vo, so + 4096); sg[3] = ld16c(rws, vo3, so + 4096);
    };
    lds_wait(L, L_CNT + 4, 4);
    __builtin_amdgcn_s_setprio(PM2);
    const int zo = ((m >> 1) * 8 + (l >> 5)) * 1024 + ((m & 1) * 32 + (l & 31)) * 16;
#pragma unroll
    for (int q = 0; q < 4; ++q) zt[q] = lds16(L, L_Z2 + zo + q * 2048);
    SB();
    if (PW < 60) VM(PW);
    ldst();
    SB();
#pragma unroll
    for (int n = 0; n < NB; ++n) lda(NS + n);
    SB();
    if constexpr (ORD.last(42, 70) + 1 + W1 >= 70 + NS) { VM(69 + NS - ORD.last(42, 70) + 4 + NB); lds_signal(L, L_CNT + 28); }
    SB();
    float zu[32];
#pragma unroll
    for (int q = 0; q < 16; ++q) { const u32 z = zt[q >> 2][q & 3]; zu[2 * q] = lo16(z); zu[2 * q + 1] = hi16(z); PIN(zu[2 * q]); PIN(zu[2 * q + 1]); }
    int zso = WS_ZB + (j * 64 + 2 * i) * 8192 + zo;
    PIN(zso);
    STAMP(5);
    auto late = [&]() {
      u32 d = 0;
#pragma unroll
      for (int q = 0; q < 4; ++q) d |= (sg[q].y ^ tagA) | (sg[q].w ^ tagB);
      return __builtin_amdgcn_ballot_w64(d != 0) != 0;
    };
    if (__builtin_expect(late(), 0)) {
#pragma unroll 1
      for (u32 s = 0; s < SPIN_CAP; ++s) {
        asm volatile("" ::: "memory");
        ldst();
        if (!late()) break;
      }
    }
    STAMP(2);
    float s = fadd(fadd(asf(sg[0].x), asf(sg[1].x)), fadd(asf(sg[2].x), g < 2 ? asf(sg[3].x) : 0.f));
    float u = fadd(fadd(asf(sg[0].z), asf(sg[1].z)), fadd(asf(sg[2].z), g < 2 ? asf(sg[3].z) : 0.f));
    {
      v2u a = __builtin_amdgcn_permlane16_swap(asu(s), asu(u), false, false);
      const float h = fadd(asf(a.x), asf(a.y));
      a = __builtin_amdgcn_permlane32_swap(asu(h), asu(h), false, false);
      const float k = fadd(asf(a.x), asf(a.y));
      a = __builtin_amdgcn_permlane16_swap(asu(k), asu(k), false, false);
      s = asf(a.x);
      u = asf(a.y);
    }
    const float mean = fmul(s, 1.0f / 896.0f);
    const float r = inv_sigma(ffma(-mean, mean, fmul(u, 1.0f / 896.0f)));
    if (g < 2) *(LAS float *)(L + L_ZST + g * 256 + (16 * m + c) * 4) = g ? r : -rbf(mean) * r;
    SB();
    const u32 cl = HZ && CLZ == 15 ? lds_peek(L, L_CNT + 52) : 0;
    f4 zb[2], zr[2];
#pragma unroll
    for (int q = 0; q < 2; ++q) {
      zb[q] = ldsf4(L, L_ZST + (16 * m + 8 * (g & 1) + 4 * q) * 4);
      zr[q] = ldsf4(L, L_ZST + 256 + (16 * m + 8 * (g & 1) + 4 * q) * 4);
    }
    SB();
#pragma unroll
    for (int n = 0; n < NB2; ++n) lda(NS + NB + n);
    if (HZ && CLZ == 15) lds_check(L, L_CNT + 52, 4, cl);
#pragma unroll
    for (int q = 0; q < 4; ++q) {
      u32 o[4];
#pragma unroll
      for (int e = 0; e < 4; ++e) {
        o[e] = pk(ffma(zu[8 * q + 2 * e], zr[e >> 1][(2 * e) & 3], zb[e >> 1][(2 * e) & 3]), ffma(zu[8 * q + 2 * e + 1], zr[e >> 1][(2 * e + 1) & 3], zb[e >> 1][(2 * e + 1) & 3]));
      }
      st16c(v4u{o[0], o[1], o[2], o[3]}, rws, zso, q * 2048);
      SB();
    }
    if (HZ) if (lds_last(L, L_CNT + 24)) setflag(p.ws, F_Z + j * 32 + i, tag);
    STAMP(6);
  }
  __builtin_amdgcn_s_setprio(PM3);
  constexpr int NA = NS + NB + NB2, NQ = 64 - NA;
  auto zflag = [&]() {
    if (!HZ) if (lds_last(L, L_CNT + 24)) setflag(p.ws, F_Z + j * 32 + i, tag);
    if (HZ) if (lds_last(L, L_CNT + 48)) setflag(p.ws, F_ZD + j * 32 + i, tag);
    SB(); STAMP(7);
  };
  auto ring = [&](auto nc) { constexpr int n = nc; dman<(n >> 1) * 8192 + (n & 1) * 1024, NT_RING>(swsp, (n & 1) ? avo1 : avo0, aso(n), lb2); };
  auto piece = [&](auto qc) {
    constexpr int n = qc - NR1;
    if constexpr (n < 0) lda(NA + qc); else if constexpr (n < 32) ring(IC(n < 0 || n > 31 ? 0 : n)); else lda(NA + qc - 32);
  };
  constexpr int XE[4] = {NR1 + X0 < NQ ? NR1 + X0 : NQ, NR1 + X1 < NQ ? NR1 + X1 : NQ, NR1 + X2 < NQ ? NR1 + X2 : NQ, NQ};
  static_assert(X0 >= 14 + 2 * AD && X1 >= 22 + 2 * AD && X2 >= 32 && NA + NR1 >= 19 && NA + NR1 <= 32 && W2 < NR1 + 5 + 2 * AD + RD, "");
  unr<XE[0]>([&](auto qc) {
    constexpr int n = qc - NR1;
    if constexpr (qc == W2) { VM(W2); zflag(); }
    if constexpr (qc > W2 && n < 14 + 2 * AD + RD) VM(RD - 1);
    if constexpr (n == 0) { lds_wait(L, L_CNT + 8, 4); STAMP(8); }
    if constexpr (n == 5 + 2 * AD + RD) lds_signal(L, L_CNT + 12);
    if constexpr (n == 28) lds_wait(L, L_CNT + 24, 4);
    if constexpr (n == ZP) if (m == 0) { wait_flags(p.ws, fl(F_Z + j * 32 + (l & 31)), tag); lds_signal(L, L_CNT + 16); }
    piece(qc);
  });
  STAMP(11);
  __builtin_amdgcn_s_setprio(0);
  const srd_t swout = mks(p.wout, 896 * D_ * 2);
  auto ldt = [&](auto nc) { constexpr int n = nc; dma<n * 4096>(swout, pvo, WSO(n), lb); SB(); __builtin_amdgcn_s_sleep(TS); SB(); };
  static_assert(T5 <= 8 && T6 <= 16 && T5 <= T6 && T6 <= NO0, "");
  v4u wr[28];
  const rsrc_t rwo = mk(p.wout, 896 * D_ * 2);
  auto trk = [&](auto cc) {
    constexpr int tr[7] = TRK, ck = cc;
    int n = NO1;
    for (int q = 0; q < ck; ++q) n += tr[q];
    if (RE == 3)
#pragma unroll
      for (int q = 0; q < tr[ck]; ++q) { if (q) __builtin_amdgcn_s_sleep(TRS); SB(); wr[n + q] = ld16(rwo, pvo, WSO(n + q)); SB(); }
  };
  int awo = m * 2048 + l * 16, awh = awo + 65536;
  PIN(awo); PIN(awh);
  {
    constexpr int cum[6] = {0, 6, 12, 19, 25, 32};
    unr<5>([&](auto kc) {
      constexpr int k = kc;
      if constexpr (k < 2) VM(XE[k] - NR1 - 14 - 2 * AD - 8 * k);
      if constexpr (k == 2) VM(XE[2] - NR1 - 32);
      BARW();
      if constexpr (TD && k + TD < 8) { ztouch(p, t, k + TD, m, l); SB(); }
      if constexpr (HU && k == CLU) {
        const int o = WS_UB + (i * 112 + 4 * j) * 1024 + m * 28672 + l * 16;
#pragma unroll
        for (int q = 0; q < 4; ++q) st16c(v4u{~0u, ~0u, ~0u, ~0u}, rws, o, q * 1024);
        SB();
      }
      constexpr int nq = k < 3 ? XE[k + 1] - XE[k] : 0, nw = cum[k + 1] - cum[k];
      unr<(nq > nw ? nq : nw)>([&](auto qc) {
        constexpr int n = cum[k] + qc;
        if constexpr (qc < nq) piece(IC(XE[k < 3 ? k : 0] + qc));
        if constexpr (qc < nw) sts16(L, ((n & 16) ? awh : awo) + ((n >> 1) & 7) * 8192 + (n & 1) * 1024, ar[n]);
        SB();
        __builtin_amdgcn_s_sleep(WSL);
        SB();
      });
      trk(IC(k + 1));
    });
  }
  unr<T5>(ldt);
  BARW();
  unr<T6 - T5>([&](auto q) { ldt(IC(T5 + q)); });
  VM(T6);
  trk(IC(6));
  BAR0();
  STAMP(9);
  {
    auto ldo = [&](auto nc) { constexpr int n = nc; VM(WOW); dma<n * 4096>(swout, pvo, WSO(n), lb); };
    const srd_t sws = mks(p.ws, WS_BYTES);
    const int yfo = fl(F_Y + ((64 * j + 16 * m + min(g * 7, 15)) / 14) * 4 + (l & 3));
    const int yvo = (64 * j + 16 * m + (l >> 3)) * 4096 + i * 128 + (((((l & 7) >> 1) ^ g) << 5) | ((l & 1) << 4));
    auto ldy = [&]() { dmac<L_Z2>(sws, yvo, WS_YB, lb2); dmac<L_Z2 + 1024>(sws, yvo, WS_YB + 32768, lb2); };
    if (XE_) {
      const rsrc_t rx = mk(p.x, S_ * D_ * 2);
      const int xto = (64 * i + 16 * m + (l >> 3)) * 896 + j * 128 + (((l & 7) ^ g) << 4);
      ar[0] = ld16(rx, xto, 0);
      ar[1] = ld16(rx, xto ^ 64, 7168);
      SB();
    }
    __builtin_amdgcn_s_sleep(WSLP);
    static_assert(NO1 >= 4 + OP && NO1 <= 24, "");
    unr<NO0 - T6>([&](auto q) { ldo(IC(T6 + q)); });
    VM(NO0 - K1);
    lds_signal(L, L_CNT + 44);
    STAMP(1);
    const int xto = (64 * i + 16 * m + (l >> 3)) * 896 + j * 128 + (((l & 7) ^ g) << 4);
    lds_wait(L, L_CNT + 36, 4);
    __builtin_amdgcn_s_setprio(PMT);
    if (XAT == 4 && !XE_) { dma<L_Z2 + 8192>(sx_, xto, 0, lb2); dma<L_Z2 + 9216>(sx_, xto ^ 64, 7168, lb2); }
    unr<NO1 - NO0>([&](auto q) { ldo(std::integral_constant<int, NO0 + q>{}); });
    const rsrc_t rwout = mk(p.wout, 896 * D_ * 2);
#pragma unroll
    for (int n = NO1; n < 28; ++n) {
      if (n == NO1 + SIGE) { VM(SIGE + (XAT == 4 && !XE_ && NO0 == NO1 ? 2 : 0)); lds_signal(L, L_CNT + 40); STAMP(10); }
      ar[n] = ld16(rwout, pvo, WSO(n));
      SB();
    }
    STAMP(13);
    int wwo = m * 1024 + l * 16 + 49152;
    PIN(wwo);
    if (XE_) { const int xwo = L_Z2 + 8192 + m * 2048 + l * 16; sts16(L, xwo, ar[0]); sts16(L, xwo + 1024, ar[1]); SB(); }
#pragma unroll
    for (int n = NO1; n < 28; ++n) { sts16(L, wwo + (n - 12) * 4096, ar[n]); SB(); }
    STAMP(12);
    const bool early = !YLATE && __all(ldflag(p.ws, yfo) == tag);
    if (early) ldy();
    if (YLATE && !RREG) DRAIN();
    BARW();
    if ((HZ || HU) && m == 0) setflag(p.ws, F_UD + i * 8 + j, tag);
    if (!early) { wait_flags(p.ws, yfo, tag); ldy(); }
  }
  DRAIN();
  BAR0();
  STAMP_OUT(1);
}

constexpr int C_CP = 4160, C_KV = 4 * C_CP, C_SLOT = C_KV + 4608;

constexpr int C_EX = 131072;

template <int H> static __device__ __forceinline__ void conv_compute(const Params &p, LAS unsigned char *L, int v, int w) {
  STAMP_DECL;
  STAMP(0);
  __builtin_amdgcn_s_setprio(PCV);
  u64 tag = epoch_load(p.ws);
  constexpr int ord[8] = {H ? 0 : 2, H ? 1 : 3, H ? 6 : 4, H ? 7 : 5, H ? 2 : 0, H ? 3 : 1, H ? 4 : 6, H ? 5 : 7};
  const int l = threadIdx.x & 63, g = l >> 4, c = l & 15, ch = w >> 1;
  const rsrc_t rws = mk(p.ws, WS_BYTES);
  {
    const int t = threadIdx.x;
    const srd_t sws = mks(p.ws, WS_BYTES);
    touch(sws, t < 128 ? WS_ST + v * 16384 + t * 128 : WS_FL + (v * 52 + min(t & 63, 51)) * 128, 0);
    __builtin_amdgcn_s_sleep(TSLEEP);
    const srd_t swsp = mks(p.wsp, S_ * S_ * 2);
#pragma unroll
    for (int q = 0; q < 2; ++q) touch(swsp, t * 512, v * 262144 + q * 131072);
  }
  const int hro = ch * C_SLOT + (c & 3) * C_CP + g * 16 + (c >> 2) * 8 + H * 64;
  const int kro = ch * C_SLOT + C_KV + (254 - 2 * c + g) * 16 - 7 * 512 + H * 64;
  v2u hh[2][16];
  v4u kf[2][8];
  auto rd = [&](int ho, int ko, int q, int n) {
    const int d = n / 3;
    if (n % 3 == 2) kf[q & 1][7 - d] = lds16(L, ko + d * 512 + q * 128);
    else hh[q & 1][2 * d + n % 3] = *(LAS v2u *)(L + ho + d * 512 + q * 128 + (n % 3) * 8);
  };
  f4 acc[8], in[4];
#pragma unroll
  for (int n = 0; n < 8; ++n) acc[n] = f4{0, 0, 0, 0};
  int xw = C_EX + w * 4096 + l * 16, xr = xw ^ 4096;
  int yo = WS_YB + (14 * v + ch) * 4096 + c * 32 + (g & 1) * 512 + (g >> 1) * 16 + 0x40000000;
  PIN(xw); PIN(xr);
  v2u y[4];
  auto fin = [&](int k) {
    if (k < 2) { in[2 * k] = ldsf4(L, xr + k * 2048); in[2 * k + 1] = ldsf4(L, xr + k * 2048 + 1024); }
    else if (k < 10) { const int n = (k - 2) >> 1, e = (k & 1) * 2; in[n][e] = fadd(in[n][e], acc[ord[4 + n]][e]); in[n][e + 1] = fadd(in[n][e + 1], acc[ord[4 + n]][e + 1]); }
    else if (k < 14) { const int n = k - 10; y[n] = v2u{pk(in[n].x, in[n].y), pk(in[n].z, in[n].w)}; PIN(y[n]); }
    else if (k < 16) st16c(widen(y[2 * k - 28], y[2 * k - 27]), rws, yo, ord[2 * k - 24] * 512);
  };
  EPOCH_WAIT(tag);
  tag += 1;
  if (KV1) BAR0();
  BAR0();
  STAMP(2);
#pragma unroll
  for (int n = 0; n < 24; ++n) { rd(hro, kro, 0, n); SB(); }
  WAITC(63, 0);
  int set = 0;
#pragma unroll 1
  for (int rho = 0; rho < 7; ++rho) {
    const int nset = set == 4 * C_SLOT ? 0 : set + 2 * C_SLOT;
    int ho = hro + set, ko = kro + set;
    unr<4>([&](auto qc) {
      constexpr int q = qc;
      if (q == 3) { ho = hro + nset - 512; ko = kro + nset - 512; PIN(ho); PIN(ko); }
      if (q > 0) WAITC(63, 0);
      if (q == 3) BAR0();
      SB();
      int k = 0;
#pragma unroll
      for (int hf = 0; hf < 2; ++hf)
#pragma unroll
        for (int P = 0; P < 8; ++P)
#pragma unroll
          for (int n = 0; n < 4; ++n) {
            const int tl = ord[4 * hf + n];
            if (P > tl) continue;
            const v2u a = hh[q & 1][2 * P], b = hh[q & 1][2 * P + 1];
            if (q == 0 && P == 0) mma0(acc[tl], v4u{a.x, a.y, b.x, b.y}, kf[q & 1][tl - P]);
            else mma(acc[tl], v4u{a.x, a.y, b.x, b.y}, kf[q & 1][tl - P]);
            SB();
            if (q == 0) {
              if (k < 2) fin(k);
              else if (k < 9) { rd(ho, ko, 1, 2 * k - 4); SB(); rd(ho, ko, 1, 2 * k - 3); }
              else if (k < 17) fin(k - 7);
              else if (k < 22) { rd(ho, ko, 1, 2 * k - 20); SB(); rd(ho, ko, 1, 2 * k - 19); }
              else if (k < 26) fin(k - 12);
            } else if (k < 24) rd(ho, ko, q + 1, k);
            else if (q == 2 && (k == 27 || k == 30)) fin(k / 3 + 5);
            else if (q == 3 && k < 28) sts16(L, xw + (k - 24) * 1024, __builtin_bit_cast(v4u, acc[ord[k - 24]]));
            SB();
            ++k;
          }
    });
    yo += rho ? 8192 : -0x40000000;
    set = nset;
    WAITC(63, 0);
    BAR0();
    if (rho == 0) STAMP(3);
    if (rho == 1) STAMP(4);
    if (rho == 3) STAMP(5);
    if (rho == 5) STAMP(6);
  }
  SETTLE();
#pragma unroll
  for (int k = 0; k < 16; ++k) fin(k);
  STAMP(7);
  DRAIN();
  setflag(p.ws, F_Y + v * 4 + w, tag);
  STAMP_OUT(0);
}

static __device__ __forceinline__ void conv_stage(const Params &p, LAS unsigned char *L, int v, int m) {
  STAMP_DECL;
  STAMP(0);
  const int l = threadIdx.x & 63;
  const srd_t sh = mks(p.h, D_ * S_ * 2), sws = mks(p.ws, WS_BYTES), swout = mks(p.wout, 896 * D_ * 2);
  const rsrc_t rws = mk(p.ws, WS_BYTES);
  const int vo = m * 1024 + l * 16;
  auto stage_h = [&](int rho, int set) {
    const int so = (14 * v + 2 * rho) * 4096, lb = set + m * 1024;
    unr<8>([&](auto qc) { constexpr int q = qc; dman<(q >> 2) * C_SLOT + (q & 3) * C_CP + 32, NT_H>(sh, vo, so + (q >> 2) * 4096 + (q & 3) * 2 + 2, lb); });
  };
  auto stage_kv = [&](int rho, int set) {
    const int so = WS_KVR + (14 * v + 2 * rho) * 4096, lb = set + m * 1024;
    dmac<C_KV>(sws, vo, so, lb);
    dmac<C_SLOT + C_KV>(sws, vo, so + 4096, lb);
  };
  u64 head;
  asm volatile("global_load_dwordx2 %0, %1, %2" : "=&v"(head) : "v"((14 * v + (l < 56 ? l >> 2 : 0)) * 4096), "s"(p.h) : "memory");
  stage_h(0, 0);
  u64 tag = epoch_load(p.ws);
  stage_h(1, 2 * C_SLOT);
  asm volatile("s_waitcnt vmcnt(16)" : "+v"(head)::"memory");
  head <<= 48 - 16 * (l & 3);
  auto put_head = [&](int rho, int set) {
    if (m == 0 && (l >> 3) == rho) *(LAS u64 *)(L + set + ((l >> 2) & 1) * C_SLOT + (l & 3) * C_CP + 24) = head;
  };
  {
    const v4u z = {0, 0, 0, 0};
    const int t = m * 64 + l;
    if (t < 192) sts16(L, (t >> 5) * C_SLOT + C_KV + 4096 + (t & 31) * 16, z);
    if (t < 24) { sts16(L, (t >> 2) * C_SLOT + (t & 3) * C_CP, z); *(LAS u64 *)(L + (t >> 2) * C_SLOT + (t & 3) * C_CP + 16) = 0; }
  }
  put_head(0, 0);
  put_head(1, 2 * C_SLOT);
  put_head(2, 4 * C_SLOT);
  EPOCH_WAIT(tag);
  tag += 1;
  if (m == 0) {
    __builtin_amdgcn_s_sleep(KSLEEP);
    wait_flags(p.ws, flk(F_KV + ((14 * v + (l < 32 ? 0 : 13)) >> 4) * 32 + (l & 31)), tag);
  }
  BAR0();
  STAMP(1);
  stage_kv(0, 0);
  stage_kv(1, 2 * C_SLOT);
  VM(2);
  BARW();
  stage_h(2, 4 * C_SLOT);
  stage_kv(2, 4 * C_SLOT);
  STAMP(2);
  setflagk(p.ws, F_HI + v * 4 + m, tag);
#define TSP2 0
  int set = 0;
#pragma unroll 1
  for (int rho = 0; rho < 7; ++rho) {
    if (rho == 2) STAMP(3);
    if (rho == 3) STAMP(8);
    if (rho == 0) VM(11 + TSP2); else if (rho >= 5) DRAIN(); else if (rho > WOR && rho <= WOR + 4 / WOK) VM(10 + WTN * WOK); else VM(10);
    if (rho == 2) STAMP(4);
    if (rho == 3) STAMP(9);
    BARW();
    if (rho == 2) STAMP(5);
    if (rho == 3) STAMP(10);
    BAR0();
    if (rho == 2) STAMP(6);
    if (rho == 3) STAMP(11);
    if (rho < 4) {
      stage_h(rho + 3, set);
      if (rho == 2) STAMP(7);
      if (rho == 3) STAMP(12);
      stage_kv(rho + 3, set);
      put_head(rho + 3, set);
    }
    if (rho >= WOR && rho < WOR + 4 / WOK) {
#pragma unroll
      for (int q = 0; q < WOK; ++q) {
        const int n = ((v & 3) * 4 + (rho - WOR) * WOK + q) * 224 + m * 56 + min(l, 55);
        touch(swout, (n >> 2) * 896 + ((n & 3) + 3 * ((v >> 2) & 1)) * 128, 0);
      }
    }
    set = set == 4 * C_SLOT ? 0 : set + 2 * C_SLOT;
    if (rho == 4 && v == 0 && m == 0) {
#pragma unroll 1
      for (u32 s = 0; s < SPIN_CAP; ++s) {
        u64 f[16], d = 0;
#pragma unroll
        for (int q = 0; q < 16; ++q) f[q] = ldflag(p.ws, flk(q * 64 + l));
#pragma unroll
        for (int q = 0; q < 16; ++q) d |= f[q] ^ tag;
        if (__all(d == 0)) break;
        asm volatile("" ::: "memory");
      }
      if (l == 0) stflag(p.ws, WS_EPOCH, tag);
    }
  }
  STAMP_OUT(1);
}

#define CPFW 4
__global__ void __launch_bounds__(512, 2) fused_kernel(Params p) {
  extern __shared__ __attribute__((aligned(16))) unsigned char smem[];
  LAS unsigned char *L = (LAS unsigned char *)smem;
  const int b = blockIdx.x, x = b & 7, r = b >> 3, wv = __builtin_amdgcn_readfirstlane(threadIdx.x >> 6);
  {
    u64 pc;
    const u32 line = min((u32)(r * 16 + (threadIdx.x & 15)), (u32)CODE_LINES);
    asm volatile("s_cmp_lg_u32 %2, %4\n\ts_cbranch_scc1 1f\n\ts_getpc_b64 %0\n\ts_mov_b32 m0, %3\n\ts_nop 0\n\tglobal_load_lds_dword %1, %0\n1:\n\t"
                 "s_setprio 3\n\ts_cmp_lt_u32 %2, 4\n\ts_cbranch_scc1 2f\n\ts_setprio 1\n2:"
                 : "=&s"(pc) : "v"(line * 128), "s"(wv), "n"(LDS_BYTES - 256), "n"(CPFW) : "m0", "scc", "memory");
  }
  if (r >= 28) {
    const int v = x * 4 + (r - 28);
    if (wv < 4) { if (wv & 1) conv_compute<1>(p, L, v, wv); else conv_compute<0>(p, L, v, wv); }
    else conv_stage(p, L, v, wv - 4);
  } else {
    Tile t;
    const int pg = x >> 1, o = x & 1;
    if (r < 24) { t.i = 8 * pg + r / 3; t.j = 4 * o + r % 3; }
    else { t.i = 8 * pg + 4 * o + (r - 24); t.j = 3; }
    t.rot1 = 2 * ((t.i + t.j) & 3);
    t.rot2 = (8 * pg + 8 + (32 * (t.j == 3 ? 6 : 2 * (t.j & 3) + o)) / 7 + DELTA * (t.i & 7)) & 31;
    t.e = r < 24 ? t.i & 7 : 2 * (t.i & 3);
    t.ne = r < 24 ? 1 : 2;
    v2u shk = {0, 0};
    u32 dvr = 0;
    if (wv < 4) kv_fetch(p, t, wv, shk, dvr);
    SB();
    asm volatile("" ::: "memory");
    if (wv < 4) gemm_compute(p, L, t, wv, shk, dvr);
    else gemm_move(p, L, t, wv - 4);
  }
}

extern "C" void kernel_launch(void *const *d_in, const int *in_sizes, int n_in, void *d_out, int out_size, void *d_ws, size_t ws_size,
                              hipStream_t stream) {
  Params p;
  p.x = (const u16 *)d_in[0];
  p.k = (const u16 *)d_in[1];
  p.v = (const u16 *)d_in[2];
  p.shk = (const u16 *)d_in[3];
  p.h = (const u16 *)d_in[4];
  p.dv = (const u16 *)d_in[5];
  p.win = (const u16 *)d_in[7];
  p.wsp = (const u16 *)d_in[8];
  p.bsp = (const u16 *)d_in[9];
  p.wout = (const u16 *)d_in[10];
  p.out = (u16 *)d_out;
  p.ws = (unsigned char *)d_ws;
  (void)hipFuncSetAttribute((const void *)fused_kernel, hipFuncAttributeMaxDynamicSharedMemorySize, LDS_BYTES);
  fused_kernel<<<256, 512, LDS_BYTES, stream>>>(p);
}
```
